```python
import torch

def _softmax(x, axis=-1):
    return torch.softmax(x, dim=axis)
def _gelu(x):
    return torch.nn.functional.gelu(x, approximate="tanh")
def _silu(x):
    return torch.nn.functional.silu(x)
def _sigmoid(x):
    return torch.sigmoid(x)
def _layernorm(x, axis=-1, eps=1e-5):
    m = x.mean(dim=axis, keepdim=True)
    v = x.var(dim=axis, keepdim=True, unbiased=False)
    return (x - m) / torch.sqrt(v + eps)
def _scatter_add(tgt, idx, src, axis=0):
    return tgt.clone().index_add_(axis, idx, src)
def _segment_sum(src, seg, n):
    out = torch.zeros((n,) + src.shape[1:], dtype=src.dtype)
    return out.index_add_(0, seg, src)
def _pad(x, pw):
    flat = tuple(v for pair in reversed(pw) for v in pair)
    return torch.nn.functional.pad(x, flat)

kernel_name = 'recurrent_scan__spectral_gfnet__glu_gated_reglu__prompt'

def setup_inputs():

    _w = (lambda a: (lambda t: t.to(torch.bfloat16) if t.is_floating_point() else t)(torch.as_tensor(a)))
    def _A_setup():
        B, S, D = 1, 2048, 512
        Df = D // 2 + 1
        return {
            "x": _w((lambda *s: torch.randn(*s, dtype=torch.bfloat16) * 0.2)(B, S, D)),
            "w_r": _w((lambda *s: torch.randn(*s, dtype=torch.bfloat16) * 1.6)(S, Df) * 0.02),
            "w_i": _w((lambda *s: torch.randn(*s, dtype=torch.bfloat16) * 1.6)(S, Df) * 0.02),
        }
    _A_in = _A_setup()

    def _B_setup():
        B, S, D, F = 1, 2048, 512, 1792
        return {
            "x": _w((lambda *s: torch.randn(*s, dtype=torch.bfloat16) * 0.2)(B, S, D)),
            "W_gate": _w((lambda *s: torch.randn(*s, dtype=torch.bfloat16) * 0.8)(D, F) * (2.0 / D) ** 0.5),
            "W_up": _w((lambda *s: torch.randn(*s, dtype=torch.bfloat16) * 0.8)(D, F) * (2.0 / D) ** 0.5),
            "W_down": _w((lambda *s: torch.randn(*s, dtype=torch.bfloat16) * 0.8)(F, D) * (1.0 / F) ** 0.5),
        }
    _B_in = _B_setup()

    return {
        "A_x": _A_in["x"],
        "A_w_r": _A_in["w_r"],
        "A_w_i": _A_in["w_i"],
        "B_x": _B_in["x"],
        "B_W_gate": _B_in["W_gate"],
        "B_W_up": _B_in["W_up"],
        "B_W_down": _B_in["W_down"],
    }

def reference(A_x, A_w_r, A_w_i, B_x, B_W_gate, B_W_up, B_W_down):
    def _A_ref(x, w_r, w_i):
        B, S, D = x.shape
        X = (lambda x, axes=(-2,-1): torch.fft.rfft2(x.float(), dim=axes))(x, axes=(-2, -1))
        W = w_r + 1j * w_i
        X = X * W
        y = (lambda x, s, axes=(-2,-1): torch.fft.irfft2(x, s=s, dim=axes).to(torch.bfloat16))(X, (S, D), axes=(-2, -1))
        return y

    def _B_ref(x, W_gate, W_up, W_down):
        gate = torch.einsum("bsd,df->bsf", x, W_gate)
        up = torch.einsum("bsd,df->bsf", x, W_up)
        gate = gate * (lambda x, dt: x.to(dt))(gate > 0, gate.dtype)
        h = gate * up
        return torch.einsum("bsf,fd->bsd", h, W_down)

    x = A_x
    xn = _layernorm(x, axis=-1)
    s = _A_ref(xn, A_w_r, A_w_i)
    g = _silu(_B_ref(xn, B_W_gate, B_W_up, B_W_down))
    return x + s * g
```

```cpp
#include <hip/hip_runtime.h>
#include <hip/hip_bf16.h>
#include <math.h>

typedef __hip_bfloat16 bf16;
constexpr int S = 2048, D = 512, DF = 257, F = 1792;

__device__ __forceinline__ float bf2f(bf16 v) { return __bfloat162float(v); }
__device__ __forceinline__ float rbf(float v) { return __bfloat162float(__float2bfloat16(v)); }

__global__ void k_ln(const bf16* x, float* xn) {
  int s = blockIdx.x; int t = threadIdx.x;
  float v[8]; float sum = 0;
  for (int j = 0; j < 8; ++j) { v[j] = bf2f(x[s * D + t * 8 + j]); sum += v[j]; }
  for (int o = 32; o; o >>= 1) sum += __shfl_xor(sum, o);
  float m = sum / D; float q = 0;
  for (int j = 0; j < 8; ++j) { float d = v[j] - m; q += d * d; }
  for (int o = 32; o; o >>= 1) q += __shfl_xor(q, o);
  float var = rbf(q / D); float mb = rbf(m);
  float sd = rbf(sqrtf(rbf(var + 1e-5f)));
  for (int j = 0; j < 8; ++j) xn[s * D + t * 8 + j] = rbf(rbf(v[j] - mb) / sd);
}
__global__ void k_rowdft(const float* xn, float2* X1) {
  __shared__ float row[D]; __shared__ float2 tw[D];
  int s = blockIdx.x; int t = threadIdx.x;
  for (int i = t; i < D; i += blockDim.x) { row[i] = xn[s * D + i]; float sn, cs; sincospif(-2.0f * i / D, &sn, &cs); tw[i] = make_float2(cs, sn); }
  __syncthreads();
  if (t < DF) {
    float re = 0, im = 0;
    for (int d = 0; d < D; ++d) { float2 w = tw[(d * t) & (D - 1)]; re += row[d] * w.x; im += row[d] * w.y; }
    X1[(size_t)t * S + s] = make_float2(re, im);
  }
}
__global__ void k_coldft(const float2* in, float2* out, const bf16* wr, const bf16* wi, int dir) {
  __shared__ float2 col[S]; __shared__ float2 tw[S];
  int k = blockIdx.x; int t = threadIdx.x;
  for (int i = t; i < S; i += blockDim.x) { col[i] = in[(size_t)k * S + i]; float sn, cs; sincospif(dir * 2.0f * i / S, &sn, &cs); tw[i] = make_float2(cs, sn); }
  __syncthreads();
  int o = blockIdx.y * 256 + t;
  float re = 0, im = 0;
  for (int n = 0; n < S; ++n) { float2 w = tw[(n * o) & (S - 1)]; float2 c = col[n]; re += c.x * w.x - c.y * w.y; im += c.x * w.y + c.y * w.x; }
  if (dir < 0) { float a = bf2f(wr[o * DF + k]), b = bf2f(wi[o * DF + k]); float r2 = re * a - im * b, i2 = re * b + im * a; re = r2; im = i2; }
  out[(size_t)k * S + o] = make_float2(re, im);
}
__global__ void k_gemm1(const float* xn, const bf16* Wg, const bf16* Wu, float* h) {
  __shared__ float rows[8][D];
  int s0 = blockIdx.x * 8; int f = blockIdx.y * 256 + threadIdx.x;
  for (int i = threadIdx.x; i < 8 * D; i += 256) rows[i / D][i % D] = xn[s0 * D + i];
  __syncthreads();
  float g[8] = {}, u[8] = {};
  for (int d = 0; d < D; ++d) { float a = bf2f(Wg[d * F + f]), b = bf2f(Wu[d * F + f]);
    for (int r = 0; r < 8; ++r) { g[r] += rows[r][d] * a; u[r] += rows[r][d] * b; } }
  for (int r = 0; r < 8; ++r) { float gg = rbf(g[r]), uu = rbf(u[r]); gg = gg > 0 ? gg : 0.f; h[(size_t)(s0 + r) * F + f] = rbf(gg * uu); }
}
__global__ void k_final(const bf16* x, const float* h, const bf16* Wd, const float2* X3, bf16* out) {
  __shared__ float hr[4][F]; __shared__ float2 sp[4][DF]; __shared__ float2 tw[D];
  int s0 = blockIdx.x * 4; int d = threadIdx.x;
  for (int i = d; i < 4 * F; i += 512) hr[i / F][i % F] = h[(size_t)s0 * F + i];
  for (int i = d; i < 4 * DF; i += 512) sp[i / DF][i % DF] = X3[(size_t)(i % DF) * S + s0 + i / DF];
  { float sn, cs; sincospif(2.0f * d / D, &sn, &cs); tw[d] = make_float2(cs, sn); }
  __syncthreads();
  float acc[4] = {};
  for (int f = 0; f < F; ++f) { float w = bf2f(Wd[f * D + d]); for (int r = 0; r < 4; ++r) acc[r] += hr[r][f] * w; }
  for (int r = 0; r < 4; ++r) {
    float y = sp[r][0].x + ((d & 1) ? -sp[r][256].x : sp[r][256].x);
    for (int k = 1; k < 256; ++k) { float2 w = tw[(k * d) & (D - 1)]; y += 2.f * (sp[r][k].x * w.x - sp[r][k].y * w.y); }
    y *= 1.0f / (float)(S * D);
    float sv = rbf(y); float o = rbf(acc[r]); float g = rbf(o / (1.f + expf(-o)));
    float xv = bf2f(x[(s0 + r) * D + d]);
    out[(s0 + r) * D + d] = __float2bfloat16(xv + rbf(sv * g));
  }
}

extern "C" void kernel_launch(void* const* d_in, const int* in_sizes, int n_in,
                              void* d_out, int out_size, void* d_ws, size_t ws_size,
                              hipStream_t stream) {
  const bf16* x = (const bf16*)d_in[0]; const bf16* wr = (const bf16*)d_in[1]; const bf16* wi = (const bf16*)d_in[2];
  const bf16* Wg = (const bf16*)d_in[4]; const bf16* Wu = (const bf16*)d_in[5]; const bf16* Wd = (const bf16*)d_in[6];
  char* ws = (char*)d_ws;
  float* xn = (float*)ws; float2* X1 = (float2*)(ws + (8 << 20)); float2* X2 = (float2*)(ws + (16 << 20)); float2* X3 = (float2*)(ws + (24 << 20));
  float* h = (float*)(ws + (32 << 20));
  k_ln<<<S, 64, 0, stream>>>(x, xn);
  k_rowdft<<<S, 320, 0, stream>>>(xn, X1);
  k_coldft<<<dim3(DF, 8), 256, 0, stream>>>(X1, X2, wr, wi, -1);
  k_coldft<<<dim3(DF, 8), 256, 0, stream>>>(X2, X3, wr, wi, 1);
  k_gemm1<<<dim3(S / 8, F / 256), 256, 0, stream>>>(xn, Wg, Wu, h);
  k_final<<<S / 4, 512, 0, stream>>>(x, h, Wd, X3, (bf16*)d_out);
}
```

```cpp
#include <hip/hip_runtime.h>
#include <stdint.h>

typedef unsigned short u16;
typedef unsigned int u32;
typedef unsigned long long u64;
typedef short v4s __attribute__((ext_vector_type(4)));
typedef short v8s __attribute__((ext_vector_type(8)));
typedef _Float16 v2h __attribute__((ext_vector_type(2)));
typedef _Float16 v4h __attribute__((ext_vector_type(4)));
typedef _Float16 v8h __attribute__((ext_vector_type(8)));
typedef __bf16 v2b __attribute__((ext_vector_type(2)));
typedef __bf16 v8b __attribute__((ext_vector_type(8)));
typedef float v2f __attribute__((ext_vector_type(2)));
typedef float v4f __attribute__((ext_vector_type(4)));
typedef float v16f __attribute__((ext_vector_type(16)));
typedef u32 v2u __attribute__((ext_vector_type(2)));
typedef u32 v4u __attribute__((ext_vector_type(4)));

#define LAS __attribute__((address_space(3)))
#define GAS __attribute__((address_space(1)))
#define DEVI __device__ __forceinline__

#define SPLIT_P2 0

constexpr int S_ = 2048, D_ = 512, F_ = 1792, DF_ = 257;
constexpr size_t OFF_H = 0;
constexpr size_t OFF_S1 = (size_t)8 << 20;
constexpr size_t OFF_WT = (size_t)10 << 20;
constexpr size_t OFF_S2 = (size_t)13 << 20;
constexpr size_t OFF_MAX = (size_t)15 << 20;
constexpr size_t OFF_FLAG = ((size_t)15 << 20) + 4096;

template <int I> struct IC { static constexpr int v = I; constexpr operator int() const { return I; } };
template <int B, int E, class Fn> DEVI void static_for(Fn&& f) {
  if constexpr (B < E) { f(IC<B>{}); static_for<B + 1, E>(f); }
}

#define WAIT_VM(n) asm volatile("s_waitcnt vmcnt(%0)" ::"n"(n) : "memory")
#define BARRIER() asm volatile("s_waitcnt lgkmcnt(0)\n\ts_barrier" ::: "memory")

DEVI int s_of_pi(int pi) {
  int RG = pi >> 6, i = pi & 63;
  return 128 * (4 * (RG & 3) + (i & 3)) + 8 * (i >> 2) + (RG >> 2);
}
DEVI void glds16(const void* g, LAS char* dst) { __builtin_amdgcn_global_load_lds((const GAS void*)g, (LAS void*)dst, 16, 0, 0); }
DEVI void glds16_sc1(const void* g, LAS char* dst) { __builtin_amdgcn_global_load_lds((const GAS void*)g, (LAS void*)dst, 16, 0, 16); }
DEVI v4s trread(LAS char* p) { return __builtin_amdgcn_ds_read_tr16_b64_v4i16((LAS v4s*)p); }
DEVI v8s cat8(v4s a, v4s b) { return __builtin_shufflevector(a, b, 0, 1, 2, 3, 4, 5, 6, 7); }
DEVI float bflo(u32 v) { return __builtin_bit_cast(float, v << 16); }
DEVI float bfhi(u32 v) { return __builtin_bit_cast(float, v & 0xffff0000u); }
DEVI float rbf(float v) { return (float)(__bf16)v; }
DEVI u32 pkbf(float a, float b) { v2b p = {(__bf16)a, (__bf16)b}; return __builtin_bit_cast(u32, p); }
DEVI float cosr(float rev) { return __builtin_amdgcn_cosf(rev); }
DEVI float sinr(float rev) { return __builtin_amdgcn_sinf(rev); }
DEVI v4f mfma16h(v8h a, v8h b, v4f c) { return __builtin_amdgcn_mfma_f32_16x16x32_f16(a, b, c, 0, 0, 0); }
DEVI v16f mfma32b(v8s a, v8s b, v16f c) { return __builtin_amdgcn_mfma_f32_32x32x16_bf16(__builtin_bit_cast(v8b, a), __builtin_bit_cast(v8b, b), c, 0, 0, 0); }
DEVI v16f mfma32h(v8h a, v8s b, v16f c) { return __builtin_amdgcn_mfma_f32_32x32x16_f16(a, __builtin_bit_cast(v8h, b), c, 0, 0, 0); }
DEVI v8h pack_ri(v4f re, v4f im) {
  v8h r;
  r[0] = (_Float16)re[0]; r[1] = (_Float16)re[1]; r[2] = (_Float16)re[2]; r[3] = (_Float16)re[3];
  r[4] = (_Float16)im[0]; r[5] = (_Float16)im[1]; r[6] = (_Float16)im[2]; r[7] = (_Float16)im[3];
  return r;
}
DEVI v4h pack4h(v4f a) { v4h r; r[0] = (_Float16)a[0]; r[1] = (_Float16)a[1]; r[2] = (_Float16)a[2]; r[3] = (_Float16)a[3]; return r; }
DEVI void twid(v4f& re, v4f& im, const v4f& c, const v4f& s) {
#pragma unroll
  for (int r = 0; r < 4; ++r) { float a = re[r], b = im[r]; re[r] = a * c[r] + b * s[r]; im[r] = b * c[r] - a * s[r]; }
}
template <int CTRL> DEVI float dppadd(float v) {
  return v + __builtin_bit_cast(float, __builtin_amdgcn_update_dpp(0, __builtin_bit_cast(int, v), CTRL, 0xf, 0xf, true));
}
DEVI float wave_sum(float v) {
  v = dppadd<0xB1>(v); v = dppadd<0x4E>(v); v = dppadd<0x141>(v); v = dppadd<0x140>(v);
  int b = __builtin_bit_cast(int, v);
  float a0 = __builtin_bit_cast(float, __builtin_amdgcn_readlane(b, 0)), a1 = __builtin_bit_cast(float, __builtin_amdgcn_readlane(b, 16));
  float a2 = __builtin_bit_cast(float, __builtin_amdgcn_readlane(b, 32)), a3 = __builtin_bit_cast(float, __builtin_amdgcn_readlane(b, 48));
  return (a0 + a1) + (a2 + a3);
}

DEVI void ffn1(const u16* __restrict__ x, const u16* __restrict__ Wg, const u16* __restrict__ Wu, char* __restrict__ H,
               int p, int j, LAS char* smem) {
  const int tid = threadIdx.x, l = tid & 63;
  const int w = __builtin_amdgcn_readfirstlane(tid >> 6);
  const int sh = w >> 2, fq = w & 3;
  u32 aoff[2];
#pragma unroll
  for (int i = 0; i < 2; ++i) {
    int row = (2 * w + i) * 8 + (l >> 3);
    aoff[i] = s_of_pi(p * 128 + row) * 1024 + (((l & 7) ^ ((row >> 1) & 7)) << 4);
  }
  const u32 boff = (u32)(2 * w * 4 + (l >> 4)) * (F_ * 2) + j * 256 + (((l & 15) ^ ((l >> 4) << 2)) << 4);
  auto issue = [&](int t, int slot) {
    LAS char* dst = smem + slot * 49152 + w * 2048;
    const char* xa = (const char*)x + t * 128;
    const char* wg = (const char*)Wg + (size_t)t * (64 * F_ * 2);
    const char* wu = (const char*)Wu + (size_t)t * (64 * F_ * 2);
    glds16(xa + aoff[0], dst);
    glds16(xa + aoff[1], dst + 1024);
    glds16(wg + boff, dst + 16384);
    glds16(wg + boff + 4 * F_ * 2, dst + 16384 + 1024);
    glds16(wu + boff, dst + 32768);
    glds16(wu + boff + 4 * F_ * 2, dst + 32768 + 1024);
  };
  issue(0, 0); issue(1, 1); issue(2, 2);

  const int h = l >> 5, blk = (l >> 4) & 1, q = (l & 15) >> 2, pp = l & 3;
  const u32 wb0 = (u32)(((8 * h + q) * 256 + ((fq ^ q) << 6) + blk * 32 + pp * 8) ^ (fq << 12));
  const u32 r1 = 16384 + sh * 16384, r2 = 32768 - sh * 16384;
  const int xrow = sh * 64 + (l & 31);
  const u32 xb0 = (u32)((xrow * 128 + ((h ^ ((xrow >> 1) & 7)) << 4)) ^ (fq << 5));

  v16f acc[2][2] = {};
  float cs = 0.f, piv[2] = {0.f, 0.f}, s1[2] = {0.f, 0.f}, s2[2] = {0.f, 0.f};
  const v2b ones = {(__bf16)1.0f, (__bf16)1.0f};

  int slot = 0;
  for (int t = 0; t < 8; ++t) {
    if (t == 0) WAIT_VM(12); else if (t == 7) WAIT_VM(0); else WAIT_VM(6);
    BARRIER();
    if (t >= 1 && t < 6) issue(t + 2, slot == 0 ? 2 : slot - 1);
    LAS char* sb = smem + slot * 49152;
#pragma unroll
    for (int i = 0; i < 4; ++i) {
      LAS char* xa = sb + (xb0 ^ (i << 5));
      LAS char* wa = sb + (wb0 ^ (i << 12));
      v8s x0 = *(LAS v8s*)xa, x1 = *(LAS v8s*)(xa + 4096);
      v8s w1 = cat8(trread(wa + r1), trread(wa + r1 + 1024));
      v8s w2 = cat8(trread(wa + r2), trread(wa + r2 + 1024));
      acc[0][0] = mfma32b(w1, x0, acc[0][0]);
      acc[0][1] = mfma32b(w1, x1, acc[0][1]);
      acc[1][0] = mfma32b(w2, x0, acc[1][0]);
      acc[1][1] = mfma32b(w2, x1, acc[1][1]);
      v4u w1u = __builtin_bit_cast(v4u, w1);
#pragma unroll
      for (int e = 0; e < 4; ++e) { u32 tw = w1u[e]; cs = __builtin_amdgcn_fdot2_f32_bf16(__builtin_bit_cast(v2b, tw), ones, cs, false); }
      if (i == 0) {
#pragma unroll
        for (int rt = 0; rt < 2; ++rt) {
          v4u xu = __builtin_bit_cast(v4u, rt ? x1 : x0);
          if (t == 0) piv[rt] = bflo(xu[0]);
#pragma unroll
          for (int e = 0; e < 4; ++e) {
            float a = bflo(xu[e]) - piv[rt], b = bfhi(xu[e]) - piv[rt];
            s1[rt] += a; s2[rt] = fmaf(a, a, s2[rt]);
            s1[rt] += b; s2[rt] = fmaf(b, b, s2[rt]);
          }
        }
      }
    }
    slot = slot == 2 ? 0 : slot + 1;
  }
  LAS char* ST = smem + 147456;
  LAS char* CS = smem + 147456 + 8192;
#pragma unroll
  for (int rt = 0; rt < 2; ++rt) {
    float mi = s1[rt] * (1.0f / 64.0f);
    v2f o = {piv[rt] + mi, fmaxf(s2[rt] - s1[rt] * mi, 0.f)};
    *(LAS v2f*)(ST + (xrow + rt * 32) * 64 + (fq * 2 + h) * 8) = o;
  }
  cs += __shfl_xor(cs, 32);
  if (l < 32) *(LAS float*)(CS + (sh * 128 + fq * 32 + l) * 4) = cs;
  BARRIER();
  float c1[16], c2[16];
#pragma unroll
  for (int g4 = 0; g4 < 4; ++g4) {
    v4f a = *(LAS v4f*)(CS + (sh * 128 + fq * 32 + g4 * 8 + h * 4) * 4);
    v4f b = *(LAS v4f*)(CS + ((1 - sh) * 128 + fq * 32 + g4 * 8 + h * 4) * 4);
#pragma unroll
    for (int e = 0; e < 4; ++e) { c1[g4 * 4 + e] = a[e]; c2[g4 * 4 + e] = b[e]; }
  }
  const float lim1 = sh ? -__builtin_inff() : 0.f, lim2 = sh ? 0.f : -__builtin_inff();
#pragma unroll
  for (int rt = 0; rt < 2; ++rt) {
    v4f st[4];
#pragma unroll
    for (int e = 0; e < 4; ++e) st[e] = *(LAS v4f*)(ST + (xrow + rt * 32) * 64 + e * 16);
    float mean = 0.f, m2 = 0.f;
#pragma unroll
    for (int e = 0; e < 4; ++e) { mean += st[e][0] + st[e][2]; m2 += st[e][1] + st[e][3]; }
    mean *= 0.125f;
    float dv = 0.f;
#pragma unroll
    for (int e = 0; e < 4; ++e) { float a = st[e][0] - mean, b = st[e][2] - mean; dv = fmaf(a, a, dv); dv = fmaf(b, b, dv); }
    float var = rbf((m2 + 64.f * dv) * (1.0f / 512.0f));
    float sd = rbf(__builtin_sqrtf(rbf(var + 1e-5f)));
    float rs = 1.0f / sd, mb = rbf(mean);
    float hv[16];
#pragma unroll
    for (int r = 0; r < 16; ++r) {
      float a = rbf(rs * fmaf(-mb, c1[r], acc[0][rt][r]));
      float b = rbf(rs * fmaf(-mb, c2[r], acc[1][rt][r]));
      hv[r] = fmaxf(a, lim1) * fmaxf(b, lim2);
    }
#pragma unroll
    for (int s2i = 0; s2i < 2; ++s2i) {
      v4u o;
#pragma unroll
      for (int e = 0; e < 4; ++e) o[e] = pkbf(hv[s2i * 8 + 2 * e], hv[s2i * 8 + 2 * e + 1]);
      size_t fr = (size_t)(p * 4 + sh * 2 + rt) * 112 + (j * 8 + fq * 2 + s2i);
      *(v4u*)(H + fr * 1024 + l * 16) = o;
    }
  }
}

DEVI void prep(const u16* __restrict__ x, const u16* __restrict__ wr, const u16* __restrict__ wi, char* __restrict__ ws,
               int sp, LAS char* smem) {
  const int tid = threadIdx.x, l = tid & 63;
  const int w = __builtin_amdgcn_readfirstlane(tid >> 6);
  const int g = l >> 4, o = l & 15;
  v4u xr[8];
#pragma unroll
  for (int i = 0; i < 8; ++i) xr[i] = *(const v4u*)(x + s_of_pi(sp * 64 + w * 8 + i) * D_ + l * 8);
  if (sp == 0 && tid < 256) ((u32*)(ws + OFF_FLAG))[tid] = 0u;

  v8h F32re[2], F32im[2], M2, M2n;
  v4f twc[2], tws[2];
#pragma unroll
  for (int e = 0; e < 8; ++e) {
    int da = 16 * (e >> 2) + 4 * g + (e & 3);
#pragma unroll
    for (int u = 0; u < 2; ++u) {
      float a = (float)((da * (16 * u + o)) & 31) * (1.0f / 32.0f);
      F32re[u][e] = (_Float16)(0.25f * cosr(a));
      F32im[u][e] = (_Float16)(-0.25f * sinr(a));
    }
    int db = 4 * g + (e & 3), part = e >> 2;
    float a = (float)((db * (o >> 1)) & 15) * (1.0f / 16.0f);
    float c = 0.25f * cosr(a), s = 0.25f * sinr(a);
    M2[e] = (_Float16)((o & 1) ? (part ? c : -s) : (part ? s : c));
    M2n[e] = (_Float16)((o == 0 && part == 0) ? ((e & 1) ? -0.25f : 0.25f) : 0.f);
  }
#pragma unroll
  for (int u = 0; u < 2; ++u)
#pragma unroll
    for (int r = 0; r < 4; ++r) {
      float a = (float)((4 * g + r) * (16 * u + o)) * (1.0f / 512.0f);
      twc[u][r] = cosr(a); tws[u][r] = sinr(a);
    }
  LAS char* img = smem + w * 8192;
#pragma unroll
  for (int i = 0; i < 8; ++i) {
    float v[8];
#pragma unroll
    for (int e = 0; e < 4; ++e) { v[2 * e] = bflo(xr[i][e]); v[2 * e + 1] = bfhi(xr[i][e]); }
    float s = ((v[0] + v[1]) + (v[2] + v[3])) + ((v[4] + v[5]) + (v[6] + v[7]));
    float mean = wave_sum(s) * (1.0f / 512.0f);
    float qq = 0.f;
#pragma unroll
    for (int e = 0; e < 8; ++e) { float d = v[e] - mean; qq = fmaf(d, d, qq); }
    float var = rbf(wave_sum(qq) * (1.0f / 512.0f));
    float sd = rbf(__builtin_sqrtf(rbf(var + 1e-5f)));
    float rs = 1.0f / sd, mb = rbf(mean);
    v8h o8;
#pragma unroll
    for (int e = 0; e < 8; ++e) o8[e] = (_Float16)rbf(rbf(v[e] - mb) * rs);
    *(LAS v8h*)(img + i * 1024 + l * 16) = o8;
  }
  char* S1 = ws + OFF_S1;
#pragma unroll
  for (int nbi = 0; nbi < 2; ++nbi) {
    v4f d2[4][2];
    float nyq[4];
#pragma unroll
    for (int r = 0; r < 4; ++r) {
      LAS char* rp = img + (nbi * 4 + r) * 1024 + l * 8;
      v8h xf = __builtin_bit_cast(v8h, cat8(trread(rp), trread(rp + 512)));
#pragma unroll
      for (int u = 0; u < 2; ++u) {
        v4f z = {0.f, 0.f, 0.f, 0.f};
        v4f re = mfma16h(xf, F32re[u], z), im = mfma16h(xf, F32im[u], z);
        twid(re, im, twc[u], tws[u]);
        v8h b = pack_ri(re, im);
        d2[r][u] = mfma16h(M2, b, z);
        if (u == 0) { v4f n = mfma16h(M2n, b, z); nyq[r] = n[0]; }
      }
      if (l == 0) d2[r][0][1] = nyq[r];
    }
#pragma unroll
    for (int u = 0; u < 2; ++u)
#pragma unroll
      for (int sub = 0; sub < 2; ++sub) {
        v8h un;
#pragma unroll
        for (int r = 0; r < 4; ++r) { un[r] = (_Float16)d2[r][u][2 * sub]; un[4 + r] = (_Float16)d2[r][u][2 * sub + 1]; }
        int kd = 16 * u + o + 32 * (2 * g + sub);
        *(v8h*)(S1 + (size_t)kd * 8192 + sp * 256 + (2 * w + nbi) * 16) = un;
      }
  }
  {
    const int ka = sp >> 1, hc = sp & 1;
    char* WT = ws + OFF_WT + (ka >> 1) * 1024 + ((ka & 1) * 2 + hc) * 256;
    u32 mx = 0;
    auto unit = [&](int kd, int kb) {
      v4u un;
#pragma unroll
      for (int r = 0; r < 4; ++r) {
        int ks = ka + 16 * kb + 256 * (4 * hc + r);
        u32 a = wr[ks * DF_ + kd], b = wi[ks * DF_ + kd];
        mx = max(mx, max(a & 0x7fffu, b & 0x7fffu));
        un[r] = a | (b << 16);
      }
      *(v4u*)(WT + (size_t)kd * 8192 + kb * 16) = un;
    };
#pragma unroll
    for (int it = 0; it < 8; ++it) unit(tid & 255, it * 2 + (tid >> 8));
    if (tid < 16) unit(256, tid);
#pragma unroll
    for (int off = 32; off; off >>= 1) mx = max(mx, (u32)__shfl_xor((int)mx, off));
    LAS u32* red = (LAS u32*)(smem + 65536);
    if (l == 0) red[w] = mx;
    BARRIER();
    if (tid == 0) {
      u32 m = 0;
#pragma unroll
      for (int i = 0; i < 8; ++i) m = max(m, red[i]);
      ((u32*)(ws + OFF_MAX))[sp] = m;
    }
  }
}

__global__ __launch_bounds__(512, 2) void k1(const u16* __restrict__ x, const u16* __restrict__ wr, const u16* __restrict__ wi,
                                             const u16* __restrict__ Wg, const u16* __restrict__ Wu, char* __restrict__ ws) {
  __shared__ __attribute__((aligned(16))) char smem_[163840];
  LAS char* smem = (LAS char*)smem_;
  const int b = blockIdx.x, xcd = b & 7, idx = b >> 3;
  if (idx < 28) ffn1(x, Wg, Wu, ws + OFF_H, (xcd >> 1) * 4 + idx / 7, (xcd & 1) * 7 + idx % 7, smem);
  else prep(x, wr, wi, ws, xcd * 4 + (idx - 28), smem);
}

constexpr int L_SREG = 98304;
constexpr int L_S1 = L_SREG, L_WT = L_SREG + 8192, L_E1 = L_SREG + 16384, L_E2 = L_SREG, L_PRE = L_SREG + 32768;
constexpr int E1P = 544, E2P = 1024 + 72;
constexpr int PUB_U = 3, POLL_U = 9, CHK_U = 12, SDAT_U = 19;
DEVI constexpr int stage_off(int u) { return u < 2 ? L_PRE + u * 16384 : ((u - 2) % 6) * 16384; }

template <int MODE>
__global__ __launch_bounds__(512, 2) void k2(const u16* __restrict__ x, const u16* __restrict__ Wd, char* __restrict__ ws, u16* __restrict__ out) {
  __shared__ __attribute__((aligned(16))) char smem_[163840];
  LAS char* smem = (LAS char*)smem_;
  const int tid = threadIdx.x, l = tid & 63;
  const int w = __builtin_amdgcn_readfirstlane(tid >> 6);
  const int b = blockIdx.x, xcd = b & 7, idx = b >> 3;
  const int RG = xcd * 4 + (idx >> 3), ct = idx & 7, kd = b;
  const int g = l >> 4, o = l & 15;

  const size_t eoff = (size_t)s_of_pi(RG * 64 + (tid >> 3)) * D_ + ct * 64 + (tid & 7) * 8;
  v4u xe = *(const v4u*)(x + eoff);

  if (MODE != 2) {
    glds16(ws + OFF_S1 + (size_t)kd * 8192 + w * 1024 + l * 16, smem + L_S1 + w * 1024);
    glds16(ws + OFF_WT + (size_t)kd * 8192 + w * 1024 + l * 16, smem + L_WT + w * 1024);
  }
  const int wq = w & 3;
  const char* hsrc = ws + OFF_H + ((size_t)(RG * 2 + (wq >> 1)) * 112 + (wq & 1) * 2) * 1024 + l * 16;
  const char* bsrc = (const char*)Wd + (size_t)(wq * 16 + (l >> 3)) * 1024 + ct * 128 + (((l & 7) ^ (((l >> 4) & 1) << 2)) << 4);
  auto issue = [&](int u, int off) {
    LAS char* dst = smem + off + wq * 2048;
    glds16(hsrc + u * 4096, dst);
    glds16(hsrc + u * 4096 + 1024, dst + 1024);
    glds16(bsrc + (size_t)u * 65536, dst + 8192);
    glds16(bsrc + (size_t)u * 65536 + 8192, dst + 8192 + 1024);
  };
  if (MODE != 1 && w < 4) static_for<0, 8>([&](auto U) { issue(U, stage_off(U)); });

  u32 wm = 0;
  {
    const u32* mxp = (const u32*)(ws + OFF_MAX);
#pragma unroll
    for (int i = 0; i < 32; ++i) wm = max(wm, mxp[i]);
  }
  int E = (int)(wm >> 7);
  E = E < 16 ? 16 : (E > 240 ? 240 : E);
  const float wscale = __builtin_bit_cast(float, (u32)(256 - E) << 23);
  const float oscale = __builtin_bit_cast(float, (u32)(E - 5) << 23);

  if (MODE != 2) {
    v8h F16re, F16im, F8re, F8im;
    v4f t1c, t1s, t2c, t2s, t3c, t3s;
#pragma unroll
    for (int e = 0; e < 4; ++e) {
      int id = 4 * g + e;
      float a = (float)((id * o) & 15) * (1.0f / 16.0f);
      float c = 0.25f * cosr(a), s = 0.25f * sinr(a);
      F16re[e] = (_Float16)c; F16re[4 + e] = (_Float16)s; F16im[e] = (_Float16)(-s); F16im[4 + e] = (_Float16)c;
      float a8 = (float)((id * o) & 7) * (1.0f / 8.0f);
      float m = ((id ^ o) & 8) ? 0.f : 0.25f;
      float c8 = m * cosr(a8), s8 = m * sinr(a8);
      F8re[e] = (_Float16)c8; F8re[4 + e] = (_Float16)s8; F8im[e] = (_Float16)(-s8); F8im[4 + e] = (_Float16)c8;
      float b1 = (float)(id * o) * (1.0f / 256.0f);
      t1c[e] = cosr(b1); t1s[e] = sinr(b1);
      float b2 = (float)(w * (o + 16 * id)) * (1.0f / 2048.0f);
      t2c[e] = cosr(b2); t2s[e] = sinr(b2);
      float b3 = (float)((o & 7) * (2 * w + (o >> 3) + 16 * id)) * (1.0f / 2048.0f);
      t3c[e] = cosr(b3); t3s[e] = sinr(b3);
    }
    if (MODE == 0 && w < 4) WAIT_VM(32); else WAIT_VM(0);
    const v4f z = {0.f, 0.f, 0.f, 0.f};
    v8h a = *(LAS v8h*)(smem + L_S1 + w * 1024 + l * 16);
    v4u wt = *(LAS v4u*)(smem + L_WT + w * 1024 + l * 16);
    v4f re = mfma16h(a, F16re, z), im = mfma16h(a, F16im, z);
    twid(re, im, t1c, t1s);
    a = pack_ri(re, im);
    re = mfma16h(F16re, a, z); im = mfma16h(F16im, a, z);
    twid(re, im, t2c, t2s);
    *(LAS v4h*)(smem + L_E1 + w * E1P + o * 32 + g * 8) = pack4h(re);
    *(LAS v4h*)(smem + L_E1 + (8 + w) * E1P + o * 32 + g * 8) = pack4h(im);
    BARRIER();
    {
      LAS char* rp = smem + L_E1 + (4 * (g & 1) + (o >> 2)) * E1P + (2 * w + (g >> 1)) * 32 + (o & 3) * 8;
      a = __builtin_bit_cast(v8h, cat8(trread(rp), trread(rp + 8 * E1P)));
    }
    re = mfma16h(F8re, a, z); im = mfma16h(F8im, a, z);
    if (kd != 0) {
#pragma unroll
      for (int r = 0; r < 4; ++r) {
        float fr = bflo(wt[r]) * wscale, fi = bfhi(wt[r]) * wscale;
        float zr = re[r], zi = im[r];
        re[r] = zr * fr - zi * fi;
        im[r] = -(zr * fi + zi * fr);
      }
    } else {
      const int ka = 2 * w + (g >> 1), kb = o;
      LAS v2f* ZX = (LAS v2f*)(smem + L_SREG);
      BARRIER();
#pragma unroll
      for (int r = 0; r < 4; ++r) { v2f t = {re[r], im[r]}; ZX[ka + 16 * kb + 256 * (4 * (g & 1) + r)] = t; }
      BARRIER();
      const u32* W0 = (const u32*)(ws + OFF_WT);
      const u32* WN = (const u32*)(ws + OFF_WT + (size_t)256 * 8192);
#pragma unroll
      for (int r = 0; r < 4; ++r) {
        int ks = ka + 16 * kb + 256 * (4 * (g & 1) + r);
        int km = (2048 - ks) & 2047;
        int ma = km & 15, mb = (km >> 4) & 15, mc = km >> 8;
        int pos = w * 256 + l * 4 + r;
        int posm = (ma >> 1) * 256 + ((ma & 1) * 2 + (mc >> 2)) * 64 + mb * 4 + (mc & 3);
        u32 a0 = wt[r], a0m = W0[posm], an = WN[pos], anm = WN[posm];
        float war = bflo(a0) + bflo(a0m), wai = bfhi(a0) - bfhi(a0m);
        float wbr = bflo(an) + bflo(anm), wbi = bfhi(an) - bfhi(anm);
        float sc = 0.25f * wscale;
        float pr = (war + wbr) * sc, pi = (wai + wbi) * sc, qr = (war - wbr) * sc, qi = (wai - wbi) * sc;
        v2f zm = ZX[km];
        float zr = re[r], zi = im[r];
        re[r] = zr * pr - zi * pi + zm[0] * qr + zm[1] * qi;
        im[r] = -(zr * pi + zi * pr + zm[0] * qi - zm[1] * qr);
      }
      BARRIER();
    }
    a = pack_ri(re, im);
    re = mfma16h(a, F8re, z); im = mfma16h(a, F8im, z);
    twid(re, im, t3c, t3s);
    *(LAS v4h*)(smem + L_E2 + (o & 7) * E2P + (2 * w + (o >> 3)) * 32 + g * 8) = pack4h(re);
    *(LAS v4h*)(smem + L_E2 + (o & 7) * E2P + 512 + (2 * w + (o >> 3)) * 32 + g * 8) = pack4h(im);
    BARRIER();
    {
      LAS char* rp = smem + L_E2 + w * E2P + o * 32 + g * 8;
      a = __builtin_bit_cast(v8h, cat8(*(LAS v4s*)rp, *(LAS v4s*)(rp + 512)));
    }
    re = mfma16h(a, F16re, z); im = mfma16h(a, F16im, z);
    twid(re, im, t1c, t1s);
    a = pack_ri(re, im);
    re = mfma16h(F16re, a, z); im = mfma16h(F16im, a, z);
    GAS u64* dst = (GAS u64*)(ws + OFF_S2 + (size_t)(2 * kd) * 4096 + w * 512 + l * 8);
    __hip_atomic_store(dst, __builtin_bit_cast(u64, pack4h(re)), __ATOMIC_RELAXED, __HIP_MEMORY_SCOPE_AGENT);
    __hip_atomic_store(dst + 512, __builtin_bit_cast(u64, pack4h(im)), __ATOMIC_RELAXED, __HIP_MEMORY_SCOPE_AGENT);
  }
  if (MODE == 1) return;

  const int h = l >> 5, blk = (l >> 4) & 1, q = (l & 15) >> 2, pp = l & 3;
  const int dh = wq >> 1, ph = wq & 1;
  v16f acc = {};
  constexpr int NST = (MODE == 0) ? 2 : 0;
  if (w < 4) {
    const u32 trb = (u32)(8192 + (4 * h + q) * 128 + ((dh ^ ((q >> 1) & 1)) << 6) + blk * 32 + pp * 8);
    const u32 hb = (u32)(ph * 4096 + l * 16);
    static_for<0, 28>([&](auto U) {
      constexpr int u = U;
      constexpr int imax = (u + 4 > 27) ? 27 : (u + 4 < 7 ? 7 : u + 4);
      constexpr int allowed = (u == PUB_U) ? 4 * (imax - 7) : 4 * (imax - u) + (u < PUB_U ? NST : 0);
      WAIT_VM(allowed);
      BARRIER();
      if constexpr (u >= 3 && u + 5 <= 27) issue(u + 5, stage_off(u + 5));
      LAS char* sb = smem + stage_off(u);
#pragma unroll
      for (int s = 0; s < 4; ++s) {
        v8s hf = *(LAS v8s*)(sb + hb + s * 1024);
        v8s wf = cat8(trread(sb + trb + s * 2048), trread(sb + trb + s * 2048 + 1024));
        acc = mfma32b(wf, hf, acc);
      }
    });
  } else {
    const u32 trb = (u32)(L_SREG + (4 * h + q) * 128 + ((ph ^ ((q >> 1) & 1)) << 6) + blk * 32 + pp * 8);
    const int d = ct * 64 + dh * 32 + (l & 31);
    const char* ssrc = ws + OFF_S2 + (size_t)(wq * 128 + (l >> 3)) * 4096 + RG * 128 + (((l & 7) ^ (((l >> 4) & 1) << 2)) << 4);
    const GAS v4u* flg = (const GAS v4u*)(ws + OFF_FLAG) + l;
    v8h G[32];
    v4u fl;
    static_for<0, 28>([&](auto U) {
      constexpr int u = U;
      if constexpr (u == PUB_U || u == SDAT_U) WAIT_VM(0);
      if constexpr (u == CHK_U && MODE == 0) {
        if (w == 4) {
          asm volatile("s_waitcnt vmcnt(0)" : "+v"(fl)::"memory");
          int spins = 0;
          while (!__all((fl[0] & fl[1] & fl[2] & fl[3]) == 1u) && ++spins < (1 << 20)) {
            __builtin_amdgcn_s_sleep(2);
            asm volatile("global_load_dwordx4 %0, %1, off sc1\n\ts_waitcnt vmcnt(0)" : "=&v"(fl) : "v"(flg) : "memory");
          }
        }
      }
      BARRIER();
      if constexpr (u == PUB_U && MODE == 0) {
        if (tid == 256) __hip_atomic_store((GAS u32*)(ws + OFF_FLAG) + b, 1u, __ATOMIC_RELAXED, __HIP_MEMORY_SCOPE_AGENT);
      }
      if constexpr (u == POLL_U && MODE == 0) {
        if (w == 4) asm volatile("global_load_dwordx4 %0, %1, off sc1" : "=&v"(fl) : "v"(flg) : "memory");
      }
      if constexpr (u == CHK_U) {
#pragma unroll
        for (int i = 0; i < 16; ++i) glds16_sc1(ssrc + (size_t)i * 32768, smem + L_SREG + wq * 16384 + i * 1024);
      }
      if constexpr (u < 16) {
#pragma unroll
        for (int i = 0; i < 2; ++i) {
          const int s = 2 * u + i;
#pragma unroll
          for (int e2 = 0; e2 < 4; ++e2) {
            int kk = 8 * s + 4 * (e2 >> 1) + 2 * h + (e2 & 1);
            float a = (float)((kk * d) & 511) * (1.0f / 512.0f);
            float c = cosr(a), sn = sinr(a);
            if (s == 0 && e2 == 0) { if (h == 0) { c = 0.5f; sn = (d & 1) ? 0.5f : -0.5f; } }
            G[s][2 * e2] = (_Float16)c; G[s][2 * e2 + 1] = (_Float16)sn;
          }
        }
      }
      if constexpr (u >= SDAT_U && u < SDAT_U + 8) {
#pragma unroll
        for (int i = 0; i < 4; ++i) {
          const int s = 4 * (u - SDAT_U) + i;
          v8s sf = cat8(trread(smem + trb + s * 2048), trread(smem + trb + s * 2048 + 1024));
          acc = mfma32h(G[s], sf, acc);
        }
      }
    });
  }
  BARRIER();
  {
    LAS char* T = smem + (w >> 2) * 17408 + (ph * 32 + (l & 31)) * 272 + (dh * 32 + h * 4) * 4;
#pragma unroll
    for (int g4 = 0; g4 < 4; ++g4) {
      v4f t = {acc[g4 * 4], acc[g4 * 4 + 1], acc[g4 * 4 + 2], acc[g4 * 4 + 3]};
      *(LAS v4f*)(T + g4 * 32) = t;
    }
  }
  BARRIER();
  {
    LAS char* T = smem + (tid >> 3) * 272 + (tid & 7) * 32;
    v4f o0 = *(LAS v4f*)T, o1 = *(LAS v4f*)(T + 16), s0 = *(LAS v4f*)(T + 17408), s1 = *(LAS v4f*)(T + 17408 + 16);
    float r[8];
#pragma unroll
    for (int e = 0; e < 8; ++e) {
      float ov = rbf(e < 4 ? o0[e & 3] : o1[e & 3]);
      float sv = rbf((e < 4 ? s0[e & 3] : s1[e & 3]) * oscale);
      float gv = rbf(ov / (1.0f + __expf(-ov)));
      float xv = (e & 1) ? bfhi(xe[e >> 1]) : bflo(xe[e >> 1]);
      r[e] = xv + rbf(sv * gv);
    }
    v4u ov4 = {pkbf(r[0], r[1]), pkbf(r[2], r[3]), pkbf(r[4], r[5]), pkbf(r[6], r[7])};
    *(v4u*)(out + eoff) = ov4;
  }
}

extern "C" void kernel_launch(void* const* d_in, const int* in_sizes, int n_in, void* d_out, int out_size, void* d_ws,
                              size_t ws_size, hipStream_t stream) {
  const u16* x = (const u16*)d_in[0];
  const u16* wr = (const u16*)d_in[1];
  const u16* wi = (const u16*)d_in[2];
  const u16* Wg = (const u16*)d_in[4];
  const u16* Wu = (const u16*)d_in[5];
  const u16* Wd = (const u16*)d_in[6];
  char* ws = (char*)d_ws;
  k1<<<256, 512, 0, stream>>>(x, wr, wi, Wg, Wu, ws);
  k2<0><<<256, 512, 0, stream>>>(x, Wd, ws, (u16*)d_out);
}
```

```cpp
#include <hip/hip_runtime.h>
#include <stdint.h>

typedef unsigned short u16;
typedef unsigned int u32;
typedef unsigned long long u64;
typedef short v4s __attribute__((ext_vector_type(4)));
typedef short v8s __attribute__((ext_vector_type(8)));
typedef _Float16 v2h __attribute__((ext_vector_type(2)));
typedef _Float16 v4h __attribute__((ext_vector_type(4)));
typedef _Float16 v8h __attribute__((ext_vector_type(8)));
typedef __bf16 v2b __attribute__((ext_vector_type(2)));
typedef __bf16 v8b __attribute__((ext_vector_type(8)));
typedef float v2f __attribute__((ext_vector_type(2)));
typedef float v4f __attribute__((ext_vector_type(4)));
typedef float v16f __attribute__((ext_vector_type(16)));
typedef u32 v2u __attribute__((ext_vector_type(2)));
typedef u32 v4u __attribute__((ext_vector_type(4)));

#define LAS __attribute__((address_space(3)))
#define GAS __attribute__((address_space(1)))
#define DEVI __device__ __forceinline__

#define SPLIT_P2 0

constexpr int S_ = 2048, D_ = 512, F_ = 1792, DF_ = 257;
constexpr size_t OFF_H = 0;
constexpr size_t OFF_S1 = (size_t)8 << 20;
constexpr size_t OFF_WT = (size_t)10 << 20;
constexpr size_t OFF_S2 = (size_t)13 << 20;
constexpr size_t OFF_MAX = (size_t)15 << 20;
constexpr size_t OFF_RS2 = ((size_t)15 << 20) + 8192;
constexpr size_t OFF_FLAG = ((size_t)15 << 20) + 4096;

#define STAMP_DECL
#define STAMPT(id)
#define STAMP_OUT(wsp, k)
template <int I> struct IC { static constexpr int v = I; constexpr operator int() const { return I; } };
template <int B, int E, class Fn> DEVI void static_for(Fn&& f) {
  if constexpr (B < E) { f(IC<B>{}); static_for<B + 1, E>(f); }
}

#define WAIT_VM(n) asm volatile("s_waitcnt vmcnt(%0)" ::"n"(n) : "memory")
#define BARRIER() asm volatile("s_waitcnt lgkmcnt(0)\n\ts_barrier" ::: "memory")

DEVI int s_of_pi(int pi) {
  int RG = pi >> 6, i = pi & 63;
  return 128 * (4 * (RG & 3) + (i & 3)) + 8 * (i >> 2) + (RG >> 2);
}
DEVI void dma(u32 voff, const char* sbase, u32 lds) {
  asm volatile("s_mov_b32 m0, %2\n\ts_nop 0\n\tglobal_load_lds_dwordx4 %0, %1" ::"v"(voff), "s"(sbase), "s"(lds) : "memory");
}
DEVI void dma_sc1(u32 voff, const char* sbase, u32 lds) {
  asm volatile("s_mov_b32 m0, %2\n\ts_nop 0\n\tglobal_load_lds_dwordx4 %0, %1 sc1" ::"v"(voff), "s"(sbase), "s"(lds) : "memory");
}
DEVI void dma4(u32 voff, const char* sbase, u32 lds) {
  asm volatile("s_mov_b32 m0, %2\n\ts_nop 0\n\tglobal_load_lds_dwordx4 %0, %1\n\tglobal_load_lds_dwordx4 %0, %1 offset:1024\n\t"
               "global_load_lds_dwordx4 %0, %1 offset:2048\n\tglobal_load_lds_dwordx4 %0, %1 offset:3072" ::"v"(voff), "s"(sbase), "s"(lds) : "memory");
}
#define SCHED() __builtin_amdgcn_sched_barrier(0)
DEVI float dot2ones(u32 v, float acc) {
  const v2b ones = {(__bf16)1.0f, (__bf16)1.0f};
  return __builtin_amdgcn_fdot2_f32_bf16(__builtin_bit_cast(v2b, v), ones, acc, false);
}
template <int CTRL> DEVI u32 dppmax(u32 v) { return max(v, (u32)__builtin_amdgcn_update_dpp(0, (int)v, CTRL, 0xf, 0xf, true)); }
DEVI u32 wave_max(u32 v) {
  v = dppmax<0xB1>(v); v = dppmax<0x4E>(v); v = dppmax<0x141>(v); v = dppmax<0x140>(v);
  u32 a0 = __builtin_amdgcn_readlane(v, 0), a1 = __builtin_amdgcn_readlane(v, 16), a2 = __builtin_amdgcn_readlane(v, 32), a3 = __builtin_amdgcn_readlane(v, 48);
  return max(max(a0, a1), max(a2, a3));
}
DEVI v4s trread(LAS char* p) { return __builtin_amdgcn_ds_read_tr16_b64_v4i16((LAS v4s*)p); }
DEVI v8s cat8(v4s a, v4s b) { return __builtin_shufflevector(a, b, 0, 1, 2, 3, 4, 5, 6, 7); }
DEVI float bflo(u32 v) { return __builtin_bit_cast(float, v << 16); }
DEVI float bfhi(u32 v) { return __builtin_bit_cast(float, v & 0xffff0000u); }
DEVI float rbf(float v) { return (float)(__bf16)v; }
DEVI u32 pkbf(float a, float b) { v2b p = {(__bf16)a, (__bf16)b}; return __builtin_bit_cast(u32, p); }
DEVI float cosr(float rev) { return __builtin_amdgcn_cosf(rev); }
DEVI float sinr(float rev) { return __builtin_amdgcn_sinf(rev); }
DEVI v4f mfma16h(v8h a, v8h b, v4f c) { return __builtin_amdgcn_mfma_f32_16x16x32_f16(a, b, c, 0, 0, 0); }
DEVI v16f mfma32b(v8s a, v8s b, v16f c) { return __builtin_amdgcn_mfma_f32_32x32x16_bf16(__builtin_bit_cast(v8b, a), __builtin_bit_cast(v8b, b), c, 0, 0, 0); }
DEVI v16f mfma32h(v8h a, v8s b, v16f c) { return __builtin_amdgcn_mfma_f32_32x32x16_f16(a, __builtin_bit_cast(v8h, b), c, 0, 0, 0); }
DEVI v8h pack_ri(v4f re, v4f im) {
  v8h r;
  r[0] = (_Float16)re[0]; r[1] = (_Float16)re[1]; r[2] = (_Float16)re[2]; r[3] = (_Float16)re[3];
  r[4] = (_Float16)im[0]; r[5] = (_Float16)im[1]; r[6] = (_Float16)im[2]; r[7] = (_Float16)im[3];
  return r;
}
DEVI v4h pack4h(v4f a) { v4h r; r[0] = (_Float16)a[0]; r[1] = (_Float16)a[1]; r[2] = (_Float16)a[2]; r[3] = (_Float16)a[3]; return r; }
DEVI void twid(v4f& re, v4f& im, const v4f& c, const v4f& s) {
#pragma unroll
  for (int r = 0; r < 4; ++r) { float a = re[r], b = im[r]; re[r] = a * c[r] + b * s[r]; im[r] = b * c[r] - a * s[r]; }
}
template <int CTRL> DEVI float dppadd(float v) {
  return v + __builtin_bit_cast(float, __builtin_amdgcn_update_dpp(0, __builtin_bit_cast(int, v), CTRL, 0xf, 0xf, true));
}
DEVI float wave_sum(float v) {
  v = dppadd<0xB1>(v); v = dppadd<0x4E>(v); v = dppadd<0x141>(v); v = dppadd<0x140>(v);
  int b = __builtin_bit_cast(int, v);
  float a0 = __builtin_bit_cast(float, __builtin_amdgcn_readlane(b, 0)), a1 = __builtin_bit_cast(float, __builtin_amdgcn_readlane(b, 16));
  float a2 = __builtin_bit_cast(float, __builtin_amdgcn_readlane(b, 32)), a3 = __builtin_bit_cast(float, __builtin_amdgcn_readlane(b, 48));
  return (a0 + a1) + (a2 + a3);
}

struct WtUnit { u32 a[4], b[4]; };
DEVI void wt_load(WtUnit& u, const u16* __restrict__ wr, const u16* __restrict__ wi, int sp, int kb, int kd) {
#pragma unroll
  for (int r = 0; r < 4; ++r) {
    int ks = (sp >> 1) + 16 * kb + 256 * (4 * (sp & 1) + r);
    u.a[r] = wr[ks * DF_ + kd]; u.b[r] = wi[ks * DF_ + kd];
  }
}
DEVI u32 wt_store(const WtUnit& u, char* __restrict__ ws, int sp, int kb, int kd) {
  v4u un; u32 mx = 0;
#pragma unroll
  for (int r = 0; r < 4; ++r) { un[r] = u.a[r] | (u.b[r] << 16); mx = max(mx, max(u.a[r] & 0x7fffu, u.b[r] & 0x7fffu)); }
  *(v4u*)(ws + OFF_WT + (size_t)kd * 8192 + (sp >> 2) * 1024 + (sp & 3) * 256 + kb * 16) = un;
  return mx;
}

struct Frag1 { v8s x0, x1, w1, w2; };

DEVI void ffn1(const u16* __restrict__ x, const u16* __restrict__ wr, const u16* __restrict__ wi, const u16* __restrict__ Wg,
               const u16* __restrict__ Wu, char* __restrict__ ws, int p, int j, LAS char* smem) {
  const int tid = threadIdx.x, l = tid & 63, b = blockIdx.x;
  const int w = __builtin_amdgcn_readfirstlane(tid >> 6);
  const int sh = w >> 2, fq = w & 3;
  STAMP_DECL; STAMPT(0);
  u32 aoff[2];
#pragma unroll
  for (int i = 0; i < 2; ++i) {
    int row = (2 * w + i) * 8 + (l >> 3);
    aoff[i] = s_of_pi(p * 128 + row) * 1024 + (((l & 7) ^ ((row >> 1) & 7)) << 4);
  }
  const u32 boff = (u32)(2 * w * 4 + (l >> 4)) * (F_ * 2) + j * 256 + (((l & 15) ^ ((l >> 4) << 2)) << 4);
  const u32 lbase = (u32)(size_t)smem;
  auto issue = [&](int t, int slot) {
    u32 dst = lbase + slot * 49152 + w * 2048;
    const char* xa = (const char*)x + t * 128;
    const char* wg = (const char*)Wg + (size_t)t * (64 * F_ * 2);
    const char* wu = (const char*)Wu + (size_t)t * (64 * F_ * 2);
    dma(aoff[0], xa, dst);
    dma(aoff[1], xa, dst + 1024);
    dma(boff, wg, dst + 16384);
    dma(boff, wg + 4 * F_ * 2, dst + 16384 + 1024);
    dma(boff, wu, dst + 32768);
    dma(boff, wu + 4 * F_ * 2, dst + 32768 + 1024);
  };
  issue(0, 0);
  WtUnit wtu;
  wt_load(wtu, wr, wi, b >> 3, 2 * (b & 7) + (tid >> 8), tid & 255);
  issue(1, 1); issue(2, 2);

  const int h = l >> 5, blk = (l >> 4) & 1, q = (l & 15) >> 2, pp = l & 3;
  const u32 wb0 = (u32)(((8 * h + q) * 256 + ((fq ^ q) << 6) + blk * 32 + pp * 8) ^ (fq << 12));
  const u32 r1 = 16384 + sh * 16384, r2 = 32768 - sh * 16384;
  const int xrow = sh * 64 + (l & 31);
  const u32 xb0 = (u32)((xrow * 128 + ((h ^ ((xrow >> 1) & 7)) << 4)) ^ (fq << 5));

  v16f acc[2][2] = {};
  float cs0 = 0.f, cs1 = 0.f, s1[2] = {0.f, 0.f};
  auto rd = [&](Frag1& f, LAS char* sb, int i) {
    LAS char* xa = sb + (xb0 ^ (i << 5));
    LAS char* wa = sb + (wb0 ^ (i << 12));
    f.w1 = cat8(trread(wa + r1), trread(wa + r1 + 1024));
    f.x0 = *(LAS v8s*)xa; f.x1 = *(LAS v8s*)(xa + 4096);
    f.w2 = cat8(trread(wa + r2), trread(wa + r2 + 1024));
  };
  auto mm = [&](const Frag1& f, bool stats) {
    acc[0][0] = mfma32b(f.w1, f.x0, acc[0][0]);
    acc[0][1] = mfma32b(f.w1, f.x1, acc[0][1]);
    acc[1][0] = mfma32b(f.w2, f.x0, acc[1][0]);
    acc[1][1] = mfma32b(f.w2, f.x1, acc[1][1]);
    v4u wu4 = __builtin_bit_cast(v4u, f.w1);
    cs0 = dot2ones(wu4[0], cs0); cs1 = dot2ones(wu4[1], cs1); cs0 = dot2ones(wu4[2], cs0); cs1 = dot2ones(wu4[3], cs1);
    if (stats) {
      v4u a = __builtin_bit_cast(v4u, f.x0), c = __builtin_bit_cast(v4u, f.x1);
#pragma unroll
      for (int e = 0; e < 4; ++e) { s1[0] = dot2ones(a[e], s1[0]); s1[1] = dot2ones(c[e], s1[1]); }
      asm volatile("" : "+v"(s1[0]), "+v"(s1[1]));
    }
    asm volatile("" : "+v"(cs0), "+v"(cs1));
  };
  Frag1 A, B;
  WAIT_VM(20);
  BARRIER();
  STAMPT(1);
  rd(A, smem, 0);
  int slot = 0;
  auto stage = [&](int t, auto MODE) {
    constexpr int mode = MODE;
    LAS char* sb = smem + slot * 49152;
    rd(B, sb, 1); SCHED(); mm(A, true); SCHED();
    rd(A, sb, 2); SCHED(); mm(B, false); SCHED();
    rd(B, sb, 3); SCHED(); mm(A, false); SCHED();
    if constexpr (mode < 3) {
      if constexpr (mode < 2) WAIT_VM(6); else WAIT_VM(0);
      BARRIER();
      const int nslot = slot == 2 ? 0 : slot + 1;
      rd(A, smem + nslot * 49152, 0);
      if constexpr (mode == 0) issue(t + 3, slot);
      slot = nslot;
      SCHED();
    }
    mm(B, false); SCHED();
  };
#pragma unroll 1
  for (int t = 0; t < 5; ++t) stage(t, IC<0>{});
  stage(5, IC<1>{}); stage(6, IC<2>{}); stage(7, IC<3>{});
  STAMPT(9);
  LAS char* ST = smem + 147456;
  LAS char* CS = smem + 147456 + 4096;
  LAS u32* RED = (LAS u32*)(smem + 147456 + 6144);
#pragma unroll
  for (int rt = 0; rt < 2; ++rt) *(LAS float*)(ST + (xrow + rt * 32) * 32 + (fq * 2 + h) * 4) = s1[rt];
  float cs = cs0 + cs1;
  cs += __shfl_xor(cs, 32);
  if (l < 32) *(LAS float*)(CS + (sh * 128 + fq * 32 + l) * 4) = cs;
  {
    u32 mx = wave_max(wt_store(wtu, ws, b >> 3, 2 * (b & 7) + (tid >> 8), tid & 255));
    if (l == 0) RED[w] = mx;
  }
  BARRIER();
  if (tid == 0) {
    u32 m = 0;
#pragma unroll
    for (int i = 0; i < 8; ++i) m = max(m, RED[i]);
    ((u32*)(ws + OFF_MAX))[b] = m;
  }
  float c1[16], c2[16];
#pragma unroll
  for (int g4 = 0; g4 < 4; ++g4) {
    v4f a = *(LAS v4f*)(CS + (sh * 128 + fq * 32 + g4 * 8 + h * 4) * 4);
    v4f c = *(LAS v4f*)(CS + ((1 - sh) * 128 + fq * 32 + g4 * 8 + h * 4) * 4);
#pragma unroll
    for (int e = 0; e < 4; ++e) { c1[g4 * 4 + e] = a[e]; c2[g4 * 4 + e] = c[e]; }
  }
  const float lim1 = sh ? -__builtin_inff() : 0.f, lim2 = sh ? 0.f : -__builtin_inff();
#pragma unroll
  for (int rt = 0; rt < 2; ++rt) {
    v4f sa = *(LAS v4f*)(ST + (xrow + rt * 32) * 32), sb2 = *(LAS v4f*)(ST + (xrow + rt * 32) * 32 + 16);
    float mb = -rbf((((sa[0] + sa[1]) + (sa[2] + sa[3])) + ((sb2[0] + sb2[1]) + (sb2[2] + sb2[3]))) * (1.0f / 512.0f));
    float hv[16];
#pragma unroll
    for (int r = 0; r < 16; ++r)
      hv[r] = fmaxf(fmaf(mb, c1[r], acc[0][rt][r]), lim1) * fmaxf(fmaf(mb, c2[r], acc[1][rt][r]), lim2);
#pragma unroll
    for (int s2i = 0; s2i < 2; ++s2i) {
      v4u o;
#pragma unroll
      for (int e = 0; e < 4; ++e) o[e] = pkbf(hv[s2i * 8 + 2 * e], hv[s2i * 8 + 2 * e + 1]);
      size_t fr = (size_t)(p * 4 + sh * 2 + rt) * 112 + (j * 8 + fq * 2 + s2i);
      *(v4u*)(ws + OFF_H + fr * 1024 + l * 16) = o;
    }
  }
  STAMPT(10);
  STAMPT(11);
  STAMP_OUT(ws, 0);
}

DEVI void prep(const u16* __restrict__ x, const u16* __restrict__ wr, const u16* __restrict__ wi, char* __restrict__ ws,
               int sp, LAS char* smem) {
  const int tid = threadIdx.x, l = tid & 63, b = blockIdx.x;
  const int w = __builtin_amdgcn_readfirstlane(tid >> 6);
  const int g = l >> 4, o = l & 15;
  STAMP_DECL; STAMPT(0);
  const int pi = sp * 64 + w * 8 + (l >> 3);
  v4u xr[8];
  {
    const u16* xp = x + s_of_pi(pi) * D_ + (l & 7) * 8;
#pragma unroll
    for (int m = 0; m < 8; ++m) xr[m] = *(const v4u*)(xp + m * 64);
  }
  WtUnit wtu, wtn;
  wt_load(wtu, wr, wi, b >> 3, 2 * (b & 7) + (tid >> 8), tid & 255);
  if (tid < 16) wt_load(wtn, wr, wi, sp, tid, 256);
  if (sp == 0 && tid < 256) ((u32*)(ws + OFF_FLAG))[tid] = 0u;

  v8h F32re[2], F32im[2], M2, M2n;
  v4f twc[2], tws[2];
#pragma unroll
  for (int e = 0; e < 8; ++e) {
    int da = 16 * (e >> 2) + 4 * g + (e & 3);
    float a = (float)((da * o) & 31) * (1.0f / 32.0f);
    float c = 0.25f * cosr(a), s = -0.25f * sinr(a);
    F32re[0][e] = (_Float16)c; F32im[0][e] = (_Float16)s;
    F32re[1][e] = (_Float16)((e & 1) ? -c : c); F32im[1][e] = (_Float16)((e & 1) ? -s : s);
  }
#pragma unroll
  for (int e = 0; e < 4; ++e) {
    int db = 4 * g + e;
    float a = (float)((db * (o >> 1)) & 15) * (1.0f / 16.0f);
    float c = 0.25f * cosr(a), s = 0.25f * sinr(a);
    M2[e] = (_Float16)((o & 1) ? -s : c); M2[4 + e] = (_Float16)((o & 1) ? c : s);
    M2n[e] = (_Float16)(o == 0 ? ((e & 1) ? -0.25f : 0.25f) : 0.f); M2n[4 + e] = (_Float16)0.f;
#pragma unroll
    for (int u = 0; u < 2; ++u) {
      float t = (float)(db * (16 * u + o)) * (1.0f / 512.0f);
      twc[u][e] = cosr(t); tws[u][e] = sinr(t);
    }
  }
  STAMPT(1);
  LAS char* img = smem + w * 8192;
  {
    float s = 0.f;
#pragma unroll
    for (int m = 0; m < 8; ++m)
#pragma unroll
      for (int e = 0; e < 4; ++e) s = dot2ones(xr[m][e], s);
    s = dppadd<0xB1>(s); s = dppadd<0x4E>(s); s = dppadd<0x141>(s);
    const float mean = s * (1.0f / 512.0f), mb = rbf(mean);
    float q0 = 0.f, q1 = 0.f;
#pragma unroll
    for (int m = 0; m < 8; ++m)
#pragma unroll
      for (int e = 0; e < 4; ++e) {
        float a = bflo(xr[m][e]) - mb, c = bfhi(xr[m][e]) - mb;
        q0 = fmaf(a, a, q0); q1 = fmaf(c, c, q1);
        xr[m][e] = pkbf(a, c);
      }
    float qq = q0 + q1;
    qq = dppadd<0xB1>(qq); qq = dppadd<0x4E>(qq); qq = dppadd<0x141>(qq);
    const float dm = mean - mb;
    const float var = rbf(fmaxf(qq * (1.0f / 512.0f) - dm * dm, 0.f));
    const float sd = rbf(__builtin_sqrtf(rbf(var + 1e-5f)));
    const float rs = 1.0f / sd;
    if ((l & 7) == 0) ((float*)(ws + OFF_RS2))[pi] = rs * rs;
#pragma unroll
    for (int m = 0; m < 8; ++m) {
      v4u o4;
#pragma unroll
      for (int e = 0; e < 4; ++e) {
        u32 t = pkbf(bflo(xr[m][e]) * rs, bfhi(xr[m][e]) * rs);
        o4[e] = __builtin_bit_cast(u32, __builtin_amdgcn_cvt_pkrtz(bflo(t), bfhi(t)));
      }
      *(LAS v4u*)(img + (l >> 3) * 1024 + ((l & 7) + 8 * m) * 16) = o4;
    }
  }
  char* S1 = ws + OFF_S1;
  STAMPT(2);
#pragma unroll 1
  for (int nbi = 0; nbi < 2; ++nbi) {
    v4f d2[4][2];
#pragma unroll
    for (int r = 0; r < 4; ++r) {
      LAS char* rp = img + (nbi * 4 + r) * 1024 + l * 8;
      v8h xf = __builtin_bit_cast(v8h, cat8(trread(rp), trread(rp + 512)));
      float nyq = 0.f;
#pragma unroll
      for (int u = 0; u < 2; ++u) {
        v4f z = {0.f, 0.f, 0.f, 0.f};
        v4f re = mfma16h(xf, F32re[u], z), im = mfma16h(xf, F32im[u], z);
        twid(re, im, twc[u], tws[u]);
        v8h bb = pack_ri(re, im);
        d2[r][u] = mfma16h(M2, bb, z);
        if (u == 0) { v4f n = mfma16h(M2n, bb, z); nyq = n[0]; }
      }
      if (l == 0) d2[r][0][1] = nyq;
    }
#pragma unroll
    for (int u = 0; u < 2; ++u)
#pragma unroll
      for (int sub = 0; sub < 2; ++sub) {
        v8h un;
#pragma unroll
        for (int r = 0; r < 4; ++r) { un[r] = (_Float16)d2[r][u][2 * sub]; un[4 + r] = (_Float16)d2[r][u][2 * sub + 1]; }
        int kd = 16 * u + o + 32 * (2 * g + sub);
        *(v8h*)(S1 + (size_t)kd * 8192 + sp * 256 + (2 * w + nbi) * 16) = un;
      }
  }
  STAMPT(3);
  {
    u32 mx = wt_store(wtu, ws, b >> 3, 2 * (b & 7) + (tid >> 8), tid & 255);
    if (tid < 16) mx = max(mx, wt_store(wtn, ws, sp, tid, 256));
    mx = wave_max(mx);
    LAS u32* red = (LAS u32*)(smem + 65536);
    if (l == 0) red[w] = mx;
    BARRIER();
    if (tid == 0) {
      u32 m = 0;
#pragma unroll
      for (int i = 0; i < 8; ++i) m = max(m, red[i]);
      ((u32*)(ws + OFF_MAX))[b] = m;
    }
  }
  STAMPT(4);
  STAMPT(5);
  STAMP_OUT(ws, 0);
}

__global__ __launch_bounds__(512, 2) void k1(const u16* __restrict__ x, const u16* __restrict__ wr, const u16* __restrict__ wi,
                                             const u16* __restrict__ Wg, const u16* __restrict__ Wu, char* __restrict__ ws) {
  __shared__ __attribute__((aligned(16))) char smem_[163840];
  LAS char* smem = (LAS char*)smem_;
  const int b = blockIdx.x, xcd = b & 7, idx = b >> 3;
  if (idx < 28) ffn1(x, wr, wi, Wg, Wu, ws, (xcd >> 1) * 4 + idx / 7, (xcd & 1) * 7 + idx % 7, smem);
  else prep(x, wr, wi, ws, xcd * 4 + (idx - 28), smem);
}

constexpr int L_X = 131072;
constexpr int L_E1 = L_X, L_E2 = L_X + 16384;
constexpr int E1P = 544, E2P = 1024 + 72;
constexpr int PUB_V = 1, POLL_V = 6, CHK_V = 9;
DEVI constexpr bool is_s(int v) { return v == 12 || v == 13; }
DEVI constexpr int gidx(int v) { return v < 12 ? v : v - 2; }

__global__ __launch_bounds__(512, 2) void k2(const u16* __restrict__ x, const u16* __restrict__ Wd, char* __restrict__ ws, u16* __restrict__ out) {
  __shared__ __attribute__((aligned(16))) char smem_[163840];
  LAS char* smem = (LAS char*)smem_;
  const u32 lbase = (u32)(size_t)smem;
  const int tid = threadIdx.x, l = tid & 63;
  const int w = __builtin_amdgcn_readfirstlane(tid >> 6);
  const int b = blockIdx.x, xcd = b & 7, idx = b >> 3;
  const int RG = xcd * 4 + (idx >> 3), ct = idx & 7, kd = b;
  const int g = l >> 4, o = l & 15;
  const int h = l >> 5, blk = (l >> 4) & 1, q = (l & 15) >> 2, pp = l & 3;
  const int kh = w >> 2, wq = w & 3, dh = wq >> 1, ph = wq & 1;
  STAMP_DECL; STAMPT(0);

  v4u s1v, wt, mxv;
  {
    const u32 vo = (u32)(w * 1024 + l * 16);
    asm volatile("global_load_dwordx4 %0, %1, %2" : "=v"(s1v) : "v"(vo), "s"(ws + OFF_S1 + (size_t)kd * 8192) : "memory");
    asm volatile("global_load_dwordx4 %0, %1, %2" : "=v"(wt) : "v"(vo), "s"(ws + OFF_WT + (size_t)kd * 8192) : "memory");
    asm volatile("global_load_dwordx4 %0, %1, %2" : "=v"(mxv) : "v"((u32)(l * 16)), "s"(ws + OFF_MAX) : "memory");
  }
  const int epi = RG * 64 + ph * 32 + (l & 31);
  const size_t eoff = (size_t)s_of_pi(epi) * D_ + ct * 64 + dh * 32 + kh * 16 + h * 8;
  v4u xe; float rs2;
  asm volatile("global_load_dwordx4 %0, %1, %2" : "=v"(xe) : "v"((u32)(eoff * 2)), "s"(x) : "memory");
  asm volatile("global_load_dword %0, %1, %2" : "=v"(rs2) : "v"((u32)(epi * 4)), "s"(ws + OFF_RS2) : "memory");

  const u32 rowoff = (u32)((l >> 3) * 1024) + (((l & 7) ^ (((l >> 4) & 1) << 2)) << 4);
  const u32 rowoff4 = (u32)((l >> 3) * 4096) + (((l & 7) ^ (((l >> 4) & 1) << 2)) << 4);
  const char* hsrc = ws + OFF_H + ((size_t)(RG * 2 + (wq >> 1)) * 112 + (wq & 1) * 4) * 1024;
  const char* bsrc = (const char*)Wd + (size_t)(wq * 32) * 1024 + ct * 128;
  const char* ssrc = ws + OFF_S2 + (size_t)(wq * 64) * 4096 + RG * 128;
  auto issueG = [&](int u, int slot) {
    u32 dst = lbase + slot * 32768 + wq * 4096;
    dma4((u32)(l * 16), hsrc + u * 8192, dst);
#pragma unroll
    for (int i = 0; i < 4; ++i) dma(rowoff, bsrc + (size_t)u * 131072 + i * 8192, dst + 16384 + i * 1024);
  };
  auto issueS = [&](int u, int slot) {
    u32 dst = lbase + slot * 32768 + wq * 8192;
#pragma unroll
    for (int i = 0; i < 8; ++i) dma_sc1(rowoff4, ssrc + (size_t)u * (256 * 4096) + i * 32768, dst + i * 1024);
  };
  issueG(2 * kh, 2 * kh); issueG(2 * kh + 1, 2 * kh + 1);

  float wscale, oscale;
  {
    v8h F16re, F16im, F8re, F8im;
    v4f t1c, t1s, t2c, t2s, t3c, t3s;
#pragma unroll
    for (int e = 0; e < 4; ++e) {
      int id = 4 * g + e;
      float a = (float)((id * o) & 15) * (1.0f / 16.0f);
      float c = 0.25f * cosr(a), s = 0.25f * sinr(a);
      F16re[e] = (_Float16)c; F16re[4 + e] = (_Float16)s; F16im[e] = (_Float16)(-s); F16im[4 + e] = (_Float16)c;
      float a8 = (float)((id * o) & 7) * (1.0f / 8.0f);
      float m = ((id ^ o) & 8) ? 0.f : 0.25f;
      float c8 = m * cosr(a8), s8 = m * sinr(a8);
      F8re[e] = (_Float16)c8; F8re[4 + e] = (_Float16)s8; F8im[e] = (_Float16)(-s8); F8im[4 + e] = (_Float16)c8;
      float b1 = (float)(id * o) * (1.0f / 256.0f);
      t1c[e] = cosr(b1); t1s[e] = sinr(b1);
      float b2 = (float)(w * (o + 16 * id)) * (1.0f / 2048.0f);
      t2c[e] = cosr(b2); t2s[e] = sinr(b2);
      float b3 = (float)((o & 7) * (2 * w + (o >> 3) + 16 * id)) * (1.0f / 2048.0f);
      t3c[e] = cosr(b3); t3s[e] = sinr(b3);
    }
    asm volatile("" : "+v"(F16re), "+v"(F16im), "+v"(F8re), "+v"(F8im), "+v"(t1c), "+v"(t1s), "+v"(t2c), "+v"(t2s), "+v"(t3c), "+v"(t3s));
    asm volatile("s_waitcnt vmcnt(18)" : "+v"(s1v), "+v"(wt), "+v"(mxv)::"memory");
    STAMPT(1);
    const v4f z = {0.f, 0.f, 0.f, 0.f};
    v8h a = __builtin_bit_cast(v8h, s1v);
    v4f re = mfma16h(a, F16re, z), im = mfma16h(a, F16im, z);
    {
      int E = (int)(wave_max(max(max(mxv[0], mxv[1]), max(mxv[2], mxv[3]))) >> 7);
      E = E < 16 ? 16 : (E > 240 ? 240 : E);
      wscale = __builtin_bit_cast(float, (u32)(256 - E) << 23);
      oscale = __builtin_bit_cast(float, (u32)(E - 5) << 23);
    }
    twid(re, im, t1c, t1s);
    a = pack_ri(re, im);
    re = mfma16h(F16re, a, z); im = mfma16h(F16im, a, z);
    twid(re, im, t2c, t2s);
    *(LAS v4h*)(smem + L_E1 + w * E1P + o * 32 + g * 8) = pack4h(re);
    *(LAS v4h*)(smem + L_E1 + (8 + w) * E1P + o * 32 + g * 8) = pack4h(im);
    BARRIER();
    {
      LAS char* rp = smem + L_E1 + (4 * (g & 1) + (o >> 2)) * E1P + (2 * w + (g >> 1)) * 32 + (o & 3) * 8;
      a = __builtin_bit_cast(v8h, cat8(trread(rp), trread(rp + 8 * E1P)));
    }
    re = mfma16h(F8re, a, z); im = mfma16h(F8im, a, z);
    if (kd != 0) {
#pragma unroll
      for (int r = 0; r < 4; ++r) {
        float fr = bflo(wt[r]) * wscale, fi = bfhi(wt[r]) * wscale;
        float zr = re[r], zi = im[r];
        re[r] = zr * fr - zi * fi;
        im[r] = -(zr * fi + zi * fr);
      }
    } else {
      const int ka = 2 * w + (g >> 1), kb = o;
      LAS v2f* ZX = (LAS v2f*)(smem + L_X);
      BARRIER();
#pragma unroll
      for (int r = 0; r < 4; ++r) { v2f t = {re[r], im[r]}; ZX[ka + 16 * kb + 256 * (4 * (g & 1) + r)] = t; }
      BARRIER();
      const u32* W0 = (const u32*)(ws + OFF_WT);
      const u32* WN = (const u32*)(ws + OFF_WT + (size_t)256 * 8192);
#pragma unroll
      for (int r = 0; r < 4; ++r) {
        int ks = ka + 16 * kb + 256 * (4 * (g & 1) + r);
        int km = (2048 - ks) & 2047;
        int ma = km & 15, mb = (km >> 4) & 15, mc = km >> 8;
        int pos = w * 256 + l * 4 + r;
        int posm = (ma >> 1) * 256 + ((ma & 1) * 2 + (mc >> 2)) * 64 + mb * 4 + (mc & 3);
        u32 a0 = wt[r], a0m = W0[posm], an = WN[pos], anm = WN[posm];
        float war = bflo(a0) + bflo(a0m), wai = bfhi(a0) - bfhi(a0m);
        float wbr = bflo(an) + bflo(anm), wbi = bfhi(an) - bfhi(anm);
        float sc = 0.25f * wscale;
        float pr = (war + wbr) * sc, pi = (wai + wbi) * sc, qr = (war - wbr) * sc, qi = (wai - wbi) * sc;
        v2f zm = ZX[km];
        float zr = re[r], zi = im[r];
        re[r] = zr * pr - zi * pi + zm[0] * qr + zm[1] * qi;
        im[r] = -(zr * pi + zi * pr + zm[0] * qi - zm[1] * qr);
      }
    }
    a = pack_ri(re, im);
    re = mfma16h(a, F8re, z); im = mfma16h(a, F8im, z);
    twid(re, im, t3c, t3s);
    *(LAS v4h*)(smem + L_E2 + (o & 7) * E2P + (2 * w + (o >> 3)) * 32 + g * 8) = pack4h(re);
    *(LAS v4h*)(smem + L_E2 + (o & 7) * E2P + 512 + (2 * w + (o >> 3)) * 32 + g * 8) = pack4h(im);
    BARRIER();
    {
      LAS char* rp = smem + L_E2 + w * E2P + o * 32 + g * 8;
      a = __builtin_bit_cast(v8h, cat8(*(LAS v4s*)rp, *(LAS v4s*)(rp + 512)));
    }
    re = mfma16h(a, F16re, z); im = mfma16h(a, F16im, z);
    twid(re, im, t1c, t1s);
    a = pack_ri(re, im);
    re = mfma16h(F16re, a, z); im = mfma16h(F16im, a, z);
    GAS u64* dst = (GAS u64*)(ws + OFF_S2 + (size_t)(2 * kd) * 4096 + w * 512 + l * 8);
    __hip_atomic_store(dst, __builtin_bit_cast(u64, pack4h(re)), __ATOMIC_RELAXED, __HIP_MEMORY_SCOPE_AGENT);
    __hip_atomic_store(dst + 512, __builtin_bit_cast(u64, pack4h(im)), __ATOMIC_RELAXED, __HIP_MEMORY_SCOPE_AGENT);
  }
  STAMPT(2);

  const u32 trG = (u32)(16384 + kh * 8192 + (4 * h + q) * 128 + ((dh ^ ((q >> 1) & 1)) << 6) + (blk ^ kh) * 32 + pp * 8);
  const u32 trS = (u32)(kh * 16384 + (4 * h + q) * 128 + ((ph ^ ((q >> 1) & 1)) << 6) + blk * 32 + pp * 8);
  const u32 hb = (u32)(ph * 8192 + kh * 4096 + l * 16);
  const int d = ct * 64 + dh * 32 + ((l & 31) ^ (kh << 4));
  const GAS v4u* flg = (const GAS v4u*)(ws + OFF_FLAG) + l;
  v16f accG = {}, accS = {};
  v8h G[16];
  v8s fa[8], fb[8];
  v4u fl;
  static_for<0, 17>([&](auto V) {
    constexpr int v = V;
    v8s(&cur)[8] = (v & 1) ? fb : fa;
    v8s(&prv)[8] = (v & 1) ? fa : fb;
    if constexpr (v < 16) {
      constexpr int imax = v + 2 < 3 ? 3 : (v + 2 > 15 ? 15 : v + 2);
      if (kh == 0) {
        if constexpr (v == PUB_V) WAIT_VM(0);
        else if constexpr (v < PUB_V) WAIT_VM(8 + 2);
        else if constexpr (!is_s(v)) {
          constexpr int ng = ((v + 1 <= imax && !is_s(v + 1)) ? 1 : 0) + ((v + 2 <= imax && !is_s(v + 2)) ? 1 : 0);
          WAIT_VM(8 * ng);
        }
      } else {
        if constexpr (v == PUB_V || v == 13) WAIT_VM(0);
        if constexpr (v == 12) WAIT_VM(8);
        if constexpr (v == CHK_V) {
          if (w == 4) {
            asm volatile("s_waitcnt vmcnt(0)" : "+v"(fl)::"memory");
            int spins = 0;
            while (!__all((fl[0] & fl[1] & fl[2] & fl[3]) == 1u) && ++spins < (1 << 20)) {
              __builtin_amdgcn_s_sleep(2);
              asm volatile("global_load_dwordx4 %0, %1, off sc1\n\ts_waitcnt vmcnt(0)" : "=&v"(fl) : "v"(flg) : "memory");
            }
          }
        }
      }
      BARRIER();
      if constexpr (v % 2 == 0) STAMPT(3 + v / 2);
      LAS char* sb = smem + (v & 3) * 32768;
      if constexpr (is_s(v)) {
#pragma unroll
        for (int s = 0; s < 8; ++s) cur[s] = cat8(trread(sb + trS + s * 2048), trread(sb + trS + s * 2048 + 1024));
      } else {
#pragma unroll
        for (int s = 0; s < 4; ++s) {
          cur[2 * s] = cat8(trread(sb + trG + s * 2048), trread(sb + trG + s * 2048 + 1024));
          cur[2 * s + 1] = *(LAS v8s*)(sb + hb + s * 1024);
        }
      }
      if constexpr (v >= 1 && v + 3 <= 15) {
        if constexpr (is_s(v + 3)) { if (kh == 1) issueS(v + 3 - 12, (v + 3) & 3); }
        else { if (kh == 0) issueG(gidx(v + 3), (v + 3) & 3); }
      }
      if constexpr (v == PUB_V) {
        if (tid == 256) __hip_atomic_store((GAS u32*)(ws + OFF_FLAG) + b, 1u, __ATOMIC_RELAXED, __HIP_MEMORY_SCOPE_AGENT);
      }
      if constexpr (v == POLL_V) {
        if (w == 4) asm volatile("global_load_dwordx4 %0, %1, off sc1" : "=&v"(fl) : "v"(flg) : "memory");
      }
      SCHED();
    }
    if constexpr (v >= 1) {
      if constexpr (is_s(v - 1)) {
#pragma unroll
        for (int s = 0; s < 8; ++s) accS = mfma32h(G[(v - 1 - 12) * 8 + s], prv[s], accS);
      } else {
#pragma unroll
        for (int s = 0; s < 4; ++s) accG = mfma32b(prv[2 * s], prv[2 * s + 1], accG);
      }
    }
    if constexpr (v >= 2 && v < 10) {
#pragma unroll
      for (int i = 0; i < 2; ++i) {
        constexpr int dummy = 0;
        const int f = 2 * (v - 2) + i + dummy;
        const int s = (f >> 3) * 16 + kh * 8 + (f & 7);
#pragma unroll
        for (int e2 = 0; e2 < 4; ++e2) {
          int kk = 8 * s + 4 * (e2 >> 1) + 2 * h + (e2 & 1);
          float a = (float)((kk * d) & 511) * (1.0f / 512.0f);
          float c = cosr(a), sn = sinr(a);
          if (f == 0 && e2 == 0) { if (kk == 0) { c = 0.5f; sn = (d & 1) ? 0.5f : -0.5f; } }
          G[f][2 * e2] = (_Float16)c; G[f][2 * e2 + 1] = (_Float16)sn;
        }
        asm volatile("" : "+v"(G[f]));
      }
    }
    if constexpr (v == 14) {
      LAS char* X = smem + L_X + w * 2048 + l * 16;
      v4f t0 = {accS[8], accS[9], accS[10], accS[11]}, t1 = {accS[12], accS[13], accS[14], accS[15]};
      *(LAS v4f*)X = t0; *(LAS v4f*)(X + 1024) = t1;
    }
    SCHED();
  });
  STAMPT(11);
  {
    LAS char* X = smem + L_X + 16384 + w * 2048 + l * 16;
    v4f t0 = {accG[8], accG[9], accG[10], accG[11]}, t1 = {accG[12], accG[13], accG[14], accG[15]};
    *(LAS v4f*)X = t0; *(LAS v4f*)(X + 1024) = t1;
  }
  float sv[8];
  {
    LAS char* X = smem + L_X + (w ^ 4) * 2048 + l * 16;
    v4f t0 = *(LAS v4f*)X, t1 = *(LAS v4f*)(X + 1024);
#pragma unroll
    for (int e = 0; e < 8; ++e) sv[e] = rbf((accS[e] + (e < 4 ? t0[e & 3] : t1[e & 3])) * oscale);
  }
  BARRIER();
  asm volatile("s_waitcnt vmcnt(0)" : "+v"(xe), "+v"(rs2)::"memory");
  {
    LAS char* X = smem + L_X + 16384 + (w ^ 4) * 2048 + l * 16;
    v4f t0 = *(LAS v4f*)X, t1 = *(LAS v4f*)(X + 1024);
    float pv[8];
#pragma unroll
    for (int e = 0; e < 8; ++e) {
      float ov = rbf((accG[e] + (e < 4 ? t0[e & 3] : t1[e & 3])) * rs2);
      float gv = rbf(ov * __builtin_amdgcn_rcpf(1.0f + __expf(-ov)));
      pv[e] = sv[e] * gv;
    }
    u32 a0 = pkbf(pv[0], pv[1]), a1 = pkbf(pv[2], pv[3]), b0 = pkbf(pv[4], pv[5]), b1 = pkbf(pv[6], pv[7]);
    auto r0 = __builtin_amdgcn_permlane32_swap(a0, b0, false, false);
    auto r1 = __builtin_amdgcn_permlane32_swap(a1, b1, false, false);
    v4u pq = {r0[0], r1[0], r0[1], r1[1]};
    v4u ov4;
#pragma unroll
    for (int e = 0; e < 4; ++e) ov4[e] = pkbf(bflo(xe[e]) + bflo(pq[e]), bfhi(xe[e]) + bfhi(pq[e]));
    *(v4u*)(out + eoff) = ov4;
  }
  STAMPT(12);
  STAMPT(13);
  STAMP_OUT(ws, 1);
}

extern "C" void kernel_launch(void* const* d_in, const int* in_sizes, int n_in, void* d_out, int out_size, void* d_ws,
                              size_t ws_size, hipStream_t stream) {
  const u16* x = (const u16*)d_in[0];
  const u16* wr = (const u16*)d_in[1];
  const u16* wi = (const u16*)d_in[2];
  const u16* Wg = (const u16*)d_in[4];
  const u16* Wu = (const u16*)d_in[5];
  const u16* Wd = (const u16*)d_in[6];
  char* ws = (char*)d_ws;
  k1<<<256, 512, 0, stream>>>(x, wr, wi, Wg, Wu, ws);
  k2<<<256, 512, 0, stream>>>(x, Wd, ws, (u16*)d_out);
}
```

```cpp
#include <hip/hip_runtime.h>
#include <stdint.h>

typedef unsigned short u16;
typedef unsigned int u32;
typedef unsigned long long u64;
typedef short v4s __attribute__((ext_vector_type(4)));
typedef short v8s __attribute__((ext_vector_type(8)));
typedef _Float16 v2h __attribute__((ext_vector_type(2)));
typedef _Float16 v4h __attribute__((ext_vector_type(4)));
typedef _Float16 v8h __attribute__((ext_vector_type(8)));
typedef __bf16 v2b __attribute__((ext_vector_type(2)));
typedef __bf16 v8b __attribute__((ext_vector_type(8)));
typedef float v2f __attribute__((ext_vector_type(2)));
typedef float v4f __attribute__((ext_vector_type(4)));
typedef float v16f __attribute__((ext_vector_type(16)));
typedef u32 v2u __attribute__((ext_vector_type(2)));
typedef u32 v4u __attribute__((ext_vector_type(4)));

#define LAS __attribute__((address_space(3)))
#define GAS __attribute__((address_space(1)))
#define DEVI __device__ __forceinline__

#define SPLIT_P2 0

constexpr int S_ = 2048, D_ = 512, F_ = 1792, DF_ = 257;
constexpr size_t OFF_H = 0;
constexpr size_t OFF_S1 = (size_t)8 << 20;
constexpr size_t OFF_WT = (size_t)10 << 20;
constexpr size_t OFF_S2 = (size_t)13 << 20;
constexpr size_t OFF_MAX = (size_t)15 << 20;
constexpr size_t OFF_RS2 = ((size_t)15 << 20) + 8192;
constexpr size_t OFF_PQ = ((size_t)15 << 20) + 65536;
constexpr size_t OFF_TAB = ((size_t)15 << 20) + 32768;
constexpr size_t OFF_FLAG = ((size_t)15 << 20) + 4096;

#define STAMP_DECL
#define STAMPT(id)
#define STAMPC(id)
#define STAMP_OUT(wsp, k)
template <int I> struct IC { static constexpr int v = I; constexpr operator int() const { return I; } };
template <int B, int E, class Fn> DEVI void static_for(Fn&& f) {
  if constexpr (B < E) { f(IC<B>{}); static_for<B + 1, E>(f); }
}

#define WAIT_VM(n) asm volatile("s_waitcnt vmcnt(%0)" ::"n"(n) : "memory")
#define BARRIER() asm volatile("s_waitcnt lgkmcnt(0)\n\ts_barrier" ::: "memory")

DEVI int s_of_pi(int pi) {
  int RG = pi >> 6, i = pi & 63;
  return 128 * (4 * (RG & 3) + (i & 3)) + 8 * (i >> 2) + (RG >> 2);
}
DEVI void dma(u32 voff, const char* sbase, u32 lds) {
  asm volatile("s_mov_b32 m0, %2\n\ts_nop 0\n\tglobal_load_lds_dwordx4 %0, %1" ::"v"(voff), "s"(sbase), "s"(lds) : "memory");
}
DEVI void dma_sc1(u32 voff, const char* sbase, u32 lds) {
  asm volatile("s_mov_b32 m0, %2\n\ts_nop 0\n\tglobal_load_lds_dwordx4 %0, %1 sc1" ::"v"(voff), "s"(sbase), "s"(lds) : "memory");
}
DEVI void dma2(u32 voff0, u32 voff1, const char* sbase, u32 lds) {
  asm volatile("s_mov_b32 m0, %3\n\ts_nop 0\n\tglobal_load_lds_dwordx4 %0, %2\n\tglobal_load_lds_dwordx4 %1, %2 offset:1024" ::"v"(voff0), "v"(voff1), "s"(sbase), "s"(lds) : "memory");
}
DEVI u32 pkbf_o(float a, float b) { u32 r; asm("v_cvt_pk_bf16_f32 %0, %1, %2" : "=v"(r) : "v"(a), "v"(b)); return r; }
DEVI void dma4(u32 voff, const char* sbase, u32 lds) {
  asm volatile("s_mov_b32 m0, %2\n\ts_nop 0\n\tglobal_load_lds_dwordx4 %0, %1\n\tglobal_load_lds_dwordx4 %0, %1 offset:1024\n\t"
               "global_load_lds_dwordx4 %0, %1 offset:2048\n\tglobal_load_lds_dwordx4 %0, %1 offset:3072" ::"v"(voff), "s"(sbase), "s"(lds) : "memory");
}
#define SCHED() __builtin_amdgcn_sched_barrier(0)
DEVI v4f mfma16b(v8b a, v4u b, v4f c) { return __builtin_amdgcn_mfma_f32_16x16x32_bf16(a, __builtin_bit_cast(v8b, b), c, 0, 0, 0); }
DEVI float dot2ones(u32 v, float acc) {
  const v2b ones = {(__bf16)1.0f, (__bf16)1.0f};
  return __builtin_amdgcn_fdot2_f32_bf16(__builtin_bit_cast(v2b, v), ones, acc, false);
}
template <int CTRL> DEVI u32 dppmax(u32 v) { return max(v, (u32)__builtin_amdgcn_update_dpp(0, (int)v, CTRL, 0xf, 0xf, true)); }
DEVI u32 wave_max(u32 v) {
  v = dppmax<0xB1>(v); v = dppmax<0x4E>(v); v = dppmax<0x141>(v); v = dppmax<0x140>(v);
  u32 a0 = __builtin_amdgcn_readlane(v, 0), a1 = __builtin_amdgcn_readlane(v, 16), a2 = __builtin_amdgcn_readlane(v, 32), a3 = __builtin_amdgcn_readlane(v, 48);
  return max(max(a0, a1), max(a2, a3));
}
DEVI v4s trread(LAS char* p) { return __builtin_amdgcn_ds_read_tr16_b64_v4i16((LAS v4s*)p); }
DEVI v8s cat8(v4s a, v4s b) { return __builtin_shufflevector(a, b, 0, 1, 2, 3, 4, 5, 6, 7); }
DEVI float bflo(u32 v) { return __builtin_bit_cast(float, v << 16); }
DEVI float bfhi(u32 v) { return __builtin_bit_cast(float, v & 0xffff0000u); }
DEVI float rbf(float v) { return (float)(__bf16)v; }
DEVI u32 pkbf(float a, float b) { v2b p = {(__bf16)a, (__bf16)b}; return __builtin_bit_cast(u32, p); }
DEVI float cosr(float rev) { return __builtin_amdgcn_cosf(rev); }
DEVI float sinr(float rev) { return __builtin_amdgcn_sinf(rev); }
DEVI v4f mfma16h(v8h a, v8h b, v4f c) { return __builtin_amdgcn_mfma_f32_16x16x32_f16(a, b, c, 0, 0, 0); }
DEVI v16f mfma32b(v8s a, v8s b, v16f c) { return __builtin_amdgcn_mfma_f32_32x32x16_bf16(__builtin_bit_cast(v8b, a), __builtin_bit_cast(v8b, b), c, 0, 0, 0); }
DEVI v16f mfma32h(v8h a, v8s b, v16f c) { return __builtin_amdgcn_mfma_f32_32x32x16_f16(a, __builtin_bit_cast(v8h, b), c, 0, 0, 0); }
DEVI v8h pack_ri(v4f re, v4f im) {
  v8h r;
  r[0] = (_Float16)re[0]; r[1] = (_Float16)re[1]; r[2] = (_Float16)re[2]; r[3] = (_Float16)re[3];
  r[4] = (_Float16)im[0]; r[5] = (_Float16)im[1]; r[6] = (_Float16)im[2]; r[7] = (_Float16)im[3];
  return r;
}
DEVI v4h pack4h(v4f a) { v4h r; r[0] = (_Float16)a[0]; r[1] = (_Float16)a[1]; r[2] = (_Float16)a[2]; r[3] = (_Float16)a[3]; return r; }
DEVI void twid(v4f& re, v4f& im, const v4f& c, const v4f& s) {
#pragma unroll
  for (int r = 0; r < 4; ++r) { float a = re[r], b = im[r]; re[r] = a * c[r] + b * s[r]; im[r] = b * c[r] - a * s[r]; }
}
template <int CTRL> DEVI float dppadd(float v) {
  return v + __builtin_bit_cast(float, __builtin_amdgcn_update_dpp(0, __builtin_bit_cast(int, v), CTRL, 0xf, 0xf, true));
}
DEVI float wave_sum(float v) {
  v = dppadd<0xB1>(v); v = dppadd<0x4E>(v); v = dppadd<0x141>(v); v = dppadd<0x140>(v);
  int b = __builtin_bit_cast(int, v);
  float a0 = __builtin_bit_cast(float, __builtin_amdgcn_readlane(b, 0)), a1 = __builtin_bit_cast(float, __builtin_amdgcn_readlane(b, 16));
  float a2 = __builtin_bit_cast(float, __builtin_amdgcn_readlane(b, 32)), a3 = __builtin_bit_cast(float, __builtin_amdgcn_readlane(b, 48));
  return (a0 + a1) + (a2 + a3);
}

struct WtUnit { u32 a[4], b[4]; };
DEVI void wt_load(WtUnit& u, const u16* __restrict__ wr, const u16* __restrict__ wi, int sp, int kb, int kd) {
#pragma unroll
  for (int r = 0; r < 4; ++r) {
    int ks = (sp >> 1) + 16 * kb + 256 * (4 * (sp & 1) + r);
    u.a[r] = wr[ks * DF_ + kd]; u.b[r] = wi[ks * DF_ + kd];
  }
}
DEVI u32 wt_store(const WtUnit& u, char* __restrict__ ws, int sp, int kb, int kd) {
  v4u un; u32 mx = 0;
#pragma unroll
  for (int r = 0; r < 4; ++r) { un[r] = u.a[r] | (u.b[r] << 16); mx = max(mx, max(u.a[r] & 0x7fffu, u.b[r] & 0x7fffu)); }
  *(v4u*)(ws + OFF_WT + (size_t)kd * 8192 + (sp >> 2) * 1024 + (sp & 3) * 256 + kb * 16) = un;
  return mx;
}

struct Frag1 { v8s x0, x1, w1, w2; };

DEVI void ffn1(const u16* __restrict__ x, const u16* __restrict__ wr, const u16* __restrict__ wi, const u16* __restrict__ Wg,
               const u16* __restrict__ Wu, char* __restrict__ ws, int p, int j, LAS char* smem) {
  const int tid = threadIdx.x, l = tid & 63, b = blockIdx.x;
  const int w = __builtin_amdgcn_readfirstlane(tid >> 6);
  const int sh = w >> 2, fq = w & 3;
  STAMP_DECL; STAMPT(0);
  u32 aoff[2];
#pragma unroll
  for (int i = 0; i < 2; ++i) {
    int row = (2 * w + i) * 8 + (l >> 3);
    aoff[i] = s_of_pi(p * 128 + row) * 1024 + (((l & 7) ^ ((row >> 1) & 7)) << 4);
  }
  const u32 boff = (u32)(2 * w * 4 + (l >> 4)) * (F_ * 2) + j * 256 + (((l & 15) ^ ((l >> 4) << 2)) << 4);
  const u32 boff1 = boff + 4 * F_ * 2 - 1024;
  aoff[1] -= 1024;
  const u32 lbase = (u32)(size_t)smem;
  auto issue = [&](int t, int slot) {
    u32 dst = lbase + slot * 49152 + w * 2048;
    const char* xa = (const char*)x + t * 128;
    const char* wg = (const char*)Wg + (size_t)t * (64 * F_ * 2);
    const char* wu = (const char*)Wu + (size_t)t * (64 * F_ * 2);
    dma2(aoff[0], aoff[1], xa, dst);
    dma2(boff, boff1, wg, dst + 16384);
    dma2(boff, boff1, wu, dst + 32768);
  };
  issue(0, 0);
  WtUnit wtu;
  wt_load(wtu, wr, wi, b >> 3, 2 * (b & 7) + (tid >> 8), tid & 255);
  issue(1, 1); issue(2, 2);

  const int h = l >> 5, blk = (l >> 4) & 1, q = (l & 15) >> 2, pp = l & 3;
  const u32 wb0 = (u32)(((8 * h + q) * 256 + ((fq ^ q) << 6) + blk * 32 + pp * 8) ^ (fq << 12));
  const u32 r1 = 16384 + sh * 16384, r2 = 32768 - sh * 16384;
  const int xrow = sh * 64 + (l & 31);
  const u32 xb0 = (u32)((xrow * 128 + ((h ^ ((xrow >> 1) & 7)) << 4)) ^ (fq << 5));

  v16f acc[2][2] = {};
  v4f csa = {0.f, 0.f, 0.f, 0.f};
  v2f s1[2] = {{0.f, 0.f}, {0.f, 0.f}};
  v8s sel;
  {
    short o1 = ((l & 15) == ((l >> 4) & 1)) ? (short)0x3f80 : (short)0;
    sel = v8s{o1, o1, o1, o1, o1, o1, o1, o1};
  }
  auto rd = [&](Frag1& f, LAS char* sb, int i) {
    LAS char* xa = sb + (xb0 ^ (i << 5));
    LAS char* wa = sb + (wb0 ^ (i << 12));
    f.w1 = cat8(trread(wa + r1), trread(wa + r1 + 1024));
    f.x0 = *(LAS v8s*)xa; f.x1 = *(LAS v8s*)(xa + 4096);
    f.w2 = cat8(trread(wa + r2), trread(wa + r2 + 1024));
  };
  auto mm = [&](const Frag1& f, int i) {
    acc[0][0] = mfma32b(f.w1, f.x0, acc[0][0]);
    acc[0][1] = mfma32b(f.w1, f.x1, acc[0][1]);
    csa = __builtin_amdgcn_mfma_f32_16x16x32_bf16(__builtin_bit_cast(v8b, f.w1), __builtin_bit_cast(v8b, sel), csa, 0, 0, 0);
    acc[1][0] = mfma32b(f.w2, f.x0, acc[1][0]);
    acc[1][1] = mfma32b(f.w2, f.x1, acc[1][1]);
    v4u a = __builtin_bit_cast(v4u, f.x0), c = __builtin_bit_cast(v4u, f.x1);
    s1[0] += v2f{bflo(a[i]), bfhi(a[i])}; s1[1] += v2f{bflo(c[i]), bfhi(c[i])};
    asm volatile("" : "+v"(s1[0]), "+v"(s1[1]));
  };
  if (b == 0 && w == 0) {
    const int g = l >> 4, o = l & 15;
    const float st1 = (float)o * (1.0f / 256.0f);
    const bool off = ((g >> 1) != (o >> 3));
    v8h F16, F8; v4f tc, ts;
#pragma unroll
    for (int e = 0; e < 4; ++e) {
      float b1 = (float)(4 * g + e) * st1;
      tc[e] = cosr(b1); ts[e] = sinr(b1);
      F16[e] = (_Float16)cosr(16.f * b1); F16[4 + e] = (_Float16)sinr(16.f * b1);
      F8[e] = (_Float16)(off ? 0.f : cosr(32.f * b1)); F8[4 + e] = (_Float16)(off ? 0.f : sinr(32.f * b1));
    }
    char* T = ws + OFF_TAB + l * 16;
    *(v8h*)T = F16; *(v8h*)(T + 1024) = F8; *(v4f*)(T + 2048) = tc; *(v4f*)(T + 3072) = ts;
  }
  Frag1 A, B;
  WAIT_VM(20);
  BARRIER();
  STAMPT(1); STAMPC(14);
  rd(A, smem, 0);
  int slot = 0;
  auto stage = [&](int t, auto MODE) {
    constexpr int mode = MODE;
    LAS char* sb = smem + slot * 49152;
    rd(B, sb, 1); SCHED(); mm(A, 0); SCHED();
    rd(A, sb, 2); SCHED(); mm(B, 1); SCHED();
    rd(B, sb, 3); SCHED(); mm(A, 2); SCHED();
    if constexpr (mode < 3) {
      if constexpr (mode < 2) WAIT_VM(6); else WAIT_VM(0);
      BARRIER();
      const int nslot = slot == 2 ? 0 : slot + 1;
      const u32 dst = lbase + slot * 49152 + w * 2048;
      const char* xa = (const char*)x + (t + 3) * 128;
      const char* wg = (const char*)Wg + (size_t)(t + 3) * (64 * F_ * 2);
      const char* wu = (const char*)Wu + (size_t)(t + 3) * (64 * F_ * 2);
      acc[0][0] = mfma32b(B.w1, B.x0, acc[0][0]); SCHED();
      rd(A, smem + nslot * 49152, 0); SCHED();
      acc[0][1] = mfma32b(B.w1, B.x1, acc[0][1]); SCHED();
      if constexpr (mode == 0) dma2(aoff[0], aoff[1], xa, dst);
      csa = __builtin_amdgcn_mfma_f32_16x16x32_bf16(__builtin_bit_cast(v8b, B.w1), __builtin_bit_cast(v8b, sel), csa, 0, 0, 0);
      acc[1][0] = mfma32b(B.w2, B.x0, acc[1][0]); SCHED();
      if constexpr (mode == 0) dma2(boff, boff1, wg, dst + 16384);
      acc[1][1] = mfma32b(B.w2, B.x1, acc[1][1]); SCHED();
      if constexpr (mode == 0) dma2(boff, boff1, wu, dst + 32768);
      v4u a = __builtin_bit_cast(v4u, B.x0), c = __builtin_bit_cast(v4u, B.x1);
      s1[0] += v2f{bflo(a[3]), bfhi(a[3])}; s1[1] += v2f{bflo(c[3]), bfhi(c[3])};
      asm volatile("" : "+v"(s1[0]), "+v"(s1[1]));
      slot = nslot;
      SCHED();
    } else {
      mm(B, 3); SCHED();
    }
  };
#pragma unroll 1
  for (int t = 0; t < 5; ++t) stage(t, IC<0>{});
  stage(5, IC<1>{}); stage(6, IC<2>{});
  LAS u32* RED = (LAS u32*)(smem + 147456 + 6144);
  {
    u32 mx = wave_max(wt_store(wtu, ws, b >> 3, 2 * (b & 7) + (tid >> 8), tid & 255));
    if (l == 0) RED[w] = mx;
  }
  SCHED();
  stage(7, IC<3>{});
  STAMPT(9); STAMPC(15);
  LAS char* ST = smem + 147456;
  LAS char* CS = smem + 147456 + 4096;
#pragma unroll
  for (int rt = 0; rt < 2; ++rt) *(LAS float*)(ST + (xrow + rt * 32) * 32 + (fq * 2 + h) * 4) = s1[rt][0] + s1[rt][1];
  if ((l & 15) < 2) *(LAS v4f*)(CS + (sh * 128 + fq * 32 + (l & 15) * 16 + (l >> 4) * 4) * 4) = csa;
  BARRIER();
  if (tid == 0) {
    u32 m = 0;
#pragma unroll
    for (int i = 0; i < 8; ++i) m = max(m, RED[i]);
    ((u32*)(ws + OFF_MAX))[b] = m;
  }
  v4f cg[4], cu[4];
#pragma unroll
  for (int g4 = 0; g4 < 4; ++g4) {
    cg[g4] = *(LAS v4f*)(CS + (fq * 32 + g4 * 8 + h * 4) * 4);
    cu[g4] = *(LAS v4f*)(CS + (128 + fq * 32 + g4 * 8 + h * 4) * 4);
  }
  auto fin = [&](const v16f& ag, const v16f& au, int rt) {
    v4f sa = *(LAS v4f*)(ST + (xrow + rt * 32) * 32), sb2 = *(LAS v4f*)(ST + (xrow + rt * 32) * 32 + 16);
    sa += sb2;
    const float mb = -rbf(((sa[0] + sa[1]) + (sa[2] + sa[3])) * (1.0f / 512.0f));
    const v2f mb2 = {mb, mb};
#pragma unroll
    for (int s2i = 0; s2i < 2; ++s2i) {
      v4u o;
#pragma unroll
      for (int e = 0; e < 4; ++e) {
        const int r = s2i * 8 + 2 * e;
        v2f gg = __builtin_elementwise_fma(mb2, v2f{cg[r >> 2][r & 3], cg[r >> 2][(r & 3) + 1]}, v2f{ag[r], ag[r + 1]});
        v2f uu = __builtin_elementwise_fma(mb2, v2f{cu[r >> 2][r & 3], cu[r >> 2][(r & 3) + 1]}, v2f{au[r], au[r + 1]});
        gg[0] = fmaxf(gg[0], 0.f); gg[1] = fmaxf(gg[1], 0.f);
        gg *= uu;
        o[e] = pkbf(gg[0], gg[1]);
      }
      size_t fr = (size_t)(p * 4 + sh * 2 + rt) * 112 + (j * 8 + fq * 2 + s2i);
      *(v4u*)(ws + OFF_H + fr * 1024 + l * 16) = o;
    }
  };
  if (sh == 0) { fin(acc[0][0], acc[1][0], 0); fin(acc[0][1], acc[1][1], 1); }
  else { fin(acc[1][0], acc[0][0], 0); fin(acc[1][1], acc[0][1], 1); }
  STAMPT(10);
  STAMPT(11);
  STAMP_OUT(ws, 0);
}

DEVI void prep(const u16* __restrict__ x, const u16* __restrict__ wr, const u16* __restrict__ wi, char* __restrict__ ws,
               int sp, LAS char* smem) {
  const int tid = threadIdx.x, l = tid & 63, b = blockIdx.x;
  const int w = __builtin_amdgcn_readfirstlane(tid >> 6);
  const int g = l >> 4, o = l & 15;
  STAMP_DECL; STAMPT(0);
  const int pi = sp * 64 + w * 8 + (l >> 3);
  v4u xr[8];
  {
    const u16* xp = x + s_of_pi(pi) * D_ + (l & 7) * 8;
#pragma unroll
    for (int m = 0; m < 8; ++m) xr[m] = *(const v4u*)(xp + m * 64);
  }
  WtUnit wtu;
  wt_load(wtu, wr, wi, b >> 3, 2 * (b & 7) + (tid >> 8), tid & 255);
  u32 sq[8];
  const int kss = (sp >> 1) + 16 * (l >> 2) + 256 * (4 * (sp & 1) + (l & 3));
  if (w == 7) {
    const int km = (2048 - kss) & 2047;
    sq[0] = wr[kss * DF_]; sq[1] = wi[kss * DF_]; sq[2] = wr[km * DF_]; sq[3] = wi[km * DF_];
    sq[4] = wr[kss * DF_ + 256]; sq[5] = wi[kss * DF_ + 256]; sq[6] = wr[km * DF_ + 256]; sq[7] = wi[km * DF_ + 256];
  }
  if (sp == 0 && tid < 256) ((u32*)(ws + OFF_FLAG))[tid] = 0u;

  v4u Fh[2][2], Fl[2][2];
  v8h M2, M2n;
  v4f twc[2], tws[2];
  {
    const float fo = (float)o, fg = (float)(4 * g);
    const float st = fo * (1.0f / 32.0f), a0 = fg * st;
#pragma unroll
    for (int e2 = 0; e2 < 4; ++e2) {
      float c[2], sn[2];
#pragma unroll
      for (int i = 0; i < 2; ++i) {
        const int e = 2 * e2 + i;
        float a = fmaf((float)(16 * (e >> 2) + (e & 3)), st, a0);
        c[i] = cosr(a); sn[i] = -sinr(a);
      }
      u32 ch = pkbf(c[0], c[1]), shh = pkbf(sn[0], sn[1]);
      u32 cl = pkbf(c[0] - bflo(ch), c[1] - bfhi(ch)), sl = pkbf(sn[0] - bflo(shh), sn[1] - bfhi(shh));
      Fh[0][0][e2] = ch; Fh[0][1][e2] = shh; Fl[0][0][e2] = cl; Fl[0][1][e2] = sl;
      Fh[1][0][e2] = ch ^ 0x80000000u; Fh[1][1][e2] = shh ^ 0x80000000u; Fl[1][0][e2] = cl ^ 0x80000000u; Fl[1][1][e2] = sl ^ 0x80000000u;
    }
    const float st2 = (float)(o >> 1) * (1.0f / 16.0f), qo = (o & 1) ? 0.25f : 0.f;
#pragma unroll
    for (int e = 0; e < 4; ++e) {
      const float db = fg + (float)e;
      float a = fmaf(db, st2, qo);
      M2[e] = (_Float16)(0.0625f * cosr(a)); M2[4 + e] = (_Float16)(0.0625f * sinr(a));
      M2n[e] = (_Float16)(o == 0 ? ((e & 1) ? -0.0625f : 0.0625f) : 0.f); M2n[4 + e] = (_Float16)0.f;
#pragma unroll
      for (int u = 0; u < 2; ++u) {
        float t = db * ((fo + 16.f * u) * (1.0f / 512.0f));
        twc[u][e] = cosr(t); tws[u][e] = sinr(t);
      }
    }
  }
  STAMPT(1); STAMPC(14);
  LAS char* img = smem + w * 8192;
  {
    float s = 0.f;
#pragma unroll
    for (int m = 0; m < 8; ++m)
#pragma unroll
      for (int e = 0; e < 4; ++e) s = dot2ones(xr[m][e], s);
    s = dppadd<0xB1>(s); s = dppadd<0x4E>(s); s = dppadd<0x141>(s);
    const float mean = s * (1.0f / 512.0f), mb = rbf(mean);
    const v2f mb2 = {mb, mb};
    v2f q2 = {0.f, 0.f};
#pragma unroll
    for (int m = 0; m < 8; ++m)
#pragma unroll
      for (int e = 0; e < 4; ++e) {
        v2f t = v2f{bflo(xr[m][e]), bfhi(xr[m][e])} - mb2;
        q2 = __builtin_elementwise_fma(t, t, q2);
        xr[m][e] = pkbf_o(t[0], t[1]);
      }
    float qq = q2[0] + q2[1];
    qq = dppadd<0xB1>(qq); qq = dppadd<0x4E>(qq); qq = dppadd<0x141>(qq);
    const float dm = mean - mb;
    const float var = rbf(fmaxf(qq * (1.0f / 512.0f) - dm * dm, 0.f));
    const float sd = rbf(__builtin_sqrtf(rbf(var + 1e-5f)));
    const float rs = 1.0f / sd;
    const v2f rs2 = {rs, rs};
    if ((l & 7) == 0) ((float*)(ws + OFF_RS2))[pi] = rs * rs;
#pragma unroll
    for (int m = 0; m < 8; ++m) {
      v4u o4;
#pragma unroll
      for (int e = 0; e < 4; ++e) {
        v2f t = v2f{bflo(xr[m][e]), bfhi(xr[m][e])} * rs2;
        o4[e] = pkbf(t[0], t[1]);
      }
      *(LAS v4u*)(img + (l >> 3) * 1024 + ((l & 7) + 8 * m) * 16) = o4;
    }
  }
  char* S1 = ws + OFF_S1;
  STAMPT(2);
#pragma unroll 1
  for (int nbi = 0; nbi < 2; ++nbi) {
    v4f d2[4][2];
#pragma unroll
    for (int r = 0; r < 4; ++r) {
      LAS char* rp = img + (nbi * 4 + r) * 1024 + l * 8;
      v8b xf = __builtin_bit_cast(v8b, cat8(trread(rp), trread(rp + 512)));
      float nyq = 0.f;
#pragma unroll
      for (int u = 0; u < 2; ++u) {
        v4f z = {0.f, 0.f, 0.f, 0.f};
        v4f re = mfma16b(xf, Fl[u][0], z), im = mfma16b(xf, Fl[u][1], z);
        re = mfma16b(xf, Fh[u][0], re); im = mfma16b(xf, Fh[u][1], im);
        twid(re, im, twc[u], tws[u]);
        v8h bb = pack_ri(re, im);
        d2[r][u] = mfma16h(M2, bb, z);
        if (u == 0) { v4f n = mfma16h(M2n, bb, z); nyq = n[0]; }
      }
      if (l == 0) d2[r][0][1] = nyq;
    }
#pragma unroll
    for (int u = 0; u < 2; ++u)
#pragma unroll
      for (int sub = 0; sub < 2; ++sub) {
        v8h un;
#pragma unroll
        for (int r = 0; r < 4; ++r) { un[r] = (_Float16)d2[r][u][2 * sub]; un[4 + r] = (_Float16)d2[r][u][2 * sub + 1]; }
        int kd = 16 * u + o + 32 * (2 * g + sub);
        *(v8h*)(S1 + (size_t)kd * 8192 + sp * 256 + (2 * w + nbi) * 16) = un;
      }
  }
  STAMPT(3); STAMPT(9); STAMPC(15);
  {
    u32 mx = wt_store(wtu, ws, b >> 3, 2 * (b & 7) + (tid >> 8), tid & 255);
    if (w == 7) {
      mx = max(mx, max(sq[4] & 0x7fffu, sq[5] & 0x7fffu));
      float war = bflo(sq[0]) + bflo(sq[2]), wai = bflo(sq[1]) - bflo(sq[3]);
      float wbr = bflo(sq[4]) + bflo(sq[6]), wbi = bflo(sq[5]) - bflo(sq[7]);
      v4f pq = {0.25f * (war + wbr), 0.25f * (wai + wbi), 0.25f * (war - wbr), 0.25f * (wai - wbi)};
      *(v4f*)(ws + OFF_PQ + (((sp >> 2) * 4 + (l & 3)) * 64 + (sp & 3) * 16 + (l >> 2)) * 16) = pq;
    }
    mx = wave_max(mx);
    LAS u32* red = (LAS u32*)(smem + 65536);
    if (l == 0) red[w] = mx;
    BARRIER();
    if (tid == 0) {
      u32 m = 0;
#pragma unroll
      for (int i = 0; i < 8; ++i) m = max(m, red[i]);
      ((u32*)(ws + OFF_MAX))[b] = m;
    }
  }
  STAMPT(4);
  STAMPT(5);
  STAMP_OUT(ws, 0);
}

__global__ __launch_bounds__(512, 2) void k1(const u16* __restrict__ x, const u16* __restrict__ wr, const u16* __restrict__ wi,
                                             const u16* __restrict__ Wg, const u16* __restrict__ Wu, char* __restrict__ ws) {
  __shared__ __attribute__((aligned(16))) char smem_[163840];
  LAS char* smem = (LAS char*)smem_;
  const int b = blockIdx.x, xcd = b & 7, idx = b >> 3;
  if (idx < 28) ffn1(x, wr, wi, Wg, Wu, ws, (xcd >> 1) * 4 + idx / 7, (xcd & 1) * 7 + idx % 7, smem);
  else prep(x, wr, wi, ws, xcd * 4 + (idx - 28), smem);
}

constexpr int L_X = 131072;
constexpr int L_E1 = L_X, L_E2 = L_X + 16384, L_ZX = L_X + 16384;
constexpr int E1P = 544, E2P = 1024 + 72;
constexpr int PUB_V = 1, POLL_V = 6, CHK_V = 9;
DEVI constexpr bool is_s(int v) { return v == 12 || v == 13; }
DEVI constexpr int gidx(int v) { return v < 12 ? v : v - 2; }

__global__ __launch_bounds__(512, 2) void k2(const u16* __restrict__ x, const u16* __restrict__ Wd, char* __restrict__ ws, u16* __restrict__ out) {
  __shared__ __attribute__((aligned(16))) char smem_[163840];
  LAS char* smem = (LAS char*)smem_;
  const u32 lbase = (u32)(size_t)smem;
  const int tid = threadIdx.x, l = tid & 63;
  const int w = __builtin_amdgcn_readfirstlane(tid >> 6);
  const int b = blockIdx.x, xcd = b & 7, idx = b >> 3;
  const int RG = xcd * 4 + (idx >> 3), ct = idx & 7, kd = b;
  const int g = l >> 4, o = l & 15;
  const int h = l >> 5, blk = (l >> 4) & 1, q = (l & 15) >> 2, pp = l & 3;
  const int kh = w >> 2, wq = w & 3, dh = wq >> 1, ph = wq & 1;
  STAMP_DECL; STAMPT(0);

  v4u s1v, wt, mxv, f16, f8;
  v4f t1c, t1s;
  {
    const u32 vo = (u32)(w * 1024 + l * 16);
    asm volatile("global_load_dwordx4 %0, %1, %2" : "=v"(s1v) : "v"(vo), "s"(ws + OFF_S1 + (size_t)kd * 8192) : "memory");
    asm volatile("global_load_dwordx4 %0, %1, %2" : "=v"(wt) : "v"(vo), "s"(ws + OFF_WT + (size_t)kd * 8192) : "memory");
    asm volatile("global_load_dwordx4 %0, %1, %2" : "=v"(mxv) : "v"((u32)(l * 16)), "s"(ws + OFF_MAX) : "memory");
    asm volatile("global_load_dwordx4 %0, %4, %5\n\tglobal_load_dwordx4 %1, %4, %5 offset:1024\n\tglobal_load_dwordx4 %2, %4, %5 offset:2048\n\tglobal_load_dwordx4 %3, %4, %5 offset:3072"
                 : "=&v"(f16), "=&v"(f8), "=&v"(t1c), "=&v"(t1s) : "v"((u32)(l * 16)), "s"(ws + OFF_TAB) : "memory");
  }
  v4f pq[4];
  if (kd == 0) {
#pragma unroll
    for (int r = 0; r < 4; ++r)
      asm volatile("global_load_dwordx4 %0, %1, %2" : "=v"(pq[r]) : "v"((u32)(w * 4096 + r * 1024 + l * 16)), "s"(ws + OFF_PQ) : "memory");
  }
  const int epi = RG * 64 + ph * 32 + (l & 31);
  const size_t eoff = (size_t)s_of_pi(epi) * D_ + ct * 64 + dh * 32 + kh * 16 + h * 8;
  v4u xe; float rs2;
  asm volatile("global_load_dwordx4 %0, %1, %2" : "=v"(xe) : "v"((u32)(eoff * 2)), "s"(x) : "memory");
  asm volatile("global_load_dword %0, %1, %2" : "=v"(rs2) : "v"((u32)(epi * 4)), "s"(ws + OFF_RS2) : "memory");

  const u32 swz = ((l & 7) ^ (((l >> 4) & 1) << 2)) << 4;
  const u32 ro0 = (u32)((l >> 3) * 1024) + swz, ro1 = ro0 + 8192 - 1024;
  const u32 so0 = (u32)((l >> 3) * 4096) + swz;
  const char* hsrc = ws + OFF_H + ((size_t)(RG * 2 + (w >> 2)) * 112 + (w & 3) * 2) * 1024;
  const char* bsrc = (const char*)Wd + (size_t)(w * 16) * 1024 + ct * 128;
  const char* ssrc = ws + OFF_S2 + (size_t)(w * 32) * 4096 + RG * 128;
  auto issueG = [&](int u, int slot) {
    u32 dst = lbase + slot * 32768 + w * 2048;
    dma2((u32)(l * 16), (u32)(l * 16), hsrc + u * 8192, dst);
    dma2(ro0, ro1, bsrc + (size_t)u * 131072, dst + 16384);
  };
  auto issueS = [&](int u, int slot) {
    u32 dst = lbase + slot * 32768 + w * 4096;
#pragma unroll
    for (int i = 0; i < 4; ++i) dma_sc1(so0, ssrc + (size_t)u * (256 * 4096) + i * 32768, dst + i * 1024);
  };
  issueG(0, 0); issueG(1, 1); issueG(2, 2); issueG(3, 3);

  float wscale, oscale;
  {
    v4f t2c, t2s, t3c, t3s;
    {
      const float fo = (float)o, fg = (float)(4 * g), fw = (float)w;
      const float st2 = fw * (1.0f / 128.0f), a2 = fw * fo * (1.0f / 2048.0f);
      const float f7 = (float)(o & 7), st3 = f7 * (1.0f / 128.0f), a3 = f7 * (float)(2 * w + (o >> 3)) * (1.0f / 2048.0f);
#pragma unroll
      for (int e = 0; e < 4; ++e) {
        const float id = fg + (float)e;
        float b2 = fmaf(id, st2, a2);
        t2c[e] = cosr(b2); t2s[e] = sinr(b2);
        float b3 = fmaf(id, st3, a3);
        t3c[e] = cosr(b3); t3s[e] = sinr(b3);
      }
    }
    asm volatile("" : "+v"(t2c), "+v"(t2s), "+v"(t3c), "+v"(t3s));
    asm volatile("s_waitcnt vmcnt(18)" : "+v"(s1v), "+v"(wt), "+v"(mxv), "+v"(f16), "+v"(f8), "+v"(t1c), "+v"(t1s)::"memory");
    const v8h F16re = __builtin_bit_cast(v8h, f16), F8re = __builtin_bit_cast(v8h, f8);
    const v8h F16im = __builtin_bit_cast(v8h, v4u{f16[2] ^ 0x80008000u, f16[3] ^ 0x80008000u, f16[0], f16[1]});
    const v8h F8im = __builtin_bit_cast(v8h, v4u{f8[2] ^ 0x80008000u, f8[3] ^ 0x80008000u, f8[0], f8[1]});
    STAMPT(1); STAMPC(14);
    const v4f z = {0.f, 0.f, 0.f, 0.f};
    v8h a = __builtin_bit_cast(v8h, s1v);
    v4f re = mfma16h(a, F16re, z), im = mfma16h(a, F16im, z);
    {
      int E = (int)(wave_max(max(max(mxv[0], mxv[1]), max(mxv[2], mxv[3]))) >> 7);
      E = E < 16 ? 16 : (E > 240 ? 240 : E);
      wscale = __builtin_bit_cast(float, (u32)(246 - E) << 23);
      oscale = __builtin_bit_cast(float, (u32)(E - 7) << 23);
    }
    twid(re, im, t1c, t1s);
    a = pack_ri(re, im);
    re = mfma16h(F16re, a, z); im = mfma16h(F16im, a, z);
    twid(re, im, t2c, t2s);
    *(LAS v4h*)(smem + L_E1 + w * E1P + o * 32 + g * 8) = pack4h(re);
    *(LAS v4h*)(smem + L_E1 + (8 + w) * E1P + o * 32 + g * 8) = pack4h(im);
    BARRIER();
    {
      LAS char* rp = smem + L_E1 + (4 * (g & 1) + (o >> 2)) * E1P + (2 * w + (g >> 1)) * 32 + (o & 3) * 8;
      a = __builtin_bit_cast(v8h, cat8(trread(rp), trread(rp + 8 * E1P)));
    }
    re = mfma16h(F8re, a, z); im = mfma16h(F8im, a, z);
    if (kd != 0) {
#pragma unroll
      for (int r = 0; r < 4; ++r) {
        float fr = bflo(wt[r]) * wscale, fi = bfhi(wt[r]) * wscale;
        float zr = re[r], zi = im[r];
        re[r] = zr * fr - zi * fi;
        im[r] = -(zr * fi + zi * fr);
      }
    } else {
      const int k0 = 2 * w + (g >> 1) + 16 * o + 1024 * (g & 1);
      LAS v2f* ZX = (LAS v2f*)(smem + L_ZX);
#pragma unroll
      for (int r = 0; r < 4; ++r) { v2f t = {re[r], im[r]}; ZX[k0 + 256 * r] = t; }
      BARRIER();
      asm volatile("" : "+v"(pq[0]), "+v"(pq[1]), "+v"(pq[2]), "+v"(pq[3]));
#pragma unroll
      for (int r = 0; r < 4; ++r) {
        v2f zm = ZX[(2048 - k0 - 256 * r) & 2047];
        float zr = re[r], zi = im[r];
        re[r] = (zr * pq[r][0] - zi * pq[r][1] + zm[0] * pq[r][2] + zm[1] * pq[r][3]) * wscale;
        im[r] = -(zr * pq[r][1] + zi * pq[r][0] + zm[0] * pq[r][3] - zm[1] * pq[r][2]) * wscale;
      }
    }
    LAS char* E2 = smem + (kd == 0 ? L_E1 : L_E2);
    a = pack_ri(re, im);
    re = mfma16h(a, F8re, z); im = mfma16h(a, F8im, z);
    twid(re, im, t3c, t3s);
    *(LAS v4h*)(E2 + (o & 7) * E2P + (2 * w + (o >> 3)) * 32 + g * 8) = pack4h(re);
    *(LAS v4h*)(E2 + (o & 7) * E2P + 512 + (2 * w + (o >> 3)) * 32 + g * 8) = pack4h(im);
    BARRIER();
    {
      LAS char* rp = E2 + w * E2P + o * 32 + g * 8;
      a = __builtin_bit_cast(v8h, cat8(*(LAS v4s*)rp, *(LAS v4s*)(rp + 512)));
    }
    re = mfma16h(a, F16re, z); im = mfma16h(a, F16im, z);
    twid(re, im, t1c, t1s);
    a = pack_ri(re, im);
    re = mfma16h(F16re, a, z); im = mfma16h(F16im, a, z);
    GAS u64* dst = (GAS u64*)(ws + OFF_S2 + (size_t)(2 * kd) * 4096 + w * 512 + l * 8);
    __hip_atomic_store(dst, __builtin_bit_cast(u64, pack4h(re)), __ATOMIC_RELAXED, __HIP_MEMORY_SCOPE_AGENT);
    __hip_atomic_store(dst + 512, __builtin_bit_cast(u64, pack4h(im)), __ATOMIC_RELAXED, __HIP_MEMORY_SCOPE_AGENT);
  }
  STAMPT(2);

  const u32 trG = (u32)(16384 + kh * 8192 + (4 * h + q) * 128 + ((dh ^ ((q >> 1) & 1)) << 6) + (blk ^ kh) * 32 + pp * 8);
  const u32 trS = (u32)(kh * 16384 + (4 * h + q) * 128 + ((ph ^ ((q >> 1) & 1)) << 6) + blk * 32 + pp * 8);
  const u32 hb = (u32)(ph * 8192 + kh * 4096 + l * 16);
  const int d = ct * 64 + dh * 32 + ((l & 31) ^ (kh << 4));
  const float gst = (float)d * (1.0f / 512.0f), ga0 = (float)(64 * kh + 2 * h) * gst;
  const GAS v4u* flg = (const GAS v4u*)(ws + OFF_FLAG) + l;
  v16f accG = {}, accS = {};
  v8h G[16];
  v8s fa[8], fb[8];
  v4u fl;
  static_for<0, 17>([&](auto V) {
    constexpr int v = V;
    v8s(&cur)[8] = (v & 1) ? fb : fa;
    v8s(&prv)[8] = (v & 1) ? fa : fb;
    auto mmp = [&](int s0, int s1) {
      if constexpr (v >= 1) {
        if constexpr (is_s(v - 1)) {
#pragma unroll
          for (int s = 2 * s0; s < 2 * s1; ++s) accS = mfma32h(G[(v - 1 - 12) * 8 + s], prv[s], accS);
        } else {
#pragma unroll
          for (int s = s0; s < s1; ++s) accG = mfma32b(prv[2 * s], prv[2 * s + 1], accG);
        }
      }
    };
    if constexpr (v < 16) {
      if constexpr (v == 0) WAIT_VM(12 + 2);
      else if constexpr (v == PUB_V) WAIT_VM(0);
      else if constexpr (v >= 4) WAIT_VM(v <= 13 ? 8 : (v == 14 ? 4 : 0));
      if constexpr (v == CHK_V) {
        if (w == 4) {
          asm volatile("" : "+v"(fl));
          int spins = 0;
          while (!__all((fl[0] & fl[1] & fl[2] & fl[3]) == 1u) && ++spins < (1 << 20)) {
            __builtin_amdgcn_s_sleep(2);
            asm volatile("global_load_dwordx4 %0, %1, off sc1\n\ts_waitcnt vmcnt(0)" : "=&v"(fl) : "v"(flg) : "memory");
          }
        }
      }
      BARRIER();
      if constexpr (v % 2 == 0) STAMPT(3 + v / 2);
      if constexpr (v == 12) STAMPC(15);
      LAS char* sb = smem + (v & 3) * 32768;
      mmp(0, 1); SCHED();
      if constexpr (v >= 1 && v + 3 <= 15) {
        if constexpr (is_s(v + 3)) issueS(v + 3 - 12, (v + 3) & 3);
        else issueG(gidx(v + 3), (v + 3) & 3);
      }
      if constexpr (v == PUB_V) {
        if (tid == 256) __hip_atomic_store((GAS u32*)(ws + OFF_FLAG) + b, 1u, __ATOMIC_RELAXED, __HIP_MEMORY_SCOPE_AGENT);
      }
      if constexpr (v == POLL_V) {
        if (w == 4) asm volatile("global_load_dwordx4 %0, %1, off sc1" : "=&v"(fl) : "v"(flg) : "memory");
      }
      mmp(1, 2); SCHED();
      if constexpr (is_s(v)) {
#pragma unroll
        for (int s = 0; s < 8; ++s) cur[s] = cat8(trread(sb + trS + s * 2048), trread(sb + trS + s * 2048 + 1024));
      } else {
#pragma unroll
        for (int s = 0; s < 4; ++s) {
          cur[2 * s] = cat8(trread(sb + trG + s * 2048), trread(sb + trG + s * 2048 + 1024));
          cur[2 * s + 1] = *(LAS v8s*)(sb + hb + s * 1024);
        }
      }
      SCHED();
      mmp(2, 4);
    } else {
      mmp(0, 4);
    }
    if constexpr (v >= 1 && v <= 11) {
      constexpr int f0 = ((v - 1) * 16) / 11, f1 = (v * 16) / 11;
      static_for<f0, f1>([&](auto FI) {
        constexpr int f = FI;
#pragma unroll
        for (int e2 = 0; e2 < 4; ++e2) {
          float a = fmaf((float)(128 * (f >> 3) + 8 * (f & 7) + 4 * (e2 >> 1) + (e2 & 1)), gst, ga0);
          float c = cosr(a), sn = sinr(a);
          if (f == 0 && e2 == 0) { if (kh == 0 && h == 0) { c = 0.5f; sn = (d & 1) ? 0.5f : -0.5f; } }
          G[f][2 * e2] = (_Float16)c; G[f][2 * e2 + 1] = (_Float16)sn;
        }
        asm volatile("" : "+v"(G[f]));
      });
    }
    if constexpr (v == 14) {
      LAS char* X = smem + L_X + w * 2048 + l * 16;
      v4f t0 = {accS[8], accS[9], accS[10], accS[11]}, t1 = {accS[12], accS[13], accS[14], accS[15]};
      *(LAS v4f*)X = t0; *(LAS v4f*)(X + 1024) = t1;
    }
    SCHED();
  });
  STAMPT(11);
  {
    LAS char* X = smem + L_X + 16384 + w * 2048 + l * 16;
    v4f t0 = {accG[8], accG[9], accG[10], accG[11]}, t1 = {accG[12], accG[13], accG[14], accG[15]};
    *(LAS v4f*)X = t0; *(LAS v4f*)(X + 1024) = t1;
  }
  float sv[8];
  {
    LAS char* X = smem + L_X + (w ^ 4) * 2048 + l * 16;
    v4f t0 = *(LAS v4f*)X, t1 = *(LAS v4f*)(X + 1024);
#pragma unroll
    for (int e = 0; e < 8; ++e) sv[e] = rbf((accS[e] + (e < 4 ? t0[e & 3] : t1[e & 3])) * oscale);
  }
  BARRIER();
  asm volatile("s_waitcnt vmcnt(0)" : "+v"(xe), "+v"(rs2)::"memory");
  {
    LAS char* X = smem + L_X + 16384 + (w ^ 4) * 2048 + l * 16;
    v4f t0 = *(LAS v4f*)X, t1 = *(LAS v4f*)(X + 1024);
    float pv[8];
#pragma unroll
    for (int e = 0; e < 8; ++e) {
      float ov = rbf((accG[e] + (e < 4 ? t0[e & 3] : t1[e & 3])) * rs2);
      float gv = rbf(ov * __builtin_amdgcn_rcpf(1.0f + __expf(-ov)));
      pv[e] = sv[e] * gv;
    }
    u32 a0 = pkbf(pv[0], pv[1]), a1 = pkbf(pv[2], pv[3]), b0 = pkbf(pv[4], pv[5]), b1 = pkbf(pv[6], pv[7]);
    auto r0 = __builtin_amdgcn_permlane32_swap(a0, b0, false, false);
    auto r1 = __builtin_amdgcn_permlane32_swap(a1, b1, false, false);
    v4u pq = {r0[0], r1[0], r0[1], r1[1]};
    v4u ov4;
#pragma unroll
    for (int e = 0; e < 4; ++e) ov4[e] = pkbf(bflo(xe[e]) + bflo(pq[e]), bfhi(xe[e]) + bfhi(pq[e]));
    *(v4u*)(out + eoff) = ov4;
  }
  STAMPT(12);
  STAMPT(13);
  STAMP_OUT(ws, 1);
}

extern "C" void kernel_launch(void* const* d_in, const int* in_sizes, int n_in, void* d_out, int out_size, void* d_ws,
                              size_t ws_size, hipStream_t stream) {
  const u16* x = (const u16*)d_in[0];
  const u16* wr = (const u16*)d_in[1];
  const u16* wi = (const u16*)d_in[2];
  const u16* Wg = (const u16*)d_in[4];
  const u16* Wu = (const u16*)d_in[5];
  const u16* Wd = (const u16*)d_in[6];
  char* ws = (char*)d_ws;
  k1<<<256, 512, 0, stream>>>(x, wr, wi, Wg, Wu, ws);
  k2<<<256, 512, 0, stream>>>(x, Wd, ws, (u16*)d_out);
}
```

```cpp
#include <hip/hip_runtime.h>
#include <stdint.h>

typedef unsigned short u16;
typedef unsigned int u32;
typedef unsigned long long u64;
typedef short v4s __attribute__((ext_vector_type(4)));
typedef short v8s __attribute__((ext_vector_type(8)));
typedef _Float16 v4h __attribute__((ext_vector_type(4)));
typedef _Float16 v8h __attribute__((ext_vector_type(8)));
typedef __bf16 v2b __attribute__((ext_vector_type(2)));
typedef __bf16 v8b __attribute__((ext_vector_type(8)));
typedef float v2f __attribute__((ext_vector_type(2)));
typedef float v4f __attribute__((ext_vector_type(4)));
typedef float v16f __attribute__((ext_vector_type(16)));
typedef u32 v2u __attribute__((ext_vector_type(2)));
typedef u32 v4u __attribute__((ext_vector_type(4)));

#define LAS __attribute__((address_space(3)))
#define GAS __attribute__((address_space(1)))
#define DEVI __device__ __forceinline__

constexpr int D_ = 512, F_ = 1792, DF_ = 257;
constexpr size_t OFF_H = 0;
constexpr size_t OFF_S1 = (size_t)8 << 20;
constexpr size_t OFF_WT = (size_t)10 << 20;
constexpr size_t OFF_S2 = (size_t)13 << 20;
constexpr size_t OFF_M = (size_t)15 << 20;
constexpr size_t OFF_FL = OFF_M;
constexpr u32 FL_W = 0, FL_H = 2048, FL_S = 3072, FL_P = 4096;
constexpr size_t OFF_BC = OFF_M + 8192;
constexpr size_t OFF_MB = OFF_M + 16384;
constexpr size_t OFF_RS2 = OFF_M + 24576;
constexpr size_t OFF_TAB = OFF_M + 32768;
constexpr size_t OFF_PQ = OFF_M + 65536;
constexpr size_t OFF_CSP = OFF_M + 131072;
constexpr int SPIN_CAP = 1 << 16;

#define STAMP_DECL
#define STAMPT(id)
#define STAMPC(id)
#define STAMP_OUT(wsp)

template <int I> struct IC { static constexpr int v = I; constexpr operator int() const { return I; } };
template <int B, int E, class Fn> DEVI void static_for(Fn&& f) {
  if constexpr (B < E) { f(IC<B>{}); static_for<B + 1, E>(f); }
}
#define WAIT_VM(n) asm volatile("s_waitcnt vmcnt(%0)" ::"n"(n) : "memory")
#define BARRIER() asm volatile("s_waitcnt lgkmcnt(0)\n\ts_barrier" ::: "memory")
#define SCHED() __builtin_amdgcn_sched_barrier(0)
#define PIN(x) asm volatile("" : "+v"(x))

DEVI int s_of_pi(int pi) {
  int RG = pi >> 6, i = pi & 63;
  return 128 * (4 * (RG & 3) + (i & 3)) + 8 * (i >> 2) + (RG >> 2);
}
DEVI void dma2(u32 voff0, u32 voff1, const char* sbase, u32 lds) {
  asm volatile("s_mov_b32 m0, %3\n\ts_nop 0\n\tglobal_load_lds_dwordx4 %0, %2\n\tglobal_load_lds_dwordx4 %1, %2 offset:1024" ::"v"(voff0), "v"(voff1), "s"(sbase), "s"(lds) : "memory");
}
DEVI void dma2c(u32 voff0, u32 voff1, const char* sbase, u32 lds) {
  asm volatile("s_mov_b32 m0, %3\n\ts_nop 0\n\tglobal_load_lds_dwordx4 %0, %2 sc1\n\tglobal_load_lds_dwordx4 %1, %2 offset:1024 sc1" ::"v"(voff0), "v"(voff1), "s"(sbase), "s"(lds) : "memory");
}
DEVI void dma1c(u32 voff, const char* sbase, u32 lds) {
  asm volatile("s_mov_b32 m0, %2\n\ts_nop 0\n\tglobal_load_lds_dword %0, %1 sc1" ::"v"(voff), "s"(sbase), "s"(lds) : "memory");
}
DEVI v2u ld8c_sync(u32 voff, const char* sb) { v2u d; asm volatile("global_load_dwordx2 %0, %1, %2 sc1\n\ts_waitcnt vmcnt(0)" : "=&v"(d) : "v"(voff), "s"(sb) : "memory"); return d; }
DEVI u32 ld4c_sync(u32 voff, const char* sb) { u32 d; asm volatile("global_load_dword %0, %1, %2 sc1\n\ts_waitcnt vmcnt(0)" : "=&v"(d) : "v"(voff), "s"(sb) : "memory"); return d; }
DEVI v4u ld16c_sync(u32 voff, const char* sb) { v4u d; asm volatile("global_load_dwordx4 %0, %1, %2 sc1\n\ts_waitcnt vmcnt(0)" : "=&v"(d) : "v"(voff), "s"(sb) : "memory"); return d; }
template <class T> DEVI void ld16c(T& d, u32 voff, const char* sb) { asm volatile("global_load_dwordx4 %0, %1, %2 sc1" : "=&v"(d) : "v"(voff), "s"(sb) : "memory"); }
template <class T> DEVI void ld8c(T& d, u32 voff, const char* sb) { asm volatile("global_load_dwordx2 %0, %1, %2 sc1" : "=&v"(d) : "v"(voff), "s"(sb) : "memory"); }
template <class T> DEVI void ld4c(T& d, u32 voff, const char* sb) { asm volatile("global_load_dword %0, %1, %2 sc1" : "=&v"(d) : "v"(voff), "s"(sb) : "memory"); }
template <class T> DEVI void ld16(T& d, u32 voff, const char* sb) { asm volatile("global_load_dwordx4 %0, %1, %2" : "=&v"(d) : "v"(voff), "s"(sb) : "memory"); }
DEVI void ld2(u32& d, u32 voff, const char* sb) { asm volatile("global_load_ushort %0, %1, %2" : "=&v"(d) : "v"(voff), "s"(sb) : "memory"); }
template <class T> DEVI void st16c(u32 voff, const char* sb, T v) { asm volatile("global_store_dwordx4 %0, %1, %2 sc1\n\ts_nop 1" ::"v"(voff), "v"(v), "s"(sb) : "memory"); }
template <class T> DEVI void st8c(u32 voff, const char* sb, T v) { asm volatile("global_store_dwordx2 %0, %1, %2 sc1" ::"v"(voff), "v"(v), "s"(sb) : "memory"); }
template <class T> DEVI void st4c(u32 voff, const char* sb, T v) { asm volatile("global_store_dword %0, %1, %2 sc1" ::"v"(voff), "v"(v), "s"(sb) : "memory"); }

DEVI v4s trread(LAS char* p) { return __builtin_amdgcn_ds_read_tr16_b64_v4i16((LAS v4s*)p); }
DEVI v8s cat8(v4s a, v4s b) { return __builtin_shufflevector(a, b, 0, 1, 2, 3, 4, 5, 6, 7); }
DEVI float bflo(u32 v) { return __builtin_bit_cast(float, v << 16); }
DEVI float bfhi(u32 v) { return __builtin_bit_cast(float, v & 0xffff0000u); }
DEVI float rbf(float v) { return (float)(__bf16)v; }
DEVI u32 pkbf(float a, float b) { v2b p = {(__bf16)a, (__bf16)b}; return __builtin_bit_cast(u32, p); }
DEVI u32 pkbf_o(float a, float b) { u32 r = pkbf(a, b); asm("" : "+v"(r)); return r; }
DEVI float cosr(float rev) { return __builtin_amdgcn_cosf(rev); }
DEVI float sinr(float rev) { return __builtin_amdgcn_sinf(rev); }
DEVI v4f mfma16h(v8h a, v8h b, v4f c) { return __builtin_amdgcn_mfma_f32_16x16x32_f16(a, b, c, 0, 0, 0); }
DEVI v4f mfma16b(v8s a, v4u b, v4f c) { return __builtin_amdgcn_mfma_f32_16x16x32_bf16(__builtin_bit_cast(v8b, a), __builtin_bit_cast(v8b, b), c, 0, 0, 0); }
DEVI v16f mfma32b(v8s a, v8s b, v16f c) { return __builtin_amdgcn_mfma_f32_32x32x16_bf16(__builtin_bit_cast(v8b, a), __builtin_bit_cast(v8b, b), c, 0, 0, 0); }
DEVI v16f mfma32h(v8h a, v8s b, v16f c) { return __builtin_amdgcn_mfma_f32_32x32x16_f16(a, __builtin_bit_cast(v8h, b), c, 0, 0, 0); }
DEVI v8h pack_ri(v4f re, v4f im) {
  v8h r;
  r[0] = (_Float16)re[0]; r[1] = (_Float16)re[1]; r[2] = (_Float16)re[2]; r[3] = (_Float16)re[3];
  r[4] = (_Float16)im[0]; r[5] = (_Float16)im[1]; r[6] = (_Float16)im[2]; r[7] = (_Float16)im[3];
  return r;
}
DEVI v4h pack4h(v4f a) { v4h r; r[0] = (_Float16)a[0]; r[1] = (_Float16)a[1]; r[2] = (_Float16)a[2]; r[3] = (_Float16)a[3]; return r; }
DEVI void twid(v4f& re, v4f& im, const v4f& c, const v4f& s) {
#pragma unroll
  for (int r = 0; r < 4; ++r) { float a = re[r], b = im[r]; re[r] = a * c[r] + b * s[r]; im[r] = b * c[r] - a * s[r]; }
}
template <int CTRL> DEVI float dppadd(float v) {
  return v + __builtin_bit_cast(float, __builtin_amdgcn_update_dpp(0, __builtin_bit_cast(int, v), CTRL, 0xf, 0xf, true));
}
DEVI float dot2ones(u32 v, float acc) {
  const v2b ones = {(__bf16)1.0f, (__bf16)1.0f};
  return __builtin_amdgcn_fdot2_f32_bf16(__builtin_bit_cast(v2b, v), ones, acc, false);
}
template <int CTRL> DEVI u32 dppmax(u32 v) { return max(v, (u32)__builtin_amdgcn_update_dpp(0, (int)v, CTRL, 0xf, 0xf, true)); }
DEVI u32 wave_max(u32 v) {
  v = dppmax<0xB1>(v); v = dppmax<0x4E>(v); v = dppmax<0x141>(v); v = dppmax<0x140>(v);
  u32 a0 = __builtin_amdgcn_readlane(v, 0), a1 = __builtin_amdgcn_readlane(v, 16), a2 = __builtin_amdgcn_readlane(v, 32), a3 = __builtin_amdgcn_readlane(v, 48);
  return max(max(a0, a1), max(a2, a3));
}

struct Frag1 { v8s x0, x1, w1, w2; };

constexpr int L_SCR = 147456;
constexpr int L_CS = L_SCR, L_RED = L_SCR + 1024, L_MX = L_SCR + 1088, L_BC0 = L_SCR + 2048, L_BC1 = L_SCR + 2304, L_FW = L_SCR + 4096;
constexpr int L_E1 = 49152, L_E2 = 65536, L_ZX = 81920 - 1024;
constexpr int E1P = 544, E2P = 1024 + 72, ZXP = 1024 + 8;
DEVI constexpr int bslot(int n) { return (n % 6) < 3 ? 98304 + (n % 6) * 16384 : ((n % 6) - 3) * 16384; }
DEVI constexpr int aslot(int n) { return (n & 3) < 3 ? 49152 + (n & 3) * 16384 : L_SCR; }

__global__ __launch_bounds__(512, 2) void kmain(const u16* __restrict__ x, const u16* __restrict__ wr, const u16* __restrict__ wi,
                                                const u16* __restrict__ Wg, const u16* __restrict__ Wu, const u16* __restrict__ Wd,
                                                char* __restrict__ ws, u16* __restrict__ out) {
  __shared__ __attribute__((aligned(16))) char smem_[163840];
  LAS char* smem = (LAS char*)smem_;
  const u32 lbase = (u32)(size_t)smem;
  const int tid = threadIdx.x, l = tid & 63;
  const int w = __builtin_amdgcn_readfirstlane(tid >> 6);
  const int b = blockIdx.x, xcd = b & 7, idx = b >> 3;
  const bool ffn = idx < 28;
  const int g = l >> 4, o = l & 15;
  const int h = l >> 5, blk = (l >> 4) & 1, q = (l & 15) >> 2, pp = l & 3;
  const char* FL = ws + OFF_FL;
  const char* BC = ws + OFF_BC;
  STAMP_DECL; STAMPT(0);

  const int RG = xcd * 4 + (idx >> 3), ct = idx & 7, kd = b;
  const u32 swz = ((l & 7) ^ (((l >> 4) & 1) << 2)) << 4;
  const u32 ro0 = (u32)((l >> 3) * 1024) + swz, ro1 = ro0 + 8192 - 1024;
  const char* bsrc = (const char*)Wd + (size_t)(w * 16) * 1024 + ct * 128;
  auto issueW = [&](int n, u32 slotoff) { dma2(ro0, ro1, bsrc + (size_t)n * 131072, lbase + slotoff + w * 2048); };

  u32 E = 0;
  if (w == 0) dma1c((u32)((l & 1) * 4), BC, lbase + L_BC0);

  u32 wa[4], wb[4];
  const int tkb = tid & 15, tkd = (b & 7) * 32 + (tid >> 4), tsp = b >> 3;
  auto wt_load = [&]() {
    const u32 v0 = (u32)(((tsp >> 1) + 16 * tkb + 1024 * (tsp & 1)) * DF_ + tkd) * 2;
#pragma unroll
    for (int r = 0; r < 4; ++r) { ld2(wa[r], v0 + r * (256 * DF_ * 2), (const char*)wr); ld2(wb[r], v0 + r * (256 * DF_ * 2), (const char*)wi); }
  };
  auto wt_store = [&]() -> u32 {
    v4u un; u32 mx = 0;
#pragma unroll
    for (int r = 0; r < 4; ++r) { PIN(wa[r]); PIN(wb[r]); un[r] = wa[r] | (wb[r] << 16); mx = max(mx, max(wa[r] & 0x7fffu, wb[r] & 0x7fffu)); }
    st16c((u32)(tkd * 8192 + tsp * 256 + tkb * 16), ws + OFF_WT, un);
    return mx;
  };
  auto get_epoch = [&]() {
    const v2u b0 = *(LAS v2u*)(smem + L_BC0);
    v2u b1 = *(LAS v2u*)(smem + L_BC1);
    int spins = 0;
    while (__builtin_amdgcn_readfirstlane((b0[0] ^ b1[0]) | (b0[1] ^ b1[1])) == 0 && ++spins < SPIN_CAP) {
      __builtin_amdgcn_s_sleep(4);
      b1 = ld8c_sync(0u, BC);
    }
    E = __builtin_amdgcn_readfirstlane(b1[0]);
  };
  auto signal_w = [&]() {
    LAS u32* RED = (LAS u32*)(smem + L_RED);
    v4u r0 = *(LAS v4u*)RED, r1 = *(LAS v4u*)(RED + 4);
    u32 m = max(max(max(r0[0], r0[1]), max(r0[2], r0[3])), max(max(r1[0], r1[1]), max(r1[2], r1[3])));
    if (l == 0) st8c((u32)(FL_W + b * 8), FL, v2u{E, m});
  };
  u32 tick = 0;
  const bool coord = (b == 224) && (w == 0);
  auto do_tick = [&]() { if (coord) { ++tick; if (l == 0) st8c(0u, BC, v2u{E, tick}); } };
  auto poll_w_issue = [&]() { dma2c((u32)(FL_W + l * 16), (u32)(FL_W + l * 16), FL, lbase + L_FW); };
  auto poll_w_check = [&]() {
    v4u fw0 = *(LAS v4u*)(smem + L_FW + l * 16), fw1 = *(LAS v4u*)(smem + L_FW + 1024 + l * 16);
    int spins = 0;
    while (!__all(fw0[0] == E && fw0[2] == E && fw1[0] == E && fw1[2] == E) && ++spins < SPIN_CAP) {
      do_tick();
      __builtin_amdgcn_s_sleep(4);
      fw0 = ld16c_sync((u32)(FL_W + l * 16), FL); fw1 = ld16c_sync((u32)(FL_W + 1024 + l * 16), FL);
    }
    u32 m = wave_max(max(max(fw0[1], fw0[3]), max(fw1[1], fw1[3])));
    if (l == 0) *(LAS u32*)(smem + L_MX) = m;
  };
  u32 fp;
  auto poll1 = [&](u32 voff) {
    PIN(fp);
    u32 f = fp;
    int spins = 0;
    while (!__all(f == E) && ++spins < SPIN_CAP) {
      __builtin_amdgcn_s_sleep(2);
      f = ld4c_sync(voff, FL);
    }
  };
  v4u wt, f16, f8; v4f t1c, t1s, pq[4];
  auto load_r1 = [&]() {
    ld16c(wt, (u32)(w * 1024 + l * 16), ws + OFF_WT + (size_t)kd * 8192);
    ld16c(f16, (u32)(l * 16), ws + OFF_TAB); ld16c(f8, (u32)(l * 16 + 1024), ws + OFF_TAB);
    ld16c(t1c, (u32)(l * 16 + 2048), ws + OFF_TAB); ld16c(t1s, (u32)(l * 16 + 3072), ws + OFF_TAB);
  };
  auto pin_r1 = [&]() { PIN(wt); PIN(f16); PIN(f8); PIN(t1c); PIN(t1s); };
  const u32 fpoff = (u32)(FL_P + (l & 31) * 4);

  const int p = (xcd >> 1) * 4 + idx / 7, j = (xcd & 1) * 7 + idx % 7;
  const int sh = w >> 2, fq = w & 3;
  v16f acc[2][2] = {};
  float mbr[2];

  if (ffn) {
    u32 aoff[2];
#pragma unroll
    for (int i = 0; i < 2; ++i) {
      int row = (2 * w + i) * 8 + (l >> 3);
      aoff[i] = s_of_pi(p * 128 + row) * 1024 + (((l & 7) ^ ((row >> 1) & 7)) << 4) - i * 1024;
    }
    const u32 boff = (u32)(2 * w * 4 + (l >> 4)) * (F_ * 2) + j * 256 + (((l & 15) ^ ((l >> 4) << 2)) << 4);
    const u32 boff1 = boff + 4 * F_ * 2 - 1024;
    const int rot = p >> 1;
    auto issue = [&](int t, int slot) {
      const int kt = (t + rot) & 7;
      u32 dst = lbase + slot * 49152 + w * 2048;
      dma2(aoff[0], aoff[1], (const char*)x + kt * 128, dst);
      dma2(boff, boff1, (const char*)Wg + (size_t)kt * (64 * F_ * 2), dst + 16384);
      dma2(boff, boff1, (const char*)Wu + (size_t)kt * (64 * F_ * 2), dst + 32768);
    };
    issue(0, 0);
    wt_load();
    issue(1, 1); issue(2, 2);
    if (b == 0 && w == 1) {
      const float st1 = (float)o * (1.0f / 256.0f);
      const bool off = ((g >> 1) != (o >> 3));
      v8h F16, F8; v4f tc, ts;
#pragma unroll
      for (int e = 0; e < 4; ++e) {
        float b1 = (float)(4 * g + e) * st1;
        tc[e] = cosr(b1); ts[e] = sinr(b1);
        F16[e] = (_Float16)cosr(16.f * b1); F16[4 + e] = (_Float16)sinr(16.f * b1);
        F8[e] = (_Float16)(off ? 0.f : cosr(32.f * b1)); F8[4 + e] = (_Float16)(off ? 0.f : sinr(32.f * b1));
      }
      st16c((u32)(l * 16), ws + OFF_TAB, F16); st16c((u32)(l * 16 + 1024), ws + OFF_TAB, F8);
      st16c((u32)(l * 16 + 2048), ws + OFF_TAB, tc); st16c((u32)(l * 16 + 3072), ws + OFF_TAB, ts);
    }
    const u32 wb0 = (u32)((8 * h + q) * 256 + ((fq ^ q) << 6) + blk * 32 + pp * 8);
    const u32 r1 = 16384 + sh * 16384, r2 = 32768 - sh * 16384;
    const int xrow = sh * 64 + (l & 31);
    const u32 xb0 = (u32)(xrow * 128 + ((h ^ ((xrow >> 1) & 7)) << 4));
    auto rd = [&](Frag1& f, LAS char* sb, int i) {
      LAS char* xa = sb + (xb0 ^ (i << 5));
      LAS char* wa_ = sb + wb0 + (i << 12);
      f.w1 = cat8(trread(wa_ + r1), trread(wa_ + r1 + 1024));
      f.x0 = *(LAS v8s*)xa; f.x1 = *(LAS v8s*)(xa + 4096);
      f.w2 = cat8(trread(wa_ + r2), trread(wa_ + r2 + 1024));
    };
    auto mm = [&](const Frag1& f) {
      acc[0][0] = mfma32b(f.w1, f.x0, acc[0][0]);
      acc[0][1] = mfma32b(f.w1, f.x1, acc[0][1]);
      acc[1][0] = mfma32b(f.w2, f.x0, acc[1][0]);
      acc[1][1] = mfma32b(f.w2, f.x1, acc[1][1]);
    };
    Frag1 A, B;
    WAIT_VM(20);
    BARRIER();
    STAMPT(1); STAMPC(30);
    rd(A, smem, 0);
    v4f csa = {0.f, 0.f, 0.f, 0.f};
    {
      const u32 one = ((l & 15) == ((l >> 4) & 1)) ? 0x3f803f80u : 0u;
      const v4u sel = {one, one, one, one};
      LAS char* wa_ = smem + wb0 + r1 + ((p & 1) << 13);
      csa = mfma16b(cat8(trread(wa_), trread(wa_ + 1024)), sel, csa);
      csa = mfma16b(cat8(trread(wa_ + 4096), trread(wa_ + 5120)), sel, csa);
    }
    float csp[16];
    int slot = 0;
#pragma unroll 1
    for (int t = 0; t < 7; ++t) {
      LAS char* sb = smem + slot * 49152;
      rd(B, sb, 1); SCHED(); mm(A); SCHED();
      rd(A, sb, 2); SCHED(); mm(B); SCHED();
      rd(B, sb, 3); SCHED(); mm(A); SCHED();
      WAIT_VM(6);
      if (t == 6 && w == 0) poll_w_check();
      BARRIER();
      const int nslot = slot == 2 ? 0 : slot + 1;
      acc[0][0] = mfma32b(B.w1, B.x0, acc[0][0]); SCHED();
      rd(A, smem + nslot * 49152, 0); SCHED();
      acc[0][1] = mfma32b(B.w1, B.x1, acc[0][1]);
      acc[1][0] = mfma32b(B.w2, B.x0, acc[1][0]);
      acc[1][1] = mfma32b(B.w2, B.x1, acc[1][1]); SCHED();
      if (t < 5) issue(t + 3, slot);
      else { const int n0 = (t - 5) * 3; issueW(n0, slot * 49152); issueW(n0 + 1, slot * 49152 + 16384); issueW(n0 + 2, slot * 49152 + 32768); }
      if (t == 0) {
        u32 mx = wave_max(wt_store());
        if (l == 0) *(LAS u32*)(smem + L_RED + w * 4) = mx;
        if ((l & 15) < 2) st16c((u32)(((j * 16 + p) * 256 + sh * 128 + fq * 32 + (l & 15) * 16 + (l >> 4) * 4) * 4), ws + OFF_CSP, csa);
        if (w == 0) dma1c((u32)((l & 1) * 4), BC, lbase + L_BC1);
      }
      if (w == 0) {
        if (t == 2) { get_epoch(); signal_w(); }
        if (t == 4) poll_w_issue();
      }
      slot = nslot;
      SCHED();
    }
    if (w < 4) {
#pragma unroll
      for (int i = 0; i < 16; ++i) ld4c(csp[i], (u32)((j * 16 + i) * 1024 + tid * 4), ws + OFF_CSP);
    }
    ld4c(mbr[0], (u32)((p * 128 + xrow) * 4), ws + OFF_MB); ld4c(mbr[1], (u32)((p * 128 + xrow + 32) * 4), ws + OFF_MB);
    load_r1();
    if (kd == 0) {
#pragma unroll
      for (int r = 0; r < 4; ++r) ld16c(pq[r], (u32)(w * 4096 + r * 1024 + l * 16), ws + OFF_PQ);
    }
    if (w == 0) ld4c(fp, fpoff, FL);
    SCHED();
    {
      LAS char* sb = smem + slot * 49152;
      rd(B, sb, 1); SCHED(); mm(A); SCHED();
      rd(A, sb, 2); SCHED(); mm(B); SCHED();
      rd(B, sb, 3); SCHED(); mm(A); SCHED();
      mm(B); SCHED();
    }
    STAMPT(2); STAMPC(31);
    WAIT_VM(0);
    pin_r1(); PIN(mbr[0]); PIN(mbr[1]);
    if (w < 4) {
      float s = 0.f;
#pragma unroll
      for (int i = 0; i < 16; ++i) { PIN(csp[i]); s += csp[i]; }
      *(LAS float*)(smem + L_CS + tid * 4) = s;
    }
  } else {
    const int sp = xcd * 4 + (idx - 28);
    const int pi = sp * 64 + w * 8 + (l >> 3);
    v4u xr[8];
    {
      const u32 xo = (u32)(s_of_pi(pi) * 1024 + (l & 7) * 16);
#pragma unroll
      for (int m = 0; m < 8; ++m) ld16(xr[m], xo + m * 128, (const char*)x);
    }
    wt_load();
    u32 sq[8];
    if (w == 7) {
      const int kss = (sp >> 1) + 16 * (l >> 2) + 256 * (4 * (sp & 1) + (l & 3)), km = (2048 - kss) & 2047;
      const u32 o1 = (u32)(kss * DF_ * 2), o2 = (u32)(km * DF_ * 2);
      ld2(sq[0], o1, (const char*)wr); ld2(sq[1], o1, (const char*)wi); ld2(sq[2], o2, (const char*)wr); ld2(sq[3], o2, (const char*)wi);
      ld2(sq[4], o1 + 512, (const char*)wr); ld2(sq[5], o1 + 512, (const char*)wi); ld2(sq[6], o2 + 512, (const char*)wr); ld2(sq[7], o2 + 512, (const char*)wi);
    }
    if (coord) {
      v4u f[5];
#pragma unroll
      for (int i = 0; i < 5; ++i) ld16c(f[i], (u32)(i * 1024 + l * 16), FL);
      WAIT_VM(0);
      u32 c = __builtin_amdgcn_readfirstlane(*(LAS u32*)(smem + L_BC0));
      bool hit;
      do {
        ++c; hit = false;
#pragma unroll
        for (int i = 0; i < 5; ++i) { PIN(f[i]); hit |= (f[i][0] == c) | (f[i][1] == c) | (f[i][2] == c) | (f[i][3] == c); }
      } while (__any(hit));
      E = c;
      do_tick();
    }
    v4u Fh[2][2], Fl[2][2];
    v8h M2, M2n;
    v4f twc[2], tws[2];
    {
      const float fo = (float)o, fg = (float)(4 * g);
      const float st = fo * (1.0f / 32.0f), a0 = fg * st;
#pragma unroll
      for (int e2 = 0; e2 < 4; ++e2) {
        float c[2], sn[2];
#pragma unroll
        for (int i = 0; i < 2; ++i) {
          const int e = 2 * e2 + i;
          float a = fmaf((float)(16 * (e >> 2) + (e & 3)), st, a0);
          c[i] = cosr(a); sn[i] = -sinr(a);
        }
        u32 ch = pkbf_o(c[0], c[1]), shh = pkbf_o(sn[0], sn[1]);
        u32 cl = pkbf_o(c[0] - bflo(ch), c[1] - bfhi(ch)), sl = pkbf_o(sn[0] - bflo(shh), sn[1] - bfhi(shh));
        Fh[0][0][e2] = ch; Fh[0][1][e2] = shh; Fl[0][0][e2] = cl; Fl[0][1][e2] = sl;
        Fh[1][0][e2] = ch ^ 0x80000000u; Fh[1][1][e2] = shh ^ 0x80000000u; Fl[1][0][e2] = cl ^ 0x80000000u; Fl[1][1][e2] = sl ^ 0x80000000u;
      }
      const float st2 = (float)(o >> 1) * (1.0f / 16.0f), qo = (o & 1) ? 0.25f : 0.f;
#pragma unroll
      for (int e = 0; e < 4; ++e) {
        const float db = fg + (float)e;
        float a = fmaf(db, st2, qo);
        M2[e] = (_Float16)(0.0625f * cosr(a)); M2[4 + e] = (_Float16)(0.0625f * sinr(a));
        M2n[e] = (_Float16)(o == 0 ? ((e & 1) ? -0.0625f : 0.0625f) : 0.f); M2n[4 + e] = (_Float16)0.f;
#pragma unroll
        for (int u = 0; u < 2; ++u) {
          float t = db * ((fo + 16.f * u) * (1.0f / 512.0f));
          twc[u][e] = cosr(t); tws[u][e] = sinr(t);
        }
      }
      PIN(Fh[0][0]); PIN(Fh[0][1]); PIN(Fl[0][0]); PIN(Fl[0][1]); PIN(M2); PIN(twc[0]); PIN(twc[1]); PIN(tws[0]); PIN(tws[1]);
    }
    do_tick();
    WAIT_VM(0);
    STAMPT(1); STAMPC(30);
    LAS char* img = smem + w * 8192;
    {
      float s = 0.f;
#pragma unroll
      for (int m = 0; m < 8; ++m) {
        PIN(xr[m]);
#pragma unroll
        for (int e = 0; e < 4; ++e) { u32 t = xr[m][e]; s = dot2ones(t, s); }
      }
      s = dppadd<0xB1>(s); s = dppadd<0x4E>(s); s = dppadd<0x141>(s);
      const float mean = s * (1.0f / 512.0f), mb = rbf(mean);
      if ((l & 7) == 0) st4c((u32)(pi * 4), ws + OFF_MB, -mb);
      u32 mx = wt_store();
      if (w == 7) {
#pragma unroll
        for (int i = 0; i < 8; ++i) PIN(sq[i]);
        mx = max(mx, max(sq[4] & 0x7fffu, sq[5] & 0x7fffu));
        float war = bflo(sq[0]) + bflo(sq[2]), wai = bflo(sq[1]) - bflo(sq[3]);
        float wbr = bflo(sq[4]) + bflo(sq[6]), wbi = bflo(sq[5]) - bflo(sq[7]);
        v4f pv = {0.25f * (war + wbr), 0.25f * (wai + wbi), 0.25f * (war - wbr), 0.25f * (wai - wbi)};
        st16c((u32)((((sp >> 2) * 4 + (l & 3)) * 64 + (sp & 3) * 16 + (l >> 2)) * 16), ws + OFF_PQ, pv);
      }
      mx = wave_max(mx);
      if (l == 0) *(LAS u32*)(smem + L_RED + w * 4) = mx;
      if (w == 0 && !coord) dma1c((u32)((l & 1) * 4), BC, lbase + L_BC1);
      do_tick();
      const v2f mb2 = {mb, mb};
      v2f q2 = {0.f, 0.f};
#pragma unroll
      for (int m = 0; m < 8; ++m)
#pragma unroll
        for (int e = 0; e < 4; ++e) {
          v2f t = v2f{bflo(xr[m][e]), bfhi(xr[m][e])} - mb2;
          q2 = __builtin_elementwise_fma(t, t, q2);
          xr[m][e] = pkbf_o(t[0], t[1]);
        }
      float qq = q2[0] + q2[1];
      qq = dppadd<0xB1>(qq); qq = dppadd<0x4E>(qq); qq = dppadd<0x141>(qq);
      const float dm = mean - mb;
      const float var = rbf(fmaxf(qq * (1.0f / 512.0f) - dm * dm, 0.f));
      const float sd = rbf(__builtin_sqrtf(rbf(var + 1e-5f)));
      const float rs = 1.0f / sd;
      PIN(qq);
      WAIT_VM(0);
      BARRIER();
      if (w == 0) { if (!coord) get_epoch(); signal_w(); }
      do_tick();
      const v2f rs2 = {rs, rs};
      if ((l & 7) == 0) st4c((u32)(pi * 4), ws + OFF_RS2, rs * rs);
#pragma unroll
      for (int m = 0; m < 8; ++m) {
        v4u o4;
#pragma unroll
        for (int e = 0; e < 4; ++e) {
          v2f t = v2f{bflo(xr[m][e]), bfhi(xr[m][e])} * rs2;
          o4[e] = pkbf(t[0], t[1]);
        }
        *(LAS v4u*)(img + (l >> 3) * 1024 + ((l & 7) + 8 * m) * 16) = o4;
      }
    }
    STAMPT(2);
    LAS char* T = smem + 65536;
#pragma unroll 1
    for (int nbi = 0; nbi < 2; ++nbi) {
      do_tick();
      v4f d2[4][2];
#pragma unroll
      for (int r = 0; r < 4; ++r) {
        LAS char* rp = img + (nbi * 4 + r) * 1024 + l * 8;
        v8s xf = cat8(trread(rp), trread(rp + 512));
        float nyq = 0.f;
#pragma unroll
        for (int u = 0; u < 2; ++u) {
          v4f z = {0.f, 0.f, 0.f, 0.f};
          v4f re = mfma16b(xf, Fl[u][0], z), im = mfma16b(xf, Fl[u][1], z);
          re = mfma16b(xf, Fh[u][0], re); im = mfma16b(xf, Fh[u][1], im);
          twid(re, im, twc[u], tws[u]);
          v8h bb = pack_ri(re, im);
          d2[r][u] = mfma16h(M2, bb, z);
          if (u == 0) { v4f n = mfma16h(M2n, bb, z); nyq = n[0]; }
        }
        if (l == 0) d2[r][0][1] = nyq;
      }
#pragma unroll
      for (int u = 0; u < 2; ++u)
#pragma unroll
        for (int sub = 0; sub < 2; ++sub) {
          v8h un;
#pragma unroll
          for (int r = 0; r < 4; ++r) { un[r] = (_Float16)d2[r][u][2 * sub]; un[4 + r] = (_Float16)d2[r][u][2 * sub + 1]; }
          *(LAS v8h*)(T + (16 * u + o + 32 * (2 * g + sub)) * 272 + (2 * w + nbi) * 16) = un;
        }
    }
    do_tick();
    if (w == 0) poll_w_issue();
    BARRIER();
    STAMPT(3); STAMPC(31);
    {
      v4u un[8];
#pragma unroll
      for (int i = 0; i < 8; ++i) un[i] = *(LAS v4u*)(T + (w * 32 + i * 4 + g) * 272 + o * 16);
#pragma unroll
      for (int i = 0; i < 8; ++i) st16c((u32)((w * 32 + i * 4 + g) * 8192 + sp * 256 + o * 16), ws + OFF_S1, un[i]);
    }
    WAIT_VM(0);
    if (w == 0) poll_w_check();
    BARRIER();
    if (tid == 0) st4c((u32)(FL_P + sp * 4), FL, E);
    static_for<0, 6>([&](auto N) { issueW(N, bslot(N)); });
    load_r1();
    if (w == 0) ld4c(fp, fpoff, FL);
    WAIT_VM(0);
    pin_r1();
    STAMPT(4);
  }

  if (w == 0) poll1(fpoff);
  BARRIER();
  STAMPT(5);
  v4u s1v;
  ld16c(s1v, (u32)(w * 1024 + l * 16), ws + OFF_S1 + (size_t)kd * 8192);
  const int kh = w >> 2, wq = w & 3, dh = wq >> 1, ph = wq & 1;
  const int epi = RG * 64 + ph * 32 + (l & 31);
  const u32 eoff = (u32)(s_of_pi(epi) * D_ + ct * 64 + dh * 32 + kh * 16 + h * 8) * 2;
  v4u xe; float rs2;
  ld16(xe, eoff, (const char*)x);
  ld4c(rs2, (u32)(epi * 4), ws + OFF_RS2);

  if (ffn) {
    v4f cg[4], cu[4];
#pragma unroll
    for (int g4 = 0; g4 < 4; ++g4) {
      cg[g4] = *(LAS v4f*)(smem + L_CS + (fq * 32 + g4 * 8 + h * 4) * 4);
      cu[g4] = *(LAS v4f*)(smem + L_CS + (128 + fq * 32 + g4 * 8 + h * 4) * 4);
    }
    auto fin = [&](const v16f& ag, const v16f& au, int rt) {
      const v2f mb2 = {mbr[rt], mbr[rt]};
#pragma unroll
      for (int s2i = 0; s2i < 2; ++s2i) {
        v4u ov;
#pragma unroll
        for (int e = 0; e < 4; ++e) {
          const int r = s2i * 8 + 2 * e;
          v2f gg = __builtin_elementwise_fma(mb2, v2f{cg[r >> 2][r & 3], cg[r >> 2][(r & 3) + 1]}, v2f{ag[r], ag[r + 1]});
          v2f uu = __builtin_elementwise_fma(mb2, v2f{cu[r >> 2][r & 3], cu[r >> 2][(r & 3) + 1]}, v2f{au[r], au[r + 1]});
          gg[0] = fmaxf(gg[0], 0.f); gg[1] = fmaxf(gg[1], 0.f);
          gg *= uu;
          ov[e] = pkbf(gg[0], gg[1]);
        }
        st16c((u32)(((p * 4 + sh * 2 + rt) * 112 + (j * 8 + fq * 2 + s2i)) * 1024 + l * 16), ws + OFF_H, ov);
      }
    };
    if (sh == 0) { fin(acc[0][0], acc[1][0], 0); fin(acc[0][1], acc[1][1], 1); }
    else { fin(acc[1][0], acc[0][0], 0); fin(acc[1][1], acc[0][1], 1); }
  }
  STAMPT(6);

  float oscale;
  const u32 fhoff = (u32)(FL_H + ((RG >> 1) * 16 + min(l, 13)) * 4);
  {
    v4f t2c, t2s, t3c, t3s;
    {
      const float fo = (float)o, fg = (float)(4 * g), fw = (float)w;
      const float st2 = fw * (1.0f / 128.0f), a2 = fw * fo * (1.0f / 2048.0f);
      const float f7 = (float)(o & 7), st3 = f7 * (1.0f / 128.0f), a3 = f7 * (float)(2 * w + (o >> 3)) * (1.0f / 2048.0f);
#pragma unroll
      for (int e = 0; e < 4; ++e) {
        const float id = fg + (float)e;
        float b2 = fmaf(id, st2, a2);
        t2c[e] = cosr(b2); t2s[e] = sinr(b2);
        float b3 = fmaf(id, st3, a3);
        t3c[e] = cosr(b3); t3s[e] = sinr(b3);
      }
    }
    int Ex = (int)(*(LAS u32*)(smem + L_MX) >> 7);
    Ex = Ex < 16 ? 16 : (Ex > 240 ? 240 : Ex);
    const float wscale = __builtin_bit_cast(float, (u32)(246 - Ex) << 23);
    oscale = __builtin_bit_cast(float, (u32)(Ex - 7) << 23);
    const v8h F16re = __builtin_bit_cast(v8h, f16), F8re = __builtin_bit_cast(v8h, f8);
    const v8h F16im = __builtin_bit_cast(v8h, v4u{f16[2] ^ 0x80008000u, f16[3] ^ 0x80008000u, f16[0], f16[1]});
    const v8h F8im = __builtin_bit_cast(v8h, v4u{f8[2] ^ 0x80008000u, f8[3] ^ 0x80008000u, f8[0], f8[1]});
    PIN(t2c); PIN(t2s);
    if (ffn) WAIT_VM(6); else WAIT_VM(2);
    PIN(s1v);
    STAMPT(7);
    const v4f z = {0.f, 0.f, 0.f, 0.f};
    v8h a = __builtin_bit_cast(v8h, s1v);
    v4f re = mfma16h(a, F16re, z), im = mfma16h(a, F16im, z);
    twid(re, im, t1c, t1s);
    a = pack_ri(re, im);
    re = mfma16h(F16re, a, z); im = mfma16h(F16im, a, z);
    twid(re, im, t2c, t2s);
    *(LAS v4h*)(smem + L_E1 + w * E1P + o * 32 + g * 8) = pack4h(re);
    *(LAS v4h*)(smem + L_E1 + (8 + w) * E1P + o * 32 + g * 8) = pack4h(im);
    BARRIER();
    {
      LAS char* rp = smem + L_E1 + (4 * (g & 1) + (o >> 2)) * E1P + (2 * w + (g >> 1)) * 32 + (o & 3) * 8;
      a = __builtin_bit_cast(v8h, cat8(trread(rp), trread(rp + 8 * E1P)));
    }
    re = mfma16h(F8re, a, z); im = mfma16h(F8im, a, z);
    if (kd != 0) {
#pragma unroll
      for (int r = 0; r < 4; ++r) {
        float fr = bflo(wt[r]) * wscale, fi = bfhi(wt[r]) * wscale;
        float zr = re[r], zi = im[r];
        re[r] = zr * fr - zi * fi;
        im[r] = -(zr * fi + zi * fr);
      }
    } else {
      const int ka = 2 * w + (g >> 1), kr = o + 64 * (g & 1);
#pragma unroll
      for (int r = 0; r < 4; ++r) { v2f t = {re[r], im[r]}; *(LAS v2f*)(smem + L_ZX + ka * ZXP + (kr + 16 * r) * 8) = t; }
      BARRIER();
#pragma unroll
      for (int r = 0; r < 4; ++r) {
        PIN(pq[r]);
        const int km = (2048 - (ka + 16 * (kr + 16 * r))) & 2047;
        v2f zm = *(LAS v2f*)(smem + L_ZX + (km & 15) * ZXP + (km >> 4) * 8);
        float zr = re[r], zi = im[r];
        re[r] = (zr * pq[r][0] - zi * pq[r][1] + zm[0] * pq[r][2] + zm[1] * pq[r][3]) * wscale;
        im[r] = -(zr * pq[r][1] + zi * pq[r][0] + zm[0] * pq[r][3] - zm[1] * pq[r][2]) * wscale;
      }
    }
    a = pack_ri(re, im);
    re = mfma16h(a, F8re, z); im = mfma16h(a, F8im, z);
    twid(re, im, t3c, t3s);
    *(LAS v4h*)(smem + L_E2 + (o & 7) * E2P + (2 * w + (o >> 3)) * 32 + g * 8) = pack4h(re);
    *(LAS v4h*)(smem + L_E2 + (o & 7) * E2P + 512 + (2 * w + (o >> 3)) * 32 + g * 8) = pack4h(im);
    WAIT_VM(0);
    BARRIER();
    STAMPT(8);
    if (w == 0) {
      if (ffn && l == 0) st4c((u32)(FL_H + (p * 16 + j) * 4), FL, E);
      ld4c(fp, fhoff, FL);
    }
    {
      LAS char* rp = smem + L_E2 + w * E2P + o * 32 + g * 8;
      a = __builtin_bit_cast(v8h, cat8(*(LAS v4s*)rp, *(LAS v4s*)(rp + 512)));
    }
    re = mfma16h(a, F16re, z); im = mfma16h(a, F16im, z);
    twid(re, im, t1c, t1s);
    a = pack_ri(re, im);
    re = mfma16h(F16re, a, z); im = mfma16h(F16im, a, z);
    st8c((u32)(2 * kd * 4096 + w * 512 + l * 8), ws + OFF_S2, pack4h(re));
    st8c((u32)((2 * kd + 1) * 4096 + w * 512 + l * 8), ws + OFF_S2, pack4h(im));
  }
  STAMPT(9);

  const int d = ct * 64 + dh * 32 + ((l & 31) ^ (kh << 4));
  const float gst = (float)d * (1.0f / 512.0f), ga0 = (float)(32 * kh + 2 * h) * gst;
  v8h G[16];
  auto gen = [&](auto FI) {
    constexpr int f = FI;
#pragma unroll
    for (int e2 = 0; e2 < 4; ++e2) {
      float a = fmaf((float)(64 * (f >> 2) + 8 * (f & 3) + 4 * (e2 >> 1) + (e2 & 1)), gst, ga0);
      float c = cosr(a), sn = sinr(a);
      if (f == 0 && e2 == 0) { if (kh == 0 && h == 0) { c = 0.5f; sn = (d & 1) ? 0.5f : -0.5f; } }
      G[f][2 * e2] = (_Float16)c; G[f][2 * e2 + 1] = (_Float16)sn;
    }
    PIN(G[f]);
  };
  if (w == 0) { WAIT_VM(0); poll1(fhoff); } else static_for<0, 2>(gen);
  BARRIER();
  STAMPT(10);
  const char* hsrc = ws + OFF_H + ((size_t)(RG * 2 + (w >> 2)) * 112 + (w & 3) * 2) * 1024;
  const u32 so0 = (u32)((l >> 3) * 4096) + swz, so1 = so0 + 32768 - 1024;
  const char* ssrc = ws + OFF_S2 + (size_t)(w * 16) * 4096 + RG * 128;
  auto issueA = [&](int n) { dma2c((u32)(l * 16), (u32)(l * 16), hsrc + n * 8192, lbase + aslot(n) + w * 2048); };
  auto issueB = [&](int n) {
    if (n < 14) issueW(n, bslot(n));
    else dma2c(so0, so1, ssrc + (size_t)(n - 14) * (128 * 4096), lbase + bslot(n) + w * 2048);
  };
  issueA(0); issueA(1); issueA(2); issueA(3);
  if (w == 0) static_for<0, 2>(gen);
  static_for<2, 6>(gen);
  const u32 trG = (u32)(kh * 8192 + (4 * h + q) * 128 + ((dh ^ ((q >> 1) & 1)) << 6) + (blk ^ kh) * 32 + pp * 8);
  const u32 trS = (u32)(kh * 8192 + (4 * h + q) * 128 + ((ph ^ ((q >> 1) & 1)) << 6) + blk * 32 + pp * 8);
  const u32 hb = (u32)(ph * 8192 + kh * 4096 + l * 16);
  v16f accG = {}, accS = {};
  v8s fa[8], fb[8];
  v4u fs;
  float gv[8];
  static_for<0, 17>([&](auto V) {
    constexpr int v = V;
    v8s(&cur)[8] = (v & 1) ? fb : fa;
    v8s(&prv)[8] = (v & 1) ? fa : fb;
    auto mmp = [&](int s0, int s1) {
      if constexpr (v >= 15) {
#pragma unroll
        for (int s = 2 * s0; s < 2 * s1; ++s) accS = mfma32h(G[(v - 15) * 8 + s], prv[s], accS);
      } else if constexpr (v >= 1) {
#pragma unroll
        for (int s = s0; s < s1; ++s) accG = mfma32b(prv[2 * s], prv[2 * s + 1], accG);
      }
    };
    if constexpr (v < 16) {
      constexpr int allowed = v == 0 ? 6 : v == 1 ? 4 : v == 2 ? 6 : v <= 11 ? 8 : v == 12 ? 6 : v == 13 ? 4 : v == 14 ? 4 : 0;
      WAIT_VM(allowed);
      if constexpr (v == 9) {
        if (w == 0) {
          PIN(fs);
          v4u f = fs;
          int spins = 0;
          while (!__all(f[0] == E && f[1] == E && f[2] == E && f[3] == E) && ++spins < SPIN_CAP) {
            __builtin_amdgcn_s_sleep(2);
            f = ld16c_sync((u32)(FL_S + l * 16), FL);
          }
        }
      }
      BARRIER();
      if constexpr (v % 2 == 0) STAMPT(11 + v / 2);
      mmp(0, 1); SCHED();
      if constexpr (v >= 1 && v + 5 <= 17) issueB(v + 5);
      if constexpr (v >= 1 && v + 3 <= 13) issueA(v + 3);
      if constexpr (v == 0) { if (tid == 0) st4c((u32)(FL_S + b * 4), FL, E); }
      if constexpr (v == 6) { if (w == 0) ld16c(fs, (u32)(FL_S + l * 16), FL); }
      mmp(1, 2); SCHED();
      if constexpr (v >= 14) {
#pragma unroll
        for (int s = 0; s < 8; ++s) {
          LAS char* sb = smem + bslot(14 + (v - 14) * 2 + (s >> 2)) + trS + (s & 3) * 2048;
          cur[s] = cat8(trread(sb), trread(sb + 1024));
        }
      } else {
#pragma unroll
        for (int s = 0; s < 4; ++s) {
          cur[2 * s] = cat8(trread(smem + bslot(v) + trG + s * 2048), trread(smem + bslot(v) + trG + s * 2048 + 1024));
          cur[2 * s + 1] = *(LAS v8s*)(smem + aslot(v) + hb + s * 1024);
        }
      }
      SCHED();
      mmp(2, 4);
    } else {
      mmp(0, 4);
    }
    if constexpr (v >= 1 && v <= 10) gen(IC<5 + v>{});
    if constexpr (v == 14) {
      LAS char* X = smem + aslot(0) + w * 2048 + l * 16;
      *(LAS v4f*)X = v4f{accG[8], accG[9], accG[10], accG[11]}; *(LAS v4f*)(X + 1024) = v4f{accG[12], accG[13], accG[14], accG[15]};
    }
    if constexpr (v == 15) {
      LAS char* X = smem + aslot(0) + (w ^ 4) * 2048 + l * 16;
      v4f t0 = *(LAS v4f*)X, t1 = *(LAS v4f*)(X + 1024);
      PIN(rs2);
#pragma unroll
      for (int e = 0; e < 8; ++e) {
        float ov = rbf((accG[e] + (e < 4 ? t0[e & 3] : t1[e & 3])) * rs2);
        gv[e] = rbf(ov * __builtin_amdgcn_rcpf(1.0f + __expf(-ov)));
        PIN(gv[e]);
      }
    }
    SCHED();
  });
  STAMPT(19);
  {
    LAS char* X = smem + aslot(1) + w * 2048 + l * 16;
    *(LAS v4f*)X = v4f{accS[8], accS[9], accS[10], accS[11]}; *(LAS v4f*)(X + 1024) = v4f{accS[12], accS[13], accS[14], accS[15]};
  }
  BARRIER();
  {
    LAS char* X = smem + aslot(1) + (w ^ 4) * 2048 + l * 16;
    v4f t0 = *(LAS v4f*)X, t1 = *(LAS v4f*)(X + 1024);
    float pv[8];
#pragma unroll
    for (int e = 0; e < 8; ++e) pv[e] = rbf((accS[e] + (e < 4 ? t0[e & 3] : t1[e & 3])) * oscale) * gv[e];
    u32 a0 = pkbf(pv[0], pv[1]), a1 = pkbf(pv[2], pv[3]), b0 = pkbf(pv[4], pv[5]), b1 = pkbf(pv[6], pv[7]);
    auto r0 = __builtin_amdgcn_permlane32_swap(a0, b0, false, false);
    auto r1 = __builtin_amdgcn_permlane32_swap(a1, b1, false, false);
    v4u pk = {r0[0], r1[0], r0[1], r1[1]};
    PIN(xe);
    v4u ov4;
#pragma unroll
    for (int e = 0; e < 4; ++e) ov4[e] = pkbf(bflo(xe[e]) + bflo(pk[e]), bfhi(xe[e]) + bfhi(pk[e]));
    *(v4u*)((char*)out + eoff) = ov4;
  }
  STAMPT(20);
  STAMPT(21);
  STAMP_OUT(ws);
}

extern "C" void kernel_launch(void* const* d_in, const int* in_sizes, int n_in, void* d_out, int out_size, void* d_ws,
                              size_t ws_size, hipStream_t stream) {
  kmain<<<256, 512, 0, stream>>>((const u16*)d_in[0], (const u16*)d_in[1], (const u16*)d_in[2], (const u16*)d_in[4],
                                 (const u16*)d_in[5], (const u16*)d_in[6], (char*)d_ws, (u16*)d_out);
}
```

```cpp
#include <hip/hip_runtime.h>
#include <stdint.h>

typedef unsigned short u16;
typedef unsigned int u32;
typedef unsigned long long u64;
typedef short v4s __attribute__((ext_vector_type(4)));
typedef short v8s __attribute__((ext_vector_type(8)));
typedef _Float16 v4h __attribute__((ext_vector_type(4)));
typedef _Float16 v8h __attribute__((ext_vector_type(8)));
typedef __bf16 v2b __attribute__((ext_vector_type(2)));
typedef __bf16 v8b __attribute__((ext_vector_type(8)));
typedef float v2f __attribute__((ext_vector_type(2)));
typedef float v4f __attribute__((ext_vector_type(4)));
typedef float v16f __attribute__((ext_vector_type(16)));
typedef u32 v2u __attribute__((ext_vector_type(2)));
typedef u32 v4u __attribute__((ext_vector_type(4)));

#define LAS __attribute__((address_space(3)))
#define GAS __attribute__((address_space(1)))
#define DEVI __device__ __forceinline__

constexpr int D_ = 512, F_ = 1792, DF_ = 257;
constexpr size_t OFF_H = 0;
constexpr size_t OFF_S1 = (size_t)8 << 20;
constexpr size_t OFF_WT = (size_t)10 << 20;
constexpr size_t OFF_S2 = (size_t)13 << 20;
constexpr size_t OFF_M = (size_t)15 << 20;
constexpr size_t OFF_FL = OFF_M;
constexpr u32 FL_W = 0, FL_H = 2048, FL_S = 3072, FL_P = 4096;
constexpr size_t OFF_BC = OFF_M + 8192;
constexpr size_t OFF_MB = OFF_M + 16384;
constexpr size_t OFF_RS2 = OFF_M + 24576;
constexpr size_t OFF_TAB = OFF_M + 32768;
constexpr size_t OFF_PQ = OFF_M + 65536;
constexpr size_t OFF_CSP = OFF_M + 131072;
constexpr int SPIN_CAP = 1 << 16;

#define STAMP_DECL
#define STAMPT(id)
#define STAMPC(id)
#define STAMP_OUT(wsp)

template <int I> struct IC { static constexpr int v = I; constexpr operator int() const { return I; } };
template <int B, int E, class Fn> DEVI void static_for(Fn&& f) {
  if constexpr (B < E) { f(IC<B>{}); static_for<B + 1, E>(f); }
}
#define WAIT_VM(n) asm volatile("s_waitcnt vmcnt(%0)" ::"n"(n) : "memory")
#define BARRIER() asm volatile("s_waitcnt lgkmcnt(0)\n\ts_barrier" ::: "memory")
#define SCHED() __builtin_amdgcn_sched_barrier(0)
#define PIN(x) asm volatile("" : "+v"(x))

DEVI int s_of_pi(int pi) {
  int RG = pi >> 6, i = pi & 63;
  return 128 * (4 * (RG & 3) + (i & 3)) + 8 * (i >> 2) + (RG >> 2);
}
DEVI void dma2(u32 voff0, u32 voff1, const char* sbase, u32 lds) {
  asm volatile("s_mov_b32 m0, %3\n\ts_nop 0\n\tglobal_load_lds_dwordx4 %0, %2\n\tglobal_load_lds_dwordx4 %1, %2 offset:1024" ::"v"(voff0), "v"(voff1), "s"(sbase), "s"(lds) : "memory");
}
DEVI void dma2c(u32 voff0, u32 voff1, const char* sbase, u32 lds) {
  asm volatile("s_mov_b32 m0, %3\n\ts_nop 0\n\tglobal_load_lds_dwordx4 %0, %2 sc1\n\tglobal_load_lds_dwordx4 %1, %2 offset:1024 sc1" ::"v"(voff0), "v"(voff1), "s"(sbase), "s"(lds) : "memory");
}
DEVI void dma1c(u32 voff, const char* sbase, u32 lds) {
  asm volatile("s_mov_b32 m0, %2\n\ts_nop 0\n\tglobal_load_lds_dword %0, %1 sc1" ::"v"(voff), "s"(sbase), "s"(lds) : "memory");
}
DEVI v2u ld8c_sync(u32 voff, const char* sb) { v2u d; asm volatile("global_load_dwordx2 %0, %1, %2 sc1\n\ts_waitcnt vmcnt(0)" : "=&v"(d) : "v"(voff), "s"(sb) : "memory"); return d; }
DEVI u32 ld4c_sync(u32 voff, const char* sb) { u32 d; asm volatile("global_load_dword %0, %1, %2 sc1\n\ts_waitcnt vmcnt(0)" : "=&v"(d) : "v"(voff), "s"(sb) : "memory"); return d; }
DEVI v4u ld16c_sync(u32 voff, const char* sb) { v4u d; asm volatile("global_load_dwordx4 %0, %1, %2 sc1\n\ts_waitcnt vmcnt(0)" : "=&v"(d) : "v"(voff), "s"(sb) : "memory"); return d; }
template <class T> DEVI void ld16c(T& d, u32 voff, const char* sb) { asm volatile("global_load_dwordx4 %0, %1, %2 sc1" : "=&v"(d) : "v"(voff), "s"(sb) : "memory"); }
template <class T> DEVI void ld8c(T& d, u32 voff, const char* sb) { asm volatile("global_load_dwordx2 %0, %1, %2 sc1" : "=&v"(d) : "v"(voff), "s"(sb) : "memory"); }
template <class T> DEVI void ld4c(T& d, u32 voff, const char* sb) { asm volatile("global_load_dword %0, %1, %2 sc1" : "=&v"(d) : "v"(voff), "s"(sb) : "memory"); }
template <class T> DEVI void ld16(T& d, u32 voff, const char* sb) { asm volatile("global_load_dwordx4 %0, %1, %2" : "=&v"(d) : "v"(voff), "s"(sb) : "memory"); }
DEVI void ld2(u32& d, u32 voff, const char* sb) { asm volatile("global_load_ushort %0, %1, %2" : "=&v"(d) : "v"(voff), "s"(sb) : "memory"); }
template <class T> DEVI void st16c(u32 voff, const char* sb, T v) { asm volatile("global_store_dwordx4 %0, %1, %2 sc1\n\ts_nop 1" ::"v"(voff), "v"(v), "s"(sb) : "memory"); }
template <class T> DEVI void st8c(u32 voff, const char* sb, T v) { asm volatile("global_store_dwordx2 %0, %1, %2 sc1" ::"v"(voff), "v"(v), "s"(sb) : "memory"); }
template <class T> DEVI void st4c(u32 voff, const char* sb, T v) { asm volatile("global_store_dword %0, %1, %2 sc1" ::"v"(voff), "v"(v), "s"(sb) : "memory"); }

DEVI v4s trread(LAS char* p) { return __builtin_amdgcn_ds_read_tr16_b64_v4i16((LAS v4s*)p); }
DEVI v8s cat8(v4s a, v4s b) { return __builtin_shufflevector(a, b, 0, 1, 2, 3, 4, 5, 6, 7); }
DEVI float bflo(u32 v) { return __builtin_bit_cast(float, v << 16); }
DEVI float bfhi(u32 v) { return __builtin_bit_cast(float, v & 0xffff0000u); }
DEVI float rbf(float v) { return (float)(__bf16)v; }
DEVI u32 pkbf(float a, float b) { v2b p = {(__bf16)a, (__bf16)b}; return __builtin_bit_cast(u32, p); }
DEVI u32 pkbf_o(float a, float b) { u32 r = pkbf(a, b); asm("" : "+v"(r)); return r; }
DEVI float cosr(float rev) { return __builtin_amdgcn_cosf(rev); }
DEVI float sinr(float rev) { return __builtin_amdgcn_sinf(rev); }
DEVI v4f mfma16h(v8h a, v8h b, v4f c) { return __builtin_amdgcn_mfma_f32_16x16x32_f16(a, b, c, 0, 0, 0); }
DEVI v4f mfma16b(v8s a, v4u b, v4f c) { return __builtin_amdgcn_mfma_f32_16x16x32_bf16(__builtin_bit_cast(v8b, a), __builtin_bit_cast(v8b, b), c, 0, 0, 0); }
DEVI v16f mfma32b(v8s a, v8s b, v16f c) { return __builtin_amdgcn_mfma_f32_32x32x16_bf16(__builtin_bit_cast(v8b, a), __builtin_bit_cast(v8b, b), c, 0, 0, 0); }
DEVI v16f mfma32h(v8h a, v8s b, v16f c) { return __builtin_amdgcn_mfma_f32_32x32x16_f16(a, __builtin_bit_cast(v8h, b), c, 0, 0, 0); }
DEVI v8h pack_ri(v4f re, v4f im) {
  v8h r;
  r[0] = (_Float16)re[0]; r[1] = (_Float16)re[1]; r[2] = (_Float16)re[2]; r[3] = (_Float16)re[3];
  r[4] = (_Float16)im[0]; r[5] = (_Float16)im[1]; r[6] = (_Float16)im[2]; r[7] = (_Float16)im[3];
  return r;
}
DEVI v4h pack4h(v4f a) { v4h r; r[0] = (_Float16)a[0]; r[1] = (_Float16)a[1]; r[2] = (_Float16)a[2]; r[3] = (_Float16)a[3]; return r; }
DEVI void twid(v4f& re, v4f& im, const v4f& c, const v4f& s) {
#pragma unroll
  for (int r = 0; r < 4; ++r) { float a = re[r], b = im[r]; re[r] = a * c[r] + b * s[r]; im[r] = b * c[r] - a * s[r]; }
}
template <int CTRL> DEVI float dppadd(float v) {
  return v + __builtin_bit_cast(float, __builtin_amdgcn_update_dpp(0, __builtin_bit_cast(int, v), CTRL, 0xf, 0xf, true));
}
DEVI float dot2ones(u32 v, float acc) {
  const v2b ones = {(__bf16)1.0f, (__bf16)1.0f};
  return __builtin_amdgcn_fdot2_f32_bf16(__builtin_bit_cast(v2b, v), ones, acc, false);
}
template <int CTRL> DEVI u32 dppmax(u32 v) { return max(v, (u32)__builtin_amdgcn_update_dpp(0, (int)v, CTRL, 0xf, 0xf, true)); }
DEVI u32 wave_max(u32 v) {
  v = dppmax<0xB1>(v); v = dppmax<0x4E>(v); v = dppmax<0x141>(v); v = dppmax<0x140>(v);
  u32 a0 = __builtin_amdgcn_readlane(v, 0), a1 = __builtin_amdgcn_readlane(v, 16), a2 = __builtin_amdgcn_readlane(v, 32), a3 = __builtin_amdgcn_readlane(v, 48);
  return max(max(a0, a1), max(a2, a3));
}

struct Frag1 { v8s x0, x1, w1, w2; };

constexpr int L_SCR = 147456;
constexpr int L_CS = L_SCR, L_RED = L_SCR + 1024, L_MX = L_SCR + 1088, L_BC0 = L_SCR + 2048, L_BC1 = L_SCR + 2304, L_FW = L_SCR + 4096;
constexpr int L_E1 = 49152, L_E2 = 65536, L_ZX = 81920 - 1024;
constexpr int E1P = 544, E2P = 1024 + 72, ZXP = 1024 + 8;
DEVI constexpr int bslot(int n) { return (n % 6) < 3 ? 98304 + (n % 6) * 16384 : ((n % 6) - 3) * 16384; }
DEVI constexpr int aslot(int n) { return (n & 3) < 3 ? 49152 + (n & 3) * 16384 : L_SCR; }

__global__ __launch_bounds__(512, 2) void kmain(const u16* __restrict__ x, const u16* __restrict__ wr, const u16* __restrict__ wi,
                                                const u16* __restrict__ Wg, const u16* __restrict__ Wu, const u16* __restrict__ Wd,
                                                char* __restrict__ ws, u16* __restrict__ out) {
  __shared__ __attribute__((aligned(16))) char smem_[163840];
  LAS char* smem = (LAS char*)smem_;
  const u32 lbase = (u32)(size_t)smem;
  const int tid = threadIdx.x, l = tid & 63;
  const int w = __builtin_amdgcn_readfirstlane(tid >> 6);
  const int b = blockIdx.x, xcd = b & 7, idx = b >> 3;
  const bool ffn = idx < 28;
  const int g = l >> 4, o = l & 15;
  const int h = l >> 5, blk = (l >> 4) & 1, q = (l & 15) >> 2, pp = l & 3;
  const char* FL = ws + OFF_FL;
  const char* BC = ws + OFF_BC;
  STAMP_DECL; STAMPT(0);

  const int RG = xcd * 4 + (idx >> 3), ct = idx & 7, kd = b;
  const u32 swz = ((l & 7) ^ (((l >> 4) & 1) << 2)) << 4;
  const u32 ro0 = (u32)((l >> 3) * 1024) + swz, ro1 = ro0 + 8192 - 1024;
  const char* bsrc = (const char*)Wd + (size_t)(w * 16) * 1024 + ct * 128;
  auto issueW = [&](int n, u32 slotoff) { dma2(ro0, ro1, bsrc + (size_t)n * 131072, lbase + slotoff + w * 2048); };

  u32 E = 0;
  if (w == 0) dma1c((u32)((l & 1) * 4), BC, lbase + L_BC0);
  if (w == 7) {
#pragma unroll
    for (int i = 0; i < 4; ++i) dma1c((u32)((i * 64 + l) * 65536 + (b & 31) * 128), ws, lbase + L_SCR + 8192);
    dma1c((u32)((l % 28) * 65536 + (b & 31) * 128), (const char*)Wd, lbase + L_SCR + 8192);
    dma1c((u32)((l & 31) * 65536 + (b & 31) * 128), (const char*)out, lbase + L_SCR + 8192);
  }

  u32 wa[4], wb[4];
  const int tkb = tid & 15, tkd = (b & 7) * 32 + (tid >> 4), tsp = b >> 3;
  auto wt_load = [&]() {
    const u32 v0 = (u32)(((tsp >> 1) + 16 * tkb + 1024 * (tsp & 1)) * DF_ + tkd) * 2;
#pragma unroll
    for (int r = 0; r < 4; ++r) { ld2(wa[r], v0 + r * (256 * DF_ * 2), (const char*)wr); ld2(wb[r], v0 + r * (256 * DF_ * 2), (const char*)wi); }
  };
  auto wt_store = [&]() -> u32 {
    v4u un; u32 mx = 0;
#pragma unroll
    for (int r = 0; r < 4; ++r) { PIN(wa[r]); PIN(wb[r]); un[r] = wa[r] | (wb[r] << 16); mx = max(mx, max(wa[r] & 0x7fffu, wb[r] & 0x7fffu)); }
    st16c((u32)(tkd * 8192 + tsp * 256 + tkb * 16), ws + OFF_WT, un);
    return mx;
  };
  auto get_epoch = [&]() {
    const v2u b0 = *(LAS v2u*)(smem + L_BC0);
    v2u b1 = *(LAS v2u*)(smem + L_BC1);
    int spins = 0;
    while (__builtin_amdgcn_readfirstlane((b0[0] ^ b1[0]) | (b0[1] ^ b1[1])) == 0 && ++spins < SPIN_CAP) {
      __builtin_amdgcn_s_sleep(4);
      b1 = ld8c_sync(0u, BC);
    }
    E = __builtin_amdgcn_readfirstlane(b1[0]);
  };
  auto signal_w = [&]() {
    LAS u32* RED = (LAS u32*)(smem + L_RED);
    v4u r0 = *(LAS v4u*)RED, r1 = *(LAS v4u*)(RED + 4);
    u32 m = max(max(max(r0[0], r0[1]), max(r0[2], r0[3])), max(max(r1[0], r1[1]), max(r1[2], r1[3])));
    if (l == 0) st8c((u32)(FL_W + b * 8), FL, v2u{E, m});
  };
  u32 tick = 0;
  const bool coord = (b == 224) && (w == 0);
  auto do_tick = [&]() { if (coord) { ++tick; if (l == 0) st8c(0u, BC, v2u{E, tick}); } };
  auto poll_w_issue = [&]() { dma2c((u32)(FL_W + l * 16), (u32)(FL_W + l * 16), FL, lbase + L_FW); };
  auto poll_w_check = [&]() {
    v4u fw0 = *(LAS v4u*)(smem + L_FW + l * 16), fw1 = *(LAS v4u*)(smem + L_FW + 1024 + l * 16);
    int spins = 0;
    while (!__all(fw0[0] == E && fw0[2] == E && fw1[0] == E && fw1[2] == E) && ++spins < SPIN_CAP) {
      do_tick();
      __builtin_amdgcn_s_sleep(4);
      fw0 = ld16c_sync((u32)(FL_W + l * 16), FL); fw1 = ld16c_sync((u32)(FL_W + 1024 + l * 16), FL);
    }
    u32 m = wave_max(max(max(fw0[1], fw0[3]), max(fw1[1], fw1[3])));
    if (l == 0) *(LAS u32*)(smem + L_MX) = m;
  };
  u32 fp;
  auto poll1 = [&](u32 voff) {
    PIN(fp);
    u32 f = fp;
    int spins = 0;
    while (!__all(f == E) && ++spins < SPIN_CAP) {
      __builtin_amdgcn_s_sleep(2);
      f = ld4c_sync(voff, FL);
    }
  };
  v4u wt, f16, f8; v4f t1c, t1s, pq[4];
  auto load_r1 = [&]() {
    ld16c(wt, (u32)(w * 1024 + l * 16), ws + OFF_WT + (size_t)kd * 8192);
    ld16c(f16, (u32)(l * 16), ws + OFF_TAB); ld16c(f8, (u32)(l * 16 + 1024), ws + OFF_TAB);
    ld16c(t1c, (u32)(l * 16 + 2048), ws + OFF_TAB); ld16c(t1s, (u32)(l * 16 + 3072), ws + OFF_TAB);
  };
  auto pin_r1 = [&]() { PIN(wt); PIN(f16); PIN(f8); PIN(t1c); PIN(t1s); };
  const u32 fpoff = (u32)(FL_P + (l & 31) * 4);

  const int p = (xcd >> 1) * 4 + idx / 7, j = (xcd & 1) * 7 + idx % 7;
  const int sh = w >> 2, fq = w & 3;
  v16f acc[2][2] = {};
  float mbr[2];

  if (ffn) {
    u32 aoff[2];
#pragma unroll
    for (int i = 0; i < 2; ++i) {
      int row = (2 * w + i) * 8 + (l >> 3);
      aoff[i] = s_of_pi(p * 128 + row) * 1024 + (((l & 7) ^ ((row >> 1) & 7)) << 4) - i * 1024;
    }
    const u32 boff = (u32)(2 * w * 4 + (l >> 4)) * (F_ * 2) + j * 256 + (((l & 15) ^ ((l >> 4) << 2)) << 4);
    const u32 boff1 = boff + 4 * F_ * 2 - 1024;
    const int rot = p >> 1;
    auto issue = [&](int t, int slot) {
      const int kt = (t + rot) & 7;
      u32 dst = lbase + slot * 49152 + w * 2048;
      dma2(aoff[0], aoff[1], (const char*)x + kt * 128, dst);
      dma2(boff, boff1, (const char*)Wg + (size_t)kt * (64 * F_ * 2), dst + 16384);
      dma2(boff, boff1, (const char*)Wu + (size_t)kt * (64 * F_ * 2), dst + 32768);
    };
    issue(0, 0);
    wt_load();
    issue(1, 1); issue(2, 2);
    if (b == 0 && w == 1) {
      const float st1 = (float)o * (1.0f / 256.0f);
      const bool off = ((g >> 1) != (o >> 3));
      v8h F16, F8; v4f tc, ts;
#pragma unroll
      for (int e = 0; e < 4; ++e) {
        float b1 = (float)(4 * g + e) * st1;
        tc[e] = cosr(b1); ts[e] = sinr(b1);
        F16[e] = (_Float16)cosr(16.f * b1); F16[4 + e] = (_Float16)sinr(16.f * b1);
        F8[e] = (_Float16)(off ? 0.f : cosr(32.f * b1)); F8[4 + e] = (_Float16)(off ? 0.f : sinr(32.f * b1));
      }
      st16c((u32)(l * 16), ws + OFF_TAB, F16); st16c((u32)(l * 16 + 1024), ws + OFF_TAB, F8);
      st16c((u32)(l * 16 + 2048), ws + OFF_TAB, tc); st16c((u32)(l * 16 + 3072), ws + OFF_TAB, ts);
    }
    const u32 wb0 = (u32)((8 * h + q) * 256 + ((fq ^ q) << 6) + blk * 32 + pp * 8);
    const u32 r1 = 16384 + sh * 16384, r2 = 32768 - sh * 16384;
    const int xrow = sh * 64 + (l & 31);
    const u32 xb0 = (u32)(xrow * 128 + ((h ^ ((xrow >> 1) & 7)) << 4));
    auto rd = [&](Frag1& f, LAS char* sb, int i) {
      LAS char* xa = sb + (xb0 ^ (i << 5));
      LAS char* wa_ = sb + wb0 + (i << 12);
      f.w1 = cat8(trread(wa_ + r1), trread(wa_ + r1 + 1024));
      f.x0 = *(LAS v8s*)xa; f.x1 = *(LAS v8s*)(xa + 4096);
      f.w2 = cat8(trread(wa_ + r2), trread(wa_ + r2 + 1024));
    };
    auto mm = [&](const Frag1& f) {
      acc[0][0] = mfma32b(f.w1, f.x0, acc[0][0]);
      acc[0][1] = mfma32b(f.w1, f.x1, acc[0][1]);
      acc[1][0] = mfma32b(f.w2, f.x0, acc[1][0]);
      acc[1][1] = mfma32b(f.w2, f.x1, acc[1][1]);
    };
    Frag1 A, B;
    WAIT_VM(20);
    BARRIER();
    STAMPT(1); STAMPC(30);
    rd(A, smem, 0);
    v4f csa = {0.f, 0.f, 0.f, 0.f};
    {
      const u32 one = ((l & 15) == ((l >> 4) & 1)) ? 0x3f803f80u : 0u;
      const v4u sel = {one, one, one, one};
      LAS char* wa_ = smem + wb0 + r1 + ((p & 1) << 13);
      csa = mfma16b(cat8(trread(wa_), trread(wa_ + 1024)), sel, csa);
      csa = mfma16b(cat8(trread(wa_ + 4096), trread(wa_ + 5120)), sel, csa);
    }
    float csp[16];
    int slot = 0;
#pragma unroll 1
    for (int t = 0; t < 7; ++t) {
      LAS char* sb = smem + slot * 49152;
      rd(B, sb, 1); SCHED(); mm(A); SCHED();
      rd(A, sb, 2); SCHED(); mm(B); SCHED();
      rd(B, sb, 3); SCHED(); mm(A); SCHED();
      WAIT_VM(6);
      if (t == 6 && w == 0) poll_w_check();
      BARRIER();
      const int nslot = slot == 2 ? 0 : slot + 1;
      acc[0][0] = mfma32b(B.w1, B.x0, acc[0][0]); SCHED();
      rd(A, smem + nslot * 49152, 0); SCHED();
      acc[0][1] = mfma32b(B.w1, B.x1, acc[0][1]);
      acc[1][0] = mfma32b(B.w2, B.x0, acc[1][0]);
      acc[1][1] = mfma32b(B.w2, B.x1, acc[1][1]); SCHED();
      if (t < 5) issue(t + 3, slot);
      else { const int n0 = (t - 5) * 3; issueW(n0, slot * 49152); issueW(n0 + 1, slot * 49152 + 16384); issueW(n0 + 2, slot * 49152 + 32768); }
      if (t == 0) {
        u32 mx = wave_max(wt_store());
        if (l == 0) *(LAS u32*)(smem + L_RED + w * 4) = mx;
        if ((l & 15) < 2) st16c((u32)(((j * 16 + p) * 256 + sh * 128 + fq * 32 + (l & 15) * 16 + (l >> 4) * 4) * 4), ws + OFF_CSP, csa);
        if (w == 0) dma1c((u32)((l & 1) * 4), BC, lbase + L_BC1);
      }
      if (w == 0) {
        if (t == 2) { get_epoch(); signal_w(); }
        if (t == 4) poll_w_issue();
      }
      slot = nslot;
      SCHED();
    }
    if (w < 4) {
#pragma unroll
      for (int i = 0; i < 16; ++i) ld4c(csp[i], (u32)((j * 16 + i) * 1024 + tid * 4), ws + OFF_CSP);
    }
    ld4c(mbr[0], (u32)((p * 128 + xrow) * 4), ws + OFF_MB); ld4c(mbr[1], (u32)((p * 128 + xrow + 32) * 4), ws + OFF_MB);
    load_r1();
    if (kd == 0) {
#pragma unroll
      for (int r = 0; r < 4; ++r) ld16c(pq[r], (u32)(w * 4096 + r * 1024 + l * 16), ws + OFF_PQ);
    }
    if (w == 0) ld4c(fp, fpoff, FL);
    SCHED();
    {
      LAS char* sb = smem + slot * 49152;
      rd(B, sb, 1); SCHED(); mm(A); SCHED();
      rd(A, sb, 2); SCHED(); mm(B); SCHED();
      rd(B, sb, 3); SCHED(); mm(A); SCHED();
      mm(B); SCHED();
    }
    STAMPT(2); STAMPC(31);
    WAIT_VM(0);
    pin_r1(); PIN(mbr[0]); PIN(mbr[1]);
    if (w < 4) {
      float s = 0.f;
#pragma unroll
      for (int i = 0; i < 16; ++i) { PIN(csp[i]); s += csp[i]; }
      *(LAS float*)(smem + L_CS + tid * 4) = s;
    }
  } else {
    const int sp = xcd * 4 + (idx - 28);
    const int pi = sp * 64 + w * 8 + (l >> 3);
    v4u xr[8];
    {
      const u32 xo = (u32)(s_of_pi(pi) * 1024 + (l & 7) * 16);
#pragma unroll
      for (int m = 0; m < 8; ++m) ld16(xr[m], xo + m * 128, (const char*)x);
    }
    wt_load();
    u32 sq[8];
    if (w == 7) {
      const int kss = (sp >> 1) + 16 * (l >> 2) + 256 * (4 * (sp & 1) + (l & 3)), km = (2048 - kss) & 2047;
      const u32 o1 = (u32)(kss * DF_ * 2), o2 = (u32)(km * DF_ * 2);
      ld2(sq[0], o1, (const char*)wr); ld2(sq[1], o1, (const char*)wi); ld2(sq[2], o2, (const char*)wr); ld2(sq[3], o2, (const char*)wi);
      ld2(sq[4], o1 + 512, (const char*)wr); ld2(sq[5], o1 + 512, (const char*)wi); ld2(sq[6], o2 + 512, (const char*)wr); ld2(sq[7], o2 + 512, (const char*)wi);
    }
    if (coord) {
      v4u f[5];
#pragma unroll
      for (int i = 0; i < 5; ++i) ld16c(f[i], (u32)(i * 1024 + l * 16), FL);
      WAIT_VM(0);
      u32 c = __builtin_amdgcn_readfirstlane(*(LAS u32*)(smem + L_BC0));
      bool hit;
      do {
        ++c; hit = false;
#pragma unroll
        for (int i = 0; i < 5; ++i) { PIN(f[i]); hit |= (f[i][0] == c) | (f[i][1] == c) | (f[i][2] == c) | (f[i][3] == c); }
      } while (__any(hit));
      E = c;
      do_tick();
    }
    v4u Fh[2][2], Fl[2][2];
    v8h M2, M2n;
    v4f twc[2], tws[2];
    {
      const float fo = (float)o, fg = (float)(4 * g);
      const float st = fo * (1.0f / 32.0f), a0 = fg * st;
#pragma unroll
      for (int e2 = 0; e2 < 4; ++e2) {
        float c[2], sn[2];
#pragma unroll
        for (int i = 0; i < 2; ++i) {
          const int e = 2 * e2 + i;
          float a = fmaf((float)(16 * (e >> 2) + (e & 3)), st, a0);
          c[i] = cosr(a); sn[i] = -sinr(a);
        }
        u32 ch = pkbf_o(c[0], c[1]), shh = pkbf_o(sn[0], sn[1]);
        u32 cl = pkbf_o(c[0] - bflo(ch), c[1] - bfhi(ch)), sl = pkbf_o(sn[0] - bflo(shh), sn[1] - bfhi(shh));
        Fh[0][0][e2] = ch; Fh[0][1][e2] = shh; Fl[0][0][e2] = cl; Fl[0][1][e2] = sl;
        Fh[1][0][e2] = ch ^ 0x80000000u; Fh[1][1][e2] = shh ^ 0x80000000u; Fl[1][0][e2] = cl ^ 0x80000000u; Fl[1][1][e2] = sl ^ 0x80000000u;
      }
      const float st2 = (float)(o >> 1) * (1.0f / 16.0f), qo = (o & 1) ? 0.25f : 0.f;
#pragma unroll
      for (int e = 0; e < 4; ++e) {
        const float db = fg + (float)e;
        float a = fmaf(db, st2, qo);
        M2[e] = (_Float16)(0.0625f * cosr(a)); M2[4 + e] = (_Float16)(0.0625f * sinr(a));
        M2n[e] = (_Float16)(o == 0 ? ((e & 1) ? -0.0625f : 0.0625f) : 0.f); M2n[4 + e] = (_Float16)0.f;
#pragma unroll
        for (int u = 0; u < 2; ++u) {
          float t = db * ((fo + 16.f * u) * (1.0f / 512.0f));
          twc[u][e] = cosr(t); tws[u][e] = sinr(t);
        }
      }
      PIN(Fh[0][0]); PIN(Fh[0][1]); PIN(Fl[0][0]); PIN(Fl[0][1]); PIN(M2); PIN(twc[0]); PIN(twc[1]); PIN(tws[0]); PIN(tws[1]);
    }
    do_tick();
    WAIT_VM(0);
    STAMPT(1); STAMPC(30);
    LAS char* img = smem + w * 8192;
    {
      float s = 0.f;
#pragma unroll
      for (int m = 0; m < 8; ++m) {
        PIN(xr[m]);
#pragma unroll
        for (int e = 0; e < 4; ++e) { u32 t = xr[m][e]; s = dot2ones(t, s); }
      }
      s = dppadd<0xB1>(s); s = dppadd<0x4E>(s); s = dppadd<0x141>(s);
      const float mean = s * (1.0f / 512.0f), mb = rbf(mean);
      if ((l & 7) == 0) st4c((u32)(pi * 4), ws + OFF_MB, -mb);
      u32 mx = wt_store();
      if (w == 7) {
#pragma unroll
        for (int i = 0; i < 8; ++i) PIN(sq[i]);
        mx = max(mx, max(sq[4] & 0x7fffu, sq[5] & 0x7fffu));
        float war = bflo(sq[0]) + bflo(sq[2]), wai = bflo(sq[1]) - bflo(sq[3]);
        float wbr = bflo(sq[4]) + bflo(sq[6]), wbi = bflo(sq[5]) - bflo(sq[7]);
        v4f pv = {0.25f * (war + wbr), 0.25f * (wai + wbi), 0.25f * (war - wbr), 0.25f * (wai - wbi)};
        st16c((u32)((((sp >> 2) * 4 + (l & 3)) * 64 + (sp & 3) * 16 + (l >> 2)) * 16), ws + OFF_PQ, pv);
      }
      mx = wave_max(mx);
      if (l == 0) *(LAS u32*)(smem + L_RED + w * 4) = mx;
      if (w == 0 && !coord) dma1c((u32)((l & 1) * 4), BC, lbase + L_BC1);
      do_tick();
      const v2f mb2 = {mb, mb};
      v2f q2 = {0.f, 0.f};
#pragma unroll
      for (int m = 0; m < 8; ++m)
#pragma unroll
        for (int e = 0; e < 4; ++e) {
          v2f t = v2f{bflo(xr[m][e]), bfhi(xr[m][e])} - mb2;
          q2 = __builtin_elementwise_fma(t, t, q2);
          xr[m][e] = pkbf_o(t[0], t[1]);
        }
      float qq = q2[0] + q2[1];
      qq = dppadd<0xB1>(qq); qq = dppadd<0x4E>(qq); qq = dppadd<0x141>(qq);
      const float dm = mean - mb;
      const float var = rbf(fmaxf(qq * (1.0f / 512.0f) - dm * dm, 0.f));
      const float sd = rbf(__builtin_sqrtf(rbf(var + 1e-5f)));
      const float rs = 1.0f / sd;
      PIN(qq);
      WAIT_VM(0);
      BARRIER();
      if (w == 0) { if (!coord) get_epoch(); signal_w(); }
      do_tick();
      const v2f rs2 = {rs, rs};
      if ((l & 7) == 0) st4c((u32)(pi * 4), ws + OFF_RS2, rs * rs);
#pragma unroll
      for (int m = 0; m < 8; ++m) {
        v4u o4;
#pragma unroll
        for (int e = 0; e < 4; ++e) {
          v2f t = v2f{bflo(xr[m][e]), bfhi(xr[m][e])} * rs2;
          o4[e] = pkbf(t[0], t[1]);
        }
        *(LAS v4u*)(img + (l >> 3) * 1024 + ((l & 7) + 8 * m) * 16) = o4;
      }
    }
    STAMPT(2);
    LAS char* T = smem + 65536;
#pragma unroll 1
    for (int nbi = 0; nbi < 2; ++nbi) {
      do_tick();
      v4f d2[4][2];
#pragma unroll
      for (int r = 0; r < 4; ++r) {
        LAS char* rp = img + (nbi * 4 + r) * 1024 + l * 8;
        v8s xf = cat8(trread(rp), trread(rp + 512));
        float nyq = 0.f;
#pragma unroll
        for (int u = 0; u < 2; ++u) {
          v4f z = {0.f, 0.f, 0.f, 0.f};
          v4f re = mfma16b(xf, Fl[u][0], z), im = mfma16b(xf, Fl[u][1], z);
          re = mfma16b(xf, Fh[u][0], re); im = mfma16b(xf, Fh[u][1], im);
          twid(re, im, twc[u], tws[u]);
          v8h bb = pack_ri(re, im);
          d2[r][u] = mfma16h(M2, bb, z);
          if (u == 0) { v4f n = mfma16h(M2n, bb, z); nyq = n[0]; }
        }
        if (l == 0) d2[r][0][1] = nyq;
      }
#pragma unroll
      for (int u = 0; u < 2; ++u)
#pragma unroll
        for (int sub = 0; sub < 2; ++sub) {
          v8h un;
#pragma unroll
          for (int r = 0; r < 4; ++r) { un[r] = (_Float16)d2[r][u][2 * sub]; un[4 + r] = (_Float16)d2[r][u][2 * sub + 1]; }
          *(LAS v8h*)(T + (16 * u + o + 32 * (2 * g + sub)) * 272 + (2 * w + nbi) * 16) = un;
        }
    }
    do_tick();
    if (w == 0) poll_w_issue();
    BARRIER();
    STAMPT(3); STAMPC(31);
    {
      v4u un[8];
#pragma unroll
      for (int i = 0; i < 8; ++i) un[i] = *(LAS v4u*)(T + (w * 32 + i * 4 + g) * 272 + o * 16);
#pragma unroll
      for (int i = 0; i < 8; ++i) st16c((u32)((w * 32 + i * 4 + g) * 8192 + sp * 256 + o * 16), ws + OFF_S1, un[i]);
    }
    WAIT_VM(0);
    if (w == 0) poll_w_check();
    BARRIER();
    if (tid == 0) st4c((u32)(FL_P + sp * 4), FL, E);
    static_for<0, 6>([&](auto N) { issueW(N, bslot(N)); });
    load_r1();
    if (w == 0) ld4c(fp, fpoff, FL);
    WAIT_VM(0);
    pin_r1();
    STAMPT(4);
  }

  if (w == 0) poll1(fpoff);
  BARRIER();
  STAMPT(5);
  v4u s1v;
  ld16c(s1v, (u32)(w * 1024 + l * 16), ws + OFF_S1 + (size_t)kd * 8192);
  const int kh = w >> 2, wq = w & 3, dh = wq >> 1, ph = wq & 1;
  const int epi = RG * 64 + ph * 32 + (l & 31);
  const u32 eoff = (u32)(s_of_pi(epi) * D_ + ct * 64 + dh * 32 + kh * 16 + h * 8) * 2;
  v4u xe; float rs2;
  ld16(xe, eoff, (const char*)x);
  ld4c(rs2, (u32)(epi * 4), ws + OFF_RS2);

  if (ffn) {
    v4f cg[4], cu[4];
#pragma unroll
    for (int g4 = 0; g4 < 4; ++g4) {
      cg[g4] = *(LAS v4f*)(smem + L_CS + (fq * 32 + g4 * 8 + h * 4) * 4);
      cu[g4] = *(LAS v4f*)(smem + L_CS + (128 + fq * 32 + g4 * 8 + h * 4) * 4);
    }
    auto fin = [&](const v16f& ag, const v16f& au, int rt) {
      const v2f mb2 = {mbr[rt], mbr[rt]};
#pragma unroll
      for (int s2i = 0; s2i < 2; ++s2i) {
        v4u ov;
#pragma unroll
        for (int e = 0; e < 4; ++e) {
          const int r = s2i * 8 + 2 * e;
          v2f gg = __builtin_elementwise_fma(mb2, v2f{cg[r >> 2][r & 3], cg[r >> 2][(r & 3) + 1]}, v2f{ag[r], ag[r + 1]});
          v2f uu = __builtin_elementwise_fma(mb2, v2f{cu[r >> 2][r & 3], cu[r >> 2][(r & 3) + 1]}, v2f{au[r], au[r + 1]});
          gg[0] = fmaxf(gg[0], 0.f); gg[1] = fmaxf(gg[1], 0.f);
          gg *= uu;
          ov[e] = pkbf(gg[0], gg[1]);
        }
        st16c((u32)(((p * 4 + sh * 2 + rt) * 112 + (j * 8 + fq * 2 + s2i)) * 1024 + l * 16), ws + OFF_H, ov);
      }
    };
    if (sh == 0) { fin(acc[0][0], acc[1][0], 0); fin(acc[0][1], acc[1][1], 1); }
    else { fin(acc[1][0], acc[0][0], 0); fin(acc[1][1], acc[0][1], 1); }
  }
  STAMPT(6);

  float oscale;
  const u32 fhoff = (u32)(FL_H + ((RG >> 1) * 16 + min(l, 13)) * 4);
  {
    v4f t2c, t2s, t3c, t3s;
    {
      const float fo = (float)o, fg = (float)(4 * g), fw = (float)w;
      const float st2 = fw * (1.0f / 128.0f), a2 = fw * fo * (1.0f / 2048.0f);
      const float f7 = (float)(o & 7), st3 = f7 * (1.0f / 128.0f), a3 = f7 * (float)(2 * w + (o >> 3)) * (1.0f / 2048.0f);
#pragma unroll
      for (int e = 0; e < 4; ++e) {
        const float id = fg + (float)e;
        float b2 = fmaf(id, st2, a2);
        t2c[e] = cosr(b2); t2s[e] = sinr(b2);
        float b3 = fmaf(id, st3, a3);
        t3c[e] = cosr(b3); t3s[e] = sinr(b3);
      }
    }
    int Ex = (int)(*(LAS u32*)(smem + L_MX) >> 7);
    Ex = Ex < 16 ? 16 : (Ex > 240 ? 240 : Ex);
    const float wscale = __builtin_bit_cast(float, (u32)(246 - Ex) << 23);
    oscale = __builtin_bit_cast(float, (u32)(Ex - 7) << 23);
    const v8h F16re = __builtin_bit_cast(v8h, f16), F8re = __builtin_bit_cast(v8h, f8);
    const v8h F16im = __builtin_bit_cast(v8h, v4u{f16[2] ^ 0x80008000u, f16[3] ^ 0x80008000u, f16[0], f16[1]});
    const v8h F8im = __builtin_bit_cast(v8h, v4u{f8[2] ^ 0x80008000u, f8[3] ^ 0x80008000u, f8[0], f8[1]});
    PIN(t2c); PIN(t2s);
    if (ffn) WAIT_VM(6); else WAIT_VM(2);
    PIN(s1v);
    STAMPT(7);
    const v4f z = {0.f, 0.f, 0.f, 0.f};
    v8h a = __builtin_bit_cast(v8h, s1v);
    v4f re = mfma16h(a, F16re, z), im = mfma16h(a, F16im, z);
    twid(re, im, t1c, t1s);
    a = pack_ri(re, im);
    re = mfma16h(F16re, a, z); im = mfma16h(F16im, a, z);
    twid(re, im, t2c, t2s);
    *(LAS v4h*)(smem + L_E1 + w * E1P + o * 32 + g * 8) = pack4h(re);
    *(LAS v4h*)(smem + L_E1 + (8 + w) * E1P + o * 32 + g * 8) = pack4h(im);
    BARRIER();
    {
      LAS char* rp = smem + L_E1 + (4 * (g & 1) + (o >> 2)) * E1P + (2 * w + (g >> 1)) * 32 + (o & 3) * 8;
      a = __builtin_bit_cast(v8h, cat8(trread(rp), trread(rp + 8 * E1P)));
    }
    re = mfma16h(F8re, a, z); im = mfma16h(F8im, a, z);
    if (kd != 0) {
#pragma unroll
      for (int r = 0; r < 4; ++r) {
        float fr = bflo(wt[r]) * wscale, fi = bfhi(wt[r]) * wscale;
        float zr = re[r], zi = im[r];
        re[r] = zr * fr - zi * fi;
        im[r] = -(zr * fi + zi * fr);
      }
    } else {
      const int ka = 2 * w + (g >> 1), kr = o + 64 * (g & 1);
#pragma unroll
      for (int r = 0; r < 4; ++r) { v2f t = {re[r], im[r]}; *(LAS v2f*)(smem + L_ZX + ka * ZXP + (kr + 16 * r) * 8) = t; }
      BARRIER();
#pragma unroll
      for (int r = 0; r < 4; ++r) {
        PIN(pq[r]);
        const int km = (2048 - (ka + 16 * (kr + 16 * r))) & 2047;
        v2f zm = *(LAS v2f*)(smem + L_ZX + (km & 15) * ZXP + (km >> 4) * 8);
        float zr = re[r], zi = im[r];
        re[r] = (zr * pq[r][0] - zi * pq[r][1] + zm[0] * pq[r][2] + zm[1] * pq[r][3]) * wscale;
        im[r] = -(zr * pq[r][1] + zi * pq[r][0] + zm[0] * pq[r][3] - zm[1] * pq[r][2]) * wscale;
      }
    }
    a = pack_ri(re, im);
    re = mfma16h(a, F8re, z); im = mfma16h(a, F8im, z);
    twid(re, im, t3c, t3s);
    *(LAS v4h*)(smem + L_E2 + (o & 7) * E2P + (2 * w + (o >> 3)) * 32 + g * 8) = pack4h(re);
    *(LAS v4h*)(smem + L_E2 + (o & 7) * E2P + 512 + (2 * w + (o >> 3)) * 32 + g * 8) = pack4h(im);
    WAIT_VM(0);
    BARRIER();
    STAMPT(8);
    if (w == 0) {
      if (ffn && l == 0) st4c((u32)(FL_H + (p * 16 + j) * 4), FL, E);
      ld4c(fp, fhoff, FL);
    }
    {
      LAS char* rp = smem + L_E2 + w * E2P + o * 32 + g * 8;
      a = __builtin_bit_cast(v8h, cat8(*(LAS v4s*)rp, *(LAS v4s*)(rp + 512)));
    }
    re = mfma16h(a, F16re, z); im = mfma16h(a, F16im, z);
    twid(re, im, t1c, t1s);
    a = pack_ri(re, im);
    re = mfma16h(F16re, a, z); im = mfma16h(F16im, a, z);
    st8c((u32)(2 * kd * 4096 + w * 512 + l * 8), ws + OFF_S2, pack4h(re));
    st8c((u32)((2 * kd + 1) * 4096 + w * 512 + l * 8), ws + OFF_S2, pack4h(im));
  }
  STAMPT(9);

  const int d = ct * 64 + dh * 32 + ((l & 31) ^ (kh << 4));
  const float gst = (float)d * (1.0f / 512.0f), ga0 = (float)(32 * kh + 2 * h) * gst;
  v8h G[16];
  auto gen = [&](auto FI) {
    constexpr int f = FI;
#pragma unroll
    for (int e2 = 0; e2 < 4; ++e2) {
      float a = fmaf((float)(64 * (f >> 2) + 8 * (f & 3) + 4 * (e2 >> 1) + (e2 & 1)), gst, ga0);
      float c = cosr(a), sn = sinr(a);
      if (f == 0 && e2 == 0) { if (kh == 0 && h == 0) { c = 0.5f; sn = (d & 1) ? 0.5f : -0.5f; } }
      G[f][2 * e2] = (_Float16)c; G[f][2 * e2 + 1] = (_Float16)sn;
    }
    PIN(G[f]);
  };
  if (w == 0) { WAIT_VM(0); poll1(fhoff); } else static_for<0, 2>(gen);
  BARRIER();
  STAMPT(10);
  const char* hsrc = ws + OFF_H + ((size_t)(RG * 2 + (w >> 2)) * 112 + (w & 3) * 2) * 1024;
  const u32 so0 = (u32)((l >> 3) * 4096) + swz, so1 = so0 + 32768 - 1024;
  const char* ssrc = ws + OFF_S2 + (size_t)(w * 16) * 4096 + RG * 128;
  auto issueA = [&](int n) { dma2c((u32)(l * 16), (u32)(l * 16), hsrc + n * 8192, lbase + aslot(n) + w * 2048); };
  auto issueB = [&](int n) {
    if (n < 14) issueW(n, bslot(n));
    else dma2c(so0, so1, ssrc + (size_t)(n - 14) * (128 * 4096), lbase + bslot(n) + w * 2048);
  };
  issueA(0); issueA(1); issueA(2); issueA(3);
  if (w == 0) static_for<0, 2>(gen);
  static_for<2, 6>(gen);
  const u32 trG = (u32)(kh * 8192 + (4 * h + q) * 128 + ((dh ^ ((q >> 1) & 1)) << 6) + (blk ^ kh) * 32 + pp * 8);
  const u32 trS = (u32)(kh * 8192 + (4 * h + q) * 128 + ((ph ^ ((q >> 1) & 1)) << 6) + blk * 32 + pp * 8);
  const u32 hb = (u32)(ph * 8192 + kh * 4096 + l * 16);
  v16f accG = {}, accS = {};
  v8s fa[8], fb[8];
  v4u fs;
  float gv[8];
  static_for<0, 17>([&](auto V) {
    constexpr int v = V;
    v8s(&cur)[8] = (v & 1) ? fb : fa;
    v8s(&prv)[8] = (v & 1) ? fa : fb;
    auto mmp = [&](int s0, int s1) {
      if constexpr (v >= 15) {
#pragma unroll
        for (int s = 2 * s0; s < 2 * s1; ++s) accS = mfma32h(G[(v - 15) * 8 + s], prv[s], accS);
      } else if constexpr (v >= 1) {
#pragma unroll
        for (int s = s0; s < s1; ++s) accG = mfma32b(prv[2 * s], prv[2 * s + 1], accG);
      }
    };
    if constexpr (v < 16) {
      constexpr int allowed = v == 0 ? 6 : v == 1 ? 4 : v == 2 ? 6 : v <= 11 ? 8 : v == 12 ? 6 : v == 13 ? 4 : v == 14 ? 4 : 0;
      WAIT_VM(allowed);
      if constexpr (v == 9) {
        if (w == 0) {
          PIN(fs);
          v4u f = fs;
          int spins = 0;
          while (!__all(f[0] == E && f[1] == E && f[2] == E && f[3] == E) && ++spins < SPIN_CAP) {
            __builtin_amdgcn_s_sleep(2);
            f = ld16c_sync((u32)(FL_S + l * 16), FL);
          }
        }
      }
      BARRIER();
      if constexpr (v % 2 == 0) STAMPT(11 + v / 2);
      mmp(0, 1); SCHED();
      if constexpr (v >= 1 && v + 5 <= 17) issueB(v + 5);
      if constexpr (v >= 1 && v + 3 <= 13) issueA(v + 3);
      if constexpr (v == 0) { if (tid == 0) st4c((u32)(FL_S + b * 4), FL, E); }
      if constexpr (v == 6) { if (w == 0) ld16c(fs, (u32)(FL_S + l * 16), FL); }
      mmp(1, 2); SCHED();
      if constexpr (v >= 14) {
#pragma unroll
        for (int s = 0; s < 8; ++s) {
          LAS char* sb = smem + bslot(14 + (v - 14) * 2 + (s >> 2)) + trS + (s & 3) * 2048;
          cur[s] = cat8(trread(sb), trread(sb + 1024));
        }
      } else {
#pragma unroll
        for (int s = 0; s < 4; ++s) {
          cur[2 * s] = cat8(trread(smem + bslot(v) + trG + s * 2048), trread(smem + bslot(v) + trG + s * 2048 + 1024));
          cur[2 * s + 1] = *(LAS v8s*)(smem + aslot(v) + hb + s * 1024);
        }
      }
      SCHED();
      mmp(2, 4);
    } else {
      mmp(0, 4);
    }
    if constexpr (v >= 1 && v <= 10) gen(IC<5 + v>{});
    if constexpr (v == 14) {
      LAS char* X = smem + aslot(0) + w * 2048 + l * 16;
      *(LAS v4f*)X = v4f{accG[8], accG[9], accG[10], accG[11]}; *(LAS v4f*)(X + 1024) = v4f{accG[12], accG[13], accG[14], accG[15]};
    }
    if constexpr (v == 15) {
      LAS char* X = smem + aslot(0) + (w ^ 4) * 2048 + l * 16;
      v4f t0 = *(LAS v4f*)X, t1 = *(LAS v4f*)(X + 1024);
      PIN(rs2);
#pragma unroll
      for (int e = 0; e < 8; ++e) {
        float ov = rbf((accG[e] + (e < 4 ? t0[e & 3] : t1[e & 3])) * rs2);
        gv[e] = rbf(ov * __builtin_amdgcn_rcpf(1.0f + __expf(-ov)));
        PIN(gv[e]);
      }
    }
    SCHED();
  });
  STAMPT(19);
  {
    LAS char* X = smem + aslot(1) + w * 2048 + l * 16;
    *(LAS v4f*)X = v4f{accS[8], accS[9], accS[10], accS[11]}; *(LAS v4f*)(X + 1024) = v4f{accS[12], accS[13], accS[14], accS[15]};
  }
  BARRIER();
  {
    LAS char* X = smem + aslot(1) + (w ^ 4) * 2048 + l * 16;
    v4f t0 = *(LAS v4f*)X, t1 = *(LAS v4f*)(X + 1024);
    float pv[8];
#pragma unroll
    for (int e = 0; e < 8; ++e) pv[e] = rbf((accS[e] + (e < 4 ? t0[e & 3] : t1[e & 3])) * oscale) * gv[e];
    u32 a0 = pkbf(pv[0], pv[1]), a1 = pkbf(pv[2], pv[3]), b0 = pkbf(pv[4], pv[5]), b1 = pkbf(pv[6], pv[7]);
    auto r0 = __builtin_amdgcn_permlane32_swap(a0, b0, false, false);
    auto r1 = __builtin_amdgcn_permlane32_swap(a1, b1, false, false);
    v4u pk = {r0[0], r1[0], r0[1], r1[1]};
    PIN(xe);
    v4u ov4;
#pragma unroll
    for (int e = 0; e < 4; ++e) ov4[e] = pkbf(bflo(xe[e]) + bflo(pk[e]), bfhi(xe[e]) + bfhi(pk[e]));
    *(v4u*)((char*)out + eoff) = ov4;
  }
  STAMPT(20);
  STAMPT(21);
  STAMP_OUT(ws);
}

extern "C" void kernel_launch(void* const* d_in, const int* in_sizes, int n_in, void* d_out, int out_size, void* d_ws,
                              size_t ws_size, hipStream_t stream) {
  kmain<<<256, 512, 0, stream>>>((const u16*)d_in[0], (const u16*)d_in[1], (const u16*)d_in[2], (const u16*)d_in[4],
                                 (const u16*)d_in[5], (const u16*)d_in[6], (char*)d_ws, (u16*)d_out);
}
```

```cpp
#include <hip/hip_runtime.h>
#include <stdint.h>

typedef unsigned short u16;
typedef unsigned int u32;
typedef unsigned long long u64;
typedef short v4s __attribute__((ext_vector_type(4)));
typedef short v8s __attribute__((ext_vector_type(8)));
typedef _Float16 v4h __attribute__((ext_vector_type(4)));
typedef _Float16 v8h __attribute__((ext_vector_type(8)));
typedef __bf16 v2b __attribute__((ext_vector_type(2)));
typedef __bf16 v8b __attribute__((ext_vector_type(8)));
typedef float v2f __attribute__((ext_vector_type(2)));
typedef float v4f __attribute__((ext_vector_type(4)));
typedef float v16f __attribute__((ext_vector_type(16)));
typedef u32 v2u __attribute__((ext_vector_type(2)));
typedef u32 v4u __attribute__((ext_vector_type(4)));

#define LAS __attribute__((address_space(3)))
#define GAS __attribute__((address_space(1)))
#define DEVI __device__ __forceinline__

constexpr int D_ = 512, F_ = 1792, DF_ = 257;
constexpr size_t OFF_H = 0;
constexpr size_t OFF_S1 = (size_t)8 << 20;
constexpr size_t OFF_WT = (size_t)10 << 20;
constexpr size_t OFF_S2 = (size_t)13 << 20;
constexpr size_t OFF_M = (size_t)15 << 20;
constexpr size_t OFF_FL = OFF_M;
constexpr u32 FL_W = 0, FL_H = 2048, FL_S = 3072, FL_P = 4096;
constexpr size_t OFF_BC = OFF_M + 8192;
constexpr size_t OFF_MB = OFF_M + 16384;
constexpr size_t OFF_RS2 = OFF_M + 24576;
constexpr size_t OFF_TAB = OFF_M + 32768;
constexpr size_t OFF_PQ = OFF_M + 65536;
constexpr size_t OFF_CSP = OFF_M + 131072;
constexpr int SPIN_CAP = 1 << 16;

#define STAMP 1
#define PROBE 0
#define PROBE_B 5
constexpr size_t OFF_STAMP = (size_t)32 << 20;
#define STAMP_DECL u32 stamps_[32] = {}
#define STAMPT(id) do { u64 t_; asm volatile("s_memrealtime %0\n\ts_waitcnt lgkmcnt(0)" : "=s"(t_) :: "memory"); stamps_[id] = (u32)t_; } while (0)
#define STAMPC(id) do { u64 t_; asm volatile("s_memtime %0\n\ts_waitcnt lgkmcnt(0)" : "=s"(t_) :: "memory"); stamps_[id] = (u32)t_; } while (0)
#define STAMP_OUT(wsp) do { if (blockIdx.x == PROBE_B && threadIdx.x < 64) { u32 dl_ = 500u + (PROBE == 99 ? (stamps_[31] - stamps_[30]) / 20u : PROBE == 98 ? (stamps_[2] - stamps_[1]) : (stamps_[PROBE % 32] - stamps_[0])); u32 t1_; do { u64 t_; asm volatile("s_memrealtime %0\n\ts_waitcnt lgkmcnt(0)" : "=s"(t_) :: "memory"); t1_ = (u32)t_; __builtin_amdgcn_s_sleep(1); } while (t1_ - stamps_[21] < dl_); } } while (0)

template <int I> struct IC { static constexpr int v = I; constexpr operator int() const { return I; } };
template <int B, int E, class Fn> DEVI void static_for(Fn&& f) {
  if constexpr (B < E) { f(IC<B>{}); static_for<B + 1, E>(f); }
}
#define WAIT_VM(n) asm volatile("s_waitcnt vmcnt(%0)" ::"n"(n) : "memory")
#define BARRIER() asm volatile("s_waitcnt lgkmcnt(0)\n\ts_barrier" ::: "memory")
#define SCHED() __builtin_amdgcn_sched_barrier(0)
#define PIN(x) asm volatile("" : "+v"(x))

DEVI int s_of_pi(int pi) {
  int RG = pi >> 6, i = pi & 63;
  return 128 * (4 * (RG & 3) + (i & 3)) + 8 * (i >> 2) + (RG >> 2);
}
DEVI void dma2(u32 voff0, u32 voff1, const char* sbase, u32 lds) {
  asm volatile("s_mov_b32 m0, %3\n\ts_nop 0\n\tglobal_load_lds_dwordx4 %0, %2\n\tglobal_load_lds_dwordx4 %1, %2 offset:1024" ::"v"(voff0), "v"(voff1), "s"(sbase), "s"(lds) : "memory");
}
DEVI void dma2c(u32 voff0, u32 voff1, const char* sbase, u32 lds) {
  asm volatile("s_mov_b32 m0, %3\n\ts_nop 0\n\tglobal_load_lds_dwordx4 %0, %2 sc1\n\tglobal_load_lds_dwordx4 %1, %2 offset:1024 sc1" ::"v"(voff0), "v"(voff1), "s"(sbase), "s"(lds) : "memory");
}
DEVI void dma1c(u32 voff, const char* sbase, u32 lds) {
  asm volatile("s_mov_b32 m0, %2\n\ts_nop 0\n\tglobal_load_lds_dword %0, %1 sc1" ::"v"(voff), "s"(sbase), "s"(lds) : "memory");
}
DEVI v2u ld8c_sync(u32 voff, const char* sb) { v2u d; asm volatile("global_load_dwordx2 %0, %1, %2 sc1\n\ts_waitcnt vmcnt(0)" : "=&v"(d) : "v"(voff), "s"(sb) : "memory"); return d; }
DEVI u32 ld4c_sync(u32 voff, const char* sb) { u32 d; asm volatile("global_load_dword %0, %1, %2 sc1\n\ts_waitcnt vmcnt(0)" : "=&v"(d) : "v"(voff), "s"(sb) : "memory"); return d; }
DEVI v4u ld16c_sync(u32 voff, const char* sb) { v4u d; asm volatile("global_load_dwordx4 %0, %1, %2 sc1\n\ts_waitcnt vmcnt(0)" : "=&v"(d) : "v"(voff), "s"(sb) : "memory"); return d; }
template <class T> DEVI void ld16c(T& d, u32 voff, const char* sb) { asm volatile("global_load_dwordx4 %0, %1, %2 sc1" : "=&v"(d) : "v"(voff), "s"(sb) : "memory"); }
template <class T> DEVI void ld8c(T& d, u32 voff, const char* sb) { asm volatile("global_load_dwordx2 %0, %1, %2 sc1" : "=&v"(d) : "v"(voff), "s"(sb) : "memory"); }
template <class T> DEVI void ld4c(T& d, u32 voff, const char* sb) { asm volatile("global_load_dword %0, %1, %2 sc1" : "=&v"(d) : "v"(voff), "s"(sb) : "memory"); }
template <class T> DEVI void ld16(T& d, u32 voff, const char* sb) { asm volatile("global_load_dwordx4 %0, %1, %2" : "=&v"(d) : "v"(voff), "s"(sb) : "memory"); }
DEVI void ld2(u32& d, u32 voff, const char* sb) { asm volatile("global_load_ushort %0, %1, %2" : "=&v"(d) : "v"(voff), "s"(sb) : "memory"); }
template <class T> DEVI void st16c(u32 voff, const char* sb, T v) { asm volatile("global_store_dwordx4 %0, %1, %2 sc1\n\ts_nop 1" ::"v"(voff), "v"(v), "s"(sb) : "memory"); }
template <class T> DEVI void st8c(u32 voff, const char* sb, T v) { asm volatile("global_store_dwordx2 %0, %1, %2 sc1" ::"v"(voff), "v"(v), "s"(sb) : "memory"); }
template <class T> DEVI void st4c(u32 voff, const char* sb, T v) { asm volatile("global_store_dword %0, %1, %2 sc1" ::"v"(voff), "v"(v), "s"(sb) : "memory"); }

DEVI v4s trread(LAS char* p) { return __builtin_amdgcn_ds_read_tr16_b64_v4i16((LAS v4s*)p); }
DEVI v8s cat8(v4s a, v4s b) { return __builtin_shufflevector(a, b, 0, 1, 2, 3, 4, 5, 6, 7); }
DEVI float bflo(u32 v) { return __builtin_bit_cast(float, v << 16); }
DEVI float bfhi(u32 v) { return __builtin_bit_cast(float, v & 0xffff0000u); }
DEVI float rbf(float v) { return (float)(__bf16)v; }
DEVI u32 pkbf(float a, float b) { v2b p = {(__bf16)a, (__bf16)b}; return __builtin_bit_cast(u32, p); }
DEVI u32 pkbf_o(float a, float b) { u32 r = pkbf(a, b); asm("" : "+v"(r)); return r; }
DEVI float cosr(float rev) { return __builtin_amdgcn_cosf(rev); }
DEVI float sinr(float rev) { return __builtin_amdgcn_sinf(rev); }
DEVI v4f mfma16h(v8h a, v8h b, v4f c) { return __builtin_amdgcn_mfma_f32_16x16x32_f16(a, b, c, 0, 0, 0); }
DEVI v4f mfma16b(v8s a, v4u b, v4f c) { return __builtin_amdgcn_mfma_f32_16x16x32_bf16(__builtin_bit_cast(v8b, a), __builtin_bit_cast(v8b, b), c, 0, 0, 0); }
DEVI v16f mfma32b(v8s a, v8s b, v16f c) { return __builtin_amdgcn_mfma_f32_32x32x16_bf16(__builtin_bit_cast(v8b, a), __builtin_bit_cast(v8b, b), c, 0, 0, 0); }
DEVI v16f mfma32h(v8h a, v8s b, v16f c) { return __builtin_amdgcn_mfma_f32_32x32x16_f16(a, __builtin_bit_cast(v8h, b), c, 0, 0, 0); }
DEVI v8h pack_ri(v4f re, v4f im) {
  v8h r;
  r[0] = (_Float16)re[0]; r[1] = (_Float16)re[1]; r[2] = (_Float16)re[2]; r[3] = (_Float16)re[3];
  r[4] = (_Float16)im[0]; r[5] = (_Float16)im[1]; r[6] = (_Float16)im[2]; r[7] = (_Float16)im[3];
  return r;
}
DEVI v4h pack4h(v4f a) { v4h r; r[0] = (_Float16)a[0]; r[1] = (_Float16)a[1]; r[2] = (_Float16)a[2]; r[3] = (_Float16)a[3]; return r; }
DEVI void twid(v4f& re, v4f& im, const v4f& c, const v4f& s) {
#pragma unroll
  for (int r = 0; r < 4; ++r) { float a = re[r], b = im[r]; re[r] = a * c[r] + b * s[r]; im[r] = b * c[r] - a * s[r]; }
}
template <int CTRL> DEVI float dppadd(float v) {
  return v + __builtin_bit_cast(float, __builtin_amdgcn_update_dpp(0, __builtin_bit_cast(int, v), CTRL, 0xf, 0xf, true));
}
DEVI float dot2ones(u32 v, float acc) {
  const v2b ones = {(__bf16)1.0f, (__bf16)1.0f};
  return __builtin_amdgcn_fdot2_f32_bf16(__builtin_bit_cast(v2b, v), ones, acc, false);
}
template <int CTRL> DEVI u32 dppmax(u32 v) { return max(v, (u32)__builtin_amdgcn_update_dpp(0, (int)v, CTRL, 0xf, 0xf, true)); }
DEVI u32 wave_max(u32 v) {
  v = dppmax<0xB1>(v); v = dppmax<0x4E>(v); v = dppmax<0x141>(v); v = dppmax<0x140>(v);
  u32 a0 = __builtin_amdgcn_readlane(v, 0), a1 = __builtin_amdgcn_readlane(v, 16), a2 = __builtin_amdgcn_readlane(v, 32), a3 = __builtin_amdgcn_readlane(v, 48);
  return max(max(a0, a1), max(a2, a3));
}

struct Frag1 { v8s x0, x1, w1, w2; };

constexpr int L_SCR = 147456;
constexpr int L_CS = L_SCR, L_RED = L_SCR + 1024, L_MX = L_SCR + 1088, L_BC0 = L_SCR + 2048, L_BC1 = L_SCR + 2304, L_FW = L_SCR + 4096;
constexpr int L_E1 = 49152, L_E2 = 65536, L_ZX = 81920 - 1024;
constexpr int E1P = 544, E2P = 1024 + 72, ZXP = 1024 + 8;
DEVI constexpr int bslot(int n) { return (n % 6) < 3 ? 98304 + (n % 6) * 16384 : ((n % 6) - 3) * 16384; }
DEVI constexpr int aslot(int n) { return (n & 3) < 3 ? 49152 + (n & 3) * 16384 : L_SCR; }

__global__ __launch_bounds__(512, 2) void kmain(const u16* __restrict__ x, const u16* __restrict__ wr, const u16* __restrict__ wi,
                                                const u16* __restrict__ Wg, const u16* __restrict__ Wu, const u16* __restrict__ Wd,
                                                char* __restrict__ ws, u16* __restrict__ out) {
  __shared__ __attribute__((aligned(16))) char smem_[163840];
  LAS char* smem = (LAS char*)smem_;
  const u32 lbase = (u32)(size_t)smem;
  const int tid = threadIdx.x, l = tid & 63;
  const int w = __builtin_amdgcn_readfirstlane(tid >> 6);
  const int b = blockIdx.x, xcd = b & 7, idx = b >> 3;
  const bool ffn = idx < 28;
  const int g = l >> 4, o = l & 15;
  const int h = l >> 5, blk = (l >> 4) & 1, q = (l & 15) >> 2, pp = l & 3;
  const char* FL = ws + OFF_FL;
  const char* BC = ws + OFF_BC;
  STAMP_DECL; STAMPT(0);

  const int RG = xcd * 4 + (idx >> 3), ct = idx & 7, kd = b;
  const u32 swz = ((l & 7) ^ (((l >> 4) & 1) << 2)) << 4;
  const u32 ro0 = (u32)((l >> 3) * 1024) + swz, ro1 = ro0 + 8192 - 1024;
  const char* bsrc = (const char*)Wd + (size_t)(w * 16) * 1024 + ct * 128;
  auto issueW = [&](int n, u32 slotoff) { dma2(ro0, ro1, bsrc + (size_t)n * 131072, lbase + slotoff + w * 2048); };

  u32 E = 0;
  if (w == 0) dma1c((u32)((l & 1) * 4), BC, lbase + L_BC0);

  u32 wa[4], wb[4];
  const int tkb = tid & 15, tkd = (b & 7) * 32 + (tid >> 4), tsp = b >> 3;
  auto wt_load = [&]() {
    const u32 v0 = (u32)(((tsp >> 1) + 16 * tkb + 1024 * (tsp & 1)) * DF_ + tkd) * 2;
#pragma unroll
    for (int r = 0; r < 4; ++r) { ld2(wa[r], v0 + r * (256 * DF_ * 2), (const char*)wr); ld2(wb[r], v0 + r * (256 * DF_ * 2), (const char*)wi); }
  };
  auto wt_store = [&]() -> u32 {
    v4u un; u32 mx = 0;
#pragma unroll
    for (int r = 0; r < 4; ++r) { PIN(wa[r]); PIN(wb[r]); un[r] = wa[r] | (wb[r] << 16); mx = max(mx, max(wa[r] & 0x7fffu, wb[r] & 0x7fffu)); }
    st16c((u32)(tkd * 8192 + tsp * 256 + tkb * 16), ws + OFF_WT, un);
    return mx;
  };
  auto get_epoch = [&]() {
    const v2u b0 = *(LAS v2u*)(smem + L_BC0);
    v2u b1 = *(LAS v2u*)(smem + L_BC1);
    int spins = 0;
    while (__builtin_amdgcn_readfirstlane((b0[0] ^ b1[0]) | (b0[1] ^ b1[1])) == 0 && ++spins < SPIN_CAP) {
      __builtin_amdgcn_s_sleep(4);
      b1 = ld8c_sync(0u, BC);
    }
    E = __builtin_amdgcn_readfirstlane(b1[0]);
  };
  auto signal_w = [&]() {
    LAS u32* RED = (LAS u32*)(smem + L_RED);
    v4u r0 = *(LAS v4u*)RED, r1 = *(LAS v4u*)(RED + 4);
    u32 m = max(max(max(r0[0], r0[1]), max(r0[2], r0[3])), max(max(r1[0], r1[1]), max(r1[2], r1[3])));
    if (l == 0) st8c((u32)(FL_W + b * 8), FL, v2u{E, m});
  };
  u32 tick = 0;
  const bool coord = (b == 224) && (w == 0);
  auto do_tick = [&]() { if (coord) { ++tick; if (l == 0) st8c(0u, BC, v2u{E, tick}); } };
  auto poll_w_issue = [&]() { dma2c((u32)(FL_W + l * 16), (u32)(FL_W + l * 16), FL, lbase + L_FW); };
  auto poll_w_check = [&]() {
    v4u fw0 = *(LAS v4u*)(smem + L_FW + l * 16), fw1 = *(LAS v4u*)(smem + L_FW + 1024 + l * 16);
    int spins = 0;
    while (!__all(fw0[0] == E && fw0[2] == E && fw1[0] == E && fw1[2] == E) && ++spins < SPIN_CAP) {
      do_tick();
      __builtin_amdgcn_s_sleep(4);
      fw0 = ld16c_sync((u32)(FL_W + l * 16), FL); fw1 = ld16c_sync((u32)(FL_W + 1024 + l * 16), FL);
    }
    u32 m = wave_max(max(max(fw0[1], fw0[3]), max(fw1[1], fw1[3])));
    if (l == 0) *(LAS u32*)(smem + L_MX) = m;
  };
  u32 fp;
  auto poll1 = [&](u32 voff) {
    PIN(fp);
    u32 f = fp;
    int spins = 0;
    while (!__all(f == E) && ++spins < SPIN_CAP) {
      __builtin_amdgcn_s_sleep(2);
      f = ld4c_sync(voff, FL);
    }
  };
  v4u wt, f16, f8; v4f t1c, t1s, pq[4];
  auto load_r1 = [&]() {
    ld16c(wt, (u32)(w * 1024 + l * 16), ws + OFF_WT + (size_t)kd * 8192);
    ld16c(f16, (u32)(l * 16), ws + OFF_TAB); ld16c(f8, (u32)(l * 16 + 1024), ws + OFF_TAB);
    ld16c(t1c, (u32)(l * 16 + 2048), ws + OFF_TAB); ld16c(t1s, (u32)(l * 16 + 3072), ws + OFF_TAB);
  };
  auto pin_r1 = [&]() { PIN(wt); PIN(f16); PIN(f8); PIN(t1c); PIN(t1s); };
  const u32 fpoff = (u32)(FL_P + (l & 31) * 4);

  const int p = (xcd >> 1) * 4 + idx / 7, j = (xcd & 1) * 7 + idx % 7;
  const int sh = w >> 2, fq = w & 3;
  v16f acc[2][2] = {};
  float mbr[2];

  if (ffn) {
    u32 aoff[2];
#pragma unroll
    for (int i = 0; i < 2; ++i) {
      int row = (2 * w + i) * 8 + (l >> 3);
      aoff[i] = s_of_pi(p * 128 + row) * 1024 + (((l & 7) ^ ((row >> 1) & 7)) << 4) - i * 1024;
    }
    const u32 boff = (u32)(2 * w * 4 + (l >> 4)) * (F_ * 2) + j * 256 + (((l & 15) ^ ((l >> 4) << 2)) << 4);
    const u32 boff1 = boff + 4 * F_ * 2 - 1024;
    const int rot = p >> 1;
    auto issue = [&](int t, int slot) {
      const int kt = (t + rot) & 7;
      u32 dst = lbase + slot * 49152 + w * 2048;
      dma2(aoff[0], aoff[1], (const char*)x + kt * 128, dst);
      dma2(boff, boff1, (const char*)Wg + (size_t)kt * (64 * F_ * 2), dst + 16384);
      dma2(boff, boff1, (const char*)Wu + (size_t)kt * (64 * F_ * 2), dst + 32768);
    };
    issue(0, 0);
    wt_load();
    issue(1, 1); issue(2, 2);
    if (b == 0 && w == 1) {
      const float st1 = (float)o * (1.0f / 256.0f);
      const bool off = ((g >> 1) != (o >> 3));
      v8h F16, F8; v4f tc, ts;
#pragma unroll
      for (int e = 0; e < 4; ++e) {
        float b1 = (float)(4 * g + e) * st1;
        tc[e] = cosr(b1); ts[e] = sinr(b1);
        F16[e] = (_Float16)cosr(16.f * b1); F16[4 + e] = (_Float16)sinr(16.f * b1);
        F8[e] = (_Float16)(off ? 0.f : cosr(32.f * b1)); F8[4 + e] = (_Float16)(off ? 0.f : sinr(32.f * b1));
      }
      st16c((u32)(l * 16), ws + OFF_TAB, F16); st16c((u32)(l * 16 + 1024), ws + OFF_TAB, F8);
      st16c((u32)(l * 16 + 2048), ws + OFF_TAB, tc); st16c((u32)(l * 16 + 3072), ws + OFF_TAB, ts);
    }
    const u32 wb0 = (u32)((8 * h + q) * 256 + ((fq ^ q) << 6) + blk * 32 + pp * 8);
    const u32 r1 = 16384 + sh * 16384, r2 = 32768 - sh * 16384;
    const int xrow = sh * 64 + (l & 31);
    const u32 xb0 = (u32)(xrow * 128 + ((h ^ ((xrow >> 1) & 7)) << 4));
    auto rd = [&](Frag1& f, LAS char* sb, int i) {
      LAS char* xa = sb + (xb0 ^ (i << 5));
      LAS char* wa_ = sb + wb0 + (i << 12);
      f.w1 = cat8(trread(wa_ + r1), trread(wa_ + r1 + 1024));
      f.x0 = *(LAS v8s*)xa; f.x1 = *(LAS v8s*)(xa + 4096);
      f.w2 = cat8(trread(wa_ + r2), trread(wa_ + r2 + 1024));
    };
    auto mm = [&](const Frag1& f) {
      acc[0][0] = mfma32b(f.w1, f.x0, acc[0][0]);
      acc[0][1] = mfma32b(f.w1, f.x1, acc[0][1]);
      acc[1][0] = mfma32b(f.w2, f.x0, acc[1][0]);
      acc[1][1] = mfma32b(f.w2, f.x1, acc[1][1]);
    };
    Frag1 A, B;
    WAIT_VM(20);
    BARRIER();
    STAMPT(1); STAMPC(30);
    rd(A, smem, 0);
    v4f csa = {0.f, 0.f, 0.f, 0.f};
    {
      const u32 one = ((l & 15) == ((l >> 4) & 1)) ? 0x3f803f80u : 0u;
      const v4u sel = {one, one, one, one};
      LAS char* wa_ = smem + wb0 + r1 + ((p & 1) << 13);
      csa = mfma16b(cat8(trread(wa_), trread(wa_ + 1024)), sel, csa);
      csa = mfma16b(cat8(trread(wa_ + 4096), trread(wa_ + 5120)), sel, csa);
    }
    float csp[16];
    int slot = 0;
#pragma unroll 1
    for (int t = 0; t < 7; ++t) {
      LAS char* sb = smem + slot * 49152;
      rd(B, sb, 1); SCHED(); mm(A); SCHED();
      rd(A, sb, 2); SCHED(); mm(B); SCHED();
      rd(B, sb, 3); SCHED(); mm(A); SCHED();
      WAIT_VM(6);
      if (t == 6 && w == 0) poll_w_check();
      BARRIER();
      const int nslot = slot == 2 ? 0 : slot + 1;
      acc[0][0] = mfma32b(B.w1, B.x0, acc[0][0]); SCHED();
      rd(A, smem + nslot * 49152, 0); SCHED();
      acc[0][1] = mfma32b(B.w1, B.x1, acc[0][1]);
      acc[1][0] = mfma32b(B.w2, B.x0, acc[1][0]);
      acc[1][1] = mfma32b(B.w2, B.x1, acc[1][1]); SCHED();
      if (t < 5) issue(t + 3, slot);
      else { const int n0 = (t - 5) * 3; issueW(n0, slot * 49152); issueW(n0 + 1, slot * 49152 + 16384); issueW(n0 + 2, slot * 49152 + 32768); }
      if (t == 0) {
        u32 mx = wave_max(wt_store());
        if (l == 0) *(LAS u32*)(smem + L_RED + w * 4) = mx;
        if ((l & 15) < 2) st16c((u32)(((j * 16 + p) * 256 + sh * 128 + fq * 32 + (l & 15) * 16 + (l >> 4) * 4) * 4), ws + OFF_CSP, csa);
        if (w == 0) dma1c((u32)((l & 1) * 4), BC, lbase + L_BC1);
      }
      if (w == 0) {
        if (t == 2) { get_epoch(); signal_w(); }
        if (t == 4) poll_w_issue();
      }
      slot = nslot;
      SCHED();
    }
    if (w < 4) {
#pragma unroll
      for (int i = 0; i < 16; ++i) ld4c(csp[i], (u32)((j * 16 + i) * 1024 + tid * 4), ws + OFF_CSP);
    }
    ld4c(mbr[0], (u32)((p * 128 + xrow) * 4), ws + OFF_MB); ld4c(mbr[1], (u32)((p * 128 + xrow + 32) * 4), ws + OFF_MB);
    load_r1();
    if (kd == 0) {
#pragma unroll
      for (int r = 0; r < 4; ++r) ld16c(pq[r], (u32)(w * 4096 + r * 1024 + l * 16), ws + OFF_PQ);
    }
    if (w == 0) ld4c(fp, fpoff, FL);
    SCHED();
    {
      LAS char* sb = smem + slot * 49152;
      rd(B, sb, 1); SCHED(); mm(A); SCHED();
      rd(A, sb, 2); SCHED(); mm(B); SCHED();
      rd(B, sb, 3); SCHED(); mm(A); SCHED();
      mm(B); SCHED();
    }
    STAMPT(2); STAMPC(31);
    WAIT_VM(0);
    pin_r1(); PIN(mbr[0]); PIN(mbr[1]);
    if (w < 4) {
      float s = 0.f;
#pragma unroll
      for (int i = 0; i < 16; ++i) { PIN(csp[i]); s += csp[i]; }
      *(LAS float*)(smem + L_CS + tid * 4) = s;
    }
  } else {
    const int sp = xcd * 4 + (idx - 28);
    const int pi = sp * 64 + w * 8 + (l >> 3);
    v4u xr[8];
    {
      const u32 xo = (u32)(s_of_pi(pi) * 1024 + (l & 7) * 16);
#pragma unroll
      for (int m = 0; m < 8; ++m) ld16(xr[m], xo + m * 128, (const char*)x);
    }
    wt_load();
    u32 sq[8];
    if (w == 7) {
      const int kss = (sp >> 1) + 16 * (l >> 2) + 256 * (4 * (sp & 1) + (l & 3)), km = (2048 - kss) & 2047;
      const u32 o1 = (u32)(kss * DF_ * 2), o2 = (u32)(km * DF_ * 2);
      ld2(sq[0], o1, (const char*)wr); ld2(sq[1], o1, (const char*)wi); ld2(sq[2], o2, (const char*)wr); ld2(sq[3], o2, (const char*)wi);
      ld2(sq[4], o1 + 512, (const char*)wr); ld2(sq[5], o1 + 512, (const char*)wi); ld2(sq[6], o2 + 512, (const char*)wr); ld2(sq[7], o2 + 512, (const char*)wi);
    }
    if (coord) {
      v4u f[5];
#pragma unroll
      for (int i = 0; i < 5; ++i) ld16c(f[i], (u32)(i * 1024 + l * 16), FL);
      WAIT_VM(0);
      u32 c = __builtin_amdgcn_readfirstlane(*(LAS u32*)(smem + L_BC0));
      bool hit;
      do {
        ++c; hit = false;
#pragma unroll
        for (int i = 0; i < 5; ++i) { PIN(f[i]); hit |= (f[i][0] == c) | (f[i][1] == c) | (f[i][2] == c) | (f[i][3] == c); }
      } while (__any(hit));
      E = c;
      do_tick();
    }
    v4u Fh[2][2], Fl[2][2];
    v8h M2, M2n;
    v4f twc[2], tws[2];
    {
      const float fo = (float)o, fg = (float)(4 * g);
      const float st = fo * (1.0f / 32.0f), a0 = fg * st;
#pragma unroll
      for (int e2 = 0; e2 < 4; ++e2) {
        float c[2], sn[2];
#pragma unroll
        for (int i = 0; i < 2; ++i) {
          const int e = 2 * e2 + i;
          float a = fmaf((float)(16 * (e >> 2) + (e & 3)), st, a0);
          c[i] = cosr(a); sn[i] = -sinr(a);
        }
        u32 ch = pkbf_o(c[0], c[1]), shh = pkbf_o(sn[0], sn[1]);
        u32 cl = pkbf_o(c[0] - bflo(ch), c[1] - bfhi(ch)), sl = pkbf_o(sn[0] - bflo(shh), sn[1] - bfhi(shh));
        Fh[0][0][e2] = ch; Fh[0][1][e2] = shh; Fl[0][0][e2] = cl; Fl[0][1][e2] = sl;
        Fh[1][0][e2] = ch ^ 0x80000000u; Fh[1][1][e2] = shh ^ 0x80000000u; Fl[1][0][e2] = cl ^ 0x80000000u; Fl[1][1][e2] = sl ^ 0x80000000u;
      }
      const float st2 = (float)(o >> 1) * (1.0f / 16.0f), qo = (o & 1) ? 0.25f : 0.f;
#pragma unroll
      for (int e = 0; e < 4; ++e) {
        const float db = fg + (float)e;
        float a = fmaf(db, st2, qo);
        M2[e] = (_Float16)(0.0625f * cosr(a)); M2[4 + e] = (_Float16)(0.0625f * sinr(a));
        M2n[e] = (_Float16)(o == 0 ? ((e & 1) ? -0.0625f : 0.0625f) : 0.f); M2n[4 + e] = (_Float16)0.f;
#pragma unroll
        for (int u = 0; u < 2; ++u) {
          float t = db * ((fo + 16.f * u) * (1.0f / 512.0f));
          twc[u][e] = cosr(t); tws[u][e] = sinr(t);
        }
      }
      PIN(Fh[0][0]); PIN(Fh[0][1]); PIN(Fl[0][0]); PIN(Fl[0][1]); PIN(M2); PIN(twc[0]); PIN(twc[1]); PIN(tws[0]); PIN(tws[1]);
    }
    do_tick();
    WAIT_VM(0);
    STAMPT(1); STAMPC(30);
    LAS char* img = smem + w * 8192;
    {
      float s = 0.f;
#pragma unroll
      for (int m = 0; m < 8; ++m) {
        PIN(xr[m]);
#pragma unroll
        for (int e = 0; e < 4; ++e) { u32 t = xr[m][e]; s = dot2ones(t, s); }
      }
      s = dppadd<0xB1>(s); s = dppadd<0x4E>(s); s = dppadd<0x141>(s);
      const float mean = s * (1.0f / 512.0f), mb = rbf(mean);
      if ((l & 7) == 0) st4c((u32)(pi * 4), ws + OFF_MB, -mb);
      u32 mx = wt_store();
      if (w == 7) {
#pragma unroll
        for (int i = 0; i < 8; ++i) PIN(sq[i]);
        mx = max(mx, max(sq[4] & 0x7fffu, sq[5] & 0x7fffu));
        float war = bflo(sq[0]) + bflo(sq[2]), wai = bflo(sq[1]) - bflo(sq[3]);
        float wbr = bflo(sq[4]) + bflo(sq[6]), wbi = bflo(sq[5]) - bflo(sq[7]);
        v4f pv = {0.25f * (war + wbr), 0.25f * (wai + wbi), 0.25f * (war - wbr), 0.25f * (wai - wbi)};
        st16c((u32)((((sp >> 2) * 4 + (l & 3)) * 64 + (sp & 3) * 16 + (l >> 2)) * 16), ws + OFF_PQ, pv);
      }
      mx = wave_max(mx);
      if (l == 0) *(LAS u32*)(smem + L_RED + w * 4) = mx;
      if (w == 0 && !coord) dma1c((u32)((l & 1) * 4), BC, lbase + L_BC1);
      do_tick();
      const v2f mb2 = {mb, mb};
      v2f q2 = {0.f, 0.f};
#pragma unroll
      for (int m = 0; m < 8; ++m)
#pragma unroll
        for (int e = 0; e < 4; ++e) {
          v2f t = v2f{bflo(xr[m][e]), bfhi(xr[m][e])} - mb2;
          q2 = __builtin_elementwise_fma(t, t, q2);
          xr[m][e] = pkbf_o(t[0], t[1]);
        }
      float qq = q2[0] + q2[1];
      qq = dppadd<0xB1>(qq); qq = dppadd<0x4E>(qq); qq = dppadd<0x141>(qq);
      const float dm = mean - mb;
      const float var = rbf(fmaxf(qq * (1.0f / 512.0f) - dm * dm, 0.f));
      const float sd = rbf(__builtin_sqrtf(rbf(var + 1e-5f)));
      const float rs = 1.0f / sd;
      PIN(qq);
      WAIT_VM(0);
      BARRIER();
      if (w == 0) { if (!coord) get_epoch(); signal_w(); }
      do_tick();
      const v2f rs2 = {rs, rs};
      if ((l & 7) == 0) st4c((u32)(pi * 4), ws + OFF_RS2, rs * rs);
#pragma unroll
      for (int m = 0; m < 8; ++m) {
        v4u o4;
#pragma unroll
        for (int e = 0; e < 4; ++e) {
          v2f t = v2f{bflo(xr[m][e]), bfhi(xr[m][e])} * rs2;
          o4[e] = pkbf(t[0], t[1]);
        }
        *(LAS v4u*)(img + (l >> 3) * 1024 + ((l & 7) + 8 * m) * 16) = o4;
      }
    }
    STAMPT(2);
    LAS char* T = smem + 65536;
#pragma unroll 1
    for (int nbi = 0; nbi < 2; ++nbi) {
      do_tick();
      v4f d2[4][2];
#pragma unroll
      for (int r = 0; r < 4; ++r) {
        LAS char* rp = img + (nbi * 4 + r) * 1024 + l * 8;
        v8s xf = cat8(trread(rp), trread(rp + 512));
        float nyq = 0.f;
#pragma unroll
        for (int u = 0; u < 2; ++u) {
          v4f z = {0.f, 0.f, 0.f, 0.f};
          v4f re = mfma16b(xf, Fl[u][0], z), im = mfma16b(xf, Fl[u][1], z);
          re = mfma16b(xf, Fh[u][0], re); im = mfma16b(xf, Fh[u][1], im);
          twid(re, im, twc[u], tws[u]);
          v8h bb = pack_ri(re, im);
          d2[r][u] = mfma16h(M2, bb, z);
          if (u == 0) { v4f n = mfma16h(M2n, bb, z); nyq = n[0]; }
        }
        if (l == 0) d2[r][0][1] = nyq;
      }
#pragma unroll
      for (int u = 0; u < 2; ++u)
#pragma unroll
        for (int sub = 0; sub < 2; ++sub) {
          v8h un;
#pragma unroll
          for (int r = 0; r < 4; ++r) { un[r] = (_Float16)d2[r][u][2 * sub]; un[4 + r] = (_Float16)d2[r][u][2 * sub + 1]; }
          *(LAS v8h*)(T + (16 * u + o + 32 * (2 * g + sub)) * 272 + (2 * w + nbi) * 16) = un;
        }
    }
    do_tick();
    if (w == 0) poll_w_issue();
    BARRIER();
    STAMPT(3); STAMPC(31);
    {
      v4u un[8];
#pragma unroll
      for (int i = 0; i < 8; ++i) un[i] = *(LAS v4u*)(T + (w * 32 + i * 4 + g) * 272 + o * 16);
#pragma unroll
      for (int i = 0; i < 8; ++i) st16c((u32)((w * 32 + i * 4 + g) * 8192 + sp * 256 + o * 16), ws + OFF_S1, un[i]);
    }
    WAIT_VM(0);
    if (w == 0) poll_w_check();
    BARRIER();
    if (tid == 0) st4c((u32)(FL_P + sp * 4), FL, E);
    static_for<0, 6>([&](auto N) { issueW(N, bslot(N)); });
    load_r1();
    if (w == 0) ld4c(fp, fpoff, FL);
    WAIT_VM(0);
    pin_r1();
    STAMPT(4);
  }

  if (w == 0) poll1(fpoff);
  BARRIER();
  STAMPT(5);
  v4u s1v;
  ld16c(s1v, (u32)(w * 1024 + l * 16), ws + OFF_S1 + (size_t)kd * 8192);
  const int kh = w >> 2, wq = w & 3, dh = wq >> 1, ph = wq & 1;
  const int epi = RG * 64 + ph * 32 + (l & 31);
  const u32 eoff = (u32)(s_of_pi(epi) * D_ + ct * 64 + dh * 32 + kh * 16 + h * 8) * 2;
  v4u xe; float rs2;
  ld16(xe, eoff, (const char*)x);
  ld4c(rs2, (u32)(epi * 4), ws + OFF_RS2);

  if (ffn) {
    v4f cg[4], cu[4];
#pragma unroll
    for (int g4 = 0; g4 < 4; ++g4) {
      cg[g4] = *(LAS v4f*)(smem + L_CS + (fq * 32 + g4 * 8 + h * 4) * 4);
      cu[g4] = *(LAS v4f*)(smem + L_CS + (128 + fq * 32 + g4 * 8 + h * 4) * 4);
    }
    auto fin = [&](const v16f& ag, const v16f& au, int rt) {
      const v2f mb2 = {mbr[rt], mbr[rt]};
#pragma unroll
      for (int s2i = 0; s2i < 2; ++s2i) {
        v4u ov;
#pragma unroll
        for (int e = 0; e < 4; ++e) {
          const int r = s2i * 8 + 2 * e;
          v2f gg = __builtin_elementwise_fma(mb2, v2f{cg[r >> 2][r & 3], cg[r >> 2][(r & 3) + 1]}, v2f{ag[r], ag[r + 1]});
          v2f uu = __builtin_elementwise_fma(mb2, v2f{cu[r >> 2][r & 3], cu[r >> 2][(r & 3) + 1]}, v2f{au[r], au[r + 1]});
          gg[0] = fmaxf(gg[0], 0.f); gg[1] = fmaxf(gg[1], 0.f);
          gg *= uu;
          ov[e] = pkbf(gg[0], gg[1]);
        }
        st16c((u32)(((p * 4 + sh * 2 + rt) * 112 + (j * 8 + fq * 2 + s2i)) * 1024 + l * 16), ws + OFF_H, ov);
      }
    };
    if (sh == 0) { fin(acc[0][0], acc[1][0], 0); fin(acc[0][1], acc[1][1], 1); }
    else { fin(acc[1][0], acc[0][0], 0); fin(acc[1][1], acc[0][1], 1); }
  }
  STAMPT(6);

  float oscale;
  const u32 fhoff = (u32)(FL_H + ((RG >> 1) * 16 + min(l, 13)) * 4);
  {
    v4f t2c, t2s, t3c, t3s;
    {
      const float fo = (float)o, fg = (float)(4 * g), fw = (float)w;
      const float st2 = fw * (1.0f / 128.0f), a2 = fw * fo * (1.0f / 2048.0f);
      const float f7 = (float)(o & 7), st3 = f7 * (1.0f / 128.0f), a3 = f7 * (float)(2 * w + (o >> 3)) * (1.0f / 2048.0f);
#pragma unroll
      for (int e = 0; e < 4; ++e) {
        const float id = fg + (float)e;
        float b2 = fmaf(id, st2, a2);
        t2c[e] = cosr(b2); t2s[e] = sinr(b2);
        float b3 = fmaf(id, st3, a3);
        t3c[e] = cosr(b3); t3s[e] = sinr(b3);
      }
    }
    int Ex = (int)(*(LAS u32*)(smem + L_MX) >> 7);
    Ex = Ex < 16 ? 16 : (Ex > 240 ? 240 : Ex);
    const float wscale = __builtin_bit_cast(float, (u32)(246 - Ex) << 23);
    oscale = __builtin_bit_cast(float, (u32)(Ex - 7) << 23);
    const v8h F16re = __builtin_bit_cast(v8h, f16), F8re = __builtin_bit_cast(v8h, f8);
    const v8h F16im = __builtin_bit_cast(v8h, v4u{f16[2] ^ 0x80008000u, f16[3] ^ 0x80008000u, f16[0], f16[1]});
    const v8h F8im = __builtin_bit_cast(v8h, v4u{f8[2] ^ 0x80008000u, f8[3] ^ 0x80008000u, f8[0], f8[1]});
    PIN(t2c); PIN(t2s);
    if (ffn) WAIT_VM(6); else WAIT_VM(2);
    PIN(s1v);
    STAMPT(7);
    const v4f z = {0.f, 0.f, 0.f, 0.f};
    v8h a = __builtin_bit_cast(v8h, s1v);
    v4f re = mfma16h(a, F16re, z), im = mfma16h(a, F16im, z);
    twid(re, im, t1c, t1s);
    a = pack_ri(re, im);
    re = mfma16h(F16re, a, z); im = mfma16h(F16im, a, z);
    twid(re, im, t2c, t2s);
    *(LAS v4h*)(smem + L_E1 + w * E1P + o * 32 + g * 8) = pack4h(re);
    *(LAS v4h*)(smem + L_E1 + (8 + w) * E1P + o * 32 + g * 8) = pack4h(im);
    BARRIER();
    {
      LAS char* rp = smem + L_E1 + (4 * (g & 1) + (o >> 2)) * E1P + (2 * w + (g >> 1)) * 32 + (o & 3) * 8;
      a = __builtin_bit_cast(v8h, cat8(trread(rp), trread(rp + 8 * E1P)));
    }
    re = mfma16h(F8re, a, z); im = mfma16h(F8im, a, z);
    if (kd != 0) {
#pragma unroll
      for (int r = 0; r < 4; ++r) {
        float fr = bflo(wt[r]) * wscale, fi = bfhi(wt[r]) * wscale;
        float zr = re[r], zi = im[r];
        re[r] = zr * fr - zi * fi;
        im[r] = -(zr * fi + zi * fr);
      }
    } else {
      const int ka = 2 * w + (g >> 1), kr = o + 64 * (g & 1);
#pragma unroll
      for (int r = 0; r < 4; ++r) { v2f t = {re[r], im[r]}; *(LAS v2f*)(smem + L_ZX + ka * ZXP + (kr + 16 * r) * 8) = t; }
      BARRIER();
#pragma unroll
      for (int r = 0; r < 4; ++r) {
        PIN(pq[r]);
        const int km = (2048 - (ka + 16 * (kr + 16 * r))) & 2047;
        v2f zm = *(LAS v2f*)(smem + L_ZX + (km & 15) * ZXP + (km >> 4) * 8);
        float zr = re[r], zi = im[r];
        re[r] = (zr * pq[r][0] - zi * pq[r][1] + zm[0] * pq[r][2] + zm[1] * pq[r][3]) * wscale;
        im[r] = -(zr * pq[r][1] + zi * pq[r][0] + zm[0] * pq[r][3] - zm[1] * pq[r][2]) * wscale;
      }
    }
    a = pack_ri(re, im);
    re = mfma16h(a, F8re, z); im = mfma16h(a, F8im, z);
    twid(re, im, t3c, t3s);
    *(LAS v4h*)(smem + L_E2 + (o & 7) * E2P + (2 * w + (o >> 3)) * 32 + g * 8) = pack4h(re);
    *(LAS v4h*)(smem + L_E2 + (o & 7) * E2P + 512 + (2 * w + (o >> 3)) * 32 + g * 8) = pack4h(im);
    WAIT_VM(0);
    BARRIER();
    STAMPT(8);
    if (w == 0) {
      if (ffn && l == 0) st4c((u32)(FL_H + (p * 16 + j) * 4), FL, E);
      ld4c(fp, fhoff, FL);
    }
    {
      LAS char* rp = smem + L_E2 + w * E2P + o * 32 + g * 8;
      a = __builtin_bit_cast(v8h, cat8(*(LAS v4s*)rp, *(LAS v4s*)(rp + 512)));
    }
    re = mfma16h(a, F16re, z); im = mfma16h(a, F16im, z);
    twid(re, im, t1c, t1s);
    a = pack_ri(re, im);
    re = mfma16h(F16re, a, z); im = mfma16h(F16im, a, z);
    st8c((u32)(2 * kd * 4096 + w * 512 + l * 8), ws + OFF_S2, pack4h(re));
    st8c((u32)((2 * kd + 1) * 4096 + w * 512 + l * 8), ws + OFF_S2, pack4h(im));
  }
  STAMPT(9);

  const int d = ct * 64 + dh * 32 + ((l & 31) ^ (kh << 4));
  const float gst = (float)d * (1.0f / 512.0f), ga0 = (float)(32 * kh + 2 * h) * gst;
  v8h G[16];
  auto gen = [&](auto FI) {
    constexpr int f = FI;
#pragma unroll
    for (int e2 = 0; e2 < 4; ++e2) {
      float a = fmaf((float)(64 * (f >> 2) + 8 * (f & 3) + 4 * (e2 >> 1) + (e2 & 1)), gst, ga0);
      float c = cosr(a), sn = sinr(a);
      if (f == 0 && e2 == 0) { if (kh == 0 && h == 0) { c = 0.5f; sn = (d & 1) ? 0.5f : -0.5f; } }
      G[f][2 * e2] = (_Float16)c; G[f][2 * e2 + 1] = (_Float16)sn;
    }
    PIN(G[f]);
  };
  if (w == 0) { WAIT_VM(0); poll1(fhoff); } else static_for<0, 2>(gen);
  BARRIER();
  STAMPT(10);
  const char* hsrc = ws + OFF_H + ((size_t)(RG * 2 + (w >> 2)) * 112 + (w & 3) * 2) * 1024;
  const u32 so0 = (u32)((l >> 3) * 4096) + swz, so1 = so0 + 32768 - 1024;
  const char* ssrc = ws + OFF_S2 + (size_t)(w * 16) * 4096 + RG * 128;
  auto issueA = [&](int n) { dma2c((u32)(l * 16), (u32)(l * 16), hsrc + n * 8192, lbase + aslot(n) + w * 2048); };
  auto issueB = [&](int n) {
    if (n < 14) issueW(n, bslot(n));
    else dma2c(so0, so1, ssrc + (size_t)(n - 14) * (128 * 4096), lbase + bslot(n) + w * 2048);
  };
  issueA(0); issueA(1); issueA(2); issueA(3);
  if (w == 0) static_for<0, 2>(gen);
  static_for<2, 6>(gen);
  const u32 trG = (u32)(kh * 8192 + (4 * h + q) * 128 + ((dh ^ ((q >> 1) & 1)) << 6) + (blk ^ kh) * 32 + pp * 8);
  const u32 trS = (u32)(kh * 8192 + (4 * h + q) * 128 + ((ph ^ ((q >> 1) & 1)) << 6) + blk * 32 + pp * 8);
  const u32 hb = (u32)(ph * 8192 + kh * 4096 + l * 16);
  v16f accG = {}, accS = {};
  v8s fa[8], fb[8];
  v4u fs;
  float gv[8];
  static_for<0, 17>([&](auto V) {
    constexpr int v = V;
    v8s(&cur)[8] = (v & 1) ? fb : fa;
    v8s(&prv)[8] = (v & 1) ? fa : fb;
    auto mmp = [&](int s0, int s1) {
      if constexpr (v >= 15) {
#pragma unroll
        for (int s = 2 * s0; s < 2 * s1; ++s) accS = mfma32h(G[(v - 15) * 8 + s], prv[s], accS);
      } else if constexpr (v >= 1) {
#pragma unroll
        for (int s = s0; s < s1; ++s) accG = mfma32b(prv[2 * s], prv[2 * s + 1], accG);
      }
    };
    if constexpr (v < 16) {
      constexpr int allowed = v == 0 ? 6 : v == 1 ? 4 : v == 2 ? 6 : v <= 11 ? 8 : v == 12 ? 6 : v == 13 ? 4 : v == 14 ? 4 : 0;
      WAIT_VM(allowed);
      if constexpr (v == 9) {
        if (w == 0) {
          PIN(fs);
          v4u f = fs;
          int spins = 0;
          while (!__all(f[0] == E && f[1] == E && f[2] == E && f[3] == E) && ++spins < SPIN_CAP) {
            __builtin_amdgcn_s_sleep(2);
            f = ld16c_sync((u32)(FL_S + l * 16), FL);
          }
        }
      }
      BARRIER();
      if constexpr (v % 2 == 0) STAMPT(11 + v / 2);
      mmp(0, 1); SCHED();
      if constexpr (v >= 1 && v + 5 <= 17) issueB(v + 5);
      if constexpr (v >= 1 && v + 3 <= 13) issueA(v + 3);
      if constexpr (v == 0) { if (tid == 0) st4c((u32)(FL_S + b * 4), FL, E); }
      if constexpr (v == 6) { if (w == 0) ld16c(fs, (u32)(FL_S + l * 16), FL); }
      mmp(1, 2); SCHED();
      if constexpr (v >= 14) {
#pragma unroll
        for (int s = 0; s < 8; ++s) {
          LAS char* sb = smem + bslot(14 + (v - 14) * 2 + (s >> 2)) + trS + (s & 3) * 2048;
          cur[s] = cat8(trread(sb), trread(sb + 1024));
        }
      } else {
#pragma unroll
        for (int s = 0; s < 4; ++s) {
          cur[2 * s] = cat8(trread(smem + bslot(v) + trG + s * 2048), trread(smem + bslot(v) + trG + s * 2048 + 1024));
          cur[2 * s + 1] = *(LAS v8s*)(smem + aslot(v) + hb + s * 1024);
        }
      }
      SCHED();
      mmp(2, 4);
    } else {
      mmp(0, 4);
    }
    if constexpr (v >= 1 && v <= 10) gen(IC<5 + v>{});
    if constexpr (v == 14) {
      LAS char* X = smem + aslot(0) + w * 2048 + l * 16;
      *(LAS v4f*)X = v4f{accG[8], accG[9], accG[10], accG[11]}; *(LAS v4f*)(X + 1024) = v4f{accG[12], accG[13], accG[14], accG[15]};
    }
    if constexpr (v == 15) {
      LAS char* X = smem + aslot(0) + (w ^ 4) * 2048 + l * 16;
      v4f t0 = *(LAS v4f*)X, t1 = *(LAS v4f*)(X + 1024);
      PIN(rs2);
#pragma unroll
      for (int e = 0; e < 8; ++e) {
        float ov = rbf((accG[e] + (e < 4 ? t0[e & 3] : t1[e & 3])) * rs2);
        gv[e] = rbf(ov * __builtin_amdgcn_rcpf(1.0f + __expf(-ov)));
        PIN(gv[e]);
      }
    }
    SCHED();
  });
  STAMPT(19);
  {
    LAS char* X = smem + aslot(1) + w * 2048 + l * 16;
    *(LAS v4f*)X = v4f{accS[8], accS[9], accS[10], accS[11]}; *(LAS v4f*)(X + 1024) = v4f{accS[12], accS[13], accS[14], accS[15]};
  }
  BARRIER();
  {
    LAS char* X = smem + aslot(1) + (w ^ 4) * 2048 + l * 16;
    v4f t0 = *(LAS v4f*)X, t1 = *(LAS v4f*)(X + 1024);
    float pv[8];
#pragma unroll
    for (int e = 0; e < 8; ++e) pv[e] = rbf((accS[e] + (e < 4 ? t0[e & 3] : t1[e & 3])) * oscale) * gv[e];
    u32 a0 = pkbf(pv[0], pv[1]), a1 = pkbf(pv[2], pv[3]), b0 = pkbf(pv[4], pv[5]), b1 = pkbf(pv[6], pv[7]);
    auto r0 = __builtin_amdgcn_permlane32_swap(a0, b0, false, false);
    auto r1 = __builtin_amdgcn_permlane32_swap(a1, b1, false, false);
    v4u pk = {r0[0], r1[0], r0[1], r1[1]};
    PIN(xe);
    v4u ov4;
#pragma unroll
    for (int e = 0; e < 4; ++e) ov4[e] = pkbf(bflo(xe[e]) + bflo(pk[e]), bfhi(xe[e]) + bfhi(pk[e]));
    *(v4u*)((char*)out + eoff) = ov4;
  }
  STAMPT(20);
  WAIT_VM(0);
  STAMPT(21);
  STAMP_OUT(ws);
}

extern "C" void kernel_launch(void* const* d_in, const int* in_sizes, int n_in, void* d_out, int out_size, void* d_ws,
                              size_t ws_size, hipStream_t stream) {
  kmain<<<256, 512, 0, stream>>>((const u16*)d_in[0], (const u16*)d_in[1], (const u16*)d_in[2], (const u16*)d_in[4],
                                 (const u16*)d_in[5], (const u16*)d_in[6], (char*)d_ws, (u16*)d_out);
}
```

```cpp
#include <hip/hip_runtime.h>
#include <stdint.h>

typedef unsigned short u16;
typedef unsigned int u32;
typedef unsigned long long u64;
typedef short v4s __attribute__((ext_vector_type(4)));
typedef short v8s __attribute__((ext_vector_type(8)));
typedef _Float16 v4h __attribute__((ext_vector_type(4)));
typedef _Float16 v8h __attribute__((ext_vector_type(8)));
typedef __bf16 v2b __attribute__((ext_vector_type(2)));
typedef __bf16 v8b __attribute__((ext_vector_type(8)));
typedef float v2f __attribute__((ext_vector_type(2)));
typedef float v4f __attribute__((ext_vector_type(4)));
typedef float v16f __attribute__((ext_vector_type(16)));
typedef u32 v2u __attribute__((ext_vector_type(2)));
typedef u32 v4u __attribute__((ext_vector_type(4)));

#define LAS __attribute__((address_space(3)))
#define GAS __attribute__((address_space(1)))
#define DEVI __device__ __forceinline__

constexpr int D_ = 512, F_ = 1792, DF_ = 257;
constexpr size_t OFF_H = 0;
constexpr size_t OFF_S1 = (size_t)8 << 20;
constexpr size_t OFF_WT = (size_t)10 << 20;
constexpr size_t OFF_S2 = (size_t)13 << 20;
constexpr size_t OFF_M = (size_t)15 << 20;
constexpr size_t OFF_FL = OFF_M;
constexpr u32 FL_W = 0, FL_S = 1024, FL_A = 2048, FL_H = 3072;
constexpr size_t OFF_BC = OFF_M + 12288;
constexpr size_t OFF_MB = OFF_M + 16384;
constexpr size_t OFF_RS2 = OFF_M + 24576;
constexpr size_t OFF_PQ = OFF_M + 65536;
constexpr size_t OFF_CSP = OFF_M + 131072;
constexpr int SPIN_CAP = 1 << 16;

#define STAMP_DECL
#define STAMPT(id)
#define STAMPC(id)
#define STAMP_OUT(wsp)

template <int I> struct IC { static constexpr int v = I; constexpr operator int() const { return I; } };
template <int B, int E, class Fn> DEVI void static_for(Fn&& f) {
  if constexpr (B < E) { f(IC<B>{}); static_for<B + 1, E>(f); }
}
#define WAIT_VM(n) asm volatile("s_waitcnt vmcnt(%0)" ::"n"(n) : "memory")
#define BARRIER() asm volatile("s_waitcnt lgkmcnt(0)\n\ts_barrier" ::: "memory")
#define SCHED() __builtin_amdgcn_sched_barrier(0)
#define PIN(x) asm volatile("" : "+v"(x))

DEVI int s_of_pi(int pi) {
  int RG = pi >> 6, i = pi & 63;
  return 128 * (4 * (RG & 3) + (i & 3)) + 8 * (i >> 2) + (RG >> 2);
}
DEVI void dma2(u32 voff0, u32 voff1, const char* sbase, u32 lds) {
  asm volatile("s_mov_b32 m0, %3\n\ts_nop 0\n\tglobal_load_lds_dwordx4 %0, %2\n\tglobal_load_lds_dwordx4 %1, %2 offset:1024" ::"v"(voff0), "v"(voff1), "s"(sbase), "s"(lds) : "memory");
}
DEVI void dma2c(u32 voff0, u32 voff1, const char* sbase, u32 lds) {
  asm volatile("s_mov_b32 m0, %3\n\ts_nop 0\n\tglobal_load_lds_dwordx4 %0, %2 sc1\n\tglobal_load_lds_dwordx4 %1, %2 offset:1024 sc1" ::"v"(voff0), "v"(voff1), "s"(sbase), "s"(lds) : "memory");
}
template <int OFF> DEVI void dma1(u32 voff, const char* sbase, u32 lds) {
  asm volatile("s_mov_b32 m0, %2\n\ts_nop 0\n\tglobal_load_lds_dwordx4 %0, %1 offset:%3" ::"v"(voff), "s"(sbase), "s"(lds), "n"(OFF) : "memory");
}
template <int OFF> DEVI void dma1s(u32 voff, const char* sbase, u32 lds) {
  asm volatile("s_mov_b32 m0, %2\n\ts_nop 0\n\tglobal_load_lds_dwordx4 %0, %1 offset:%3 sc1" ::"v"(voff), "s"(sbase), "s"(lds), "n"(OFF) : "memory");
}
DEVI void dma1c(u32 voff, const char* sbase, u32 lds) {
  asm volatile("s_mov_b32 m0, %2\n\ts_nop 0\n\tglobal_load_lds_dword %0, %1 sc1" ::"v"(voff), "s"(sbase), "s"(lds) : "memory");
}
DEVI v2u ld8c_sync(u32 voff, const char* sb) { v2u d; asm volatile("global_load_dwordx2 %0, %1, %2 sc1\n\ts_waitcnt vmcnt(0)" : "=&v"(d) : "v"(voff), "s"(sb) : "memory"); return d; }
DEVI u32 ld4c_sync(u32 voff, const char* sb) { u32 d; asm volatile("global_load_dword %0, %1, %2 sc1\n\ts_waitcnt vmcnt(0)" : "=&v"(d) : "v"(voff), "s"(sb) : "memory"); return d; }
DEVI v4u ld16c_sync(u32 voff, const char* sb) { v4u d; asm volatile("global_load_dwordx4 %0, %1, %2 sc1\n\ts_waitcnt vmcnt(0)" : "=&v"(d) : "v"(voff), "s"(sb) : "memory"); return d; }
template <class T> DEVI void ld16c(T& d, u32 voff, const char* sb) { asm volatile("global_load_dwordx4 %0, %1, %2 sc1" : "=&v"(d) : "v"(voff), "s"(sb) : "memory"); }
template <class T> DEVI void ld8c(T& d, u32 voff, const char* sb) { asm volatile("global_load_dwordx2 %0, %1, %2 sc1" : "=&v"(d) : "v"(voff), "s"(sb) : "memory"); }
template <class T> DEVI void ld4c(T& d, u32 voff, const char* sb) { asm volatile("global_load_dword %0, %1, %2 sc1" : "=&v"(d) : "v"(voff), "s"(sb) : "memory"); }
template <class T> DEVI void ld16(T& d, u32 voff, const char* sb) { asm volatile("global_load_dwordx4 %0, %1, %2" : "=&v"(d) : "v"(voff), "s"(sb) : "memory"); }
DEVI void ld2(u32& d, u32 voff, const char* sb) { asm volatile("global_load_ushort %0, %1, %2" : "=&v"(d) : "v"(voff), "s"(sb) : "memory"); }
template <class T> DEVI void st16c(u32 voff, const char* sb, T v) { asm volatile("global_store_dwordx4 %0, %1, %2 sc1\n\ts_nop 1" ::"v"(voff), "v"(v), "s"(sb) : "memory"); }
template <class T> DEVI void st8c(u32 voff, const char* sb, T v) { asm volatile("global_store_dwordx2 %0, %1, %2 sc1" ::"v"(voff), "v"(v), "s"(sb) : "memory"); }
template <class T> DEVI void st4c(u32 voff, const char* sb, T v) { asm volatile("global_store_dword %0, %1, %2 sc1" ::"v"(voff), "v"(v), "s"(sb) : "memory"); }

DEVI v4s trread(LAS char* p) { return __builtin_amdgcn_ds_read_tr16_b64_v4i16((LAS v4s*)p); }
DEVI v8s cat8(v4s a, v4s b) { return __builtin_shufflevector(a, b, 0, 1, 2, 3, 4, 5, 6, 7); }
DEVI float bflo(u32 v) { return __builtin_bit_cast(float, v << 16); }
DEVI float bfhi(u32 v) { return __builtin_bit_cast(float, v & 0xffff0000u); }
DEVI float rbf(float v) { return (float)(__bf16)v; }
DEVI u32 pkbf(float a, float b) { v2b p = {(__bf16)a, (__bf16)b}; return __builtin_bit_cast(u32, p); }
DEVI u32 pkbf_o(float a, float b) { u32 r = pkbf(a, b); asm("" : "+v"(r)); return r; }
DEVI float cosr(float rev) { return __builtin_amdgcn_cosf(rev); }
DEVI float sinr(float rev) { return __builtin_amdgcn_sinf(rev); }
DEVI v4f mfma16h(v8h a, v8h b, v4f c) { return __builtin_amdgcn_mfma_f32_16x16x32_f16(a, b, c, 0, 0, 0); }
DEVI v4f mfma16b(v8s a, v4u b, v4f c) { return __builtin_amdgcn_mfma_f32_16x16x32_bf16(__builtin_bit_cast(v8b, a), __builtin_bit_cast(v8b, b), c, 0, 0, 0); }
DEVI v16f mfma32b(v8s a, v8s b, v16f c) { return __builtin_amdgcn_mfma_f32_32x32x16_bf16(__builtin_bit_cast(v8b, a), __builtin_bit_cast(v8b, b), c, 0, 0, 0); }
DEVI v16f mfma32h(v8h a, v8s b, v16f c) { return __builtin_amdgcn_mfma_f32_32x32x16_f16(a, __builtin_bit_cast(v8h, b), c, 0, 0, 0); }
DEVI v8h pack_ri(v4f re, v4f im) {
  v8h r;
  r[0] = (_Float16)re[0]; r[1] = (_Float16)re[1]; r[2] = (_Float16)re[2]; r[3] = (_Float16)re[3];
  r[4] = (_Float16)im[0]; r[5] = (_Float16)im[1]; r[6] = (_Float16)im[2]; r[7] = (_Float16)im[3];
  return r;
}
DEVI v4h pack4h(v4f a) { v4h r; r[0] = (_Float16)a[0]; r[1] = (_Float16)a[1]; r[2] = (_Float16)a[2]; r[3] = (_Float16)a[3]; return r; }
DEVI void twid(v4f& re, v4f& im, const v4f& c, const v4f& s) {
#pragma unroll
  for (int r = 0; r < 4; ++r) { float a = re[r], b = im[r]; re[r] = a * c[r] + b * s[r]; im[r] = b * c[r] - a * s[r]; }
}
template <int CTRL> DEVI float dppadd(float v) {
  return v + __builtin_bit_cast(float, __builtin_amdgcn_update_dpp(0, __builtin_bit_cast(int, v), CTRL, 0xf, 0xf, true));
}
DEVI float wave_sum(float v) {
  v = dppadd<0xB1>(v); v = dppadd<0x4E>(v); v = dppadd<0x141>(v); v = dppadd<0x140>(v);
  int bb = __builtin_bit_cast(int, v);
  float a0 = __builtin_bit_cast(float, __builtin_amdgcn_readlane(bb, 0)), a1 = __builtin_bit_cast(float, __builtin_amdgcn_readlane(bb, 16));
  float a2 = __builtin_bit_cast(float, __builtin_amdgcn_readlane(bb, 32)), a3 = __builtin_bit_cast(float, __builtin_amdgcn_readlane(bb, 48));
  return (a0 + a1) + (a2 + a3);
}
DEVI float dot2ones(u32 v, float acc) {
  const v2b ones = {(__bf16)1.0f, (__bf16)1.0f};
  return __builtin_amdgcn_fdot2_f32_bf16(__builtin_bit_cast(v2b, v), ones, acc, false);
}
template <int CTRL> DEVI u32 dppmax(u32 v) { return max(v, (u32)__builtin_amdgcn_update_dpp(0, (int)v, CTRL, 0xf, 0xf, true)); }
DEVI u32 wave_max(u32 v) {
  v = dppmax<0xB1>(v); v = dppmax<0x4E>(v); v = dppmax<0x141>(v); v = dppmax<0x140>(v);
  u32 a0 = __builtin_amdgcn_readlane(v, 0), a1 = __builtin_amdgcn_readlane(v, 16), a2 = __builtin_amdgcn_readlane(v, 32), a3 = __builtin_amdgcn_readlane(v, 48);
  return max(max(a0, a1), max(a2, a3));
}

struct Frag1 { v8s x0, x1, w1, w2; };

constexpr int L_SCR = 147456;
constexpr int L_CS = L_SCR, L_RED = L_SCR + 1024, L_MX = L_SCR + 1088, L_BC0 = L_SCR + 2048, L_BC1 = L_SCR + 2304;
constexpr int L_TAB = L_SCR + 3072;
constexpr int L_T = L_SCR + 7168, L_FW = L_T, L_FA = L_T + 2048, L_E1 = L_T, L_E2 = 32768, L_ZX = 32768;
constexpr int L_C1 = 98304, L_IMG = L_C1 + 9216;
constexpr int WXP = 544 + 32;
constexpr int E1P = 544, E2P = 1024 + 72;
DEVI constexpr int wslot(int n) { return n < 3 ? 98304 + n * 16384 : n < 6 ? (n - 3) * 16384 : n < 9 ? 49152 + (n - 6) * 16384 : n < 12 ? 98304 + (n - 9) * 16384 : (n - 12) * 16384; }
DEVI constexpr int aslot(int n) {
  constexpr int u[18] = {3, 4, 5, 9, 2, 6, 7, 8, 0, 1, 3, 4, 5, 9, 2, 6, 7, 8};
  return u[n] * 16384;
}

__global__ __launch_bounds__(512, 2) void kmain(const u16* __restrict__ x, const u16* __restrict__ wr, const u16* __restrict__ wi,
                                                const u16* __restrict__ Wg, const u16* __restrict__ Wu, const u16* __restrict__ Wd,
                                                char* __restrict__ ws, u16* __restrict__ out) {
  __shared__ __attribute__((aligned(16))) char smem_[163840];
  LAS char* smem = (LAS char*)smem_;
  const u32 lbase = (u32)(size_t)smem;
  const int tid = threadIdx.x, l = tid & 63;
  const int w = __builtin_amdgcn_readfirstlane(tid >> 6);
  const int b = blockIdx.x, xcd = b & 7, idx = b >> 3;
  const bool ffn = idx < 28;
  const int g = l >> 4, o = l & 15;
  const int h = l >> 5, blk = (l >> 4) & 1, q = (l & 15) >> 2, pp = l & 3;
  const char* FL = ws + OFF_FL;
  const char* BC = ws + OFF_BC;
  STAMP_DECL; STAMPT(0);

  const int RG = xcd * 4 + (idx >> 3), ct = idx & 7;
  const int pos = xcd * 32 + idx, kd = (pos >> 3) + 32 * (pos & 7);
  const u32 swz = ((l & 7) ^ (((l >> 4) & 1) << 2)) << 4;
  const u32 ro0 = (u32)((l >> 3) * 1024) + swz, ro1 = ro0 + 8192 - 1024;
  const char* bsrc = (const char*)Wd + (size_t)(w * 16) * 1024 + ct * 128;
  auto issueW = [&](int n, u32 slotoff) { dma2(ro0, ro1, bsrc + (size_t)n * 131072, lbase + slotoff + w * 2048); };

  u32 E = 0;
  if (w == 0) dma1c((u32)((l & 1) * 4), BC, lbase + L_BC0);

  auto get_epoch = [&]() {
    const v2u b0 = *(LAS v2u*)(smem + L_BC0);
    v2u b1 = *(LAS v2u*)(smem + L_BC1);
    int spins = 0;
    while (__builtin_amdgcn_readfirstlane((b0[0] ^ b1[0]) | (b0[1] ^ b1[1])) == 0 && ++spins < SPIN_CAP) {
      __builtin_amdgcn_s_sleep(4);
      b1 = ld8c_sync(0u, BC);
    }
    E = __builtin_amdgcn_readfirstlane(b1[0]);
  };
  auto signal_a = [&]() { if (l == 0) { st4c((u32)(FL_A + b * 4), FL, E); *(LAS u32*)(smem + L_MX + 4) = E; } };
  u32 tick = 0;
  const bool coord = (b == 224) && (w == 0);
  auto do_tick = [&]() { if (coord) { ++tick; if (l == 0) st8c(0u, BC, v2u{E, tick}); } };
  auto poll_w_issue = [&]() { dma1c((u32)(FL_W + l * 4), FL, lbase + L_FW); };
  auto poll_w_check = [&]() {
    u32 f = *(LAS u32*)(smem + L_FW + l * 4);
    int spins = 0;
    while (!__all((l & 1) || f == E) && ++spins < SPIN_CAP) {
      __builtin_amdgcn_s_sleep(12);
      f = ld4c_sync((u32)(FL_W + l * 4), FL);
    }
    u32 m = wave_max((l & 1) ? f : 0u);
    if (l == 0) *(LAS u32*)(smem + L_MX) = m;
  };
  auto poll_a_issue = [&]() { dma1s<0>((u32)(FL_A + l * 16), FL, lbase + L_FA); };
  auto poll4 = [&](v4u f, u32 voff) {
    int spins = 0;
    while (!__all(f[0] == E && f[1] == E && f[2] == E && f[3] == E) && ++spins < SPIN_CAP) {
      do_tick();
      __builtin_amdgcn_s_sleep(12);
      f = ld16c_sync(voff, FL);
    }
  };
  u32 fp;
  auto poll1 = [&](u32 voff) {
    PIN(fp);
    u32 f = fp;
    int spins = 0;
    while (!__all(f == E) && ++spins < SPIN_CAP) {
      __builtin_amdgcn_s_sleep(2);
      f = ld4c_sync(voff, FL);
    }
  };
  const int dh = w & 1, kq = w >> 1, ph = kq >> 1, kh = kq & 1;
  const int epi = RG * 64 + ph * 32 + (l & 31);
  const u32 eoff = (u32)(s_of_pi(epi) * D_ + ct * 64 + dh * 32 + kh * 16 + h * 8) * 2;
  v4u wt, s1v, xe; v4f pq[4]; float rs2;
  auto load_r1 = [&]() {
    ld16c(s1v, (u32)((((4 * w + g) * 8 + (o >> 1)) * 256 + pos) * 32 + (o & 1) * 16), ws + OFF_S1);
    ld16c(wt, (u32)((w * 64 + l) * 4608 + (kd + ((2 * w + (g >> 1)) & 7)) * 16), ws + OFF_WT);
    ld16(xe, eoff, (const char*)x);
    ld4c(rs2, (u32)(epi * 4), ws + OFF_RS2);
  };
  auto pin_r1 = [&]() { PIN(wt); PIN(s1v); PIN(xe); PIN(rs2); };

  const int p = (xcd >> 1) * 4 + idx / 7, j = (xcd & 1) * 7 + idx % 7;
  const int sh = w >> 2, fq = w & 3;
  v16f acc[2][2] = {};
  float mbr[2];

  u32 aoff[2];
#pragma unroll
  for (int i = 0; i < 2; ++i) {
    int row = (2 * w + i) * 8 + (l >> 3);
    aoff[i] = s_of_pi((p & 15) * 128 + row) * 1024 + (((l & 7) ^ ((row >> 1) & 7)) << 4) - i * 1024;
  }
  const u32 boff = (u32)(2 * w * 4 + (l >> 4)) * (F_ * 2) + j * 256 + (((l & 15) ^ ((l >> 4) << 2)) << 4);
  const u32 boff1 = boff + 4 * F_ * 2 - 1024;
  const int rot = p >> 1;
  auto issue = [&](int t, int slot) {
    const int kt = (t + rot) & 7;
    u32 dst = lbase + slot * 49152 + w * 2048;
    dma2(aoff[0], aoff[1], (const char*)x + kt * 128, dst);
    dma2(boff, boff1, (const char*)Wg + (size_t)kt * (64 * F_ * 2), dst + 16384);
    dma2(boff, boff1, (const char*)Wu + (size_t)kt * (64 * F_ * 2), dst + 32768);
  };
  const int sp = xcd * 4 + (idx - 28);
  v4u Fhr, Flr, Fhi, Fli; v8h M2; v4f twc[2], tws[2]; v8s xf;
  {
    const int pi = b * 8 + w;
    v4u xr;
    ld16(xr, (u32)(s_of_pi(pi) * 1024 + l * 16), (const char*)x);
    if (ffn) issue(0, 0);
    if (coord) {
      v4u f[11];
#pragma unroll
      for (int i = 0; i < 11; ++i) ld16c(f[i], (u32)(i * 1024 + l * 16), FL);
      WAIT_VM(0);
      u32 c = __builtin_amdgcn_readfirstlane(*(LAS u32*)(smem + L_BC0));
      bool hit;
      do {
        ++c; hit = false;
#pragma unroll
        for (int i = 0; i < 11; ++i) { PIN(f[i]); hit |= (f[i][0] == c) | (f[i][1] == c) | (f[i][2] == c) | (f[i][3] == c); }
      } while (__any(hit));
      E = c;
      do_tick();
    }
    {
      const float fo = (float)o, fg = (float)(4 * g);
      if (w < 2) {
        const float st = fo * (1.0f / 32.0f), a0 = fmaf(fg, st, w ? 0.25f : 0.f);
        v4u hi, lo;
#pragma unroll
        for (int e2 = 0; e2 < 4; ++e2) {
          float c0 = cosr(fmaf((float)(16 * (e2 >> 1) + 2 * (e2 & 1)), st, a0)), c1 = cosr(fmaf((float)(16 * (e2 >> 1) + 2 * (e2 & 1) + 1), st, a0));
          u32 ch = pkbf_o(c0, c1);
          hi[e2] = ch; lo[e2] = pkbf(c0 - bflo(ch), c1 - bfhi(ch));
        }
        *(LAS v4u*)(smem + L_C1 + w * 2048 + l * 16) = hi; *(LAS v4u*)(smem + L_C1 + w * 2048 + 1024 + l * 16) = lo;
      } else if (w < 5) {
        const float st = w == 2 ? (float)(o & 7) * (1.0f / 16.0f) : fo * (w == 3 ? 1.0f / 16.0f : 1.0f / 8.0f);
        const float a0 = (w == 2 && o >= 8) ? 0.25f : 0.f;
        const float sc = w == 2 ? 0.0625f : ((w == 4 && (g >> 1) != (o >> 3)) ? 0.f : 1.0f);
        v8h m;
#pragma unroll
        for (int e = 0; e < 4; ++e) { float a = fmaf(fg + (float)e, st, a0); m[e] = (_Float16)(sc * cosr(a)); m[4 + e] = (_Float16)(sc * sinr(a)); }
        *(LAS v8h*)(smem + (w == 2 ? L_C1 + 4096 : L_TAB + (w - 3) * 1024) + l * 16) = m;
      } else {
        const float st = w == 7 ? fo * (1.0f / 256.0f) : (fo + (w == 6 ? 16.f : 0.f)) * (1.0f / 512.0f);
        v4f c, sn;
#pragma unroll
        for (int e = 0; e < 4; ++e) { float a = (fg + (float)e) * st; c[e] = cosr(a); sn[e] = sinr(a); }
        LAS char* dst = smem + (w == 7 ? L_TAB + 2048 : L_C1 + 5120 + (w - 5) * 2048) + l * 16;
        *(LAS v4f*)dst = c; *(LAS v4f*)(dst + 1024) = sn;
      }
    }
    BARRIER();
    if (ffn) issue(1, 1);
    LAS char* C1 = smem + L_C1 + l * 16;
    Fhr = *(LAS v4u*)C1; Flr = *(LAS v4u*)(C1 + 1024); Fhi = *(LAS v4u*)(C1 + 2048); Fli = *(LAS v4u*)(C1 + 3072);
    M2 = *(LAS v8h*)(C1 + 4096);
#pragma unroll
    for (int u = 0; u < 2; ++u) { twc[u] = *(LAS v4f*)(C1 + 5120 + u * 2048); tws[u] = *(LAS v4f*)(C1 + 6144 + u * 2048); }
    if (ffn) WAIT_VM(12); else WAIT_VM(0);
    PIN(xr);
    STAMPT(3);
    const float c0 = bflo(__builtin_amdgcn_readfirstlane(xr[0]));
    v2f xv[4], s2 = {0.f, 0.f}, q2 = {0.f, 0.f};
#pragma unroll
    for (int e = 0; e < 4; ++e) {
      xv[e] = v2f{bflo(xr[e]), bfhi(xr[e])};
      v2f t = xv[e] - v2f{c0, c0};
      s2 += t; q2 = __builtin_elementwise_fma(t, t, q2);
    }
    const float dmean = wave_sum(s2[0] + s2[1]) * (1.0f / 512.0f), msq = wave_sum(q2[0] + q2[1]) * (1.0f / 512.0f);
    do_tick();
    const float mb = rbf(c0 + dmean);
    const float var = rbf(fmaxf(msq - dmean * dmean, 0.f));
    const float sd = rbf(__builtin_amdgcn_sqrtf(rbf(var + 1e-5f)));
    const float rs = __builtin_amdgcn_rcpf(sd);
#pragma unroll
    for (int e = 0; e < 4; ++e) {
      v2f t = xv[e] - v2f{mb, mb};
      u32 tb = pkbf_o(t[0], t[1]);
      t = v2f{bflo(tb), bfhi(tb)} * v2f{rs, rs};
      xr[e] = pkbf(t[0], t[1]);
    }
    *(LAS v4u*)(smem + L_IMG + w * 1024 + l * 16) = xr;
    if (l < 2) st4c((u32)(pi * 4 + l * 8192), ws + OFF_MB, l ? rs * rs : -mb);
    xf = cat8(trread(smem + L_IMG + w * 1024 + l * 8), trread(smem + L_IMG + w * 1024 + 512 + l * 8));
    STAMPT(4);
  }
  v4f dre[2], dim[2], d2[2];
  auto dft_a = [&]() {
    const v4u sg = {0x80000000u, 0x80000000u, 0x80000000u, 0x80000000u};
    const v4f z = {0.f, 0.f, 0.f, 0.f};
#pragma unroll
    for (int u = 0; u < 2; ++u) { dre[u] = mfma16b(xf, u ? Flr ^ sg : Flr, z); dim[u] = mfma16b(xf, u ? Fli ^ sg : Fli, z); }
#pragma unroll
    for (int u = 0; u < 2; ++u) { dre[u] = mfma16b(xf, u ? Fhr ^ sg : Fhr, dre[u]); dim[u] = mfma16b(xf, u ? Fhi ^ sg : Fhi, dim[u]); }
  };
  v4f dnq;
  auto dft_b = [&]() {
    const v4f z = {0.f, 0.f, 0.f, 0.f};
#pragma unroll
    for (int u = 0; u < 2; ++u) {
      twid(dre[u], dim[u], twc[u], tws[u]);
      const v8h bb = pack_ri(dre[u], dim[u]);
      d2[u] = mfma16h(M2, bb, z);
      if (u == 0) {
        const u32 nv = o == 8 ? 0xac002c00u : 0u;
        dnq = mfma16h(__builtin_bit_cast(v8h, v4u{nv, nv, 0u, 0u}), bb, z);
      }
    }
  };
  auto dft_c = [&]() {
    if (l == 32) d2[0][0] = dnq[0];
#pragma unroll
    for (int u = 0; u < 2; ++u) *(LAS v4h*)(smem + L_T + ((g >> 1) * 8 + w) * 544 + (16 * u + o) * 16 + (g & 1) * 8) = pack4h(d2[u]);
  };
  auto dft_d = [&]() {
    LAS char* rp = smem + L_T + (4 * (g & 1) + (o >> 2)) * 544 + (w * 32 + (g >> 1) * 16) * 2 + (o & 3) * 8;
    v8s un = cat8(trread(rp), trread(rp + 8 * 544));
    st16c((u32)(b * 8192 + (w * 32 + (g >> 1) * 16 + o) * 32 + (g & 1) * 16), ws + OFF_S1, un);
  };

  dft_a(); dft_b(); dft_c();
  STAMPT(29);

  if (ffn) {
    const u32 wb0 = (u32)((8 * h + q) * 256 + ((fq ^ q) << 6) + blk * 32 + pp * 8);
    const u32 r1 = 16384 + sh * 16384, r2 = 32768 - sh * 16384;
    const int xrow = sh * 64 + (l & 31);
    const u32 xb0 = (u32)(xrow * 128 + ((h ^ ((xrow >> 1) & 7)) << 4));
    auto rd = [&](Frag1& f, LAS char* sb, int i) {
      LAS char* xa = sb + (xb0 ^ (i << 5));
      LAS char* wa_ = sb + wb0 + (i << 12);
      f.w1 = cat8(trread(wa_ + r1), trread(wa_ + r1 + 1024));
      f.x0 = *(LAS v8s*)xa; f.x1 = *(LAS v8s*)(xa + 4096);
      f.w2 = cat8(trread(wa_ + r2), trread(wa_ + r2 + 1024));
    };
    auto mm = [&](const Frag1& f) {
      acc[0][0] = mfma32b(f.w1, f.x0, acc[0][0]);
      acc[0][1] = mfma32b(f.w1, f.x1, acc[0][1]);
      acc[1][0] = mfma32b(f.w2, f.x0, acc[1][0]);
      acc[1][1] = mfma32b(f.w2, f.x1, acc[1][1]);
    };
    Frag1 A, B;
    WAIT_VM(7);
    BARRIER();
    STAMPT(1); STAMPC(30);
    rd(A, smem, 0);
    dft_d();
    if (w == 0) dma1c((u32)((l & 1) * 4), BC, lbase + L_BC1);
    dma2(aoff[0], aoff[1], (const char*)x + ((2 + rot) & 7) * 128, lbase + 2 * 49152 + w * 2048);
    v4f csa = {0.f, 0.f, 0.f, 0.f};
    {
      const u32 one = ((l & 15) == ((l >> 4) & 1)) ? 0x3f803f80u : 0u;
      const v4u sel = {one, one, one, one};
      LAS char* wa_ = smem + wb0 + r1 + ((p & 1) << 13);
      csa = mfma16b(cat8(trread(wa_), trread(wa_ + 1024)), sel, csa);
      csa = mfma16b(cat8(trread(wa_ + 4096), trread(wa_ + 5120)), sel, csa);
    }
    float csp[16];
    v4f cg[4], cu[4];
    static_for<0, 8>([&](auto T) {
      constexpr int t = T, slot = t % 3, nslot = (t + 1) % 3, pslot = (t + 2) % 3;
      LAS char* sb = smem + slot * 49152;
      {
        const int kt = (t + 2 + rot) & 7;
        const char* s1 = t < 6 ? (const char*)Wg + (size_t)kt * (64 * F_ * 2) : bsrc + (size_t)((t - 6) * 3 + 1) * 131072;
        const char* s2 = t < 6 ? (const char*)Wu + (size_t)kt * (64 * F_ * 2) : bsrc + (size_t)((t - 6) * 3 + 2) * 131072;
        const u32 o2 = t < 6 ? boff : ro0, o3 = t < 6 ? boff1 : ro1;
        const u32 dst = lbase + pslot * 49152 + w * 2048;
        rd(B, sb, 1); SCHED();
        acc[0][0] = mfma32b(A.w1, A.x0, acc[0][0]); acc[0][1] = mfma32b(A.w1, A.x1, acc[0][1]); SCHED();
        dma1<0>(o2, s1, dst + 16384);
        if constexpr (t == 7) { load_r1(); }
        acc[1][0] = mfma32b(A.w2, A.x0, acc[1][0]); acc[1][1] = mfma32b(A.w2, A.x1, acc[1][1]); SCHED();
        if constexpr (t == 0) {
          if ((l & 15) < 2) st16c((u32)(((j * 16 + p) * 256 + sh * 128 + fq * 32 + (l & 15) * 16 + (l >> 4) * 4) * 4), ws + OFF_CSP, csa);
          SCHED();
        }
        rd(A, sb, 2); SCHED();
        acc[0][0] = mfma32b(B.w1, B.x0, acc[0][0]); SCHED();
        dma1<1024>(o3, s1, dst + 16384);
        if constexpr (t == 7) {
          if (kd == 0) {
#pragma unroll
            for (int r = 0; r < 4; ++r) ld16c(pq[r], (u32)(w * 4096 + r * 1024 + l * 16), ws + OFF_PQ);
          }
        }
        acc[0][1] = mfma32b(B.w1, B.x1, acc[0][1]); acc[1][0] = mfma32b(B.w2, B.x0, acc[1][0]); acc[1][1] = mfma32b(B.w2, B.x1, acc[1][1]); SCHED();
        dma1<0>(o2, s2, dst + 32768);
        rd(B, sb, 3); SCHED();
        if constexpr (t == 7) {
#pragma unroll
          for (int g4 = 0; g4 < 4; ++g4) {
            cg[g4] = *(LAS v4f*)(smem + L_CS + (fq * 32 + g4 * 8 + h * 4) * 4);
            cu[g4] = *(LAS v4f*)(smem + L_CS + (128 + fq * 32 + g4 * 8 + h * 4) * 4);
          }
          SCHED();
        }
        acc[0][0] = mfma32b(A.w1, A.x0, acc[0][0]); acc[0][1] = mfma32b(A.w1, A.x1, acc[0][1]); SCHED();
        dma1<1024>(o3, s2, dst + 32768);
        acc[1][0] = mfma32b(A.w2, A.x0, acc[1][0]); acc[1][1] = mfma32b(A.w2, A.x1, acc[1][1]); SCHED();
      }
      if constexpr (t < 7) {
        const char* s0 = t < 5 ? (const char*)x + ((t + 3 + rot) & 7) * 128 : bsrc + (size_t)((t - 5) * 3) * 131072;
        const u32 o0 = t < 5 ? aoff[0] : ro0, o1 = t < 5 ? aoff[1] : ro1;
        const u32 dst = lbase + slot * 49152 + w * 2048;
        WAIT_VM(6);
        if constexpr (t == 5) { if (w == 0) poll4(*(LAS v4u*)(smem + L_FA + l * 16), (u32)(FL_A + l * 16)); }
        if constexpr (t == 6) { if (w == 0) poll_w_check(); }
        if constexpr (t == 6) {
          PIN(mbr[0]); PIN(mbr[1]);
          if (w < 4) {
            float sm = 0.f;
#pragma unroll
            for (int i = 0; i < 16; ++i) { PIN(csp[i]); sm += csp[i]; }
            *(LAS float*)(smem + L_CS + tid * 4) = sm;
          }
        }
        BARRIER();
        STAMPT(22 + t);
        acc[0][0] = mfma32b(B.w1, B.x0, acc[0][0]); SCHED();
        rd(A, smem + nslot * 49152, 0); SCHED();
        if constexpr (t == 4) { if (w == 0) poll_a_issue(); }
        if constexpr (t == 5) { if (w == 0) poll_w_issue(); }
        if constexpr (t == 5) {
          if (w < 4) {
#pragma unroll
            for (int i = 0; i < 16; ++i) ld4c(csp[i], (u32)((j * 16 + i) * 1024 + tid * 4), ws + OFF_CSP);
          }
          ld4c(mbr[0], (u32)((p * 128 + xrow) * 4), ws + OFF_MB); ld4c(mbr[1], (u32)((p * 128 + xrow + 32) * 4), ws + OFF_MB);
        }
        dma1<0>(o0, s0, dst);
        acc[0][1] = mfma32b(B.w1, B.x1, acc[0][1]);
        acc[1][0] = mfma32b(B.w2, B.x0, acc[1][0]);
        acc[1][1] = mfma32b(B.w2, B.x1, acc[1][1]); SCHED();
        dma1<1024>(o1, s0, dst);
        if constexpr (t == 1) { if (w == 0) { get_epoch(); signal_a(); } }
        SCHED();
      } else {
        mm(B); SCHED();
      }
    });
    STAMPT(2); STAMPC(31);
    auto fin = [&](const v16f& ag, const v16f& au, int rt) {
      const v2f mb2 = {mbr[rt], mbr[rt]};
#pragma unroll
      for (int s2i = 0; s2i < 2; ++s2i) {
        v4u ov;
#pragma unroll
        for (int e = 0; e < 4; ++e) {
          const int r = s2i * 8 + 2 * e;
          v2f gg = __builtin_elementwise_fma(mb2, v2f{cg[r >> 2][r & 3], cg[r >> 2][(r & 3) + 1]}, v2f{ag[r], ag[r + 1]});
          v2f uu = __builtin_elementwise_fma(mb2, v2f{cu[r >> 2][r & 3], cu[r >> 2][(r & 3) + 1]}, v2f{au[r], au[r + 1]});
          gg[0] = fmaxf(gg[0], 0.f); gg[1] = fmaxf(gg[1], 0.f);
          gg *= uu;
          ov[e] = pkbf(gg[0], gg[1]);
        }
        st16c((u32)(((p * 4 + sh * 2 + rt) * 112 + (j * 8 + fq * 2 + s2i)) * 1024 + l * 16), ws + OFF_H, ov);
      }
    };
    if (sh == 0) { fin(acc[0][0], acc[1][0], 0); fin(acc[0][1], acc[1][1], 1); }
    else { fin(acc[1][0], acc[0][0], 0); fin(acc[1][1], acc[0][1], 1); }
    STAMPT(6);
    WAIT_VM(4);
    pin_r1();
  } else {
    BARRIER();
    dft_d();
    if (w == 0 && !coord) dma1c((u32)((l & 1) * 4), BC, lbase + L_BC1);
    do_tick();
    if (l < 34) {
#pragma unroll
      for (int i = 0; i < 16; ++i) {
        const int kb = 2 * w + (i >> 3), r = i & 3;
        const u32 ks = (u32)((sp >> 1) + 16 * kb + 256 * (4 * (sp & 1) + r));
        const u32 off = min(((ks * (DF_ * 2)) & ~15u) + l * 16, (u32)(2048 * DF_ * 2 - 16));
        dma1<0>(off, (const char*)((i & 4) ? wi : wr), lbase + ((((i >> 2) & 1) * 16 + kb) * 4 + r) * WXP);
      }
    }
    WAIT_VM(16);
    BARRIER();
    if (w == 0) { if (!coord) get_epoch(); signal_a(); }
    STAMPT(22);
    u32 sq[8];
    if (w == 7) {
      const int kss = (sp >> 1) + 16 * (l >> 2) + 256 * (4 * (sp & 1) + (l & 3)), km = (2048 - kss) & 2047;
      const u32 o1 = (u32)(kss * DF_ * 2), o2 = (u32)(km * DF_ * 2);
      ld2(sq[0], o1, (const char*)wr); ld2(sq[1], o1, (const char*)wi); ld2(sq[2], o2, (const char*)wr); ld2(sq[3], o2, (const char*)wi);
      ld2(sq[4], o1 + 512, (const char*)wr); ld2(sq[5], o1 + 512, (const char*)wi); ld2(sq[6], o2 + 512, (const char*)wr); ld2(sq[7], o2 + 512, (const char*)wi);
    }
    WAIT_VM(0);
    STAMPT(23);
    u32 mx;
    {
      typedef unsigned short v2us __attribute__((ext_vector_type(2)));
      const int dl = (sp >> 1) & 7;
      LAS char* rp = smem + ((2 * w + (g >> 1)) * 4 + (o >> 2)) * WXP + (g & 1) * 32 + (o & 3) * 8;
      v4u un[9];
#pragma unroll
      for (int i = 0; i < 9; ++i) un[i] = __builtin_bit_cast(v4u, cat8(trread(rp + i * 64), trread(rp + 64 * WXP + i * 64)));
      v2us m2 = {0, 0};
#pragma unroll
      for (int i = 0; i < 9; ++i) {
        st16c((u32)((sp * 16 + 2 * w + (g >> 1)) * 4608 + (i * 32 + (l & 31)) * 16), ws + OFF_WT, un[i]);
        const int kk = i * 32 + (l & 31) - dl;
        const u32 msk = ((i == 0 || i == 8) && (kk < 0 || kk > 256)) ? 0u : 0x7fff7fffu;
#pragma unroll
        for (int e = 0; e < 4; ++e) { u32 t = un[i][e] & msk; m2 = __builtin_elementwise_max(m2, __builtin_bit_cast(v2us, t)); }
      }
      mx = max((u32)m2[0], (u32)m2[1]);
    }
    if (w == 7) {
#pragma unroll
      for (int i = 0; i < 8; ++i) PIN(sq[i]);
      float war = bflo(sq[0]) + bflo(sq[2]), wai = bflo(sq[1]) - bflo(sq[3]);
      float wbr = bflo(sq[4]) + bflo(sq[6]), wbi = bflo(sq[5]) - bflo(sq[7]);
      v4f pv = {0.25f * (war + wbr), 0.25f * (wai + wbi), 0.25f * (war - wbr), 0.25f * (wai - wbi)};
      st16c((u32)((((sp >> 2) * 4 + (l & 3)) * 64 + (sp & 3) * 16 + (l >> 2)) * 16), ws + OFF_PQ, pv);
    }
    mx = wave_max(mx);
    if (l == 0) *(LAS u32*)(smem + L_RED + w * 4) = mx;
    do_tick();
    STAMPT(24);
    WAIT_VM(0);
    BARRIER();
    STAMPT(1); STAMPC(30);
    if (w == 0) {
      v4u r0 = *(LAS v4u*)(smem + L_RED), r1 = *(LAS v4u*)(smem + L_RED + 16);
      u32 m = max(max(max(r0[0], r0[1]), max(r0[2], r0[3])), max(max(r1[0], r1[1]), max(r1[2], r1[3])));
      if (l == 0) st8c((u32)(FL_W + sp * 8), FL, v2u{E, m});
    }
    static_for<0, 6>([&](auto N) { issueW(N, wslot(N)); });
    if (w == 0) { poll4(v4u{~E, 0u, 0u, 0u}, (u32)(FL_A + l * 16)); poll_w_issue(); WAIT_VM(0); poll_w_check(); }
    BARRIER();
    load_r1();
    WAIT_VM(0);
    pin_r1();
    STAMPT(2); STAMPC(31);
  }

  BARRIER();
  STAMPT(5);
  issueW(6, wslot(6));
  const u32 trG = (u32)(kq * 4096 + (4 * h + q) * 128 + ((dh ^ ((q >> 1) & 1)) << 6) + (blk ^ kh) * 32 + pp * 8);
  v8s Wf[14][2];
  auto readW = [&](auto N) {
    constexpr int n = N;
    LAS char* sb = smem + wslot(n) + trG;
    Wf[n][0] = cat8(trread(sb), trread(sb + 1024)); Wf[n][1] = cat8(trread(sb + 2048), trread(sb + 3072));
  };
  static_for<0, 6>(readW);

  issueW(7, wslot(7));

  const char* hsrc = ws + OFF_H + ((size_t)(RG * 2 + (w >> 2)) * 112 + (w & 3) * 2) * 1024;
  const u32 so0 = (u32)((l >> 3) * 4096) + swz, so1 = so0 + 32768 - 1024;
  const char* ssrc = ws + OFF_S2 + (size_t)(w * 16) * 4096 + RG * 128;
  auto issueP = [&](int n, auto HALF) {
    constexpr int hf = HALF;
    if (n < 14) dma1s<hf * 1024>((u32)(l * 16), hsrc + n * 8192, lbase + aslot(n) + w * 2048);
    else dma1s<hf * 1024>(hf ? so1 : so0, ssrc + (size_t)(n - 14) * (128 * 4096), lbase + aslot(n) + w * 2048);
  };
  float oscale;
  const u32 fhoff = (u32)(FL_H + (RG * 64 + min(l, 55)) * 4);
  {
    v4f t2c, t2s, t3c, t3s;
    {
      const float fo = (float)o, fg = (float)(4 * g), fw = (float)w;
      const float st2 = fw * (1.0f / 128.0f), a2 = fw * fo * (1.0f / 2048.0f);
      const float f7 = (float)(o & 7), st3 = f7 * (1.0f / 128.0f), a3 = f7 * (float)(2 * w + (o >> 3)) * (1.0f / 2048.0f);
#pragma unroll
      for (int e = 0; e < 4; ++e) {
        const float id = fg + (float)e;
        float b2 = fmaf(id, st2, a2);
        t2c[e] = cosr(b2); t2s[e] = sinr(b2);
        float b3 = fmaf(id, st3, a3);
        t3c[e] = cosr(b3); t3s[e] = sinr(b3);
      }
    }
    const u32 Ev = *(LAS u32*)(smem + L_MX + 4);
    int Ex = (int)(*(LAS u32*)(smem + L_MX) >> 7);
    Ex = Ex < 16 ? 16 : (Ex > 240 ? 240 : Ex);
    const float wscale = __builtin_bit_cast(float, (u32)(246 - Ex) << 23);
    oscale = __builtin_bit_cast(float, (u32)(Ex - 7) << 23);
    const v4u f16 = *(LAS v4u*)(smem + L_TAB + l * 16), f8 = *(LAS v4u*)(smem + L_TAB + 1024 + l * 16);
    const v4f t1c = *(LAS v4f*)(smem + L_TAB + 2048 + l * 16), t1s = *(LAS v4f*)(smem + L_TAB + 3072 + l * 16);
    const v8h F16re = __builtin_bit_cast(v8h, f16), F8re = __builtin_bit_cast(v8h, f8);
    const v8h F16im = __builtin_bit_cast(v8h, v4u{f16[2] ^ 0x80008000u, f16[3] ^ 0x80008000u, f16[0], f16[1]});
    const v8h F8im = __builtin_bit_cast(v8h, v4u{f8[2] ^ 0x80008000u, f8[3] ^ 0x80008000u, f8[0], f8[1]});
    STAMPT(7);
    const v4f z = {0.f, 0.f, 0.f, 0.f};
    v8h a = __builtin_bit_cast(v8h, s1v);
    v4f re = mfma16h(a, F16re, z), im = mfma16h(a, F16im, z);
    issueW(8, wslot(8));
    twid(re, im, t1c, t1s);
    a = pack_ri(re, im);
    re = mfma16h(F16re, a, z); im = mfma16h(F16im, a, z);
    WAIT_VM(6);
    if (ffn && l == 0) st4c((u32)(FL_H + ((p * 2 + sh) * 64 + j * 4 + fq) * 4), FL, Ev);
    twid(re, im, t2c, t2s);
    *(LAS v4h*)(smem + L_E1 + w * E1P + o * 32 + g * 8) = pack4h(re);
    *(LAS v4h*)(smem + L_E1 + (8 + w) * E1P + o * 32 + g * 8) = pack4h(im);
    if (ffn) WAIT_VM(1); else WAIT_VM(0);
    BARRIER();
    STAMPT(8);
    {
      LAS char* rp = smem + L_E1 + (4 * (g & 1) + (o >> 2)) * E1P + (2 * w + (g >> 1)) * 32 + (o & 3) * 8;
      a = __builtin_bit_cast(v8h, cat8(trread(rp), trread(rp + 8 * E1P)));
    }
    issueW(9, wslot(9));
    static_for<6, 9>(readW);
    re = mfma16h(F8re, a, z); im = mfma16h(F8im, a, z);
    issueW(10, wslot(10));
    if (kd != 0) {
#pragma unroll
      for (int r = 0; r < 4; ++r) {
        float fr = ((r & 1) ? bfhi(wt[r >> 1]) : bflo(wt[r >> 1])) * wscale, fi = ((r & 1) ? bfhi(wt[2 + (r >> 1)]) : bflo(wt[2 + (r >> 1)])) * wscale;
        float zr = re[r], zi = im[r];
        re[r] = zr * fr - zi * fi;
        im[r] = -(zr * fi + zi * fr);
      }
    } else {
      const int ka = 2 * w + (g >> 1), kr = o + 64 * (g & 1);
#pragma unroll
      for (int r = 0; r < 4; ++r) { v2f t = {re[r], im[r]}; *(LAS v2f*)(smem + L_ZX + ka * 1024 + (kr + 16 * r) * 8) = t; }
      BARRIER();
#pragma unroll
      for (int r = 0; r < 4; ++r) {
        PIN(pq[r]);
        const int km = (2048 - (ka + 16 * (kr + 16 * r))) & 2047;
        v2f zm = *(LAS v2f*)(smem + L_ZX + (km & 15) * 1024 + (km >> 4) * 8);
        float zr = re[r], zi = im[r];
        re[r] = (zr * pq[r][0] - zi * pq[r][1] + zm[0] * pq[r][2] + zm[1] * pq[r][3]) * wscale;
        im[r] = -(zr * pq[r][1] + zi * pq[r][0] + zm[0] * pq[r][3] - zm[1] * pq[r][2]) * wscale;
      }
    }
    LAS char* E2 = smem + (kd == 0 ? L_E1 : L_E2);
    a = pack_ri(re, im);
    issueW(11, wslot(11));
    re = mfma16h(a, F8re, z); im = mfma16h(a, F8im, z);
    if (w == 0) ld4c(fp, fhoff, FL);
    issueW(12, wslot(12));
    twid(re, im, t3c, t3s);
    *(LAS v4h*)(E2 + (o & 7) * E2P + (2 * w + (o >> 3)) * 32 + g * 8) = pack4h(re);
    *(LAS v4h*)(E2 + (o & 7) * E2P + 512 + (2 * w + (o >> 3)) * 32 + g * 8) = pack4h(im);
    issueW(13, wslot(13));
    if (w == 0) { WAIT_VM(4); poll1(fhoff); }
    BARRIER();
    STAMPT(10);
    {
      LAS char* rp = E2 + w * E2P + o * 32 + g * 8;
      a = __builtin_bit_cast(v8h, cat8(*(LAS v4s*)rp, *(LAS v4s*)(rp + 512)));
    }
    issueP(0, IC<0>{}); issueP(0, IC<1>{});
    re = mfma16h(a, F16re, z); im = mfma16h(a, F16im, z);
    issueP(1, IC<0>{}); issueP(1, IC<1>{});
    twid(re, im, t1c, t1s);
    a = pack_ri(re, im);
    re = mfma16h(F16re, a, z); im = mfma16h(F16im, a, z);
    st8c((u32)(2 * kd * 4096 + w * 512 + l * 8), ws + OFF_S2, pack4h(re));
    st8c((u32)((2 * kd + 1) * 4096 + w * 512 + l * 8), ws + OFF_S2, pack4h(im));
  }
  STAMPT(9);

  const int d = ct * 64 + dh * 32 + ((l & 31) ^ (kh << 4));
  const float gst = (float)d * (1.0f / 512.0f), ga0 = (float)(16 * kq + 2 * h) * gst;
  v8h G[8];
  auto gen = [&](auto FI) {
    constexpr int f = FI;
#pragma unroll
    for (int e2 = 0; e2 < 4; ++e2) {
      float a = fmaf((float)(64 * (f >> 1) + 8 * (f & 1) + 4 * (e2 >> 1) + (e2 & 1)), gst, ga0);
      float c = cosr(a), sn = sinr(a);
      if (f == 0 && e2 == 0) { if (kq == 0 && h == 0) { c = 0.5f; sn = (d & 1) ? 0.5f : -0.5f; } }
      G[f][2 * e2] = (_Float16)c; G[f][2 * e2 + 1] = (_Float16)sn;
    }
    PIN(G[f]);
  };
  issueP(2, IC<0>{}); issueP(2, IC<1>{});
  static_for<0, 3>(gen);
  const u32 trS = (u32)(kq * 4096 + (4 * h + q) * 128 + ((ph ^ ((q >> 1) & 1)) << 6) + blk * 32 + pp * 8);
  const u32 hb = (u32)(ph * 8192 + kq * 2048 + l * 16);
  v16f accG[2] = {}, accS[2] = {};
  v8s fa[8], fb[8];
  v4u fs;
  float gv[8];
  const u32 xw = (u32)(w * 6144 + l * 16);
  auto xchg_out = [&](const v16f (&ac)[2], int base) {
    LAS char* X = smem + base + xw;
#pragma unroll
    for (int i = 0; i < 6; ++i) {
      const v16f& a = ac[(i + 2) >> 2]; const int r = ((i + 2) & 3) * 4;
      *(LAS v4f*)(X + i * 1024) = v4f{a[r], a[r + 1], a[r + 2], a[r + 3]};
    }
  };
  auto xchg_in = [&](const v16f (&ac)[2], int base, float (&o8)[8]) {
    v4f s0 = {ac[0][0], ac[0][1], ac[0][2], ac[0][3]}, s1 = {ac[0][4], ac[0][5], ac[0][6], ac[0][7]};
#pragma unroll
    for (int x_ = 1; x_ < 4; ++x_) {
      LAS char* X = smem + base + (w ^ (2 * x_)) * 6144 + (x_ - 1) * 2048 + l * 16;
      s0 += *(LAS v4f*)X; s1 += *(LAS v4f*)(X + 1024);
    }
#pragma unroll
    for (int e = 0; e < 4; ++e) { o8[e] = s0[e]; o8[4 + e] = s1[e]; }
  };
  static_for<0, 10>([&](auto V) {
    constexpr int v = V;
    v8s(&cur)[8] = (v & 1) ? fb : fa;
    v8s(&prv)[8] = (v & 1) ? fa : fb;
    auto mmp = [&](int s0, int s1) {
#pragma unroll
      for (int s = s0; s < s1; ++s) {
        if constexpr (v >= 8) {
          accS[0] = mfma32h(G[(v - 8) * 4 + s], prv[2 * s], accS[0]); accS[1] = mfma32h(G[(v - 8) * 4 + s], prv[2 * s + 1], accS[1]);
        } else if constexpr (v >= 1) {
          accG[0] = mfma32b(Wf[2 * (v - 1) + (s >> 1)][s & 1], prv[2 * s], accG[0]);
          accG[1] = mfma32b(Wf[2 * (v - 1) + (s >> 1)][s & 1], prv[2 * s + 1], accG[1]);
        }
      }
    };
    if constexpr (v < 9) {
      WAIT_VM(v == 0 ? 4 : v == 1 ? 2 : v <= 7 ? 4 : 0);
      if constexpr (v == 5) { if (w == 0) { PIN(fs); poll4(fs, (u32)(FL_S + l * 16)); } }
      BARRIER();
      STAMPT(11 + v);
      if constexpr (v == 1) { if (tid == 0) st4c((u32)(FL_S + b * 4), FL, E); }
      if constexpr (v == 4) { if (w == 0) ld16c(fs, (u32)(FL_S + l * 16), FL); }
      mmp(0, 1); SCHED();
#pragma unroll
      for (int s = 0; s < 4; ++s) {
        if constexpr (v >= 7) {
          LAS char* sb = smem + aslot(2 * v + (s >> 1)) + (s & 1) * 2048;
          cur[2 * s] = cat8(trread(sb + trS), trread(sb + trS + 1024));
          cur[2 * s + 1] = cat8(trread(sb + (trS ^ 64)), trread(sb + (trS ^ 64) + 1024));
        } else {
          LAS char* sb = smem + aslot(2 * v + (s >> 1)) + (s & 1) * 1024;
          cur[2 * s] = *(LAS v8s*)(sb + hb);
          cur[2 * s + 1] = *(LAS v8s*)(sb + (hb ^ 8192));
        }
      }
      if constexpr (v == 0) {
        issueP(3, IC<0>{}); issueP(3, IC<1>{});
        static_for<9, 14>(readW);
        issueP(4, IC<0>{}); issueP(4, IC<1>{});
        gen(IC<3>{}); gen(IC<4>{});
      }
      SCHED();
      if constexpr (v == 1) { issueP(5, IC<0>{}); issueP(5, IC<1>{}); }
      if constexpr (v >= 1 && v <= 6) issueP(2 * v + 4, IC<0>{});
      mmp(1, 2); SCHED();
      if constexpr (v >= 1 && v <= 6) issueP(2 * v + 4, IC<1>{});
      mmp(2, 3); SCHED();
      if constexpr (v >= 1 && v <= 6) issueP(2 * v + 5, IC<0>{});
      mmp(3, 4); SCHED();
      if constexpr (v >= 1 && v <= 6) issueP(2 * v + 5, IC<1>{});
    } else {
      mmp(0, 4);
    }
    if constexpr (v >= 1 && v <= 3) gen(IC<4 + v>{});
    if constexpr (v == 7) xchg_out(accG, 49152);
    if constexpr (v == 8) {
      float o8[8];
      xchg_in(accG, 49152, o8);
      PIN(rs2);
#pragma unroll
      for (int e = 0; e < 8; ++e) {
        float ov = rbf(o8[e] * rs2);
        gv[e] = rbf(ov * __builtin_amdgcn_rcpf(1.0f + __expf(-ov)));
        PIN(gv[e]);
      }
    }
    SCHED();
  });
  xchg_out(accS, 0);
  BARRIER();
  {
    float o8[8], pv[8];
    xchg_in(accS, 0, o8);
#pragma unroll
    for (int e = 0; e < 8; ++e) pv[e] = rbf(o8[e] * oscale) * gv[e];
    u32 a0 = pkbf(pv[0], pv[1]), a1 = pkbf(pv[2], pv[3]), b0 = pkbf(pv[4], pv[5]), b1 = pkbf(pv[6], pv[7]);
    auto r0 = __builtin_amdgcn_permlane32_swap(a0, b0, false, false);
    auto r1 = __builtin_amdgcn_permlane32_swap(a1, b1, false, false);
    v4u pk = {r0[0], r1[0], r0[1], r1[1]};
    PIN(xe);
    v4u ov4;
#pragma unroll
    for (int e = 0; e < 4; ++e) ov4[e] = pkbf(bflo(xe[e]) + bflo(pk[e]), bfhi(xe[e]) + bfhi(pk[e]));
    *(v4u*)((char*)out + eoff) = ov4;
  }
  STAMPT(20);
  STAMPT(21);
  STAMP_OUT(ws);
}

extern "C" void kernel_launch(void* const* d_in, const int* in_sizes, int n_in, void* d_out, int out_size, void* d_ws,
                              size_t ws_size, hipStream_t stream) {
  kmain<<<256, 512, 0, stream>>>((const u16*)d_in[0], (const u16*)d_in[1], (const u16*)d_in[2], (const u16*)d_in[4],
                                 (const u16*)d_in[5], (const u16*)d_in[6], (char*)d_ws, (u16*)d_out);
}
```

```cpp
#include <hip/hip_runtime.h>
#include <stdint.h>

typedef unsigned short u16;
typedef unsigned int u32;
typedef unsigned long long u64;
typedef short v4s __attribute__((ext_vector_type(4)));
typedef short v8s __attribute__((ext_vector_type(8)));
typedef _Float16 v4h __attribute__((ext_vector_type(4)));
typedef _Float16 v8h __attribute__((ext_vector_type(8)));
typedef __bf16 v2b __attribute__((ext_vector_type(2)));
typedef __bf16 v8b __attribute__((ext_vector_type(8)));
typedef float v2f __attribute__((ext_vector_type(2)));
typedef float v4f __attribute__((ext_vector_type(4)));
typedef float v16f __attribute__((ext_vector_type(16)));
typedef u32 v2u __attribute__((ext_vector_type(2)));
typedef u32 v4u __attribute__((ext_vector_type(4)));

#define LAS __attribute__((address_space(3)))
#define GAS __attribute__((address_space(1)))
#define DEVI __device__ __forceinline__

constexpr int D_ = 512, F_ = 1792, DF_ = 257;
constexpr size_t OFF_H = 0;
constexpr size_t OFF_S1 = (size_t)8 << 20;
constexpr size_t OFF_WT = (size_t)10 << 20;
constexpr size_t OFF_S2 = (size_t)13 << 20;
constexpr size_t OFF_M = (size_t)15 << 20;
constexpr size_t OFF_FL = OFF_M;
constexpr u32 FL_W = 0, FL_S = 1024, FL_A = 2048, FL_H = 3072;
constexpr size_t OFF_BC = OFF_M + 12288;
constexpr size_t OFF_MB = OFF_M + 16384;
constexpr size_t OFF_RS2 = OFF_M + 24576;
constexpr size_t OFF_PQ = OFF_M + 65536;
constexpr size_t OFF_CSP = OFF_M + 131072;
constexpr int SPIN_CAP = 1 << 16;

#define STAMP 1
#define PROBE 0
#define PROBE_B 5
constexpr size_t OFF_STAMP = (size_t)32 << 20;
#define STAMP_DECL u32 stamps_[32] = {}
#define STAMPT(id) do { u64 t_; asm volatile("s_memrealtime %0\n\ts_waitcnt lgkmcnt(0)" : "=s"(t_) :: "memory"); stamps_[id] = (u32)t_; } while (0)
#define STAMPC(id) do { u64 t_; asm volatile("s_memtime %0\n\ts_waitcnt lgkmcnt(0)" : "=s"(t_) :: "memory"); stamps_[id] = (u32)t_; } while (0)
#define STAMP_OUT(wsp) do { if (blockIdx.x == PROBE_B && threadIdx.x < 64) { u32 dl_ = 500u + (PROBE == 99 ? (stamps_[31] - stamps_[30]) / 20u : PROBE == 98 ? (stamps_[2] - stamps_[1]) : (stamps_[PROBE % 32] - stamps_[0])); u32 t1_; do { u64 t_; asm volatile("s_memrealtime %0\n\ts_waitcnt lgkmcnt(0)" : "=s"(t_) :: "memory"); t1_ = (u32)t_; __builtin_amdgcn_s_sleep(1); } while (t1_ - stamps_[21] < dl_); } } while (0)

template <int I> struct IC { static constexpr int v = I; constexpr operator int() const { return I; } };
template <int B, int E, class Fn> DEVI void static_for(Fn&& f) {
  if constexpr (B < E) { f(IC<B>{}); static_for<B + 1, E>(f); }
}
#define WAIT_VM(n) asm volatile("s_waitcnt vmcnt(%0)" ::"n"(n) : "memory")
#define BARRIER() asm volatile("s_waitcnt lgkmcnt(0)\n\ts_barrier" ::: "memory")
#define SCHED() __builtin_amdgcn_sched_barrier(0)
#define PIN(x) asm volatile("" : "+v"(x))

DEVI int s_of_pi(int pi) {
  int RG = pi >> 6, i = pi & 63;
  return 128 * (4 * (RG & 3) + (i & 3)) + 8 * (i >> 2) + (RG >> 2);
}
DEVI void dma2(u32 voff0, u32 voff1, const char* sbase, u32 lds) {
  asm volatile("s_mov_b32 m0, %3\n\ts_nop 0\n\tglobal_load_lds_dwordx4 %0, %2\n\tglobal_load_lds_dwordx4 %1, %2 offset:1024" ::"v"(voff0), "v"(voff1), "s"(sbase), "s"(lds) : "memory");
}
DEVI void dma2c(u32 voff0, u32 voff1, const char* sbase, u32 lds) {
  asm volatile("s_mov_b32 m0, %3\n\ts_nop 0\n\tglobal_load_lds_dwordx4 %0, %2 sc1\n\tglobal_load_lds_dwordx4 %1, %2 offset:1024 sc1" ::"v"(voff0), "v"(voff1), "s"(sbase), "s"(lds) : "memory");
}
template <int OFF> DEVI void dma1(u32 voff, const char* sbase, u32 lds) {
  asm volatile("s_mov_b32 m0, %2\n\ts_nop 0\n\tglobal_load_lds_dwordx4 %0, %1 offset:%3" ::"v"(voff), "s"(sbase), "s"(lds), "n"(OFF) : "memory");
}
template <int OFF> DEVI void dma1s(u32 voff, const char* sbase, u32 lds) {
  asm volatile("s_mov_b32 m0, %2\n\ts_nop 0\n\tglobal_load_lds_dwordx4 %0, %1 offset:%3 sc1" ::"v"(voff), "s"(sbase), "s"(lds), "n"(OFF) : "memory");
}
DEVI void dma1c(u32 voff, const char* sbase, u32 lds) {
  asm volatile("s_mov_b32 m0, %2\n\ts_nop 0\n\tglobal_load_lds_dword %0, %1 sc1" ::"v"(voff), "s"(sbase), "s"(lds) : "memory");
}
DEVI v2u ld8c_sync(u32 voff, const char* sb) { v2u d; asm volatile("global_load_dwordx2 %0, %1, %2 sc1\n\ts_waitcnt vmcnt(0)" : "=&v"(d) : "v"(voff), "s"(sb) : "memory"); return d; }
DEVI u32 ld4c_sync(u32 voff, const char* sb) { u32 d; asm volatile("global_load_dword %0, %1, %2 sc1\n\ts_waitcnt vmcnt(0)" : "=&v"(d) : "v"(voff), "s"(sb) : "memory"); return d; }
DEVI v4u ld16c_sync(u32 voff, const char* sb) { v4u d; asm volatile("global_load_dwordx4 %0, %1, %2 sc1\n\ts_waitcnt vmcnt(0)" : "=&v"(d) : "v"(voff), "s"(sb) : "memory"); return d; }
template <class T> DEVI void ld16c(T& d, u32 voff, const char* sb) { asm volatile("global_load_dwordx4 %0, %1, %2 sc1" : "=&v"(d) : "v"(voff), "s"(sb) : "memory"); }
template <class T> DEVI void ld8c(T& d, u32 voff, const char* sb) { asm volatile("global_load_dwordx2 %0, %1, %2 sc1" : "=&v"(d) : "v"(voff), "s"(sb) : "memory"); }
template <class T> DEVI void ld4c(T& d, u32 voff, const char* sb) { asm volatile("global_load_dword %0, %1, %2 sc1" : "=&v"(d) : "v"(voff), "s"(sb) : "memory"); }
template <class T> DEVI void ld16(T& d, u32 voff, const char* sb) { asm volatile("global_load_dwordx4 %0, %1, %2" : "=&v"(d) : "v"(voff), "s"(sb) : "memory"); }
DEVI void ld2(u32& d, u32 voff, const char* sb) { asm volatile("global_load_ushort %0, %1, %2" : "=&v"(d) : "v"(voff), "s"(sb) : "memory"); }
template <class T> DEVI void st16c(u32 voff, const char* sb, T v) { asm volatile("global_store_dwordx4 %0, %1, %2 sc1\n\ts_nop 1" ::"v"(voff), "v"(v), "s"(sb) : "memory"); }
template <class T> DEVI void st8c(u32 voff, const char* sb, T v) { asm volatile("global_store_dwordx2 %0, %1, %2 sc1" ::"v"(voff), "v"(v), "s"(sb) : "memory"); }
template <class T> DEVI void st4c(u32 voff, const char* sb, T v) { asm volatile("global_store_dword %0, %1, %2 sc1" ::"v"(voff), "v"(v), "s"(sb) : "memory"); }

DEVI v4s trread(LAS char* p) { return __builtin_amdgcn_ds_read_tr16_b64_v4i16((LAS v4s*)p); }
DEVI v8s cat8(v4s a, v4s b) { return __builtin_shufflevector(a, b, 0, 1, 2, 3, 4, 5, 6, 7); }
DEVI float bflo(u32 v) { return __builtin_bit_cast(float, v << 16); }
DEVI float bfhi(u32 v) { return __builtin_bit_cast(float, v & 0xffff0000u); }
DEVI float rbf(float v) { return (float)(__bf16)v; }
DEVI u32 pkbf(float a, float b) { v2b p = {(__bf16)a, (__bf16)b}; return __builtin_bit_cast(u32, p); }
DEVI u32 pkbf_o(float a, float b) { u32 r = pkbf(a, b); asm("" : "+v"(r)); return r; }
DEVI float cosr(float rev) { return __builtin_amdgcn_cosf(rev); }
DEVI float sinr(float rev) { return __builtin_amdgcn_sinf(rev); }
DEVI v4f mfma16h(v8h a, v8h b, v4f c) { return __builtin_amdgcn_mfma_f32_16x16x32_f16(a, b, c, 0, 0, 0); }
DEVI v4f mfma16b(v8s a, v4u b, v4f c) { return __builtin_amdgcn_mfma_f32_16x16x32_bf16(__builtin_bit_cast(v8b, a), __builtin_bit_cast(v8b, b), c, 0, 0, 0); }
DEVI v16f mfma32b(v8s a, v8s b, v16f c) { return __builtin_amdgcn_mfma_f32_32x32x16_bf16(__builtin_bit_cast(v8b, a), __builtin_bit_cast(v8b, b), c, 0, 0, 0); }
DEVI v16f mfma32h(v8h a, v8s b, v16f c) { return __builtin_amdgcn_mfma_f32_32x32x16_f16(a, __builtin_bit_cast(v8h, b), c, 0, 0, 0); }
DEVI v8h pack_ri(v4f re, v4f im) {
  v8h r;
  r[0] = (_Float16)re[0]; r[1] = (_Float16)re[1]; r[2] = (_Float16)re[2]; r[3] = (_Float16)re[3];
  r[4] = (_Float16)im[0]; r[5] = (_Float16)im[1]; r[6] = (_Float16)im[2]; r[7] = (_Float16)im[3];
  return r;
}
DEVI v4h pack4h(v4f a) { v4h r; r[0] = (_Float16)a[0]; r[1] = (_Float16)a[1]; r[2] = (_Float16)a[2]; r[3] = (_Float16)a[3]; return r; }
DEVI void twid(v4f& re, v4f& im, const v4f& c, const v4f& s) {
#pragma unroll
  for (int r = 0; r < 4; ++r) { float a = re[r], b = im[r]; re[r] = a * c[r] + b * s[r]; im[r] = b * c[r] - a * s[r]; }
}
template <int CTRL> DEVI float dppadd(float v) {
  return v + __builtin_bit_cast(float, __builtin_amdgcn_update_dpp(0, __builtin_bit_cast(int, v), CTRL, 0xf, 0xf, true));
}
DEVI float wave_sum(float v) {
  v = dppadd<0xB1>(v); v = dppadd<0x4E>(v); v = dppadd<0x141>(v); v = dppadd<0x140>(v);
  int bb = __builtin_bit_cast(int, v);
  float a0 = __builtin_bit_cast(float, __builtin_amdgcn_readlane(bb, 0)), a1 = __builtin_bit_cast(float, __builtin_amdgcn_readlane(bb, 16));
  float a2 = __builtin_bit_cast(float, __builtin_amdgcn_readlane(bb, 32)), a3 = __builtin_bit_cast(float, __builtin_amdgcn_readlane(bb, 48));
  return (a0 + a1) + (a2 + a3);
}
DEVI float dot2ones(u32 v, float acc) {
  const v2b ones = {(__bf16)1.0f, (__bf16)1.0f};
  return __builtin_amdgcn_fdot2_f32_bf16(__builtin_bit_cast(v2b, v), ones, acc, false);
}
template <int CTRL> DEVI u32 dppmax(u32 v) { return max(v, (u32)__builtin_amdgcn_update_dpp(0, (int)v, CTRL, 0xf, 0xf, true)); }
DEVI u32 wave_max(u32 v) {
  v = dppmax<0xB1>(v); v = dppmax<0x4E>(v); v = dppmax<0x141>(v); v = dppmax<0x140>(v);
  u32 a0 = __builtin_amdgcn_readlane(v, 0), a1 = __builtin_amdgcn_readlane(v, 16), a2 = __builtin_amdgcn_readlane(v, 32), a3 = __builtin_amdgcn_readlane(v, 48);
  return max(max(a0, a1), max(a2, a3));
}

struct Frag1 { v8s x0, x1, w1, w2; };

constexpr int L_SCR = 147456;
constexpr int L_CS = L_SCR, L_RED = L_SCR + 1024, L_MX = L_SCR + 1088, L_BC0 = L_SCR + 2048, L_BC1 = L_SCR + 2304;
constexpr int L_TAB = L_SCR + 3072;
constexpr int L_T = L_SCR + 7168, L_FW = L_T, L_FA = L_T + 2048, L_E1 = L_T, L_E2 = 32768, L_ZX = 32768;
constexpr int L_C1 = 98304, L_IMG = L_C1 + 9216;
constexpr int WXP = 544 + 32;
constexpr int E1P = 544, E2P = 1024 + 72;
DEVI constexpr int wslot(int n) { return n < 3 ? 98304 + n * 16384 : n < 6 ? (n - 3) * 16384 : n < 9 ? 49152 + (n - 6) * 16384 : n < 12 ? 98304 + (n - 9) * 16384 : (n - 12) * 16384; }
DEVI constexpr int aslot(int n) {
  constexpr int u[18] = {3, 4, 5, 9, 2, 6, 7, 8, 0, 1, 3, 4, 5, 9, 2, 6, 7, 8};
  return u[n] * 16384;
}

__global__ __launch_bounds__(512, 2) void kmain(const u16* __restrict__ x, const u16* __restrict__ wr, const u16* __restrict__ wi,
                                                const u16* __restrict__ Wg, const u16* __restrict__ Wu, const u16* __restrict__ Wd,
                                                char* __restrict__ ws, u16* __restrict__ out) {
  __shared__ __attribute__((aligned(16))) char smem_[163840];
  LAS char* smem = (LAS char*)smem_;
  const u32 lbase = (u32)(size_t)smem;
  const int tid = threadIdx.x, l = tid & 63;
  const int w = __builtin_amdgcn_readfirstlane(tid >> 6);
  const int b = blockIdx.x, xcd = b & 7, idx = b >> 3;
  const bool ffn = idx < 28;
  const int g = l >> 4, o = l & 15;
  const int h = l >> 5, blk = (l >> 4) & 1, q = (l & 15) >> 2, pp = l & 3;
  const char* FL = ws + OFF_FL;
  const char* BC = ws + OFF_BC;
  STAMP_DECL; STAMPT(0);

  const int RG = xcd * 4 + (idx >> 3), ct = idx & 7;
  const int pos = xcd * 32 + idx, kd = (pos >> 3) + 32 * (pos & 7);
  const u32 swz = ((l & 7) ^ (((l >> 4) & 1) << 2)) << 4;
  const u32 ro0 = (u32)((l >> 3) * 1024) + swz, ro1 = ro0 + 8192 - 1024;
  const char* bsrc = (const char*)Wd + (size_t)(w * 16) * 1024 + ct * 128;
  auto issueW = [&](int n, u32 slotoff) { dma2(ro0, ro1, bsrc + (size_t)n * 131072, lbase + slotoff + w * 2048); };

  u32 E = 0;
  if (w == 0) dma1c((u32)((l & 1) * 4), BC, lbase + L_BC0);

  auto get_epoch = [&]() {
    const v2u b0 = *(LAS v2u*)(smem + L_BC0);
    v2u b1 = *(LAS v2u*)(smem + L_BC1);
    int spins = 0;
    while (__builtin_amdgcn_readfirstlane((b0[0] ^ b1[0]) | (b0[1] ^ b1[1])) == 0 && ++spins < SPIN_CAP) {
      __builtin_amdgcn_s_sleep(4);
      b1 = ld8c_sync(0u, BC);
    }
    E = __builtin_amdgcn_readfirstlane(b1[0]);
  };
  auto signal_a = [&]() { if (l == 0) { st4c((u32)(FL_A + b * 4), FL, E); *(LAS u32*)(smem + L_MX + 4) = E; } };
  u32 tick = 0;
  const bool coord = (b == 224) && (w == 0);
  auto do_tick = [&]() { if (coord) { ++tick; if (l == 0) st8c(0u, BC, v2u{E, tick}); } };
  auto poll_w_issue = [&]() { dma1c((u32)(FL_W + l * 4), FL, lbase + L_FW); };
  auto poll_w_check = [&]() {
    u32 f = *(LAS u32*)(smem + L_FW + l * 4);
    int spins = 0;
    while (!__all((l & 1) || f == E) && ++spins < SPIN_CAP) {
      __builtin_amdgcn_s_sleep(12);
      f = ld4c_sync((u32)(FL_W + l * 4), FL);
    }
    u32 m = wave_max((l & 1) ? f : 0u);
    if (l == 0) *(LAS u32*)(smem + L_MX) = m;
  };
  auto poll_a_issue = [&]() { dma1s<0>((u32)(FL_A + l * 16), FL, lbase + L_FA); };
  auto poll4 = [&](v4u f, u32 voff) {
    int spins = 0;
    while (!__all(f[0] == E && f[1] == E && f[2] == E && f[3] == E) && ++spins < SPIN_CAP) {
      do_tick();
      __builtin_amdgcn_s_sleep(12);
      f = ld16c_sync(voff, FL);
    }
  };
  u32 fp;
  auto poll1 = [&](u32 voff) {
    PIN(fp);
    u32 f = fp;
    int spins = 0;
    while (!__all(f == E) && ++spins < SPIN_CAP) {
      __builtin_amdgcn_s_sleep(2);
      f = ld4c_sync(voff, FL);
    }
  };
  const int dh = w & 1, kq = w >> 1, ph = kq >> 1, kh = kq & 1;
  const int epi = RG * 64 + ph * 32 + (l & 31);
  const u32 eoff = (u32)(s_of_pi(epi) * D_ + ct * 64 + dh * 32 + kh * 16 + h * 8) * 2;
  v4u wt, s1v, xe; v4f pq[4]; float rs2;
  auto load_r1 = [&]() {
    ld16c(s1v, (u32)((((4 * w + g) * 8 + (o >> 1)) * 256 + pos) * 32 + (o & 1) * 16), ws + OFF_S1);
    ld16c(wt, (u32)((w * 64 + l) * 4608 + (kd + ((2 * w + (g >> 1)) & 7)) * 16), ws + OFF_WT);
    ld16(xe, eoff, (const char*)x);
    ld4c(rs2, (u32)(epi * 4), ws + OFF_RS2);
  };
  auto pin_r1 = [&]() { PIN(wt); PIN(s1v); PIN(xe); PIN(rs2); };

  const int p = (xcd >> 1) * 4 + idx / 7, j = (xcd & 1) * 7 + idx % 7;
  const int sh = w >> 2, fq = w & 3;
  v16f acc[2][2] = {};
  float mbr[2];

  u32 aoff[2];
#pragma unroll
  for (int i = 0; i < 2; ++i) {
    int row = (2 * w + i) * 8 + (l >> 3);
    aoff[i] = s_of_pi((p & 15) * 128 + row) * 1024 + (((l & 7) ^ ((row >> 1) & 7)) << 4) - i * 1024;
  }
  const u32 boff = (u32)(2 * w * 4 + (l >> 4)) * (F_ * 2) + j * 256 + (((l & 15) ^ ((l >> 4) << 2)) << 4);
  const u32 boff1 = boff + 4 * F_ * 2 - 1024;
  const int rot = p >> 1;
  auto issue = [&](int t, int slot) {
    const int kt = (t + rot) & 7;
    u32 dst = lbase + slot * 49152 + w * 2048;
    dma2(aoff[0], aoff[1], (const char*)x + kt * 128, dst);
    dma2(boff, boff1, (const char*)Wg + (size_t)kt * (64 * F_ * 2), dst + 16384);
    dma2(boff, boff1, (const char*)Wu + (size_t)kt * (64 * F_ * 2), dst + 32768);
  };
  const int sp = xcd * 4 + (idx - 28);
  v4u Fhr, Flr, Fhi, Fli; v8h M2; v4f twc[2], tws[2]; v8s xf;
  {
    const int pi = b * 8 + w;
    v4u xr;
    ld16(xr, (u32)(s_of_pi(pi) * 1024 + l * 16), (const char*)x);
    if (ffn) issue(0, 0);
    if (coord) {
      v4u f[11];
#pragma unroll
      for (int i = 0; i < 11; ++i) ld16c(f[i], (u32)(i * 1024 + l * 16), FL);
      WAIT_VM(0);
      u32 c = __builtin_amdgcn_readfirstlane(*(LAS u32*)(smem + L_BC0));
      bool hit;
      do {
        ++c; hit = false;
#pragma unroll
        for (int i = 0; i < 11; ++i) { PIN(f[i]); hit |= (f[i][0] == c) | (f[i][1] == c) | (f[i][2] == c) | (f[i][3] == c); }
      } while (__any(hit));
      E = c;
      do_tick();
    }
    {
      const float fo = (float)o, fg = (float)(4 * g);
      if (w < 2) {
        const float st = fo * (1.0f / 32.0f), a0 = fmaf(fg, st, w ? 0.25f : 0.f);
        v4u hi, lo;
#pragma unroll
        for (int e2 = 0; e2 < 4; ++e2) {
          float c0 = cosr(fmaf((float)(16 * (e2 >> 1) + 2 * (e2 & 1)), st, a0)), c1 = cosr(fmaf((float)(16 * (e2 >> 1) + 2 * (e2 & 1) + 1), st, a0));
          u32 ch = pkbf_o(c0, c1);
          hi[e2] = ch; lo[e2] = pkbf(c0 - bflo(ch), c1 - bfhi(ch));
        }
        *(LAS v4u*)(smem + L_C1 + w * 2048 + l * 16) = hi; *(LAS v4u*)(smem + L_C1 + w * 2048 + 1024 + l * 16) = lo;
      } else if (w < 5) {
        const float st = w == 2 ? (float)(o & 7) * (1.0f / 16.0f) : fo * (w == 3 ? 1.0f / 16.0f : 1.0f / 8.0f);
        const float a0 = (w == 2 && o >= 8) ? 0.25f : 0.f;
        const float sc = w == 2 ? 0.0625f : ((w == 4 && (g >> 1) != (o >> 3)) ? 0.f : 1.0f);
        v8h m;
#pragma unroll
        for (int e = 0; e < 4; ++e) { float a = fmaf(fg + (float)e, st, a0); m[e] = (_Float16)(sc * cosr(a)); m[4 + e] = (_Float16)(sc * sinr(a)); }
        *(LAS v8h*)(smem + (w == 2 ? L_C1 + 4096 : L_TAB + (w - 3) * 1024) + l * 16) = m;
      } else {
        const float st = w == 7 ? fo * (1.0f / 256.0f) : (fo + (w == 6 ? 16.f : 0.f)) * (1.0f / 512.0f);
        v4f c, sn;
#pragma unroll
        for (int e = 0; e < 4; ++e) { float a = (fg + (float)e) * st; c[e] = cosr(a); sn[e] = sinr(a); }
        LAS char* dst = smem + (w == 7 ? L_TAB + 2048 : L_C1 + 5120 + (w - 5) * 2048) + l * 16;
        *(LAS v4f*)dst = c; *(LAS v4f*)(dst + 1024) = sn;
      }
    }
    BARRIER();
    if (ffn) issue(1, 1);
    LAS char* C1 = smem + L_C1 + l * 16;
    Fhr = *(LAS v4u*)C1; Flr = *(LAS v4u*)(C1 + 1024); Fhi = *(LAS v4u*)(C1 + 2048); Fli = *(LAS v4u*)(C1 + 3072);
    M2 = *(LAS v8h*)(C1 + 4096);
#pragma unroll
    for (int u = 0; u < 2; ++u) { twc[u] = *(LAS v4f*)(C1 + 5120 + u * 2048); tws[u] = *(LAS v4f*)(C1 + 6144 + u * 2048); }
    if (ffn) WAIT_VM(12); else WAIT_VM(0);
    PIN(xr);
    STAMPT(3);
    const float c0 = bflo(__builtin_amdgcn_readfirstlane(xr[0]));
    v2f xv[4], s2 = {0.f, 0.f}, q2 = {0.f, 0.f};
#pragma unroll
    for (int e = 0; e < 4; ++e) {
      xv[e] = v2f{bflo(xr[e]), bfhi(xr[e])};
      v2f t = xv[e] - v2f{c0, c0};
      s2 += t; q2 = __builtin_elementwise_fma(t, t, q2);
    }
    const float dmean = wave_sum(s2[0] + s2[1]) * (1.0f / 512.0f), msq = wave_sum(q2[0] + q2[1]) * (1.0f / 512.0f);
    do_tick();
    const float mb = rbf(c0 + dmean);
    const float var = rbf(fmaxf(msq - dmean * dmean, 0.f));
    const float sd = rbf(__builtin_amdgcn_sqrtf(rbf(var + 1e-5f)));
    const float rs = __builtin_amdgcn_rcpf(sd);
#pragma unroll
    for (int e = 0; e < 4; ++e) {
      v2f t = xv[e] - v2f{mb, mb};
      u32 tb = pkbf_o(t[0], t[1]);
      t = v2f{bflo(tb), bfhi(tb)} * v2f{rs, rs};
      xr[e] = pkbf(t[0], t[1]);
    }
    *(LAS v4u*)(smem + L_IMG + w * 1024 + l * 16) = xr;
    if (l < 2) st4c((u32)(pi * 4 + l * 8192), ws + OFF_MB, l ? rs * rs : -mb);
    xf = cat8(trread(smem + L_IMG + w * 1024 + l * 8), trread(smem + L_IMG + w * 1024 + 512 + l * 8));
    STAMPT(4);
  }
  v4f dre[2], dim[2], d2[2];
  auto dft_a = [&]() {
    const v4u sg = {0x80000000u, 0x80000000u, 0x80000000u, 0x80000000u};
    const v4f z = {0.f, 0.f, 0.f, 0.f};
#pragma unroll
    for (int u = 0; u < 2; ++u) { dre[u] = mfma16b(xf, u ? Flr ^ sg : Flr, z); dim[u] = mfma16b(xf, u ? Fli ^ sg : Fli, z); }
#pragma unroll
    for (int u = 0; u < 2; ++u) { dre[u] = mfma16b(xf, u ? Fhr ^ sg : Fhr, dre[u]); dim[u] = mfma16b(xf, u ? Fhi ^ sg : Fhi, dim[u]); }
  };
  v4f dnq;
  auto dft_b = [&]() {
    const v4f z = {0.f, 0.f, 0.f, 0.f};
#pragma unroll
    for (int u = 0; u < 2; ++u) {
      twid(dre[u], dim[u], twc[u], tws[u]);
      const v8h bb = pack_ri(dre[u], dim[u]);
      d2[u] = mfma16h(M2, bb, z);
      if (u == 0) {
        const u32 nv = o == 8 ? 0xac002c00u : 0u;
        dnq = mfma16h(__builtin_bit_cast(v8h, v4u{nv, nv, 0u, 0u}), bb, z);
      }
    }
  };
  auto dft_c = [&]() {
    if (l == 32) d2[0][0] = dnq[0];
#pragma unroll
    for (int u = 0; u < 2; ++u) *(LAS v4h*)(smem + L_T + ((g >> 1) * 8 + w) * 544 + (16 * u + o) * 16 + (g & 1) * 8) = pack4h(d2[u]);
  };
  auto dft_d = [&]() {
    LAS char* rp = smem + L_T + (4 * (g & 1) + (o >> 2)) * 544 + (w * 32 + (g >> 1) * 16) * 2 + (o & 3) * 8;
    v8s un = cat8(trread(rp), trread(rp + 8 * 544));
    st16c((u32)(b * 8192 + (w * 32 + (g >> 1) * 16 + o) * 32 + (g & 1) * 16), ws + OFF_S1, un);
  };

  dft_a(); dft_b(); dft_c();
  STAMPT(29);

  if (ffn) {
    const u32 wb0 = (u32)((8 * h + q) * 256 + ((fq ^ q) << 6) + blk * 32 + pp * 8);
    const u32 r1 = 16384 + sh * 16384, r2 = 32768 - sh * 16384;
    const int xrow = sh * 64 + (l & 31);
    const u32 xb0 = (u32)(xrow * 128 + ((h ^ ((xrow >> 1) & 7)) << 4));
    auto rd = [&](Frag1& f, LAS char* sb, int i) {
      LAS char* xa = sb + (xb0 ^ (i << 5));
      LAS char* wa_ = sb + wb0 + (i << 12);
      f.w1 = cat8(trread(wa_ + r1), trread(wa_ + r1 + 1024));
      f.x0 = *(LAS v8s*)xa; f.x1 = *(LAS v8s*)(xa + 4096);
      f.w2 = cat8(trread(wa_ + r2), trread(wa_ + r2 + 1024));
    };
    auto mm = [&](const Frag1& f) {
      acc[0][0] = mfma32b(f.w1, f.x0, acc[0][0]);
      acc[0][1] = mfma32b(f.w1, f.x1, acc[0][1]);
      acc[1][0] = mfma32b(f.w2, f.x0, acc[1][0]);
      acc[1][1] = mfma32b(f.w2, f.x1, acc[1][1]);
    };
    Frag1 A, B;
    WAIT_VM(7);
    BARRIER();
    STAMPT(1); STAMPC(30);
    rd(A, smem, 0);
    dft_d();
    if (w == 0) dma1c((u32)((l & 1) * 4), BC, lbase + L_BC1);
    dma2(aoff[0], aoff[1], (const char*)x + ((2 + rot) & 7) * 128, lbase + 2 * 49152 + w * 2048);
    v4f csa = {0.f, 0.f, 0.f, 0.f};
    {
      const u32 one = ((l & 15) == ((l >> 4) & 1)) ? 0x3f803f80u : 0u;
      const v4u sel = {one, one, one, one};
      LAS char* wa_ = smem + wb0 + r1 + ((p & 1) << 13);
      csa = mfma16b(cat8(trread(wa_), trread(wa_ + 1024)), sel, csa);
      csa = mfma16b(cat8(trread(wa_ + 4096), trread(wa_ + 5120)), sel, csa);
    }
    float csp[16];
    v4f cg[4], cu[4];
    static_for<0, 8>([&](auto T) {
      constexpr int t = T, slot = t % 3, nslot = (t + 1) % 3, pslot = (t + 2) % 3;
      LAS char* sb = smem + slot * 49152;
      {
        const int kt = (t + 2 + rot) & 7;
        const char* s1 = t < 6 ? (const char*)Wg + (size_t)kt * (64 * F_ * 2) : bsrc + (size_t)((t - 6) * 3 + 1) * 131072;
        const char* s2 = t < 6 ? (const char*)Wu + (size_t)kt * (64 * F_ * 2) : bsrc + (size_t)((t - 6) * 3 + 2) * 131072;
        const u32 o2 = t < 6 ? boff : ro0, o3 = t < 6 ? boff1 : ro1;
        const u32 dst = lbase + pslot * 49152 + w * 2048;
        rd(B, sb, 1); SCHED();
        acc[0][0] = mfma32b(A.w1, A.x0, acc[0][0]); acc[0][1] = mfma32b(A.w1, A.x1, acc[0][1]); SCHED();
        dma1<0>(o2, s1, dst + 16384);
        if constexpr (t == 7) { load_r1(); }
        acc[1][0] = mfma32b(A.w2, A.x0, acc[1][0]); acc[1][1] = mfma32b(A.w2, A.x1, acc[1][1]); SCHED();
        if constexpr (t == 0) {
          if ((l & 15) < 2) st16c((u32)(((j * 16 + p) * 256 + sh * 128 + fq * 32 + (l & 15) * 16 + (l >> 4) * 4) * 4), ws + OFF_CSP, csa);
          SCHED();
        }
        rd(A, sb, 2); SCHED();
        acc[0][0] = mfma32b(B.w1, B.x0, acc[0][0]); SCHED();
        dma1<1024>(o3, s1, dst + 16384);
        if constexpr (t == 7) {
          if (kd == 0) {
#pragma unroll
            for (int r = 0; r < 4; ++r) ld16c(pq[r], (u32)(w * 4096 + r * 1024 + l * 16), ws + OFF_PQ);
          }
        }
        acc[0][1] = mfma32b(B.w1, B.x1, acc[0][1]); acc[1][0] = mfma32b(B.w2, B.x0, acc[1][0]); acc[1][1] = mfma32b(B.w2, B.x1, acc[1][1]); SCHED();
        dma1<0>(o2, s2, dst + 32768);
        rd(B, sb, 3); SCHED();
        if constexpr (t == 7) {
#pragma unroll
          for (int g4 = 0; g4 < 4; ++g4) {
            cg[g4] = *(LAS v4f*)(smem + L_CS + (fq * 32 + g4 * 8 + h * 4) * 4);
            cu[g4] = *(LAS v4f*)(smem + L_CS + (128 + fq * 32 + g4 * 8 + h * 4) * 4);
          }
          SCHED();
        }
        acc[0][0] = mfma32b(A.w1, A.x0, acc[0][0]); acc[0][1] = mfma32b(A.w1, A.x1, acc[0][1]); SCHED();
        dma1<1024>(o3, s2, dst + 32768);
        acc[1][0] = mfma32b(A.w2, A.x0, acc[1][0]); acc[1][1] = mfma32b(A.w2, A.x1, acc[1][1]); SCHED();
      }
      if constexpr (t < 7) {
        const char* s0 = t < 5 ? (const char*)x + ((t + 3 + rot) & 7) * 128 : bsrc + (size_t)((t - 5) * 3) * 131072;
        const u32 o0 = t < 5 ? aoff[0] : ro0, o1 = t < 5 ? aoff[1] : ro1;
        const u32 dst = lbase + slot * 49152 + w * 2048;
        WAIT_VM(6);
        if constexpr (t == 5) { if (w == 0) poll4(*(LAS v4u*)(smem + L_FA + l * 16), (u32)(FL_A + l * 16)); }
        if constexpr (t == 6) { if (w == 0) poll_w_check(); }
        if constexpr (t == 6) {
          PIN(mbr[0]); PIN(mbr[1]);
          if (w < 4) {
            float sm = 0.f;
#pragma unroll
            for (int i = 0; i < 16; ++i) { PIN(csp[i]); sm += csp[i]; }
            *(LAS float*)(smem + L_CS + tid * 4) = sm;
          }
        }
        BARRIER();
        STAMPT(22 + t);
        acc[0][0] = mfma32b(B.w1, B.x0, acc[0][0]); SCHED();
        rd(A, smem + nslot * 49152, 0); SCHED();
        if constexpr (t == 4) { if (w == 0) poll_a_issue(); }
        if constexpr (t == 5) { if (w == 0) poll_w_issue(); }
        if constexpr (t == 5) {
          if (w < 4) {
#pragma unroll
            for (int i = 0; i < 16; ++i) ld4c(csp[i], (u32)((j * 16 + i) * 1024 + tid * 4), ws + OFF_CSP);
          }
          ld4c(mbr[0], (u32)((p * 128 + xrow) * 4), ws + OFF_MB); ld4c(mbr[1], (u32)((p * 128 + xrow + 32) * 4), ws + OFF_MB);
        }
        dma1<0>(o0, s0, dst);
        acc[0][1] = mfma32b(B.w1, B.x1, acc[0][1]);
        acc[1][0] = mfma32b(B.w2, B.x0, acc[1][0]);
        acc[1][1] = mfma32b(B.w2, B.x1, acc[1][1]); SCHED();
        dma1<1024>(o1, s0, dst);
        if constexpr (t == 1) { if (w == 0) { get_epoch(); signal_a(); } }
        SCHED();
      } else {
        mm(B); SCHED();
      }
    });
    STAMPT(2); STAMPC(31);
    auto fin = [&](const v16f& ag, const v16f& au, int rt) {
      const v2f mb2 = {mbr[rt], mbr[rt]};
#pragma unroll
      for (int s2i = 0; s2i < 2; ++s2i) {
        v4u ov;
#pragma unroll
        for (int e = 0; e < 4; ++e) {
          const int r = s2i * 8 + 2 * e;
          v2f gg = __builtin_elementwise_fma(mb2, v2f{cg[r >> 2][r & 3], cg[r >> 2][(r & 3) + 1]}, v2f{ag[r], ag[r + 1]});
          v2f uu = __builtin_elementwise_fma(mb2, v2f{cu[r >> 2][r & 3], cu[r >> 2][(r & 3) + 1]}, v2f{au[r], au[r + 1]});
          gg[0] = fmaxf(gg[0], 0.f); gg[1] = fmaxf(gg[1], 0.f);
          gg *= uu;
          ov[e] = pkbf(gg[0], gg[1]);
        }
        st16c((u32)(((p * 4 + sh * 2 + rt) * 112 + (j * 8 + fq * 2 + s2i)) * 1024 + l * 16), ws + OFF_H, ov);
      }
    };
    if (sh == 0) { fin(acc[0][0], acc[1][0], 0); fin(acc[0][1], acc[1][1], 1); }
    else { fin(acc[1][0], acc[0][0], 0); fin(acc[1][1], acc[0][1], 1); }
    STAMPT(6);
    WAIT_VM(4);
    pin_r1();
  } else {
    BARRIER();
    dft_d();
    if (w == 0 && !coord) dma1c((u32)((l & 1) * 4), BC, lbase + L_BC1);
    do_tick();
    if (l < 34) {
#pragma unroll
      for (int i = 0; i < 16; ++i) {
        const int kb = 2 * w + (i >> 3), r = i & 3;
        const u32 ks = (u32)((sp >> 1) + 16 * kb + 256 * (4 * (sp & 1) + r));
        const u32 off = min(((ks * (DF_ * 2)) & ~15u) + l * 16, (u32)(2048 * DF_ * 2 - 16));
        dma1<0>(off, (const char*)((i & 4) ? wi : wr), lbase + ((((i >> 2) & 1) * 16 + kb) * 4 + r) * WXP);
      }
    }
    WAIT_VM(16);
    BARRIER();
    if (w == 0) { if (!coord) get_epoch(); signal_a(); }
    STAMPT(22);
    u32 sq[8];
    if (w == 7) {
      const int kss = (sp >> 1) + 16 * (l >> 2) + 256 * (4 * (sp & 1) + (l & 3)), km = (2048 - kss) & 2047;
      const u32 o1 = (u32)(kss * DF_ * 2), o2 = (u32)(km * DF_ * 2);
      ld2(sq[0], o1, (const char*)wr); ld2(sq[1], o1, (const char*)wi); ld2(sq[2], o2, (const char*)wr); ld2(sq[3], o2, (const char*)wi);
      ld2(sq[4], o1 + 512, (const char*)wr); ld2(sq[5], o1 + 512, (const char*)wi); ld2(sq[6], o2 + 512, (const char*)wr); ld2(sq[7], o2 + 512, (const char*)wi);
    }
    WAIT_VM(0);
    STAMPT(23);
    u32 mx;
    {
      typedef unsigned short v2us __attribute__((ext_vector_type(2)));
      const int dl = (sp >> 1) & 7;
      LAS char* rp = smem + ((2 * w + (g >> 1)) * 4 + (o >> 2)) * WXP + (g & 1) * 32 + (o & 3) * 8;
      v4u un[9];
#pragma unroll
      for (int i = 0; i < 9; ++i) un[i] = __builtin_bit_cast(v4u, cat8(trread(rp + i * 64), trread(rp + 64 * WXP + i * 64)));
      v2us m2 = {0, 0};
#pragma unroll
      for (int i = 0; i < 9; ++i) {
        st16c((u32)((sp * 16 + 2 * w + (g >> 1)) * 4608 + (i * 32 + (l & 31)) * 16), ws + OFF_WT, un[i]);
        const int kk = i * 32 + (l & 31) - dl;
        const u32 msk = ((i == 0 || i == 8) && (kk < 0 || kk > 256)) ? 0u : 0x7fff7fffu;
#pragma unroll
        for (int e = 0; e < 4; ++e) { u32 t = un[i][e] & msk; m2 = __builtin_elementwise_max(m2, __builtin_bit_cast(v2us, t)); }
      }
      mx = max((u32)m2[0], (u32)m2[1]);
    }
    if (w == 7) {
#pragma unroll
      for (int i = 0; i < 8; ++i) PIN(sq[i]);
      float war = bflo(sq[0]) + bflo(sq[2]), wai = bflo(sq[1]) - bflo(sq[3]);
      float wbr = bflo(sq[4]) + bflo(sq[6]), wbi = bflo(sq[5]) - bflo(sq[7]);
      v4f pv = {0.25f * (war + wbr), 0.25f * (wai + wbi), 0.25f * (war - wbr), 0.25f * (wai - wbi)};
      st16c((u32)((((sp >> 2) * 4 + (l & 3)) * 64 + (sp & 3) * 16 + (l >> 2)) * 16), ws + OFF_PQ, pv);
    }
    mx = wave_max(mx);
    if (l == 0) *(LAS u32*)(smem + L_RED + w * 4) = mx;
    do_tick();
    STAMPT(24);
    WAIT_VM(0);
    BARRIER();
    STAMPT(1); STAMPC(30);
    if (w == 0) {
      v4u r0 = *(LAS v4u*)(smem + L_RED), r1 = *(LAS v4u*)(smem + L_RED + 16);
      u32 m = max(max(max(r0[0], r0[1]), max(r0[2], r0[3])), max(max(r1[0], r1[1]), max(r1[2], r1[3])));
      if (l == 0) st8c((u32)(FL_W + sp * 8), FL, v2u{E, m});
    }
    static_for<0, 6>([&](auto N) { issueW(N, wslot(N)); });
    if (w == 0) { poll4(v4u{~E, 0u, 0u, 0u}, (u32)(FL_A + l * 16)); poll_w_issue(); WAIT_VM(0); poll_w_check(); }
    BARRIER();
    load_r1();
    WAIT_VM(0);
    pin_r1();
    STAMPT(2); STAMPC(31);
  }

  BARRIER();
  STAMPT(5);
  issueW(6, wslot(6));
  const u32 trG = (u32)(kq * 4096 + (4 * h + q) * 128 + ((dh ^ ((q >> 1) & 1)) << 6) + (blk ^ kh) * 32 + pp * 8);
  v8s Wf[14][2];
  auto readW = [&](auto N) {
    constexpr int n = N;
    LAS char* sb = smem + wslot(n) + trG;
    Wf[n][0] = cat8(trread(sb), trread(sb + 1024)); Wf[n][1] = cat8(trread(sb + 2048), trread(sb + 3072));
  };
  static_for<0, 6>(readW);

  issueW(7, wslot(7));

  const char* hsrc = ws + OFF_H + ((size_t)(RG * 2 + (w >> 2)) * 112 + (w & 3) * 2) * 1024;
  const u32 so0 = (u32)((l >> 3) * 4096) + swz, so1 = so0 + 32768 - 1024;
  const char* ssrc = ws + OFF_S2 + (size_t)(w * 16) * 4096 + RG * 128;
  auto issueP = [&](int n, auto HALF) {
    constexpr int hf = HALF;
    if (n < 14) dma1s<hf * 1024>((u32)(l * 16), hsrc + n * 8192, lbase + aslot(n) + w * 2048);
    else dma1s<hf * 1024>(hf ? so1 : so0, ssrc + (size_t)(n - 14) * (128 * 4096), lbase + aslot(n) + w * 2048);
  };
  float oscale;
  const u32 fhoff = (u32)(FL_H + (RG * 64 + min(l, 55)) * 4);
  {
    v4f t2c, t2s, t3c, t3s;
    {
      const float fo = (float)o, fg = (float)(4 * g), fw = (float)w;
      const float st2 = fw * (1.0f / 128.0f), a2 = fw * fo * (1.0f / 2048.0f);
      const float f7 = (float)(o & 7), st3 = f7 * (1.0f / 128.0f), a3 = f7 * (float)(2 * w + (o >> 3)) * (1.0f / 2048.0f);
#pragma unroll
      for (int e = 0; e < 4; ++e) {
        const float id = fg + (float)e;
        float b2 = fmaf(id, st2, a2);
        t2c[e] = cosr(b2); t2s[e] = sinr(b2);
        float b3 = fmaf(id, st3, a3);
        t3c[e] = cosr(b3); t3s[e] = sinr(b3);
      }
    }
    const u32 Ev = *(LAS u32*)(smem + L_MX + 4);
    int Ex = (int)(*(LAS u32*)(smem + L_MX) >> 7);
    Ex = Ex < 16 ? 16 : (Ex > 240 ? 240 : Ex);
    const float wscale = __builtin_bit_cast(float, (u32)(246 - Ex) << 23);
    oscale = __builtin_bit_cast(float, (u32)(Ex - 7) << 23);
    const v4u f16 = *(LAS v4u*)(smem + L_TAB + l * 16), f8 = *(LAS v4u*)(smem + L_TAB + 1024 + l * 16);
    const v4f t1c = *(LAS v4f*)(smem + L_TAB + 2048 + l * 16), t1s = *(LAS v4f*)(smem + L_TAB + 3072 + l * 16);
    const v8h F16re = __builtin_bit_cast(v8h, f16), F8re = __builtin_bit_cast(v8h, f8);
    const v8h F16im = __builtin_bit_cast(v8h, v4u{f16[2] ^ 0x80008000u, f16[3] ^ 0x80008000u, f16[0], f16[1]});
    const v8h F8im = __builtin_bit_cast(v8h, v4u{f8[2] ^ 0x80008000u, f8[3] ^ 0x80008000u, f8[0], f8[1]});
    STAMPT(7);
    const v4f z = {0.f, 0.f, 0.f, 0.f};
    v8h a = __builtin_bit_cast(v8h, s1v);
    v4f re = mfma16h(a, F16re, z), im = mfma16h(a, F16im, z);
    issueW(8, wslot(8));
    twid(re, im, t1c, t1s);
    a = pack_ri(re, im);
    re = mfma16h(F16re, a, z); im = mfma16h(F16im, a, z);
    WAIT_VM(6);
    if (ffn && l == 0) st4c((u32)(FL_H + ((p * 2 + sh) * 64 + j * 4 + fq) * 4), FL, Ev);
    twid(re, im, t2c, t2s);
    *(LAS v4h*)(smem + L_E1 + w * E1P + o * 32 + g * 8) = pack4h(re);
    *(LAS v4h*)(smem + L_E1 + (8 + w) * E1P + o * 32 + g * 8) = pack4h(im);
    if (ffn) WAIT_VM(1); else WAIT_VM(0);
    BARRIER();
    STAMPT(8);
    {
      LAS char* rp = smem + L_E1 + (4 * (g & 1) + (o >> 2)) * E1P + (2 * w + (g >> 1)) * 32 + (o & 3) * 8;
      a = __builtin_bit_cast(v8h, cat8(trread(rp), trread(rp + 8 * E1P)));
    }
    issueW(9, wslot(9));
    static_for<6, 9>(readW);
    re = mfma16h(F8re, a, z); im = mfma16h(F8im, a, z);
    issueW(10, wslot(10));
    if (kd != 0) {
#pragma unroll
      for (int r = 0; r < 4; ++r) {
        float fr = ((r & 1) ? bfhi(wt[r >> 1]) : bflo(wt[r >> 1])) * wscale, fi = ((r & 1) ? bfhi(wt[2 + (r >> 1)]) : bflo(wt[2 + (r >> 1)])) * wscale;
        float zr = re[r], zi = im[r];
        re[r] = zr * fr - zi * fi;
        im[r] = -(zr * fi + zi * fr);
      }
    } else {
      const int ka = 2 * w + (g >> 1), kr = o + 64 * (g & 1);
#pragma unroll
      for (int r = 0; r < 4; ++r) { v2f t = {re[r], im[r]}; *(LAS v2f*)(smem + L_ZX + ka * 1024 + (kr + 16 * r) * 8) = t; }
      BARRIER();
#pragma unroll
      for (int r = 0; r < 4; ++r) {
        PIN(pq[r]);
        const int km = (2048 - (ka + 16 * (kr + 16 * r))) & 2047;
        v2f zm = *(LAS v2f*)(smem + L_ZX + (km & 15) * 1024 + (km >> 4) * 8);
        float zr = re[r], zi = im[r];
        re[r] = (zr * pq[r][0] - zi * pq[r][1] + zm[0] * pq[r][2] + zm[1] * pq[r][3]) * wscale;
        im[r] = -(zr * pq[r][1] + zi * pq[r][0] + zm[0] * pq[r][3] - zm[1] * pq[r][2]) * wscale;
      }
    }
    LAS char* E2 = smem + (kd == 0 ? L_E1 : L_E2);
    a = pack_ri(re, im);
    issueW(11, wslot(11));
    re = mfma16h(a, F8re, z); im = mfma16h(a, F8im, z);
    if (w == 0) ld4c(fp, fhoff, FL);
    issueW(12, wslot(12));
    twid(re, im, t3c, t3s);
    *(LAS v4h*)(E2 + (o & 7) * E2P + (2 * w + (o >> 3)) * 32 + g * 8) = pack4h(re);
    *(LAS v4h*)(E2 + (o & 7) * E2P + 512 + (2 * w + (o >> 3)) * 32 + g * 8) = pack4h(im);
    issueW(13, wslot(13));
    if (w == 0) { WAIT_VM(4); poll1(fhoff); }
    BARRIER();
    STAMPT(10);
    {
      LAS char* rp = E2 + w * E2P + o * 32 + g * 8;
      a = __builtin_bit_cast(v8h, cat8(*(LAS v4s*)rp, *(LAS v4s*)(rp + 512)));
    }
    issueP(0, IC<0>{}); issueP(0, IC<1>{});
    re = mfma16h(a, F16re, z); im = mfma16h(a, F16im, z);
    issueP(1, IC<0>{}); issueP(1, IC<1>{});
    twid(re, im, t1c, t1s);
    a = pack_ri(re, im);
    re = mfma16h(F16re, a, z); im = mfma16h(F16im, a, z);
    st8c((u32)(2 * kd * 4096 + w * 512 + l * 8), ws + OFF_S2, pack4h(re));
    st8c((u32)((2 * kd + 1) * 4096 + w * 512 + l * 8), ws + OFF_S2, pack4h(im));
  }
  STAMPT(9);

  const int d = ct * 64 + dh * 32 + ((l & 31) ^ (kh << 4));
  const float gst = (float)d * (1.0f / 512.0f), ga0 = (float)(16 * kq + 2 * h) * gst;
  v8h G[8];
  auto gen = [&](auto FI) {
    constexpr int f = FI;
#pragma unroll
    for (int e2 = 0; e2 < 4; ++e2) {
      float a = fmaf((float)(64 * (f >> 1) + 8 * (f & 1) + 4 * (e2 >> 1) + (e2 & 1)), gst, ga0);
      float c = cosr(a), sn = sinr(a);
      if (f == 0 && e2 == 0) { if (kq == 0 && h == 0) { c = 0.5f; sn = (d & 1) ? 0.5f : -0.5f; } }
      G[f][2 * e2] = (_Float16)c; G[f][2 * e2 + 1] = (_Float16)sn;
    }
    PIN(G[f]);
  };
  issueP(2, IC<0>{}); issueP(2, IC<1>{});
  static_for<0, 3>(gen);
  const u32 trS = (u32)(kq * 4096 + (4 * h + q) * 128 + ((ph ^ ((q >> 1) & 1)) << 6) + blk * 32 + pp * 8);
  const u32 hb = (u32)(ph * 8192 + kq * 2048 + l * 16);
  v16f accG[2] = {}, accS[2] = {};
  v8s fa[8], fb[8];
  v4u fs;
  float gv[8];
  const u32 xw = (u32)(w * 6144 + l * 16);
  auto xchg_out = [&](const v16f (&ac)[2], int base) {
    LAS char* X = smem + base + xw;
#pragma unroll
    for (int i = 0; i < 6; ++i) {
      const v16f& a = ac[(i + 2) >> 2]; const int r = ((i + 2) & 3) * 4;
      *(LAS v4f*)(X + i * 1024) = v4f{a[r], a[r + 1], a[r + 2], a[r + 3]};
    }
  };
  auto xchg_in = [&](const v16f (&ac)[2], int base, float (&o8)[8]) {
    v4f s0 = {ac[0][0], ac[0][1], ac[0][2], ac[0][3]}, s1 = {ac[0][4], ac[0][5], ac[0][6], ac[0][7]};
#pragma unroll
    for (int x_ = 1; x_ < 4; ++x_) {
      LAS char* X = smem + base + (w ^ (2 * x_)) * 6144 + (x_ - 1) * 2048 + l * 16;
      s0 += *(LAS v4f*)X; s1 += *(LAS v4f*)(X + 1024);
    }
#pragma unroll
    for (int e = 0; e < 4; ++e) { o8[e] = s0[e]; o8[4 + e] = s1[e]; }
  };
  static_for<0, 10>([&](auto V) {
    constexpr int v = V;
    v8s(&cur)[8] = (v & 1) ? fb : fa;
    v8s(&prv)[8] = (v & 1) ? fa : fb;
    auto mmp = [&](int s0, int s1) {
#pragma unroll
      for (int s = s0; s < s1; ++s) {
        if constexpr (v >= 8) {
          accS[0] = mfma32h(G[(v - 8) * 4 + s], prv[2 * s], accS[0]); accS[1] = mfma32h(G[(v - 8) * 4 + s], prv[2 * s + 1], accS[1]);
        } else if constexpr (v >= 1) {
          accG[0] = mfma32b(Wf[2 * (v - 1) + (s >> 1)][s & 1], prv[2 * s], accG[0]);
          accG[1] = mfma32b(Wf[2 * (v - 1) + (s >> 1)][s & 1], prv[2 * s + 1], accG[1]);
        }
      }
    };
    if constexpr (v < 9) {
      WAIT_VM(v == 0 ? 4 : v == 1 ? 2 : v <= 7 ? 4 : 0);
      if constexpr (v == 5) { if (w == 0) { PIN(fs); poll4(fs, (u32)(FL_S + l * 16)); } }
      BARRIER();
      STAMPT(11 + v);
      if constexpr (v == 1) { if (tid == 0) st4c((u32)(FL_S + b * 4), FL, E); }
      if constexpr (v == 4) { if (w == 0) ld16c(fs, (u32)(FL_S + l * 16), FL); }
      mmp(0, 1); SCHED();
#pragma unroll
      for (int s = 0; s < 4; ++s) {
        if constexpr (v >= 7) {
          LAS char* sb = smem + aslot(2 * v + (s >> 1)) + (s & 1) * 2048;
          cur[2 * s] = cat8(trread(sb + trS), trread(sb + trS + 1024));
          cur[2 * s + 1] = cat8(trread(sb + (trS ^ 64)), trread(sb + (trS ^ 64) + 1024));
        } else {
          LAS char* sb = smem + aslot(2 * v + (s >> 1)) + (s & 1) * 1024;
          cur[2 * s] = *(LAS v8s*)(sb + hb);
          cur[2 * s + 1] = *(LAS v8s*)(sb + (hb ^ 8192));
        }
      }
      if constexpr (v == 0) {
        issueP(3, IC<0>{}); issueP(3, IC<1>{});
        static_for<9, 14>(readW);
        issueP(4, IC<0>{}); issueP(4, IC<1>{});
        gen(IC<3>{}); gen(IC<4>{});
      }
      SCHED();
      if constexpr (v == 1) { issueP(5, IC<0>{}); issueP(5, IC<1>{}); }
      if constexpr (v >= 1 && v <= 6) issueP(2 * v + 4, IC<0>{});
      mmp(1, 2); SCHED();
      if constexpr (v >= 1 && v <= 6) issueP(2 * v + 4, IC<1>{});
      mmp(2, 3); SCHED();
      if constexpr (v >= 1 && v <= 6) issueP(2 * v + 5, IC<0>{});
      mmp(3, 4); SCHED();
      if constexpr (v >= 1 && v <= 6) issueP(2 * v + 5, IC<1>{});
    } else {
      mmp(0, 4);
    }
    if constexpr (v >= 1 && v <= 3) gen(IC<4 + v>{});
    if constexpr (v == 7) xchg_out(accG, 49152);
    if constexpr (v == 8) {
      float o8[8];
      xchg_in(accG, 49152, o8);
      PIN(rs2);
#pragma unroll
      for (int e = 0; e < 8; ++e) {
        float ov = rbf(o8[e] * rs2);
        gv[e] = rbf(ov * __builtin_amdgcn_rcpf(1.0f + __expf(-ov)));
        PIN(gv[e]);
      }
    }
    SCHED();
  });
  xchg_out(accS, 0);
  BARRIER();
  {
    float o8[8], pv[8];
    xchg_in(accS, 0, o8);
#pragma unroll
    for (int e = 0; e < 8; ++e) pv[e] = rbf(o8[e] * oscale) * gv[e];
    u32 a0 = pkbf(pv[0], pv[1]), a1 = pkbf(pv[2], pv[3]), b0 = pkbf(pv[4], pv[5]), b1 = pkbf(pv[6], pv[7]);
    auto r0 = __builtin_amdgcn_permlane32_swap(a0, b0, false, false);
    auto r1 = __builtin_amdgcn_permlane32_swap(a1, b1, false, false);
    v4u pk = {r0[0], r1[0], r0[1], r1[1]};
    PIN(xe);
    v4u ov4;
#pragma unroll
    for (int e = 0; e < 4; ++e) ov4[e] = pkbf(bflo(xe[e]) + bflo(pk[e]), bfhi(xe[e]) + bfhi(pk[e]));
    *(v4u*)((char*)out + eoff) = ov4;
  }
  STAMPT(20);
  WAIT_VM(0);
  STAMPT(21);
  STAMP_OUT(ws);
}

extern "C" void kernel_launch(void* const* d_in, const int* in_sizes, int n_in, void* d_out, int out_size, void* d_ws,
                              size_t ws_size, hipStream_t stream) {
  kmain<<<256, 512, 0, stream>>>((const u16*)d_in[0], (const u16*)d_in[1], (const u16*)d_in[2], (const u16*)d_in[4],
                                 (const u16*)d_in[5], (const u16*)d_in[6], (char*)d_ws, (u16*)d_out);
}
```

```cpp
#include <hip/hip_runtime.h>
#include <stdint.h>

typedef unsigned short u16;
typedef unsigned int u32;
typedef unsigned long long u64;
typedef short v4s __attribute__((ext_vector_type(4)));
typedef short v8s __attribute__((ext_vector_type(8)));
typedef _Float16 v4h __attribute__((ext_vector_type(4)));
typedef _Float16 v8h __attribute__((ext_vector_type(8)));
typedef __bf16 v2b __attribute__((ext_vector_type(2)));
typedef __bf16 v8b __attribute__((ext_vector_type(8)));
typedef float v2f __attribute__((ext_vector_type(2)));
typedef float v4f __attribute__((ext_vector_type(4)));
typedef float v16f __attribute__((ext_vector_type(16)));
typedef u32 v2u __attribute__((ext_vector_type(2)));
typedef u32 v4u __attribute__((ext_vector_type(4)));

#define LAS __attribute__((address_space(3)))
#define GAS __attribute__((address_space(1)))
#define DEVI __device__ __forceinline__

constexpr int D_ = 512, F_ = 1792, DF_ = 257;
constexpr size_t OFF_H = 0;
constexpr size_t OFF_S1 = (size_t)8 << 20;
constexpr size_t OFF_WT = (size_t)10 << 20;
constexpr size_t OFF_S2 = (size_t)13 << 20;
constexpr size_t OFF_M = (size_t)15 << 20;
constexpr size_t OFF_FL = OFF_M;
constexpr u32 FL_W = 0, FL_S = 1024, FL_A = 2048, FL_H = 3072;
constexpr size_t OFF_BC = OFF_M + 12288;
constexpr size_t OFF_MB = OFF_M + 16384;
constexpr size_t OFF_RS2 = OFF_M + 24576;
constexpr size_t OFF_PQ = OFF_M + 65536;
constexpr size_t OFF_CSP = OFF_M + 131072;
constexpr int SPIN_CAP = 1 << 16;

#define STAMP_DECL
#define STAMPT(id)
#define STAMPC(id)
#define STAMP_OUT(wsp)

template <int I> struct IC { static constexpr int v = I; constexpr operator int() const { return I; } };
template <int B, int E, class Fn> DEVI void static_for(Fn&& f) {
  if constexpr (B < E) { f(IC<B>{}); static_for<B + 1, E>(f); }
}
#define WAIT_VM(n) asm volatile("s_waitcnt vmcnt(%0)" ::"n"(n) : "memory")
#define BARRIER() asm volatile("s_waitcnt lgkmcnt(0)\n\ts_barrier" ::: "memory")
#define SCHED() __builtin_amdgcn_sched_barrier(0)
#define PIN(x) asm volatile("" : "+v"(x))

DEVI int s_of_pi(int pi) {
  int RG = pi >> 6, i = pi & 63;
  return 128 * (4 * (RG & 3) + (i & 3)) + 8 * (i >> 2) + (RG >> 2);
}
DEVI void dma2(u32 voff0, u32 voff1, const char* sbase, u32 lds) {
  asm volatile("s_mov_b32 m0, %3\n\ts_nop 0\n\tglobal_load_lds_dwordx4 %0, %2\n\tglobal_load_lds_dwordx4 %1, %2 offset:1024" ::"v"(voff0), "v"(voff1), "s"(sbase), "s"(lds) : "memory");
}
DEVI void dma2c(u32 voff0, u32 voff1, const char* sbase, u32 lds) {
  asm volatile("s_mov_b32 m0, %3\n\ts_nop 0\n\tglobal_load_lds_dwordx4 %0, %2 sc1\n\tglobal_load_lds_dwordx4 %1, %2 offset:1024 sc1" ::"v"(voff0), "v"(voff1), "s"(sbase), "s"(lds) : "memory");
}
template <int OFF> DEVI void dma1(u32 voff, const char* sbase, u32 lds) {
  asm volatile("s_mov_b32 m0, %2\n\ts_nop 0\n\tglobal_load_lds_dwordx4 %0, %1 offset:%3" ::"v"(voff), "s"(sbase), "s"(lds), "n"(OFF) : "memory");
}
template <int OFF> DEVI void dma1s(u32 voff, const char* sbase, u32 lds) {
  asm volatile("s_mov_b32 m0, %2\n\ts_nop 0\n\tglobal_load_lds_dwordx4 %0, %1 offset:%3 sc1" ::"v"(voff), "s"(sbase), "s"(lds), "n"(OFF) : "memory");
}
DEVI void dma1c(u32 voff, const char* sbase, u32 lds) {
  asm volatile("s_mov_b32 m0, %2\n\ts_nop 0\n\tglobal_load_lds_dword %0, %1 sc1" ::"v"(voff), "s"(sbase), "s"(lds) : "memory");
}
DEVI v2u ld8c_sync(u32 voff, const char* sb) { v2u d; asm volatile("global_load_dwordx2 %0, %1, %2 sc1\n\ts_waitcnt vmcnt(0)" : "=&v"(d) : "v"(voff), "s"(sb) : "memory"); return d; }
DEVI u32 ld4c_sync(u32 voff, const char* sb) { u32 d; asm volatile("global_load_dword %0, %1, %2 sc1\n\ts_waitcnt vmcnt(0)" : "=&v"(d) : "v"(voff), "s"(sb) : "memory"); return d; }
DEVI v4u ld16c_sync(u32 voff, const char* sb) { v4u d; asm volatile("global_load_dwordx4 %0, %1, %2 sc1\n\ts_waitcnt vmcnt(0)" : "=&v"(d) : "v"(voff), "s"(sb) : "memory"); return d; }
template <class T> DEVI void ld16c(T& d, u32 voff, const char* sb) { asm volatile("global_load_dwordx4 %0, %1, %2 sc1" : "=&v"(d) : "v"(voff), "s"(sb) : "memory"); }
template <class T> DEVI void ld8c(T& d, u32 voff, const char* sb) { asm volatile("global_load_dwordx2 %0, %1, %2 sc1" : "=&v"(d) : "v"(voff), "s"(sb) : "memory"); }
template <class T> DEVI void ld4c(T& d, u32 voff, const char* sb) { asm volatile("global_load_dword %0, %1, %2 sc1" : "=&v"(d) : "v"(voff), "s"(sb) : "memory"); }
template <class T> DEVI void ld16(T& d, u32 voff, const char* sb) { asm volatile("global_load_dwordx4 %0, %1, %2" : "=&v"(d) : "v"(voff), "s"(sb) : "memory"); }
DEVI void ld2(u32& d, u32 voff, const char* sb) { asm volatile("global_load_ushort %0, %1, %2" : "=&v"(d) : "v"(voff), "s"(sb) : "memory"); }
template <class T> DEVI void st16c(u32 voff, const char* sb, T v) { asm volatile("global_store_dwordx4 %0, %1, %2 sc1\n\ts_nop 1" ::"v"(voff), "v"(v), "s"(sb) : "memory"); }
template <class T> DEVI void st8c(u32 voff, const char* sb, T v) { asm volatile("global_store_dwordx2 %0, %1, %2 sc1" ::"v"(voff), "v"(v), "s"(sb) : "memory"); }
template <class T> DEVI void st4c(u32 voff, const char* sb, T v) { asm volatile("global_store_dword %0, %1, %2 sc1" ::"v"(voff), "v"(v), "s"(sb) : "memory"); }

DEVI v4s trread(LAS char* p) { return __builtin_amdgcn_ds_read_tr16_b64_v4i16((LAS v4s*)p); }
DEVI v8s cat8(v4s a, v4s b) { return __builtin_shufflevector(a, b, 0, 1, 2, 3, 4, 5, 6, 7); }
DEVI float bflo(u32 v) { return __builtin_bit_cast(float, v << 16); }
DEVI float bfhi(u32 v) { return __builtin_bit_cast(float, v & 0xffff0000u); }
DEVI float rbf(float v) { return (float)(__bf16)v; }
DEVI u32 pkbf(float a, float b) { v2b p = {(__bf16)a, (__bf16)b}; return __builtin_bit_cast(u32, p); }
DEVI u32 pkbf_o(float a, float b) { u32 r = pkbf(a, b); asm("" : "+v"(r)); return r; }
DEVI float cosr(float rev) { return __builtin_amdgcn_cosf(rev); }
DEVI float sinr(float rev) { return __builtin_amdgcn_sinf(rev); }
DEVI v4f mfma16h(v8h a, v8h b, v4f c) { return __builtin_amdgcn_mfma_f32_16x16x32_f16(a, b, c, 0, 0, 0); }
DEVI v4f mfma16b(v8s a, v4u b, v4f c) { return __builtin_amdgcn_mfma_f32_16x16x32_bf16(__builtin_bit_cast(v8b, a), __builtin_bit_cast(v8b, b), c, 0, 0, 0); }
DEVI v16f mfma32b(v8s a, v8s b, v16f c) { return __builtin_amdgcn_mfma_f32_32x32x16_bf16(__builtin_bit_cast(v8b, a), __builtin_bit_cast(v8b, b), c, 0, 0, 0); }
DEVI v16f mfma32h(v8h a, v8s b, v16f c) { return __builtin_amdgcn_mfma_f32_32x32x16_f16(a, __builtin_bit_cast(v8h, b), c, 0, 0, 0); }
DEVI v8h pack_ri(v4f re, v4f im) {
  v8h r;
  r[0] = (_Float16)re[0]; r[1] = (_Float16)re[1]; r[2] = (_Float16)re[2]; r[3] = (_Float16)re[3];
  r[4] = (_Float16)im[0]; r[5] = (_Float16)im[1]; r[6] = (_Float16)im[2]; r[7] = (_Float16)im[3];
  return r;
}
DEVI v4h pack4h(v4f a) { v4h r; r[0] = (_Float16)a[0]; r[1] = (_Float16)a[1]; r[2] = (_Float16)a[2]; r[3] = (_Float16)a[3]; return r; }
DEVI void twid(v4f& re, v4f& im, const v4f& c, const v4f& s) {
#pragma unroll
  for (int r = 0; r < 4; ++r) { float a = re[r], b = im[r]; re[r] = a * c[r] + b * s[r]; im[r] = b * c[r] - a * s[r]; }
}
template <int CTRL> DEVI float dppadd(float v) {
  return v + __builtin_bit_cast(float, __builtin_amdgcn_update_dpp(0, __builtin_bit_cast(int, v), CTRL, 0xf, 0xf, true));
}
DEVI float wave_sum(float v) {
  v = dppadd<0xB1>(v); v = dppadd<0x4E>(v); v = dppadd<0x141>(v); v = dppadd<0x140>(v);
  int bb = __builtin_bit_cast(int, v);
  float a0 = __builtin_bit_cast(float, __builtin_amdgcn_readlane(bb, 0)), a1 = __builtin_bit_cast(float, __builtin_amdgcn_readlane(bb, 16));
  float a2 = __builtin_bit_cast(float, __builtin_amdgcn_readlane(bb, 32)), a3 = __builtin_bit_cast(float, __builtin_amdgcn_readlane(bb, 48));
  return (a0 + a1) + (a2 + a3);
}
DEVI float dot2ones(u32 v, float acc) {
  const v2b ones = {(__bf16)1.0f, (__bf16)1.0f};
  return __builtin_amdgcn_fdot2_f32_bf16(__builtin_bit_cast(v2b, v), ones, acc, false);
}
template <int CTRL> DEVI u32 dppmax(u32 v) { return max(v, (u32)__builtin_amdgcn_update_dpp(0, (int)v, CTRL, 0xf, 0xf, true)); }
DEVI u32 wave_max(u32 v) {
  v = dppmax<0xB1>(v); v = dppmax<0x4E>(v); v = dppmax<0x141>(v); v = dppmax<0x140>(v);
  u32 a0 = __builtin_amdgcn_readlane(v, 0), a1 = __builtin_amdgcn_readlane(v, 16), a2 = __builtin_amdgcn_readlane(v, 32), a3 = __builtin_amdgcn_readlane(v, 48);
  return max(max(a0, a1), max(a2, a3));
}

struct Frag1 { v8s x0, x1, w1, w2; };

constexpr int L_SCR = 147456;
constexpr int L_CS = L_SCR, L_RED = L_SCR + 1024, L_MX = L_SCR + 1088, L_BC0 = L_SCR + 2048, L_BC1 = L_SCR + 2304;
constexpr int L_TAB = L_SCR + 3072;
constexpr int L_T = L_SCR + 7168, L_FW = L_T, L_FA = L_T + 2048, L_E1 = L_T, L_E2 = 32768, L_ZX = 32768;
constexpr int L_C1 = 98304, L_IMG = L_C1 + 9216;
constexpr int WXP = 544 + 32;
constexpr int E1P = 544, E2P = 1024 + 72;
DEVI constexpr int wslot(int n) { return n < 3 ? 98304 + n * 16384 : n < 6 ? (n - 3) * 16384 : n < 9 ? 49152 + (n - 6) * 16384 : n < 12 ? 98304 + (n - 9) * 16384 : (n - 12) * 16384; }
DEVI constexpr int aslot(int n) {
  constexpr int u[18] = {3, 4, 5, 9, 2, 6, 7, 8, 0, 1, 3, 4, 5, 9, 2, 6, 7, 8};
  return u[n] * 16384;
}

__global__ __launch_bounds__(512, 2) void kmain(const u16* __restrict__ x, const u16* __restrict__ wr, const u16* __restrict__ wi,
                                                const u16* __restrict__ Wg, const u16* __restrict__ Wu, const u16* __restrict__ Wd,
                                                char* __restrict__ ws, u16* __restrict__ out) {
  __shared__ __attribute__((aligned(16))) char smem_[163840];
  LAS char* smem = (LAS char*)smem_;
  const u32 lbase = (u32)(size_t)smem;
  const int tid = threadIdx.x, l = tid & 63;
  const int w = __builtin_amdgcn_readfirstlane(tid >> 6);
  const int b = blockIdx.x, xcd = b & 7, idx = b >> 3;
  const bool ffn = idx < 28;
  const int g = l >> 4, o = l & 15;
  const int h = l >> 5, blk = (l >> 4) & 1, q = (l & 15) >> 2, pp = l & 3;
  const char* FL = ws + OFF_FL;
  const char* BC = ws + OFF_BC;
  STAMP_DECL; STAMPT(0);

  const int RG = xcd * 4 + (idx >> 3), ct = idx & 7;
  const int pos = xcd * 32 + idx, kd = (pos >> 3) + 32 * (pos & 7);
  const u32 swz = ((l & 7) ^ (((l >> 4) & 1) << 2)) << 4;
  const u32 ro0 = (u32)((l >> 3) * 1024) + swz, ro1 = ro0 + 8192 - 1024;
  const char* bsrc = (const char*)Wd + (size_t)(w * 16) * 1024 + ct * 128;
  auto issueW = [&](int n, u32 slotoff) { dma2(ro0, ro1, bsrc + (size_t)n * 131072, lbase + slotoff + w * 2048); };

  u32 E = 0;
  if (w == 0) dma1c((u32)((l & 1) * 4), BC, lbase + L_BC0);

  auto get_epoch = [&]() {
    const v2u b0 = *(LAS v2u*)(smem + L_BC0);
    v2u b1 = *(LAS v2u*)(smem + L_BC1);
    int spins = 0;
    while (__builtin_amdgcn_readfirstlane((b0[0] ^ b1[0]) | (b0[1] ^ b1[1])) == 0 && ++spins < SPIN_CAP) {
      __builtin_amdgcn_s_sleep(4);
      b1 = ld8c_sync(0u, BC);
    }
    E = __builtin_amdgcn_readfirstlane(b1[0]);
  };
  auto signal_a = [&]() { if (l == 0) { st4c((u32)(FL_A + b * 4), FL, E); *(LAS u32*)(smem + L_MX + 4) = E; } };
  u32 tick = 0;
  const bool coord = (b == 224) && (w == 0);
  auto do_tick = [&]() { if (coord) { ++tick; if (l == 0) st8c(0u, BC, v2u{E, tick}); } };
  auto poll_w_issue = [&]() { dma1c((u32)(FL_W + l * 4), FL, lbase + L_FW); };
  auto poll_w_check = [&]() {
    u32 f = *(LAS u32*)(smem + L_FW + l * 4);
    int spins = 0;
    while (!__all((l & 1) || f == E) && ++spins < SPIN_CAP) {
      __builtin_amdgcn_s_sleep(12);
      f = ld4c_sync((u32)(FL_W + l * 4), FL);
    }
    u32 m = wave_max((l & 1) ? f : 0u);
    if (l == 0) *(LAS u32*)(smem + L_MX) = m;
  };
  auto poll_a_issue = [&]() { dma1s<0>((u32)(FL_A + l * 16), FL, lbase + L_FA); };
  auto poll4 = [&](v4u f, u32 voff) {
    int spins = 0;
    while (!__all(f[0] == E && f[1] == E && f[2] == E && f[3] == E) && ++spins < SPIN_CAP) {
      do_tick();
      __builtin_amdgcn_s_sleep(12);
      f = ld16c_sync(voff, FL);
    }
  };
  u32 fp;
  auto poll1 = [&](u32 voff) {
    PIN(fp);
    u32 f = fp;
    int spins = 0;
    while (!__all(f == E) && ++spins < SPIN_CAP) {
      __builtin_amdgcn_s_sleep(2);
      f = ld4c_sync(voff, FL);
    }
  };
  const int dh = w & 1, kq = w >> 1, ph = kq >> 1, kh = kq & 1;
  const int epi = RG * 64 + ph * 32 + (l & 31);
  const u32 eoff = (u32)(s_of_pi(epi) * D_ + ct * 64 + dh * 32 + kh * 16 + h * 8) * 2;
  v4u wt, s1v, xe; v4f pq[4]; float rs2;
  auto load_xe = [&]() { ld16(xe, eoff, (const char*)x); };
  auto load_s1 = [&]() {
    ld16c(s1v, (u32)((((4 * w + g) * 8 + (o >> 1)) * 256 + pos) * 32 + (o & 1) * 16), ws + OFF_S1);
    ld4c(rs2, (u32)(epi * 4), ws + OFF_RS2);
  };
  auto pin_r1 = [&]() { PIN(s1v); PIN(xe); PIN(rs2); };

  const int p = (xcd >> 1) * 4 + idx / 7, j = (xcd & 1) * 7 + idx % 7;
  const int sh = w >> 2, fq = w & 3;
  v16f acc[2][2] = {};
  float mbr[2];

  u32 aoff[2];
#pragma unroll
  for (int i = 0; i < 2; ++i) {
    int row = (2 * w + i) * 8 + (l >> 3);
    aoff[i] = s_of_pi((p & 15) * 128 + row) * 1024 + (((l & 7) ^ ((row >> 1) & 7)) << 4) - i * 1024;
  }
  const u32 boff = (u32)(2 * w * 4 + (l >> 4)) * (F_ * 2) + j * 256 + (((l & 15) ^ ((l >> 4) << 2)) << 4);
  const u32 boff1 = boff + 4 * F_ * 2 - 1024;
  const int rot = p >> 1;
  auto issue = [&](int t, int slot) {
    const int kt = (t + rot) & 7;
    u32 dst = lbase + slot * 49152 + w * 2048;
    dma2(aoff[0], aoff[1], (const char*)x + kt * 128, dst);
    dma2(boff, boff1, (const char*)Wg + (size_t)kt * (64 * F_ * 2), dst + 16384);
    dma2(boff, boff1, (const char*)Wu + (size_t)kt * (64 * F_ * 2), dst + 32768);
  };
  const int sp = xcd * 4 + (idx - 28);
  auto filt_rows = [&](int i0, int i1) {
    if (l < 34) {
#pragma unroll
      for (int i = i0; i < i1; ++i) {
        const int kb = 2 * w + (i >> 3), r = i & 3;
        const u32 ks = (u32)((sp >> 1) + 16 * kb + 256 * (4 * (sp & 1) + r));
        const u32 off = min(((ks * (DF_ * 2)) & ~15u) + l * 16, (u32)(2048 * DF_ * 2 - 16));
        dma1<0>(off, (const char*)((i & 4) ? wi : wr), lbase + ((((i >> 2) & 1) * 16 + kb) * 4 + r) * WXP);
      }
    }
  };
  v4u Fhr, Flr, Fhi, Fli; v8h M2; v4f twc[2], tws[2]; v8s xf;
  {
    const int pi = b * 8 + w;
    v4u xr;
    ld16(xr, (u32)(s_of_pi(pi) * 1024 + l * 16), (const char*)x);
    if (ffn) issue(0, 0);
    else {
      {
        u64 pc; u32 len;
        asm volatile("s_getpc_b64 %0\nkm_pf_a:\n\ts_mov_b32 %1, km_pf_b-km_pf_a" : "=s"(pc), "=s"(len));
        const u32 o0 = (u32)(((idx & 3) * 8 + w) * 1024 + l * 16);
        dma1<0>(min(o0, len) & ~15u, (const char*)(pc & ~15ull), lbase + 81920);
        dma1<0>(min(o0 + 32768, len) & ~15u, (const char*)(pc & ~15ull), lbase + 81920);
      }
    }
    if (coord) {
      v4u f[11];
#pragma unroll
      for (int i = 0; i < 11; ++i) ld16c(f[i], (u32)(i * 1024 + l * 16), FL);
      WAIT_VM(0);
      u32 c = __builtin_amdgcn_readfirstlane(*(LAS u32*)(smem + L_BC0));
      bool hit;
      do {
        ++c; hit = false;
#pragma unroll
        for (int i = 0; i < 11; ++i) { PIN(f[i]); hit |= (f[i][0] == c) | (f[i][1] == c) | (f[i][2] == c) | (f[i][3] == c); }
      } while (__any(hit));
      E = c;
      do_tick();
    }
    {
      const float fo = (float)o, fg = (float)(4 * g);
      if (w < 2) {
        const float st = fo * (1.0f / 32.0f), a0 = fmaf(fg, st, w ? 0.25f : 0.f);
        v4u hi, lo;
#pragma unroll
        for (int e2 = 0; e2 < 4; ++e2) {
          float c0 = cosr(fmaf((float)(16 * (e2 >> 1) + 2 * (e2 & 1)), st, a0)), c1 = cosr(fmaf((float)(16 * (e2 >> 1) + 2 * (e2 & 1) + 1), st, a0));
          u32 ch = pkbf_o(c0, c1);
          hi[e2] = ch; lo[e2] = pkbf(c0 - bflo(ch), c1 - bfhi(ch));
        }
        *(LAS v4u*)(smem + L_C1 + w * 2048 + l * 16) = hi; *(LAS v4u*)(smem + L_C1 + w * 2048 + 1024 + l * 16) = lo;
      } else if (w < 5) {
        const float st = w == 2 ? (float)(o & 7) * (1.0f / 16.0f) : fo * (w == 3 ? 1.0f / 16.0f : 1.0f / 8.0f);
        const float a0 = (w == 2 && o >= 8) ? 0.25f : 0.f;
        const float sc = w == 2 ? 0.0625f : ((w == 4 && (g >> 1) != (o >> 3)) ? 0.f : 1.0f);
        v8h m;
#pragma unroll
        for (int e = 0; e < 4; ++e) { float a = fmaf(fg + (float)e, st, a0); m[e] = (_Float16)(sc * cosr(a)); m[4 + e] = (_Float16)(sc * sinr(a)); }
        *(LAS v8h*)(smem + (w == 2 ? L_C1 + 4096 : L_TAB + (w - 3) * 1024) + l * 16) = m;
      } else {
        const float st = w == 7 ? fo * (1.0f / 256.0f) : (fo + (w == 6 ? 16.f : 0.f)) * (1.0f / 512.0f);
        v4f c, sn;
#pragma unroll
        for (int e = 0; e < 4; ++e) { float a = (fg + (float)e) * st; c[e] = cosr(a); sn[e] = sinr(a); }
        LAS char* dst = smem + (w == 7 ? L_TAB + 2048 : L_C1 + 5120 + (w - 5) * 2048) + l * 16;
        *(LAS v4f*)dst = c; *(LAS v4f*)(dst + 1024) = sn;
      }
    }
    BARRIER();
    if (ffn) issue(1, 1); else filt_rows(0, 6);
    LAS char* C1 = smem + L_C1 + l * 16;
    Fhr = *(LAS v4u*)C1; Flr = *(LAS v4u*)(C1 + 1024); Fhi = *(LAS v4u*)(C1 + 2048); Fli = *(LAS v4u*)(C1 + 3072);
    M2 = *(LAS v8h*)(C1 + 4096);
#pragma unroll
    for (int u = 0; u < 2; ++u) { twc[u] = *(LAS v4f*)(C1 + 5120 + u * 2048); tws[u] = *(LAS v4f*)(C1 + 6144 + u * 2048); }
    if (ffn) WAIT_VM(12); else WAIT_VM(8);
    PIN(xr);
    STAMPT(3);
    const float c0 = bflo(__builtin_amdgcn_readfirstlane(xr[0]));
    v2f xv[4], s2 = {0.f, 0.f}, q2 = {0.f, 0.f};
#pragma unroll
    for (int e = 0; e < 4; ++e) {
      xv[e] = v2f{bflo(xr[e]), bfhi(xr[e])};
      v2f t = xv[e] - v2f{c0, c0};
      s2 += t; q2 = __builtin_elementwise_fma(t, t, q2);
    }
    const float dmean = wave_sum(s2[0] + s2[1]) * (1.0f / 512.0f), msq = wave_sum(q2[0] + q2[1]) * (1.0f / 512.0f);
    do_tick();
    const float mb = rbf(c0 + dmean);
    const float var = rbf(fmaxf(msq - dmean * dmean, 0.f));
    const float sd = rbf(__builtin_amdgcn_sqrtf(rbf(var + 1e-5f)));
    const float rs = __builtin_amdgcn_rcpf(sd);
#pragma unroll
    for (int e = 0; e < 4; ++e) {
      v2f t = xv[e] - v2f{mb, mb};
      u32 tb = pkbf_o(t[0], t[1]);
      t = v2f{bflo(tb), bfhi(tb)} * v2f{rs, rs};
      xr[e] = pkbf(t[0], t[1]);
    }
    *(LAS v4u*)(smem + L_IMG + w * 1024 + l * 16) = xr;
    if (l < 2) st4c((u32)(pi * 4 + l * 8192), ws + OFF_MB, l ? rs * rs : -mb);
    xf = cat8(trread(smem + L_IMG + w * 1024 + l * 8), trread(smem + L_IMG + w * 1024 + 512 + l * 8));
    STAMPT(4);
  }
  v4f dre[2], dim[2], d2[2];
  auto dft_a = [&]() {
    const v4u sg = {0x80000000u, 0x80000000u, 0x80000000u, 0x80000000u};
    const v4f z = {0.f, 0.f, 0.f, 0.f};
#pragma unroll
    for (int u = 0; u < 2; ++u) { dre[u] = mfma16b(xf, u ? Flr ^ sg : Flr, z); dim[u] = mfma16b(xf, u ? Fli ^ sg : Fli, z); }
#pragma unroll
    for (int u = 0; u < 2; ++u) { dre[u] = mfma16b(xf, u ? Fhr ^ sg : Fhr, dre[u]); dim[u] = mfma16b(xf, u ? Fhi ^ sg : Fhi, dim[u]); }
  };
  v4f dnq;
  auto dft_b = [&]() {
    const v4f z = {0.f, 0.f, 0.f, 0.f};
#pragma unroll
    for (int u = 0; u < 2; ++u) {
      twid(dre[u], dim[u], twc[u], tws[u]);
      const v8h bb = pack_ri(dre[u], dim[u]);
      d2[u] = mfma16h(M2, bb, z);
      if (u == 0) {
        const u32 nv = o == 8 ? 0xac002c00u : 0u;
        dnq = mfma16h(__builtin_bit_cast(v8h, v4u{nv, nv, 0u, 0u}), bb, z);
      }
    }
  };
  auto dft_c = [&]() {
    if (l == 32) d2[0][0] = dnq[0];
#pragma unroll
    for (int u = 0; u < 2; ++u) *(LAS v4h*)(smem + L_T + ((g >> 1) * 8 + w) * 544 + (16 * u + o) * 16 + (g & 1) * 8) = pack4h(d2[u]);
  };
  auto dft_d = [&]() {
    LAS char* rp = smem + L_T + (4 * (g & 1) + (o >> 2)) * 544 + (w * 32 + (g >> 1) * 16) * 2 + (o & 3) * 8;
    v8s un = cat8(trread(rp), trread(rp + 8 * 544));
    st16c((u32)(b * 8192 + (w * 32 + (g >> 1) * 16 + o) * 32 + (g & 1) * 16), ws + OFF_S1, un);
  };

  dft_a(); dft_b(); dft_c();
  STAMPT(29);

  if (ffn) {
    const u32 wb0 = (u32)((8 * h + q) * 256 + ((fq ^ q) << 6) + blk * 32 + pp * 8);
    const u32 r1 = 16384 + sh * 16384, r2 = 32768 - sh * 16384;
    const int xrow = sh * 64 + (l & 31);
    const u32 xb0 = (u32)(xrow * 128 + ((h ^ ((xrow >> 1) & 7)) << 4));
    auto rd = [&](Frag1& f, LAS char* sb, int i) {
      LAS char* xa = sb + (xb0 ^ (i << 5));
      LAS char* wa_ = sb + wb0 + (i << 12);
      f.w1 = cat8(trread(wa_ + r1), trread(wa_ + r1 + 1024));
      f.x0 = *(LAS v8s*)xa; f.x1 = *(LAS v8s*)(xa + 4096);
      f.w2 = cat8(trread(wa_ + r2), trread(wa_ + r2 + 1024));
    };
    auto mm = [&](const Frag1& f) {
      acc[0][0] = mfma32b(f.w1, f.x0, acc[0][0]);
      acc[0][1] = mfma32b(f.w1, f.x1, acc[0][1]);
      acc[1][0] = mfma32b(f.w2, f.x0, acc[1][0]);
      acc[1][1] = mfma32b(f.w2, f.x1, acc[1][1]);
    };
    Frag1 A, B;
    WAIT_VM(7);
    BARRIER();
    STAMPT(1); STAMPC(30);
    rd(A, smem, 0);
    dft_d();
    if (w == 0) dma1c((u32)((l & 1) * 4), BC, lbase + L_BC1);
    dma2(aoff[0], aoff[1], (const char*)x + ((2 + rot) & 7) * 128, lbase + 2 * 49152 + w * 2048);
    v4f csa = {0.f, 0.f, 0.f, 0.f};
    {
      const u32 one = ((l & 15) == ((l >> 4) & 1)) ? 0x3f803f80u : 0u;
      const v4u sel = {one, one, one, one};
      LAS char* wa_ = smem + wb0 + r1;
#pragma unroll
      for (int i = 0; i < 4; ++i) csa = mfma16b(cat8(trread(wa_ + i * 4096), trread(wa_ + i * 4096 + 1024)), sel, csa);
    }
    float csp[8];
    v4f cg[4], cu[4];
    static_for<0, 8>([&](auto T) {
      constexpr int t = T, slot = t % 3, nslot = (t + 1) % 3, pslot = (t + 2) % 3;
      LAS char* sb = smem + slot * 49152;
      {
        const int kt = (t + 2 + rot) & 7;
        const char* s1 = t < 6 ? (const char*)Wg + (size_t)kt * (64 * F_ * 2) : bsrc + (size_t)((t - 6) * 3 + 1) * 131072;
        const char* s2 = t < 6 ? (const char*)Wu + (size_t)kt * (64 * F_ * 2) : bsrc + (size_t)((t - 6) * 3 + 2) * 131072;
        const u32 o2 = t < 6 ? boff : ro0, o3 = t < 6 ? boff1 : ro1;
        const u32 dst = lbase + pslot * 49152 + w * 2048;
        rd(B, sb, 1); SCHED();
        acc[0][0] = mfma32b(A.w1, A.x0, acc[0][0]); acc[0][1] = mfma32b(A.w1, A.x1, acc[0][1]); SCHED();
        dma1<0>(o2, s1, dst + 16384);
        if constexpr (t == 2) load_xe();
        if constexpr (t == 7) load_s1();
        acc[1][0] = mfma32b(A.w2, A.x0, acc[1][0]); acc[1][1] = mfma32b(A.w2, A.x1, acc[1][1]); SCHED();
        if constexpr (t == 0) {
          if ((l & 15) < 2) st16c((u32)(((j * 16 + p) * 256 + sh * 128 + fq * 32 + (l & 15) * 16 + (l >> 4) * 4) * 4), ws + OFF_CSP, csa);
          SCHED();
        }
        rd(A, sb, 2); SCHED();
        acc[0][0] = mfma32b(B.w1, B.x0, acc[0][0]); SCHED();
        dma1<1024>(o3, s1, dst + 16384);
        if constexpr (t == 7) { if (w == 0) poll_w_issue(); }
        acc[0][1] = mfma32b(B.w1, B.x1, acc[0][1]); acc[1][0] = mfma32b(B.w2, B.x0, acc[1][0]); acc[1][1] = mfma32b(B.w2, B.x1, acc[1][1]); SCHED();
        dma1<0>(o2, s2, dst + 32768);
        rd(B, sb, 3); SCHED();
        if constexpr (t == 7) {
#pragma unroll
          for (int g4 = 0; g4 < 4; ++g4) {
            cg[g4] = *(LAS v4f*)(smem + L_CS + (fq * 32 + g4 * 8 + h * 4) * 4);
            cu[g4] = *(LAS v4f*)(smem + L_CS + (128 + fq * 32 + g4 * 8 + h * 4) * 4);
          }
          SCHED();
        }
        acc[0][0] = mfma32b(A.w1, A.x0, acc[0][0]); acc[0][1] = mfma32b(A.w1, A.x1, acc[0][1]); SCHED();
        dma1<1024>(o3, s2, dst + 32768);
        acc[1][0] = mfma32b(A.w2, A.x0, acc[1][0]); acc[1][1] = mfma32b(A.w2, A.x1, acc[1][1]); SCHED();
      }
      if constexpr (t < 7) {
        const char* s0 = t < 5 ? (const char*)x + ((t + 3 + rot) & 7) * 128 : bsrc + (size_t)((t - 5) * 3) * 131072;
        const u32 o0 = t < 5 ? aoff[0] : ro0, o1 = t < 5 ? aoff[1] : ro1;
        const u32 dst = lbase + slot * 49152 + w * 2048;
        WAIT_VM(6);
        if constexpr (t == 5) { if (w == 0) poll4(*(LAS v4u*)(smem + L_FA + l * 16), (u32)(FL_A + l * 16)); }
        if constexpr (t == 6) {
          PIN(mbr[0]); PIN(mbr[1]);
          if (w < 4) {
            float sm = 0.f;
#pragma unroll
            for (int i = 0; i < 8; ++i) { PIN(csp[i]); sm += csp[i]; }
            *(LAS float*)(smem + L_CS + tid * 4) = sm;
          }
        }
        BARRIER();
        STAMPT(22 + t);
        acc[0][0] = mfma32b(B.w1, B.x0, acc[0][0]); SCHED();
        rd(A, smem + nslot * 49152, 0); SCHED();
        if constexpr (t == 4) { if (w == 0) poll_a_issue(); }
        if constexpr (t == 5) {
          if (w < 4) {
#pragma unroll
            for (int i = 0; i < 8; ++i) ld4c(csp[i], (u32)((j * 16 + 2 * i + (p & 1)) * 1024 + tid * 4), ws + OFF_CSP);
          }
          ld4c(mbr[0], (u32)((p * 128 + xrow) * 4), ws + OFF_MB); ld4c(mbr[1], (u32)((p * 128 + xrow + 32) * 4), ws + OFF_MB);
        }
        dma1<0>(o0, s0, dst);
        acc[0][1] = mfma32b(B.w1, B.x1, acc[0][1]);
        acc[1][0] = mfma32b(B.w2, B.x0, acc[1][0]);
        acc[1][1] = mfma32b(B.w2, B.x1, acc[1][1]); SCHED();
        dma1<1024>(o1, s0, dst);
        if constexpr (t == 1) { if (w == 0) { get_epoch(); signal_a(); } }
        SCHED();
      } else {
        mm(B); SCHED();
      }
    });
    STAMPT(2); STAMPC(31);
    auto fin = [&](const v16f& ag, const v16f& au, int rt) {
      const v2f mb2 = {mbr[rt], mbr[rt]};
#pragma unroll
      for (int s2i = 0; s2i < 2; ++s2i) {
        v4u ov;
#pragma unroll
        for (int e = 0; e < 4; ++e) {
          const int r = s2i * 8 + 2 * e;
          v2f gg = __builtin_elementwise_fma(mb2, v2f{cg[r >> 2][r & 3], cg[r >> 2][(r & 3) + 1]}, v2f{ag[r], ag[r + 1]});
          v2f uu = __builtin_elementwise_fma(mb2, v2f{cu[r >> 2][r & 3], cu[r >> 2][(r & 3) + 1]}, v2f{au[r], au[r + 1]});
          gg[0] = fmaxf(gg[0], 0.f); gg[1] = fmaxf(gg[1], 0.f);
          gg *= uu;
          ov[e] = pkbf(gg[0], gg[1]);
        }
        st16c((u32)(((p * 4 + sh * 2 + rt) * 112 + (j * 8 + fq * 2 + s2i)) * 1024 + l * 16), ws + OFF_H, ov);
      }
    };
    if (sh == 0) { fin(acc[0][0], acc[1][0], 0); fin(acc[0][1], acc[1][1], 1); }
    else { fin(acc[1][0], acc[0][0], 0); fin(acc[1][1], acc[0][1], 1); }
    STAMPT(6);
    WAIT_VM(4);
    pin_r1();
    if (w == 0) poll_w_check();
  } else {
    BARRIER();
    dft_d();
    if (w == 0 && !coord) dma1c((u32)((l & 1) * 4), BC, lbase + L_BC1);
    do_tick();
    filt_rows(6, 16);
    WAIT_VM(10);
    BARRIER();
    if (w == 0) { if (!coord) get_epoch(); signal_a(); }
    STAMPT(22);
    dma1c((u32)(sp * 57344 + (w * 112 + l) * 64), (const char*)Wd, lbase + 81920);
    dma1c((u32)(sp * 57344 + (w * 112 + 48 + l) * 64), (const char*)Wd, lbase + 81920);
    WAIT_VM(2);
    u32 sq[8];
    if (w == 7) {
      const int kss = (sp >> 1) + 16 * (l >> 2) + 256 * (4 * (sp & 1) + (l & 3)), km = (2048 - kss) & 2047;
      const u32 o1 = (u32)(kss * DF_ * 2), o2 = (u32)(km * DF_ * 2);
      ld2(sq[0], o1, (const char*)wr); ld2(sq[1], o1, (const char*)wi); ld2(sq[2], o2, (const char*)wr); ld2(sq[3], o2, (const char*)wi);
      ld2(sq[4], o1 + 512, (const char*)wr); ld2(sq[5], o1 + 512, (const char*)wi); ld2(sq[6], o2 + 512, (const char*)wr); ld2(sq[7], o2 + 512, (const char*)wi);
    }
    STAMPT(23);
    u32 mx;
    {
      typedef unsigned short v2us __attribute__((ext_vector_type(2)));
      const int dl = (sp >> 1) & 7;
      LAS char* rp = smem + ((2 * w + (g >> 1)) * 4 + (o >> 2)) * WXP + (g & 1) * 32 + (o & 3) * 8;
      v4u un[9];
#pragma unroll
      for (int i = 0; i < 9; ++i) un[i] = __builtin_bit_cast(v4u, cat8(trread(rp + i * 64), trread(rp + 64 * WXP + i * 64)));
      v2us m2 = {0, 0};
#pragma unroll
      for (int i = 0; i < 9; ++i) {
        st16c((u32)((sp * 16 + 2 * w + (g >> 1)) * 4608 + (i * 32 + (l & 31)) * 16), ws + OFF_WT, un[i]);
        const int kk = i * 32 + (l & 31) - dl;
        const u32 msk = ((i == 0 || i == 8) && (kk < 0 || kk > 256)) ? 0u : 0x7fff7fffu;
#pragma unroll
        for (int e = 0; e < 4; ++e) { u32 t = un[i][e] & msk; m2 = __builtin_elementwise_max(m2, __builtin_bit_cast(v2us, t)); }
      }
      mx = max((u32)m2[0], (u32)m2[1]);
    }
    if (w == 7) {
      WAIT_VM(9);
#pragma unroll
      for (int i = 0; i < 8; ++i) PIN(sq[i]);
      float war = bflo(sq[0]) + bflo(sq[2]), wai = bflo(sq[1]) - bflo(sq[3]);
      float wbr = bflo(sq[4]) + bflo(sq[6]), wbi = bflo(sq[5]) - bflo(sq[7]);
      v4f pv = {0.25f * (war + wbr), 0.25f * (wai + wbi), 0.25f * (war - wbr), 0.25f * (wai - wbi)};
      st16c((u32)((((sp >> 2) * 4 + (l & 3)) * 64 + (sp & 3) * 16 + (l >> 2)) * 16), ws + OFF_PQ, pv);
    }
    mx = wave_max(mx);
    if (l == 0) *(LAS u32*)(smem + L_RED + w * 4) = mx;
    do_tick();
    STAMPT(24);
    WAIT_VM(0);
    BARRIER();
    STAMPT(1); STAMPC(30);
    if (w == 0) {
      v4u r0 = *(LAS v4u*)(smem + L_RED), r1 = *(LAS v4u*)(smem + L_RED + 16);
      u32 m = max(max(max(r0[0], r0[1]), max(r0[2], r0[3])), max(max(r1[0], r1[1]), max(r1[2], r1[3])));
      if (l == 0) st8c((u32)(FL_W + sp * 8), FL, v2u{E, m});
    }
    static_for<0, 6>([&](auto N) { issueW(N, wslot(N)); });
    if (w == 0) { poll4(v4u{~E, 0u, 0u, 0u}, (u32)(FL_A + l * 16)); poll_w_issue(); WAIT_VM(0); poll_w_check(); }
    BARRIER();
    load_xe(); load_s1();
    WAIT_VM(0);
    pin_r1();
    STAMPT(2); STAMPC(31);
  }

  BARRIER();
  STAMPT(5);
  ld16c(wt, (u32)((w * 64 + l) * 4608 + (kd + ((2 * w + (g >> 1)) & 7)) * 16), ws + OFF_WT);
  if (kd == 0) {
#pragma unroll
    for (int r = 0; r < 4; ++r) ld16c(pq[r], (u32)(w * 4096 + r * 1024 + l * 16), ws + OFF_PQ);
  }
  issueW(6, wslot(6));
  const u32 trG = (u32)(kq * 4096 + (4 * h + q) * 128 + ((dh ^ ((q >> 1) & 1)) << 6) + (blk ^ kh) * 32 + pp * 8);
  v8s Wf[14][2];
  auto readW = [&](auto N) {
    constexpr int n = N;
    LAS char* sb = smem + wslot(n) + trG;
    Wf[n][0] = cat8(trread(sb), trread(sb + 1024)); Wf[n][1] = cat8(trread(sb + 2048), trread(sb + 3072));
  };
  static_for<0, 6>(readW);

  issueW(7, wslot(7));

  const char* hsrc = ws + OFF_H + ((size_t)(RG * 2 + (w >> 2)) * 112 + (w & 3) * 2) * 1024;
  const u32 so0 = (u32)((l >> 3) * 4096) + swz, so1 = so0 + 32768 - 1024;
  const char* ssrc = ws + OFF_S2 + (size_t)(w * 16) * 4096 + RG * 128;
  auto issueP = [&](int n, auto HALF) {
    constexpr int hf = HALF;
    if (n < 14) dma1s<hf * 1024>((u32)(l * 16), hsrc + n * 8192, lbase + aslot(n) + w * 2048);
    else dma1s<hf * 1024>(hf ? so1 : so0, ssrc + (size_t)(n - 14) * (128 * 4096), lbase + aslot(n) + w * 2048);
  };
  float oscale;
  const u32 fhoff = (u32)(FL_H + (RG * 64 + min(l, 55)) * 4);
  {
    v4f t2c, t2s, t3c, t3s;
    {
      const float fo = (float)o, fg = (float)(4 * g), fw = (float)w;
      const float st2 = fw * (1.0f / 128.0f), a2 = fw * fo * (1.0f / 2048.0f);
      const float f7 = (float)(o & 7), st3 = f7 * (1.0f / 128.0f), a3 = f7 * (float)(2 * w + (o >> 3)) * (1.0f / 2048.0f);
#pragma unroll
      for (int e = 0; e < 4; ++e) {
        const float id = fg + (float)e;
        float b2 = fmaf(id, st2, a2);
        t2c[e] = cosr(b2); t2s[e] = sinr(b2);
        float b3 = fmaf(id, st3, a3);
        t3c[e] = cosr(b3); t3s[e] = sinr(b3);
      }
    }
    const u32 Ev = *(LAS u32*)(smem + L_MX + 4);
    int Ex = (int)(*(LAS u32*)(smem + L_MX) >> 7);
    Ex = Ex < 16 ? 16 : (Ex > 240 ? 240 : Ex);
    const float wscale = __builtin_bit_cast(float, (u32)(246 - Ex) << 23);
    oscale = __builtin_bit_cast(float, (u32)(Ex - 7) << 23);
    const v4u f16 = *(LAS v4u*)(smem + L_TAB + l * 16), f8 = *(LAS v4u*)(smem + L_TAB + 1024 + l * 16);
    const v4f t1c = *(LAS v4f*)(smem + L_TAB + 2048 + l * 16), t1s = *(LAS v4f*)(smem + L_TAB + 3072 + l * 16);
    const v8h F16re = __builtin_bit_cast(v8h, f16), F8re = __builtin_bit_cast(v8h, f8);
    const v8h F16im = __builtin_bit_cast(v8h, v4u{f16[2] ^ 0x80008000u, f16[3] ^ 0x80008000u, f16[0], f16[1]});
    const v8h F8im = __builtin_bit_cast(v8h, v4u{f8[2] ^ 0x80008000u, f8[3] ^ 0x80008000u, f8[0], f8[1]});
    STAMPT(7);
    const v4f z = {0.f, 0.f, 0.f, 0.f};
    v8h a = __builtin_bit_cast(v8h, s1v);
    v4f re = mfma16h(a, F16re, z), im = mfma16h(a, F16im, z);
    issueW(8, wslot(8));
    twid(re, im, t1c, t1s);
    a = pack_ri(re, im);
    re = mfma16h(F16re, a, z); im = mfma16h(F16im, a, z);
    WAIT_VM(7);
    if (ffn && l == 0) st4c((u32)(FL_H + ((p * 2 + sh) * 64 + j * 4 + fq) * 4), FL, Ev);
    twid(re, im, t2c, t2s);
    *(LAS v4h*)(smem + L_E1 + w * E1P + o * 32 + g * 8) = pack4h(re);
    *(LAS v4h*)(smem + L_E1 + (8 + w) * E1P + o * 32 + g * 8) = pack4h(im);
    if (ffn) WAIT_VM(1); else WAIT_VM(0);
    BARRIER();
    STAMPT(8);
    {
      LAS char* rp = smem + L_E1 + (4 * (g & 1) + (o >> 2)) * E1P + (2 * w + (g >> 1)) * 32 + (o & 3) * 8;
      a = __builtin_bit_cast(v8h, cat8(trread(rp), trread(rp + 8 * E1P)));
    }
    issueW(9, wslot(9));
    static_for<6, 9>(readW);
    re = mfma16h(F8re, a, z); im = mfma16h(F8im, a, z);
    issueW(10, wslot(10));
    if (kd != 0) {
      PIN(wt);
#pragma unroll
      for (int r = 0; r < 4; ++r) {
        float fr = ((r & 1) ? bfhi(wt[r >> 1]) : bflo(wt[r >> 1])) * wscale, fi = ((r & 1) ? bfhi(wt[2 + (r >> 1)]) : bflo(wt[2 + (r >> 1)])) * wscale;
        float zr = re[r], zi = im[r];
        re[r] = zr * fr - zi * fi;
        im[r] = -(zr * fi + zi * fr);
      }
    } else {
      const int ka = 2 * w + (g >> 1), kr = o + 64 * (g & 1);
#pragma unroll
      for (int r = 0; r < 4; ++r) { v2f t = {re[r], im[r]}; *(LAS v2f*)(smem + L_ZX + ka * 1024 + (kr + 16 * r) * 8) = t; }
      BARRIER();
#pragma unroll
      for (int r = 0; r < 4; ++r) {
        PIN(pq[r]);
        const int km = (2048 - (ka + 16 * (kr + 16 * r))) & 2047;
        v2f zm = *(LAS v2f*)(smem + L_ZX + (km & 15) * 1024 + (km >> 4) * 8);
        float zr = re[r], zi = im[r];
        re[r] = (zr * pq[r][0] - zi * pq[r][1] + zm[0] * pq[r][2] + zm[1] * pq[r][3]) * wscale;
        im[r] = -(zr * pq[r][1] + zi * pq[r][0] + zm[0] * pq[r][3] - zm[1] * pq[r][2]) * wscale;
      }
    }
    LAS char* E2 = smem + (kd == 0 ? L_E1 : L_E2);
    a = pack_ri(re, im);
    issueW(11, wslot(11));
    re = mfma16h(a, F8re, z); im = mfma16h(a, F8im, z);
    if (w == 0) ld4c(fp, fhoff, FL);
    issueW(12, wslot(12));
    twid(re, im, t3c, t3s);
    *(LAS v4h*)(E2 + (o & 7) * E2P + (2 * w + (o >> 3)) * 32 + g * 8) = pack4h(re);
    *(LAS v4h*)(E2 + (o & 7) * E2P + 512 + (2 * w + (o >> 3)) * 32 + g * 8) = pack4h(im);
    issueW(13, wslot(13));
    if (w == 0) { WAIT_VM(4); poll1(fhoff); }
    BARRIER();
    STAMPT(10);
    {
      LAS char* rp = E2 + w * E2P + o * 32 + g * 8;
      a = __builtin_bit_cast(v8h, cat8(*(LAS v4s*)rp, *(LAS v4s*)(rp + 512)));
    }
    issueP(0, IC<0>{}); issueP(0, IC<1>{});
    re = mfma16h(a, F16re, z); im = mfma16h(a, F16im, z);
    issueP(1, IC<0>{}); issueP(1, IC<1>{});
    twid(re, im, t1c, t1s);
    a = pack_ri(re, im);
    re = mfma16h(F16re, a, z); im = mfma16h(F16im, a, z);
    st8c((u32)(2 * kd * 4096 + w * 512 + l * 8), ws + OFF_S2, pack4h(re));
    st8c((u32)((2 * kd + 1) * 4096 + w * 512 + l * 8), ws + OFF_S2, pack4h(im));
  }
  STAMPT(9);

  const int d = ct * 64 + dh * 32 + ((l & 31) ^ (kh << 4));
  const float gst = (float)d * (1.0f / 512.0f), ga0 = (float)(16 * kq + 2 * h) * gst;
  v8h G[8];
  auto gen = [&](auto FI) {
    constexpr int f = FI;
#pragma unroll
    for (int e2 = 0; e2 < 4; ++e2) {
      float a = fmaf((float)(64 * (f >> 1) + 8 * (f & 1) + 4 * (e2 >> 1) + (e2 & 1)), gst, ga0);
      float c = cosr(a), sn = sinr(a);
      if (f == 0 && e2 == 0) { if (kq == 0 && h == 0) { c = 0.5f; sn = (d & 1) ? 0.5f : -0.5f; } }
      G[f][2 * e2] = (_Float16)c; G[f][2 * e2 + 1] = (_Float16)sn;
    }
    PIN(G[f]);
  };
  issueP(2, IC<0>{}); issueP(2, IC<1>{});
  static_for<0, 3>(gen);
  const u32 trS = (u32)(kq * 4096 + (4 * h + q) * 128 + ((ph ^ ((q >> 1) & 1)) << 6) + blk * 32 + pp * 8);
  const u32 hb = (u32)(ph * 8192 + kq * 2048 + l * 16);
  v16f accG[2] = {}, accS[2] = {};
  v8s fa[8], fb[8];
  v4u fs;
  float gv[8];
  const u32 xw = (u32)(w * 6144 + l * 16);
  auto xchg_out = [&](const v16f (&ac)[2], int base) {
    LAS char* X = smem + base + xw;
#pragma unroll
    for (int i = 0; i < 6; ++i) {
      const v16f& a = ac[(i + 2) >> 2]; const int r = ((i + 2) & 3) * 4;
      *(LAS v4f*)(X + i * 1024) = v4f{a[r], a[r + 1], a[r + 2], a[r + 3]};
    }
  };
  auto xchg_in = [&](const v16f (&ac)[2], int base, float (&o8)[8]) {
    v4f s0 = {ac[0][0], ac[0][1], ac[0][2], ac[0][3]}, s1 = {ac[0][4], ac[0][5], ac[0][6], ac[0][7]};
#pragma unroll
    for (int x_ = 1; x_ < 4; ++x_) {
      LAS char* X = smem + base + (w ^ (2 * x_)) * 6144 + (x_ - 1) * 2048 + l * 16;
      s0 += *(LAS v4f*)X; s1 += *(LAS v4f*)(X + 1024);
    }
#pragma unroll
    for (int e = 0; e < 4; ++e) { o8[e] = s0[e]; o8[4 + e] = s1[e]; }
  };
  static_for<0, 10>([&](auto V) {
    constexpr int v = V;
    v8s(&cur)[8] = (v & 1) ? fb : fa;
    v8s(&prv)[8] = (v & 1) ? fa : fb;
    auto mmp = [&](int s0, int s1) {
#pragma unroll
      for (int s = s0; s < s1; ++s) {
        if constexpr (v >= 8) {
          accS[0] = mfma32h(G[(v - 8) * 4 + s], prv[2 * s], accS[0]); accS[1] = mfma32h(G[(v - 8) * 4 + s], prv[2 * s + 1], accS[1]);
        } else if constexpr (v >= 1) {
          accG[0] = mfma32b(Wf[2 * (v - 1) + (s >> 1)][s & 1], prv[2 * s], accG[0]);
          accG[1] = mfma32b(Wf[2 * (v - 1) + (s >> 1)][s & 1], prv[2 * s + 1], accG[1]);
        }
      }
    };
    if constexpr (v < 9) {
      WAIT_VM(v == 0 ? 4 : v == 1 ? 2 : v <= 7 ? 4 : 0);
      if constexpr (v == 5) { if (w == 0) { PIN(fs); poll4(fs, (u32)(FL_S + l * 16)); } }
      BARRIER();
      STAMPT(11 + v);
      if constexpr (v == 1) { if (tid == 0) st4c((u32)(FL_S + b * 4), FL, E); }
      if constexpr (v == 4) { if (w == 0) ld16c(fs, (u32)(FL_S + l * 16), FL); }
      mmp(0, 1); SCHED();
#pragma unroll
      for (int s = 0; s < 4; ++s) {
        if constexpr (v >= 7) {
          LAS char* sb = smem + aslot(2 * v + (s >> 1)) + (s & 1) * 2048;
          cur[2 * s] = cat8(trread(sb + trS), trread(sb + trS + 1024));
          cur[2 * s + 1] = cat8(trread(sb + (trS ^ 64)), trread(sb + (trS ^ 64) + 1024));
        } else {
          LAS char* sb = smem + aslot(2 * v + (s >> 1)) + (s & 1) * 1024;
          cur[2 * s] = *(LAS v8s*)(sb + hb);
          cur[2 * s + 1] = *(LAS v8s*)(sb + (hb ^ 8192));
        }
      }
      if constexpr (v == 0) {
        issueP(3, IC<0>{}); issueP(3, IC<1>{});
        static_for<9, 14>(readW);
        issueP(4, IC<0>{}); issueP(4, IC<1>{});
        gen(IC<3>{}); gen(IC<4>{});
      }
      SCHED();
      if constexpr (v == 1) { issueP(5, IC<0>{}); issueP(5, IC<1>{}); }
      if constexpr (v >= 1 && v <= 6) issueP(2 * v + 4, IC<0>{});
      mmp(1, 2); SCHED();
      if constexpr (v >= 1 && v <= 6) issueP(2 * v + 4, IC<1>{});
      mmp(2, 3); SCHED();
      if constexpr (v >= 1 && v <= 6) issueP(2 * v + 5, IC<0>{});
      mmp(3, 4); SCHED();
      if constexpr (v >= 1 && v <= 6) issueP(2 * v + 5, IC<1>{});
    } else {
      mmp(0, 4);
    }
    if constexpr (v >= 1 && v <= 3) gen(IC<4 + v>{});
    if constexpr (v == 7) xchg_out(accG, 49152);
    if constexpr (v == 8) {
      float o8[8];
      xchg_in(accG, 49152, o8);
      PIN(rs2);
#pragma unroll
      for (int e = 0; e < 8; ++e) {
        float ov = rbf(o8[e] * rs2);
        gv[e] = rbf(ov * __builtin_amdgcn_rcpf(1.0f + __expf(-ov)));
        PIN(gv[e]);
      }
    }
    SCHED();
  });
  xchg_out(accS, 0);
  BARRIER();
  {
    float o8[8], pv[8];
    xchg_in(accS, 0, o8);
#pragma unroll
    for (int e = 0; e < 8; ++e) pv[e] = rbf(o8[e] * oscale) * gv[e];
    u32 a0 = pkbf(pv[0], pv[1]), a1 = pkbf(pv[2], pv[3]), b0 = pkbf(pv[4], pv[5]), b1 = pkbf(pv[6], pv[7]);
    auto r0 = __builtin_amdgcn_permlane32_swap(a0, b0, false, false);
    auto r1 = __builtin_amdgcn_permlane32_swap(a1, b1, false, false);
    v4u pk = {r0[0], r1[0], r0[1], r1[1]};
    PIN(xe);
    v4u ov4;
#pragma unroll
    for (int e = 0; e < 4; ++e) ov4[e] = pkbf(bflo(xe[e]) + bflo(pk[e]), bfhi(xe[e]) + bfhi(pk[e]));
    *(v4u*)((char*)out + eoff) = ov4;
  }
  asm volatile("km_pf_b:");
  STAMPT(20);
  STAMPT(21);
  STAMP_OUT(ws);
}

extern "C" void kernel_launch(void* const* d_in, const int* in_sizes, int n_in, void* d_out, int out_size, void* d_ws,
                              size_t ws_size, hipStream_t stream) {
  kmain<<<256, 512, 0, stream>>>((const u16*)d_in[0], (const u16*)d_in[1], (const u16*)d_in[2], (const u16*)d_in[4],
                                 (const u16*)d_in[5], (const u16*)d_in[6], (char*)d_ws, (u16*)d_out);
}
```

```cpp
#include <hip/hip_runtime.h>
#include <stdint.h>

typedef unsigned short u16;
typedef unsigned int u32;
typedef unsigned long long u64;
typedef short v4s __attribute__((ext_vector_type(4)));
typedef short v8s __attribute__((ext_vector_type(8)));
typedef _Float16 v4h __attribute__((ext_vector_type(4)));
typedef _Float16 v8h __attribute__((ext_vector_type(8)));
typedef __bf16 v2b __attribute__((ext_vector_type(2)));
typedef __bf16 v8b __attribute__((ext_vector_type(8)));
typedef float v2f __attribute__((ext_vector_type(2)));
typedef float v4f __attribute__((ext_vector_type(4)));
typedef float v16f __attribute__((ext_vector_type(16)));
typedef u32 v2u __attribute__((ext_vector_type(2)));
typedef u32 v4u __attribute__((ext_vector_type(4)));

#define LAS __attribute__((address_space(3)))
#define GAS __attribute__((address_space(1)))
#define DEVI __device__ __forceinline__

constexpr int D_ = 512, F_ = 1792, DF_ = 257;
constexpr size_t OFF_H = 0;
constexpr size_t OFF_S1 = (size_t)8 << 20;
constexpr size_t OFF_WT = (size_t)10 << 20;
constexpr size_t OFF_S2 = (size_t)13 << 20;
constexpr size_t OFF_M = (size_t)15 << 20;
constexpr size_t OFF_FL = OFF_M;
constexpr u32 FL_S = 0, FL_A = 1024, FL_H = 2048;
constexpr size_t OFF_BC = OFF_M + 12288;
constexpr size_t OFF_MB = OFF_M + 16384;
constexpr size_t OFF_RS2 = OFF_M + 24576;
constexpr size_t OFF_PQ = OFF_M + 65536;
constexpr size_t OFF_CSP = OFF_M + 131072;
constexpr int SPIN_CAP = 1 << 16;

#define STAMP_DECL
#define STAMPT(id)
#define STAMPC(id)
#define STAMP_OUT(wsp)

template <int I> struct IC { static constexpr int v = I; constexpr operator int() const { return I; } };
template <int B, int E, class Fn> DEVI void static_for(Fn&& f) {
  if constexpr (B < E) { f(IC<B>{}); static_for<B + 1, E>(f); }
}
#define WAIT_VM(n) asm volatile("s_waitcnt vmcnt(%0)" ::"n"(n) : "memory")
#define BARRIER() asm volatile("s_waitcnt lgkmcnt(0)\n\ts_barrier" ::: "memory")
#define SCHED() __builtin_amdgcn_sched_barrier(0)
#define PIN(x) asm volatile("" : "+v"(x))

DEVI int s_of_pi(int pi) {
  int RG = pi >> 6, i = pi & 63;
  return 128 * (4 * (RG & 3) + (i & 3)) + 8 * (i >> 2) + (RG >> 2);
}
DEVI void dma2(u32 voff0, u32 voff1, const char* sbase, u32 lds) {
  asm volatile("s_mov_b32 m0, %3\n\ts_nop 0\n\tglobal_load_lds_dwordx4 %0, %2\n\tglobal_load_lds_dwordx4 %1, %2 offset:1024" ::"v"(voff0), "v"(voff1), "s"(sbase), "s"(lds) : "memory");
}
DEVI void dma2c(u32 voff0, u32 voff1, const char* sbase, u32 lds) {
  asm volatile("s_mov_b32 m0, %3\n\ts_nop 0\n\tglobal_load_lds_dwordx4 %0, %2 sc1\n\tglobal_load_lds_dwordx4 %1, %2 offset:1024 sc1" ::"v"(voff0), "v"(voff1), "s"(sbase), "s"(lds) : "memory");
}
template <int OFF> DEVI void dma1(u32 voff, const char* sbase, u32 lds) {
  asm volatile("s_mov_b32 m0, %2\n\ts_nop 0\n\tglobal_load_lds_dwordx4 %0, %1 offset:%3" ::"v"(voff), "s"(sbase), "s"(lds), "n"(OFF) : "memory");
}
template <int OFF> DEVI void dma1s(u32 voff, const char* sbase, u32 lds) {
  asm volatile("s_mov_b32 m0, %2\n\ts_nop 0\n\tglobal_load_lds_dwordx4 %0, %1 offset:%3 sc1" ::"v"(voff), "s"(sbase), "s"(lds), "n"(OFF) : "memory");
}
DEVI void dma1c(u32 voff, const char* sbase, u32 lds) {
  asm volatile("s_mov_b32 m0, %2\n\ts_nop 0\n\tglobal_load_lds_dword %0, %1 sc1" ::"v"(voff), "s"(sbase), "s"(lds) : "memory");
}
DEVI v2u ld8c_sync(u32 voff, const char* sb) { v2u d; asm volatile("global_load_dwordx2 %0, %1, %2 sc1\n\ts_waitcnt vmcnt(0)" : "=&v"(d) : "v"(voff), "s"(sb) : "memory"); return d; }
DEVI u32 ld4c_sync(u32 voff, const char* sb) { u32 d; asm volatile("global_load_dword %0, %1, %2 sc1\n\ts_waitcnt vmcnt(0)" : "=&v"(d) : "v"(voff), "s"(sb) : "memory"); return d; }
DEVI v4u ld16c_sync(u32 voff, const char* sb) { v4u d; asm volatile("global_load_dwordx4 %0, %1, %2 sc1\n\ts_waitcnt vmcnt(0)" : "=&v"(d) : "v"(voff), "s"(sb) : "memory"); return d; }
template <class T> DEVI void ld16c(T& d, u32 voff, const char* sb) { asm volatile("global_load_dwordx4 %0, %1, %2 sc1" : "=&v"(d) : "v"(voff), "s"(sb) : "memory"); }
template <class T> DEVI void ld8c(T& d, u32 voff, const char* sb) { asm volatile("global_load_dwordx2 %0, %1, %2 sc1" : "=&v"(d) : "v"(voff), "s"(sb) : "memory"); }
template <class T> DEVI void ld4c(T& d, u32 voff, const char* sb) { asm volatile("global_load_dword %0, %1, %2 sc1" : "=&v"(d) : "v"(voff), "s"(sb) : "memory"); }
template <class T> DEVI void ld16(T& d, u32 voff, const char* sb) { asm volatile("global_load_dwordx4 %0, %1, %2" : "=&v"(d) : "v"(voff), "s"(sb) : "memory"); }
DEVI void ld2(u32& d, u32 voff, const char* sb) { asm volatile("global_load_ushort %0, %1, %2" : "=&v"(d) : "v"(voff), "s"(sb) : "memory"); }
template <class T> DEVI void st16c(u32 voff, const char* sb, T v) { asm volatile("global_store_dwordx4 %0, %1, %2 sc1\n\ts_nop 1" ::"v"(voff), "v"(v), "s"(sb) : "memory"); }
template <class T> DEVI void st8c(u32 voff, const char* sb, T v) { asm volatile("global_store_dwordx2 %0, %1, %2 sc1" ::"v"(voff), "v"(v), "s"(sb) : "memory"); }
template <class T> DEVI void st4c(u32 voff, const char* sb, T v) { asm volatile("global_store_dword %0, %1, %2 sc1" ::"v"(voff), "v"(v), "s"(sb) : "memory"); }

DEVI v4s trread(LAS char* p) { return __builtin_amdgcn_ds_read_tr16_b64_v4i16((LAS v4s*)p); }
DEVI v8s cat8(v4s a, v4s b) { return __builtin_shufflevector(a, b, 0, 1, 2, 3, 4, 5, 6, 7); }
DEVI float bflo(u32 v) { return __builtin_bit_cast(float, v << 16); }
DEVI float bfhi(u32 v) { return __builtin_bit_cast(float, v & 0xffff0000u); }
DEVI float rbf(float v) { return (float)(__bf16)v; }
DEVI u32 pkbf(float a, float b) { v2b p = {(__bf16)a, (__bf16)b}; return __builtin_bit_cast(u32, p); }
DEVI u32 pkbf_o(float a, float b) { u32 r = pkbf(a, b); asm("" : "+v"(r)); return r; }
DEVI float cosr(float rev) { return __builtin_amdgcn_cosf(rev); }
DEVI float sinr(float rev) { return __builtin_amdgcn_sinf(rev); }
DEVI v4f mfma16h(v8h a, v8h b, v4f c) { return __builtin_amdgcn_mfma_f32_16x16x32_f16(a, b, c, 0, 0, 0); }
DEVI v4f mfma16b(v8s a, v4u b, v4f c) { return __builtin_amdgcn_mfma_f32_16x16x32_bf16(__builtin_bit_cast(v8b, a), __builtin_bit_cast(v8b, b), c, 0, 0, 0); }
DEVI v16f mfma32b(v8s a, v8s b, v16f c) { return __builtin_amdgcn_mfma_f32_32x32x16_bf16(__builtin_bit_cast(v8b, a), __builtin_bit_cast(v8b, b), c, 0, 0, 0); }
DEVI v16f mfma32h(v8h a, v8s b, v16f c) { return __builtin_amdgcn_mfma_f32_32x32x16_f16(a, __builtin_bit_cast(v8h, b), c, 0, 0, 0); }
DEVI v8h pack_ri(v4f re, v4f im) {
  v8h r;
  r[0] = (_Float16)re[0]; r[1] = (_Float16)re[1]; r[2] = (_Float16)re[2]; r[3] = (_Float16)re[3];
  r[4] = (_Float16)im[0]; r[5] = (_Float16)im[1]; r[6] = (_Float16)im[2]; r[7] = (_Float16)im[3];
  return r;
}
DEVI v4h pack4h(v4f a) { v4h r; r[0] = (_Float16)a[0]; r[1] = (_Float16)a[1]; r[2] = (_Float16)a[2]; r[3] = (_Float16)a[3]; return r; }
DEVI void twid(v4f& re, v4f& im, const v4f& c, const v4f& s) {
#pragma unroll
  for (int r = 0; r < 4; ++r) { float a = re[r], b = im[r]; re[r] = a * c[r] + b * s[r]; im[r] = b * c[r] - a * s[r]; }
}
template <int CTRL> DEVI float dppadd(float v) {
  return v + __builtin_bit_cast(float, __builtin_amdgcn_update_dpp(0, __builtin_bit_cast(int, v), CTRL, 0xf, 0xf, true));
}
DEVI float wave_sum(float v) {
  v = dppadd<0xB1>(v); v = dppadd<0x4E>(v); v = dppadd<0x141>(v); v = dppadd<0x140>(v);
  int bb = __builtin_bit_cast(int, v);
  float a0 = __builtin_bit_cast(float, __builtin_amdgcn_readlane(bb, 0)), a1 = __builtin_bit_cast(float, __builtin_amdgcn_readlane(bb, 16));
  float a2 = __builtin_bit_cast(float, __builtin_amdgcn_readlane(bb, 32)), a3 = __builtin_bit_cast(float, __builtin_amdgcn_readlane(bb, 48));
  return (a0 + a1) + (a2 + a3);
}
DEVI float dot2ones(u32 v, float acc) {
  const v2b ones = {(__bf16)1.0f, (__bf16)1.0f};
  return __builtin_amdgcn_fdot2_f32_bf16(__builtin_bit_cast(v2b, v), ones, acc, false);
}
template <int CTRL> DEVI u32 dppmax(u32 v) { return max(v, (u32)__builtin_amdgcn_update_dpp(0, (int)v, CTRL, 0xf, 0xf, true)); }
DEVI u32 wave_max(u32 v) {
  v = dppmax<0xB1>(v); v = dppmax<0x4E>(v); v = dppmax<0x141>(v); v = dppmax<0x140>(v);
  u32 a0 = __builtin_amdgcn_readlane(v, 0), a1 = __builtin_amdgcn_readlane(v, 16), a2 = __builtin_amdgcn_readlane(v, 32), a3 = __builtin_amdgcn_readlane(v, 48);
  return max(max(a0, a1), max(a2, a3));
}

struct Frag1 { v8s x0, x1, w1, w2; };

constexpr int L_SCR = 147456;
constexpr int L_CS = L_SCR, L_RED = L_SCR + 1024, L_MX = L_SCR + 1088, L_BC0 = L_SCR + 2048, L_BC1 = L_SCR + 2304;
constexpr int L_TAB = L_SCR + 3072;
constexpr int L_T = L_SCR + 7168, L_FA = L_T, L_E1 = L_T, L_E2 = 81920, L_ZX = 81920;
constexpr int L_C1 = 98304, L_IMG = L_C1 + 9216;
constexpr int L_FR = 131072;
constexpr int E1P = 544, E2P = 1024 + 72;
DEVI constexpr int wslot(int n) {
  constexpr int u[14] = {6, 7, 8, 0, 1, 2, 3, 4, 6, 7, 8, 0, 1, 2};
  return u[n] * 16384;
}
DEVI constexpr int aslot(int n) {
  constexpr int u[18] = {3, 4, 5, 9, 2, 6, 7, 8, 0, 1, 3, 4, 5, 9, 2, 6, 7, 8};
  return u[n] * 16384;
}

__global__ __launch_bounds__(512, 2) void kmain(const u16* __restrict__ x, const u16* __restrict__ wr, const u16* __restrict__ wi,
                                                const u16* __restrict__ Wg, const u16* __restrict__ Wu, const u16* __restrict__ Wd,
                                                char* __restrict__ ws, u16* __restrict__ out) {
  __shared__ __attribute__((aligned(16))) char smem_[163840];
  LAS char* smem = (LAS char*)smem_;
  const u32 lbase = (u32)(size_t)smem;
  const int tid = threadIdx.x, l = tid & 63;
  const int w = __builtin_amdgcn_readfirstlane(tid >> 6);
  const int b = blockIdx.x, xcd = b & 7, idx = b >> 3;
  const bool ffn = idx < 28;
  const int g = l >> 4, o = l & 15;
  const int h = l >> 5, blk = (l >> 4) & 1, q = (l & 15) >> 2, pp = l & 3;
  const char* FL = ws + OFF_FL;
  const char* BC = ws + OFF_BC;
  STAMP_DECL; STAMPT(0);

  const int pi = b * 8 + w;
  v4u xr;
  ld16(xr, (u32)(s_of_pi(pi) * 1024 + l * 16), (const char*)x);
  if (w == 0) dma1c((u32)((l & 1) * 4), BC, lbase + L_BC0);
  const int fkb = (b & 7) * 2 + (w >> 2), fcq = w & 3;
  {
    const u32 ks = (u32)((idx >> 1) + 16 * fkb + 256 * (4 * (idx & 1) + ((l >> 3) & 3)));
    const u32 base = ((ks * (DF_ * 2)) & ~15u) + fcq * 128;
    const u32 dlt = (u32)((const char*)wi - (const char*)wr);
    (void)dlt;
    const u32 c0 = ((l & 7) ^ (((l >> 4) & 1) << 2)) << 4;
    if (l < 32) dma1<0>(base + c0, (const char*)wr, lbase + L_FR + w * 2048);
    else dma1<0>(base + c0, (const char*)wi, lbase + L_FR + w * 2048);
    if (fcq == 3) {
      const u32 t0 = min(base + 128 + (c0 & 16), (u32)(2048 * DF_ * 2 - 16));
      if (l < 32) dma1<0>(t0, (const char*)wr, lbase + L_FR + w * 2048 + 1024);
      else dma1<0>(t0, (const char*)wi, lbase + L_FR + w * 2048 + 1024);
    }
  }
  const int p = (xcd >> 1) * 4 + idx / 7, j = (xcd & 1) * 7 + idx % 7;
  const int sh = w >> 2, fq = w & 3;
  u32 aoff[2];
#pragma unroll
  for (int i = 0; i < 2; ++i) {
    int row = (2 * w + i) * 8 + (l >> 3);
    aoff[i] = s_of_pi((p & 15) * 128 + row) * 1024 + (((l & 7) ^ ((row >> 1) & 7)) << 4) - i * 1024;
  }
  const u32 boff = (u32)(2 * w * 4 + (l >> 4)) * (F_ * 2) + j * 256 + (((l & 15) ^ ((l >> 4) << 2)) << 4);
  const u32 boff1 = boff + 4 * F_ * 2 - 1024;
  const int rot = p >> 1;
  auto issue = [&](int t, int slot) {
    const int kt = (t + rot) & 7;
    u32 dst = lbase + slot * 49152 + w * 2048;
    dma2(aoff[0], aoff[1], (const char*)x + kt * 128, dst);
    dma2(boff, boff1, (const char*)Wg + (size_t)kt * (64 * F_ * 2), dst + 16384);
    dma2(boff, boff1, (const char*)Wu + (size_t)kt * (64 * F_ * 2), dst + 32768);
  };
  const int sp = xcd * 4 + (idx - 28);
  if (ffn) issue(0, 0);
  else {
    u64 pc; u32 len;
    asm volatile("s_getpc_b64 %0\nkm_pf_a:\n\ts_mov_b32 %1, km_pf_b-km_pf_a" : "=s"(pc), "=s"(len));
    dma1<0>(min((u32)(((idx & 3) * 8 + w) * 1024 + l * 16), len) & ~15u, (const char*)(pc & ~15ull), lbase);
  }

  u32 E = 0, tick = 0;
  const bool coord = (b == 224) && (w == 0);
  auto do_tick = [&]() { if (coord) { ++tick; if (l == 0) st8c(0u, BC, v2u{E, tick}); } };
  auto get_epoch = [&]() {
    const v2u b0 = *(LAS v2u*)(smem + L_BC0);
    v2u b1 = *(LAS v2u*)(smem + L_BC1);
    int spins = 0;
    while (__builtin_amdgcn_readfirstlane((b0[0] ^ b1[0]) | (b0[1] ^ b1[1])) == 0 && ++spins < SPIN_CAP) {
      __builtin_amdgcn_s_sleep(4);
      b1 = ld8c_sync(0u, BC);
    }
    E = __builtin_amdgcn_readfirstlane(b1[0]);
  };
  auto signal_a = [&]() {
    v4u r0 = *(LAS v4u*)(smem + L_RED), r1 = *(LAS v4u*)(smem + L_RED + 16);
    u32 m = max(max(max(r0[0], r0[1]), max(r0[2], r0[3])), max(max(r1[0], r1[1]), max(r1[2], r1[3])));
    if (l == 0) { st4c((u32)(FL_A + b * 4), FL, E | (m >> 7)); *(LAS u32*)(smem + L_MX + 4) = E; }
  };
  auto poll4 = [&](v4u f, u32 voff) -> v4u {
    int spins = 0;
    while (!__all((((f[0] ^ E) | (f[1] ^ E) | (f[2] ^ E) | (f[3] ^ E)) >> 8) == 0) && ++spins < SPIN_CAP) {
      do_tick();
      __builtin_amdgcn_s_sleep(12);
      f = ld16c_sync(voff, FL);
    }
    return f;
  };
  auto check_a = [&](v4u f) {
    f = poll4(f, (u32)(FL_A + l * 16));
    u32 m = wave_max(max(max(f[0], f[1]), max(f[2], f[3]))) & 0xffu;
    if (l == 0) *(LAS u32*)(smem + L_MX) = m;
  };
  if (coord) {
    v4u f[10];
#pragma unroll
    for (int i = 0; i < 10; ++i) ld16c(f[i], (u32)(i * 1024 + l * 16), FL);
    WAIT_VM(0);
    u32 c = __builtin_amdgcn_readfirstlane(*(LAS u32*)(smem + L_BC0)) >> 8;
    bool hit;
    do {
      c = (c + 1) & 0xffffffu; hit = false;
#pragma unroll
      for (int i = 0; i < 10; ++i) { PIN(f[i]); hit |= ((f[i][0] >> 8) == c) | ((f[i][1] >> 8) == c) | ((f[i][2] >> 8) == c) | ((f[i][3] >> 8) == c); }
    } while (__any(hit));
    E = c << 8;
    do_tick();
  }

  {
    const float fo = (float)o, fg = (float)(4 * g);
    if (w < 2) {
      const float st = fo * (1.0f / 32.0f), a0 = fmaf(fg, st, w ? 0.25f : 0.f);
      v4u hi, lo;
#pragma unroll
      for (int e2 = 0; e2 < 4; ++e2) {
        float c0 = cosr(fmaf((float)(16 * (e2 >> 1) + 2 * (e2 & 1)), st, a0)), c1 = cosr(fmaf((float)(16 * (e2 >> 1) + 2 * (e2 & 1) + 1), st, a0));
        u32 ch = pkbf_o(c0, c1);
        hi[e2] = ch; lo[e2] = pkbf(c0 - bflo(ch), c1 - bfhi(ch));
      }
      *(LAS v4u*)(smem + L_C1 + w * 2048 + l * 16) = hi; *(LAS v4u*)(smem + L_C1 + w * 2048 + 1024 + l * 16) = lo;
    } else if (w < 5) {
      const float st = w == 2 ? (float)(o & 7) * (1.0f / 16.0f) : fo * (w == 3 ? 1.0f / 16.0f : 1.0f / 8.0f);
      const float a0 = (w == 2 && o >= 8) ? 0.25f : 0.f;
      const float sc = w == 2 ? 0.0625f : ((w == 4 && (g >> 1) != (o >> 3)) ? 0.f : 1.0f);
      v8h m;
#pragma unroll
      for (int e = 0; e < 4; ++e) { float a = fmaf(fg + (float)e, st, a0); m[e] = (_Float16)(sc * cosr(a)); m[4 + e] = (_Float16)(sc * sinr(a)); }
      *(LAS v8h*)(smem + (w == 2 ? L_C1 + 4096 : L_TAB + (w - 3) * 1024) + l * 16) = m;
    } else {
      const float st = w == 7 ? fo * (1.0f / 256.0f) : (fo + (w == 6 ? 16.f : 0.f)) * (1.0f / 512.0f);
      v4f c, sn;
#pragma unroll
      for (int e = 0; e < 4; ++e) { float a = (fg + (float)e) * st; c[e] = cosr(a); sn[e] = sinr(a); }
      LAS char* dst = smem + (w == 7 ? L_TAB + 2048 : L_C1 + 5120 + (w - 5) * 2048) + l * 16;
      *(LAS v4f*)dst = c; *(LAS v4f*)(dst + 1024) = sn;
    }
    BARRIER();
    if (ffn) issue(1, 1);
    LAS char* C1 = smem + L_C1 + l * 16;
    const v4u Fhr = *(LAS v4u*)C1, Flr = *(LAS v4u*)(C1 + 1024), Fhi = *(LAS v4u*)(C1 + 2048), Fli = *(LAS v4u*)(C1 + 3072);
    const v8h M2 = *(LAS v8h*)(C1 + 4096);
    v4f twc[2], tws[2];
#pragma unroll
    for (int u = 0; u < 2; ++u) { twc[u] = *(LAS v4f*)(C1 + 5120 + u * 2048); tws[u] = *(LAS v4f*)(C1 + 6144 + u * 2048); }
    if (ffn) WAIT_VM(14); else WAIT_VM(3);
    PIN(xr);
    STAMPT(3);
    const float c0 = bflo(__builtin_amdgcn_readfirstlane(xr[0]));
    v2f xv[4], s2 = {0.f, 0.f}, q2 = {0.f, 0.f};
#pragma unroll
    for (int e = 0; e < 4; ++e) {
      xv[e] = v2f{bflo(xr[e]), bfhi(xr[e])};
      v2f t = xv[e] - v2f{c0, c0};
      s2 += t; q2 = __builtin_elementwise_fma(t, t, q2);
    }
    const float dmean = wave_sum(s2[0] + s2[1]) * (1.0f / 512.0f), msq = wave_sum(q2[0] + q2[1]) * (1.0f / 512.0f);
    do_tick();
    const float mb = rbf(c0 + dmean);
    const float var = rbf(fmaxf(msq - dmean * dmean, 0.f));
    const float sd = rbf(__builtin_amdgcn_sqrtf(rbf(var + 1e-5f)));
    const float rs = __builtin_amdgcn_rcpf(sd);
#pragma unroll
    for (int e = 0; e < 4; ++e) {
      v2f t = xv[e] - v2f{mb, mb};
      u32 tb = pkbf_o(t[0], t[1]);
      t = v2f{bflo(tb), bfhi(tb)} * v2f{rs, rs};
      xr[e] = pkbf(t[0], t[1]);
    }
    *(LAS v4u*)(smem + L_IMG + w * 1024 + l * 16) = xr;
    if (l < 2) st4c((u32)(pi * 4 + l * 8192), ws + OFF_MB, l ? rs * rs : -mb);
    const v8s xf = cat8(trread(smem + L_IMG + w * 1024 + l * 8), trread(smem + L_IMG + w * 1024 + 512 + l * 8));
    const v4u sg = {0x80000000u, 0x80000000u, 0x80000000u, 0x80000000u};
    const v4f z = {0.f, 0.f, 0.f, 0.f};
    v4f dre[2], dim[2];
#pragma unroll
    for (int u = 0; u < 2; ++u) { dre[u] = mfma16b(xf, u ? Flr ^ sg : Flr, z); dim[u] = mfma16b(xf, u ? Fli ^ sg : Fli, z); }
#pragma unroll
    for (int u = 0; u < 2; ++u) { dre[u] = mfma16b(xf, u ? Fhr ^ sg : Fhr, dre[u]); dim[u] = mfma16b(xf, u ? Fhi ^ sg : Fhi, dim[u]); }
#pragma unroll
    for (int u = 0; u < 2; ++u) {
      twid(dre[u], dim[u], twc[u], tws[u]);
      const v8h bb = pack_ri(dre[u], dim[u]);
      v4f d2 = mfma16h(bb, M2, z);
      if (u == 0) {
        const u32 nv = o == 8 ? 0xac002c00u : 0u;
        v4f n = mfma16h(bb, __builtin_bit_cast(v8h, v4u{nv, nv, 0u, 0u}), z);
        if (l == 8) d2[0] = n[0];
      }
      *(LAS v4h*)(smem + L_T + ((o >> 3) * 8 + w) * 544 + (o & 7) * 64 + u * 32 + g * 8) = pack4h(d2);
    }
    STAMPT(4);
  }
  auto round1_out = [&]() {
    LAS char* rp = smem + L_T + (4 * (g & 1) + (o >> 2)) * 544 + (w * 32 + (g >> 1) * 16) * 2 + (o & 3) * 8;
    const v8s su = cat8(trread(rp), trread(rp + 8 * 544));
    LAS char* fp_ = smem + L_FR + w * 2048 + q * 128 + (((2 * g + (pp >> 1)) ^ ((q >> 1) << 2)) << 4) + (pp & 1) * 8;
    const v4u fu = __builtin_bit_cast(v4u, cat8(trread(fp_), trread(fp_ + 512)));
    st16c((u32)(b * 8192 + (w * 32 + (g >> 1) * 16 + o) * 32 + (g & 1) * 16), ws + OFF_S1, su);
    st16c((u32)((idx * 16 + fkb) * 4608 + (fcq * 64 + l) * 16), ws + OFF_WT, fu);
    typedef unsigned short v2us __attribute__((ext_vector_type(2)));
    const int dl = (idx >> 1) & 7;
    u32 msk = (fcq == 0 && l < dl) ? 0u : 0x7fff7fffu;
    v2us m2 = {0, 0};
#pragma unroll
    for (int e = 0; e < 4; ++e) { u32 t = fu[e] & msk; m2 = __builtin_elementwise_max(m2, __builtin_bit_cast(v2us, t)); }
    if (fcq == 3) {
      LAS char* tp = smem + L_FR + w * 2048 + 1024 + q * 128 + (pp >> 1) * 16 + (pp & 1) * 8;
      const v4u tu = __builtin_bit_cast(v4u, cat8(trread(tp), trread(tp + 512)));
      if (l < 16) st16c((u32)((idx * 16 + fkb) * 4608 + (256 + l) * 16), ws + OFF_WT, tu);
      msk = (o <= dl) ? 0x7fff7fffu : 0u;
#pragma unroll
      for (int e = 0; e < 4; ++e) { u32 t = tu[e] & msk; m2 = __builtin_elementwise_max(m2, __builtin_bit_cast(v2us, t)); }
    }
    const u32 mx = wave_max(max((u32)m2[0], (u32)m2[1]));
    if (l == 0) *(LAS u32*)(smem + L_RED + w * 4) = mx;
    if (w == 0 && !coord) dma1c((u32)((l & 1) * 4), BC, lbase + L_BC1);
  };

  const int RG = xcd * 4 + (idx >> 3), ct = idx & 7;
  const int kd = xcd * 32 + idx;
  const u32 swz = ((l & 7) ^ (((l >> 4) & 1) << 2)) << 4;
  const u32 ro0 = (u32)((l >> 3) * 1024) + swz, ro1 = ro0 + 8192 - 1024;
  const char* bsrc = (const char*)Wd + (size_t)(w * 16) * 1024 + ct * 128;
  auto issueW = [&](int n) { dma2(ro0, ro1, bsrc + (size_t)n * 131072, lbase + wslot(n) + w * 2048); };
  const int dh = w & 1, kq = w >> 1, ph = kq >> 1, kh = kq & 1;
  const int epi = RG * 64 + ph * 32 + (l & 31);
  const u32 eoff = (u32)(s_of_pi(epi) * D_ + ct * 64 + dh * 32 + kh * 16 + h * 8) * 2;
  v4u wt, s1v, xe; v4f pq[4]; float rs2;
  auto load_xe = [&]() { ld16(xe, eoff, (const char*)x); };
  auto load_s1 = [&]() { ld16c(s1v, (u32)((((4 * w + g) * 8 + (o >> 1)) * 256 + kd) * 32 + (o & 1) * 16), ws + OFF_S1); };
  auto load_wt = [&]() { ld16c(wt, (u32)((w * 64 + l) * 4608 + (kd + ((2 * w + (g >> 1)) & 7)) * 16), ws + OFF_WT); };
  auto load_rs = [&]() { ld4c(rs2, (u32)(epi * 4), ws + OFF_RS2); };
  v4f t2c, t2s, t3c, t3s;
  auto trig2 = [&]() {
    const float fw = (float)w, st2 = fw * (1.0f / 128.0f), a2 = fw * (float)(o + 64 * g) * (1.0f / 2048.0f);
#pragma unroll
    for (int e = 0; e < 4; ++e) { float b2 = fmaf((float)e, st2, a2); t2c[e] = cosr(b2); t2s[e] = sinr(b2); }
    PIN(t2c); PIN(t2s);
  };
  auto trig3 = [&]() {
    const float f7 = (float)(o & 7), st3 = f7 * (1.0f / 128.0f), a3 = f7 * (float)(2 * w + (o >> 3) + 64 * g) * (1.0f / 2048.0f);
#pragma unroll
    for (int e = 0; e < 4; ++e) { float b3 = fmaf((float)e, st3, a3); t3c[e] = cosr(b3); t3s[e] = sinr(b3); }
    PIN(t3c); PIN(t3s);
  };
  v4u f16; v4f t1c, t1s, re, im;
  const v4f z4 = {0.f, 0.f, 0.f, 0.f};
  auto imf = [](v4u f) { return __builtin_bit_cast(v8h, v4u{f[2] ^ 0x80008000u, f[3] ^ 0x80008000u, f[0], f[1]}); };
  auto segA0 = [&]() {
    f16 = *(LAS v4u*)(smem + L_TAB + l * 16);
    t1c = *(LAS v4f*)(smem + L_TAB + 2048 + l * 16); t1s = *(LAS v4f*)(smem + L_TAB + 3072 + l * 16);
  };
  auto segA1 = [&]() {
    PIN(s1v);
    const v8h a = __builtin_bit_cast(v8h, s1v);
    re = mfma16h(a, __builtin_bit_cast(v8h, f16), z4); im = mfma16h(a, imf(f16), z4);
  };
  auto segA2 = [&]() {
    twid(re, im, t1c, t1s);
    const v8h a = pack_ri(re, im);
    re = mfma16h(__builtin_bit_cast(v8h, f16), a, z4); im = mfma16h(imf(f16), a, z4);
  };
  auto segA3 = [&]() {
    twid(re, im, t2c, t2s);
    *(LAS v4h*)(smem + L_E1 + w * E1P + o * 32 + g * 8) = pack4h(re);
    *(LAS v4h*)(smem + L_E1 + (8 + w) * E1P + o * 32 + g * 8) = pack4h(im);
  };

  if (ffn) {
    v16f acc[2][2] = {};
    float mbr[2];
    const u32 wb0 = (u32)((8 * h + q) * 256 + ((fq ^ q) << 6) + blk * 32 + pp * 8);
    const u32 r1 = 16384 + sh * 16384, r2 = 32768 - sh * 16384;
    const int xrow = sh * 64 + (l & 31);
    const u32 xb0 = (u32)(xrow * 128 + ((h ^ ((xrow >> 1) & 7)) << 4));
    auto rd = [&](Frag1& f, LAS char* sb, int i) {
      LAS char* xa = sb + (xb0 ^ (i << 5));
      LAS char* wa_ = sb + wb0 + (i << 12);
      f.w1 = cat8(trread(wa_ + r1), trread(wa_ + r1 + 1024));
      f.x0 = *(LAS v8s*)xa; f.x1 = *(LAS v8s*)(xa + 4096);
      f.w2 = cat8(trread(wa_ + r2), trread(wa_ + r2 + 1024));
    };
    const u32 one = ((l & 15) == ((l >> 4) & 1)) ? 0x3f803f80u : 0u;
    const v8s sel = __builtin_bit_cast(v8s, v4u{one, one, one, one});
    v4f csa = {0.f, 0.f, 0.f, 0.f};
    Frag1 A, B;
    WAIT_VM(7);
    BARRIER();
    STAMPT(1); STAMPC(30);
    rd(A, smem, 0);
    dma2(aoff[0], aoff[1], (const char*)x + ((2 + rot) & 7) * 128, lbase + 2 * 49152 + w * 2048);
    v4u cs1, cs2;
    static_for<0, 8>([&](auto T) {
      constexpr int t = T, slot = t % 3, nslot = (t + 1) % 3, pslot = (t + 2) % 3;
      LAS char* sb = smem + slot * 49152;
      {
        const int kt = (t + 2 + rot) & 7;
        const char* s1 = t < 6 ? (const char*)Wg + (size_t)kt * (64 * F_ * 2) : bsrc + (size_t)((t - 6) * 3 + 1) * 131072;
        const char* s2 = t < 6 ? (const char*)Wu + (size_t)kt * (64 * F_ * 2) : bsrc + (size_t)((t - 6) * 3 + 2) * 131072;
        const u32 o2 = t < 6 ? boff : ro0, o3 = t < 6 ? boff1 : ro1;
        const u32 dst = lbase + pslot * 49152 + w * 2048;
        rd(B, sb, 1); SCHED();
        acc[0][0] = mfma32b(A.w1, A.x0, acc[0][0]); acc[0][1] = mfma32b(A.w1, A.x1, acc[0][1]);
        if constexpr (t == 0) csa = mfma16b(sel, __builtin_bit_cast(v4u, A.w1), csa);
        SCHED();
        dma1<0>(o2, s1, dst + 16384);
        if constexpr (t == 2) load_xe();
        if constexpr (t == 6) load_wt();
        acc[1][0] = mfma32b(A.w2, A.x0, acc[1][0]); acc[1][1] = mfma32b(A.w2, A.x1, acc[1][1]); SCHED();
        if constexpr (t == 0) { round1_out(); SCHED(); }
        if constexpr (t == 3) { trig2(); SCHED(); }
        if constexpr (t == 4) { trig3(); SCHED(); }
        rd(A, sb, 2); SCHED();
        acc[0][0] = mfma32b(B.w1, B.x0, acc[0][0]); SCHED();
        dma1<1024>(o3, s1, dst + 16384);
        acc[0][1] = mfma32b(B.w1, B.x1, acc[0][1]);
        if constexpr (t == 0) csa = mfma16b(sel, __builtin_bit_cast(v4u, B.w1), csa);
        acc[1][0] = mfma32b(B.w2, B.x0, acc[1][0]); acc[1][1] = mfma32b(B.w2, B.x1, acc[1][1]); SCHED();
        dma1<0>(o2, s2, dst + 32768);
        if constexpr (t == 6) {
          load_rs();
          if (kd == 0) {
#pragma unroll
            for (int r = 0; r < 4; ++r) ld16c(pq[r], (u32)(w * 4096 + r * 1024 + l * 16), ws + OFF_PQ);
          }
        }
        if constexpr (t == 7) { WAIT_VM(13); segA1(); SCHED(); }
        rd(B, sb, 3); SCHED();
        acc[0][0] = mfma32b(A.w1, A.x0, acc[0][0]); acc[0][1] = mfma32b(A.w1, A.x1, acc[0][1]);
        if constexpr (t == 0) csa = mfma16b(sel, __builtin_bit_cast(v4u, A.w1), csa);
        SCHED();
        dma1<1024>(o3, s2, dst + 32768);
        acc[1][0] = mfma32b(A.w2, A.x0, acc[1][0]); acc[1][1] = mfma32b(A.w2, A.x1, acc[1][1]); SCHED();
      }
      if constexpr (t < 7) {
        const char* s0 = t < 5 ? (const char*)x + ((t + 3 + rot) & 7) * 128 : bsrc + (size_t)((t - 5) * 3) * 131072;
        const u32 o0 = t < 5 ? aoff[0] : ro0, o1 = t < 5 ? aoff[1] : ro1;
        const u32 dst = lbase + slot * 49152 + w * 2048;
        if constexpr (t == 0) csa = mfma16b(sel, __builtin_bit_cast(v4u, B.w1), csa);
        WAIT_VM(t == 0 ? 8 : t == 2 ? 7 : t == 6 ? 13 : 6);
        if constexpr (t == 5) { if (w == 0) check_a(*(LAS v4u*)(smem + L_FA + l * 16)); }
        BARRIER();
        STAMPT(22 + t);
        acc[0][0] = mfma32b(B.w1, B.x0, acc[0][0]); SCHED();
        rd(A, smem + nslot * 49152, 0);
        if constexpr (t == 6) segA0();
        SCHED();
        if constexpr (t == 0) {
          if (l < 16) {
#pragma unroll
            for (int i = 0; i < 2; ++i) {
              const float hi = rbf(csa[i]);
              st4c((u32)((((j * 2 + (p & 1)) * 256 + sh * 128 + fq * 32 + i * 16 + l) * 8 + rot) * 4), ws + OFF_CSP, pkbf(hi, csa[i] - hi));
            }
          }
        }
        if constexpr (t == 4) { if (w == 0) dma1s<0>((u32)(FL_A + l * 16), FL, lbase + L_FA); }
        if constexpr (t == 5) {
          const u32 co = (u32)(((j * 2 + (p & 1)) * 256 + fq * 32 + (l & 31)) * 32 + h * 16);
          ld16c(cs1, co + sh * 4096, ws + OFF_CSP); ld16c(cs2, co + (1 - sh) * 4096, ws + OFF_CSP);
          ld4c(mbr[0], (u32)((p * 128 + xrow) * 4), ws + OFF_MB); ld4c(mbr[1], (u32)((p * 128 + xrow + 32) * 4), ws + OFF_MB);
          load_s1();
        }
        dma1<0>(o0, s0, dst);
        acc[0][1] = mfma32b(B.w1, B.x1, acc[0][1]);
        acc[1][0] = mfma32b(B.w2, B.x0, acc[1][0]);
        acc[1][1] = mfma32b(B.w2, B.x1, acc[1][1]); SCHED();
        dma1<1024>(o1, s0, dst);
        if constexpr (t == 1) { if (w == 0) { get_epoch(); signal_a(); } }
        SCHED();
      } else {
        segA2(); SCHED();
        PIN(cs1); PIN(cs2); PIN(mbr[0]); PIN(mbr[1]);
        const u32 m0_ = pkbf(mbr[0], mbr[0]), m1_ = pkbf(mbr[1], mbr[1]);
        const v8s mb0 = __builtin_bit_cast(v8s, v4u{m0_, m0_, m0_, m0_}), mb1 = __builtin_bit_cast(v8s, v4u{m1_, m1_, m1_, m1_});
        acc[0][0] = mfma32b(B.w1, B.x0, acc[0][0]); acc[1][0] = mfma32b(B.w2, B.x0, acc[1][0]);
        acc[0][0] = mfma32b(__builtin_bit_cast(v8s, cs1), mb0, acc[0][0]); acc[1][0] = mfma32b(__builtin_bit_cast(v8s, cs2), mb0, acc[1][0]);
        acc[0][1] = mfma32b(B.w1, B.x1, acc[0][1]); acc[1][1] = mfma32b(B.w2, B.x1, acc[1][1]);
        acc[0][1] = mfma32b(__builtin_bit_cast(v8s, cs1), mb1, acc[0][1]); acc[1][1] = mfma32b(__builtin_bit_cast(v8s, cs2), mb1, acc[1][1]); SCHED();
      }
    });
    STAMPT(2); STAMPC(31);
    auto fin = [&](const v16f& ag, const v16f& au, int rt) {
#pragma unroll
      for (int s2i = 0; s2i < 2; ++s2i) {
        v4u ov;
#pragma unroll
        for (int e = 0; e < 4; ++e) {
          const int r = s2i * 8 + 2 * e;
          v2f gg = v2f{fmaxf(ag[r], 0.f), fmaxf(ag[r + 1], 0.f)} * v2f{au[r], au[r + 1]};
          ov[e] = pkbf(gg[0], gg[1]);
        }
        st16c((u32)(((p * 4 + sh * 2 + rt) * 112 + (j * 8 + fq * 2 + s2i)) * 1024 + l * 16), ws + OFF_H, ov);
      }
    };
    if (sh == 0) { fin(acc[0][0], acc[1][0], 0); fin(acc[0][1], acc[1][1], 1); }
    else { fin(acc[1][0], acc[0][0], 0); fin(acc[1][1], acc[0][1], 1); }
    STAMPT(6);
    segA3();
    WAIT_VM(4);
  } else {
    WAIT_VM(1);
    BARRIER();
    round1_out();
    do_tick();
    if (w == 7) {
      u32 sq[8];
      const int kss = (sp >> 1) + 16 * (l >> 2) + 256 * (4 * (sp & 1) + (l & 3)), km = (2048 - kss) & 2047;
      const u32 o1 = (u32)(kss * DF_ * 2), o2 = (u32)(km * DF_ * 2);
      ld2(sq[0], o1, (const char*)wr); ld2(sq[1], o1, (const char*)wi); ld2(sq[2], o2, (const char*)wr); ld2(sq[3], o2, (const char*)wi);
      ld2(sq[4], o1 + 512, (const char*)wr); ld2(sq[5], o1 + 512, (const char*)wi); ld2(sq[6], o2 + 512, (const char*)wr); ld2(sq[7], o2 + 512, (const char*)wi);
      WAIT_VM(0);
#pragma unroll
      for (int i = 0; i < 8; ++i) PIN(sq[i]);
      float war = bflo(sq[0]) + bflo(sq[2]), wai = bflo(sq[1]) - bflo(sq[3]);
      float wbr = bflo(sq[4]) + bflo(sq[6]), wbi = bflo(sq[5]) - bflo(sq[7]);
      v4f pv = {0.25f * (war + wbr), 0.25f * (wai + wbi), 0.25f * (war - wbr), 0.25f * (wai - wbi)};
      st16c((u32)((((sp >> 2) * 4 + (l & 3)) * 64 + (sp & 3) * 16 + (l >> 2)) * 16), ws + OFF_PQ, pv);
    }
    WAIT_VM(0);
    BARRIER();
    STAMPT(1); STAMPC(30);
    if (w == 0) { if (!coord) get_epoch(); signal_a(); }
    dma1c((u32)(sp * 57344 + (w * 112 + l) * 64), (const char*)Wd, lbase + 49152);
    dma1c((u32)(sp * 57344 + (w * 112 + 48 + l) * 64), (const char*)Wd, lbase + 49152);
    static_for<0, 6>([&](auto N) { issueW(N); });
#pragma unroll
    for (int i = 0; i < 8; ++i) dma1c((u32)(786432 + (idx & 3) * 262144 + ((i * 8 + w) * 64 + l) * 64), (const char*)Wd, lbase + 49152);
    load_xe();
    trig2(); trig3(); segA0();
    if (w == 0) check_a(v4u{~E, 0u, 0u, 0u});
    BARRIER();
    load_s1(); load_wt(); load_rs();
    WAIT_VM(0);
    segA1(); segA2(); segA3();
    STAMPT(2); STAMPC(31);
  }
  PIN(wt); PIN(xe); PIN(rs2);
  BARRIER();
  STAMPT(5);

  const u32 trG = (u32)(kq * 4096 + (4 * h + q) * 128 + ((dh ^ ((q >> 1) & 1)) << 6) + (blk ^ kh) * 32 + pp * 8);
  v8s Wf[14][2];
  auto readW = [&](auto N) {
    constexpr int n = N;
    LAS char* sb = smem + wslot(n) + trG;
    Wf[n][0] = cat8(trread(sb), trread(sb + 1024)); Wf[n][1] = cat8(trread(sb + 2048), trread(sb + 3072));
  };
  const char* hsrc = ws + OFF_H + ((size_t)(RG * 2 + (w >> 2)) * 112 + (w & 3) * 2) * 1024;
  const u32 so0 = (u32)((l >> 3) * 4096) + swz, so1 = so0 + 32768 - 1024;
  const char* ssrc = ws + OFF_S2 + (size_t)(w * 16) * 4096 + RG * 128;
  auto issueP = [&](int n, auto HALF) {
    constexpr int hf = HALF;
    if (n < 14) dma1s<hf * 1024>((u32)(l * 16), hsrc + n * 8192, lbase + aslot(n) + w * 2048);
    else dma1s<hf * 1024>(hf ? so1 : so0, ssrc + (size_t)(n - 14) * (128 * 4096), lbase + aslot(n) + w * 2048);
  };
  const int d = ct * 64 + dh * 32 + ((l & 31) ^ (kh << 4));
  const float gst = (float)d * (1.0f / 512.0f), ga0 = (float)(16 * kq + 2 * h) * gst;
  v8h G[8];
  auto gen = [&](auto FI) {
    constexpr int f = FI;
#pragma unroll
    for (int e2 = 0; e2 < 4; ++e2) {
      float a = fmaf((float)(64 * (f >> 1) + 8 * (f & 1) + 4 * (e2 >> 1) + (e2 & 1)), gst, ga0);
      float c = cosr(a), sn = sinr(a);
      if (f == 0 && e2 == 0) { if (kq == 0 && h == 0) { c = 0.5f; sn = (d & 1) ? 0.5f : -0.5f; } }
      G[f][2 * e2] = (_Float16)c; G[f][2 * e2 + 1] = (_Float16)sn;
    }
    PIN(G[f]);
  };

  float oscale;
  const u32 fhoff = (u32)(FL_H + (RG * 64 + min(l, 55)) * 4);
  u32 fp;
  {
    v8h a;
    {
      LAS char* rp = smem + L_E1 + (4 * (g & 1) + (o >> 2)) * E1P + (2 * w + (g >> 1)) * 32 + (o & 3) * 8;
      a = __builtin_bit_cast(v8h, cat8(trread(rp), trread(rp + 8 * E1P)));
    }
    const v4u f8 = *(LAS v4u*)(smem + L_TAB + 1024 + l * 16);
    const v2u me = *(LAS v2u*)(smem + L_MX);
    issueW(6);
    static_for<0, 3>(readW);
    const int Ex = (int)me[0] < 16 ? 16 : ((int)me[0] > 240 ? 240 : (int)me[0]);
    const float wscale = __builtin_bit_cast(float, (u32)(246 - Ex) << 23);
    oscale = __builtin_bit_cast(float, (u32)(Ex - 7) << 23);
    re = mfma16h(__builtin_bit_cast(v8h, f8), a, z4); im = mfma16h(imf(f8), a, z4);
    issueW(7);
    static_for<3, 6>(readW);
    if (kd != 0) {
#pragma unroll
      for (int r = 0; r < 4; ++r) {
        float fr = ((r & 1) ? bfhi(wt[r >> 1]) : bflo(wt[r >> 1])) * wscale, fi = ((r & 1) ? bfhi(wt[2 + (r >> 1)]) : bflo(wt[2 + (r >> 1)])) * wscale;
        float zr = re[r], zi = im[r];
        re[r] = zr * fr - zi * fi;
        im[r] = -(zr * fi + zi * fr);
      }
    } else {
      const int ka = 2 * w + (g >> 1), kr = o + 64 * (g & 1);
#pragma unroll
      for (int r = 0; r < 4; ++r) { v2f t = {re[r], im[r]}; *(LAS v2f*)(smem + L_ZX + ka * 1024 + (kr + 16 * r) * 8) = t; }
      BARRIER();
#pragma unroll
      for (int r = 0; r < 4; ++r) {
        PIN(pq[r]);
        const int km = (2048 - (ka + 16 * (kr + 16 * r))) & 2047;
        v2f zm = *(LAS v2f*)(smem + L_ZX + (km & 15) * 1024 + (km >> 4) * 8);
        float zr = re[r], zi = im[r];
        re[r] = (zr * pq[r][0] - zi * pq[r][1] + zm[0] * pq[r][2] + zm[1] * pq[r][3]) * wscale;
        im[r] = -(zr * pq[r][1] + zi * pq[r][0] + zm[0] * pq[r][3] - zm[1] * pq[r][2]) * wscale;
      }
    }
    LAS char* E2 = smem + (kd == 0 ? L_E1 : L_E2);
    a = pack_ri(re, im);
    re = mfma16h(a, __builtin_bit_cast(v8h, f8), z4); im = mfma16h(a, imf(f8), z4);
    twid(re, im, t3c, t3s);
    *(LAS v4h*)(E2 + (o & 7) * E2P + (2 * w + (o >> 3)) * 32 + g * 8) = pack4h(re);
    *(LAS v4h*)(E2 + (o & 7) * E2P + 512 + (2 * w + (o >> 3)) * 32 + g * 8) = pack4h(im);
    BARRIER();
    STAMPT(8);
    {
      LAS char* rp = E2 + w * E2P + o * 32 + g * 8;
      a = __builtin_bit_cast(v8h, cat8(*(LAS v4s*)rp, *(LAS v4s*)(rp + 512)));
    }
    issueW(8);
    re = mfma16h(a, __builtin_bit_cast(v8h, f16), z4); im = mfma16h(a, imf(f16), z4);
    WAIT_VM(6);
    if (ffn && l == 0) st4c((u32)(FL_H + ((p * 2 + sh) * 64 + j * 4 + fq) * 4), FL, me[1]);
    issueW(9);
    twid(re, im, t1c, t1s);
    a = pack_ri(re, im);
    re = mfma16h(__builtin_bit_cast(v8h, f16), a, z4); im = mfma16h(imf(f16), a, z4);
    issueW(10);
    st8c((u32)(2 * kd * 4096 + w * 512 + l * 8), ws + OFF_S2, pack4h(re));
    st8c((u32)((2 * kd + 1) * 4096 + w * 512 + l * 8), ws + OFF_S2, pack4h(im));
  }
  STAMPT(9);
  gen(IC<0>{}); issueW(11);
  WAIT_VM(8);
  BARRIER();
  static_for<6, 9>(readW);
  gen(IC<1>{}); issueW(12); gen(IC<2>{}); issueW(13);
  if (w == 0) ld4c(fp, fhoff, FL);
  gen(IC<3>{}); gen(IC<4>{});
  WAIT_VM(0);
  if (w == 0) {
    PIN(fp);
    u32 f = fp;
    int spins = 0;
    while (!__all(f == E) && ++spins < SPIN_CAP) {
      __builtin_amdgcn_s_sleep(2);
      f = ld4c_sync(fhoff, FL);
    }
  }
  BARRIER();
  STAMPT(10);
  if (tid == 0) st4c((u32)(FL_S + b * 4), FL, E);
  issueP(0, IC<0>{}); issueP(0, IC<1>{}); issueP(1, IC<0>{}); issueP(1, IC<1>{});
  static_for<9, 14>(readW);
  gen(IC<5>{});
  issueP(2, IC<0>{}); issueP(2, IC<1>{});
  gen(IC<6>{});
  issueP(3, IC<0>{}); issueP(3, IC<1>{});
  gen(IC<7>{});

  const u32 trS = (u32)(kq * 4096 + (4 * h + q) * 128 + ((ph ^ ((q >> 1) & 1)) << 6) + blk * 32 + pp * 8);
  const u32 hb = (u32)(ph * 8192 + kq * 2048 + l * 16);
  v16f accG[2] = {}, accS[2] = {};
  v8s fa[8], fb[8];
  v4u fs;
  float gv[8];
  const u32 xw = (u32)(w * 6144 + l * 16);
  auto xchg_out = [&](const v16f (&ac)[2], int base) {
    LAS char* X = smem + base + xw;
#pragma unroll
    for (int i = 0; i < 6; ++i) {
      const v16f& a = ac[(i + 2) >> 2]; const int r = ((i + 2) & 3) * 4;
      *(LAS v4f*)(X + i * 1024) = v4f{a[r], a[r + 1], a[r + 2], a[r + 3]};
    }
  };
  auto xchg_in = [&](const v16f (&ac)[2], int base, float (&o8)[8]) {
    v4f s0 = {ac[0][0], ac[0][1], ac[0][2], ac[0][3]}, s1 = {ac[0][4], ac[0][5], ac[0][6], ac[0][7]};
#pragma unroll
    for (int x_ = 1; x_ < 4; ++x_) {
      LAS char* X = smem + base + (w ^ (2 * x_)) * 6144 + (x_ - 1) * 2048 + l * 16;
      s0 += *(LAS v4f*)X; s1 += *(LAS v4f*)(X + 1024);
    }
#pragma unroll
    for (int e = 0; e < 4; ++e) { o8[e] = s0[e]; o8[4 + e] = s1[e]; }
  };
  v4f xg[6];
  float ov[8], ex[8];
  auto sadd = [](float a, float b) { float r; asm("v_add_f32 %0, %1, %2" : "=v"(r) : "v"(a), "v"(b)); return r; };
  auto smul = [](float a, float b) { float r; asm("v_mul_f32 %0, %1, %2" : "=v"(r) : "v"(a), "v"(b)); return r; };
  auto silu_step = [&](int i) {
    if (i < 3) {
#pragma unroll
      for (int e = 0; e < 8; ++e) { ov[e] = sadd(i == 0 ? accG[0][e] : ov[e], xg[2 * i + (e >> 2)][e & 3]); PIN(ov[e]); }
    } else if (i == 3) {
#pragma unroll
      for (int e = 0; e < 8; e += 2) { u32 t = pkbf_o(smul(ov[e], rs2), smul(ov[e + 1], rs2)); ov[e] = bflo(t); ov[e + 1] = bfhi(t); PIN(ov[e]); PIN(ov[e + 1]); }
    } else if (i == 4) {
#pragma unroll
      for (int e = 0; e < 8; ++e) { ex[e] = __builtin_amdgcn_exp2f(ov[e] * -1.44269504088896f); PIN(ex[e]); }
    } else if (i == 5) {
#pragma unroll
      for (int e = 0; e < 8; ++e) { ex[e] = __builtin_amdgcn_rcpf(1.0f + ex[e]); PIN(ex[e]); }
    } else if (i == 6) {
#pragma unroll
      for (int e = 0; e < 8; e += 2) { u32 t = pkbf_o(ov[e] * ex[e], ov[e + 1] * ex[e + 1]); gv[e] = bflo(t); gv[e + 1] = bfhi(t); PIN(gv[e]); PIN(gv[e + 1]); }
    }
  };
  static_for<0, 10>([&](auto V) {
    constexpr int v = V;
    v8s(&cur)[8] = (v & 1) ? fb : fa;
    v8s(&prv)[8] = (v & 1) ? fa : fb;
    auto mmp = [&](int s) {
      if constexpr (v >= 8) {
        accS[0] = mfma32h(G[(v - 8) * 4 + s], prv[2 * s], accS[0]); accS[1] = mfma32h(G[(v - 8) * 4 + s], prv[2 * s + 1], accS[1]);
        SCHED();
        if ((v - 8) * 4 + s >= 1) silu_step((v - 8) * 4 + s - 1);
      } else if constexpr (v >= 1) {
        accG[0] = mfma32b(Wf[2 * (v - 1) + (s >> 1)][s & 1], prv[2 * s], accG[0]);
        accG[1] = mfma32b(Wf[2 * (v - 1) + (s >> 1)][s & 1], prv[2 * s + 1], accG[1]);
      }
      SCHED();
    };
    if constexpr (v < 9) {
      WAIT_VM(v <= 7 ? 4 : 0);
      if constexpr (v == 5) { if (w == 0) { PIN(fs); poll4(fs, (u32)(FL_S + l * 16)); } }
      BARRIER();
      STAMPT(11 + v);
      if constexpr (v == 4) { if (w == 0) ld16c(fs, (u32)(FL_S + l * 16), FL); }
      if constexpr (v == 8) {
#pragma unroll
        for (int x_ = 1; x_ < 4; ++x_) {
          LAS char* X = smem + 49152 + (w ^ (2 * x_)) * 6144 + (x_ - 1) * 2048 + l * 16;
          xg[2 * x_ - 2] = *(LAS v4f*)X; xg[2 * x_ - 1] = *(LAS v4f*)(X + 1024);
        }
        SCHED();
      }
      mmp(0);
#pragma unroll
      for (int s = 0; s < 4; ++s) {
        if constexpr (v >= 7) {
          LAS char* sb = smem + aslot(2 * v + (s >> 1)) + (s & 1) * 2048;
          cur[2 * s] = cat8(trread(sb + trS), trread(sb + trS + 1024));
          cur[2 * s + 1] = cat8(trread(sb + (trS ^ 64)), trread(sb + (trS ^ 64) + 1024));
        } else {
          LAS char* sb = smem + aslot(2 * v + (s >> 1)) + (s & 1) * 1024;
          cur[2 * s] = *(LAS v8s*)(sb + hb);
          cur[2 * s + 1] = *(LAS v8s*)(sb + (hb ^ 8192));
        }
      }
      SCHED();
      if constexpr (v <= 6) issueP(2 * v + 4, IC<0>{});
      mmp(1);
      if constexpr (v <= 6) issueP(2 * v + 4, IC<1>{});
      mmp(2);
      if constexpr (v <= 6) issueP(2 * v + 5, IC<0>{});
      mmp(3);
      if constexpr (v <= 6) issueP(2 * v + 5, IC<1>{});
    } else {
      mmp(0); mmp(1); mmp(2); mmp(3);
    }
    if constexpr (v == 7) xchg_out(accG, 49152);
    SCHED();
  });
  xchg_out(accS, 0);
  BARRIER();
  {
    float o8[8], pv[8];
    xchg_in(accS, 0, o8);
#pragma unroll
    for (int e = 0; e < 8; e += 2) { u32 t = pkbf_o(o8[e] * oscale, o8[e + 1] * oscale); pv[e] = bflo(t) * gv[e]; pv[e + 1] = bfhi(t) * gv[e + 1]; }
    u32 a0 = pkbf(pv[0], pv[1]), a1 = pkbf(pv[2], pv[3]), b0 = pkbf(pv[4], pv[5]), b1 = pkbf(pv[6], pv[7]);
    auto r0 = __builtin_amdgcn_permlane32_swap(a0, b0, false, false);
    auto r1 = __builtin_amdgcn_permlane32_swap(a1, b1, false, false);
    v4u pk = {r0[0], r1[0], r0[1], r1[1]};
    v4u ov4;
#pragma unroll
    for (int e = 0; e < 4; ++e) ov4[e] = pkbf(bflo(xe[e]) + bflo(pk[e]), bfhi(xe[e]) + bfhi(pk[e]));
    *(v4u*)((char*)out + eoff) = ov4;
  }
  asm volatile("km_pf_b:");
  STAMPT(20);
  STAMPT(21);
  STAMP_OUT(ws);
}

extern "C" void kernel_launch(void* const* d_in, const int* in_sizes, int n_in, void* d_out, int out_size, void* d_ws,
                              size_t ws_size, hipStream_t stream) {
  kmain<<<256, 512, 0, stream>>>((const u16*)d_in[0], (const u16*)d_in[1], (const u16*)d_in[2], (const u16*)d_in[4],
                                 (const u16*)d_in[5], (const u16*)d_in[6], (char*)d_ws, (u16*)d_out);
}
```

```cpp
#include <hip/hip_runtime.h>
#include <stdint.h>

typedef unsigned short u16;
typedef unsigned int u32;
typedef unsigned long long u64;
typedef short v4s __attribute__((ext_vector_type(4)));
typedef short v8s __attribute__((ext_vector_type(8)));
typedef _Float16 v4h __attribute__((ext_vector_type(4)));
typedef _Float16 v8h __attribute__((ext_vector_type(8)));
typedef __bf16 v2b __attribute__((ext_vector_type(2)));
typedef __bf16 v8b __attribute__((ext_vector_type(8)));
typedef float v2f __attribute__((ext_vector_type(2)));
typedef float v4f __attribute__((ext_vector_type(4)));
typedef float v16f __attribute__((ext_vector_type(16)));
typedef u32 v2u __attribute__((ext_vector_type(2)));
typedef u32 v4u __attribute__((ext_vector_type(4)));

#define LAS __attribute__((address_space(3)))
#define GAS __attribute__((address_space(1)))
#define DEVI __device__ __forceinline__

constexpr int D_ = 512, F_ = 1792, DF_ = 257;
constexpr size_t OFF_H = 0;
constexpr size_t OFF_S1 = (size_t)8 << 20;
constexpr size_t OFF_WT = (size_t)10 << 20;
constexpr size_t OFF_S2 = (size_t)13 << 20;
constexpr size_t OFF_M = (size_t)15 << 20;
constexpr size_t OFF_FL = OFF_M;
constexpr u32 FL_S = 0, FL_A = 1024, FL_H = 2048;
constexpr size_t OFF_BC = OFF_M + 12288;
constexpr size_t OFF_MB = OFF_M + 16384;
constexpr size_t OFF_RS2 = OFF_M + 24576;
constexpr size_t OFF_PQ = OFF_M + 65536;
constexpr size_t OFF_CSP = OFF_M + 131072;
constexpr int SPIN_CAP = 1 << 16;

#define STAMP_DECL
#define STAMPT(id)
#define STAMPC(id)
#define STAMP_OUT(wsp)

template <int I> struct IC { static constexpr int v = I; constexpr operator int() const { return I; } };
template <int B, int E, class Fn> DEVI void static_for(Fn&& f) {
  if constexpr (B < E) { f(IC<B>{}); static_for<B + 1, E>(f); }
}
#define WAIT_VM(n) asm volatile("s_waitcnt vmcnt(%0)" ::"n"(n) : "memory")
#define BARRIER() asm volatile("s_waitcnt lgkmcnt(0)\n\ts_barrier" ::: "memory")
#define SCHED() __builtin_amdgcn_sched_barrier(0)
#define PIN(x) asm volatile("" : "+v"(x))

DEVI int s_of_pi(int pi) {
  int RG = pi >> 6, i = pi & 63;
  return 128 * (4 * (RG & 3) + (i & 3)) + 8 * (i >> 2) + (RG >> 2);
}
DEVI void dma2(u32 voff0, u32 voff1, const char* sbase, u32 lds) {
  asm volatile("s_mov_b32 m0, %3\n\ts_nop 0\n\tglobal_load_lds_dwordx4 %0, %2\n\tglobal_load_lds_dwordx4 %1, %2 offset:1024" ::"v"(voff0), "v"(voff1), "s"(sbase), "s"(lds) : "memory");
}
DEVI void dma2c(u32 voff0, u32 voff1, const char* sbase, u32 lds) {
  asm volatile("s_mov_b32 m0, %3\n\ts_nop 0\n\tglobal_load_lds_dwordx4 %0, %2 sc1\n\tglobal_load_lds_dwordx4 %1, %2 offset:1024 sc1" ::"v"(voff0), "v"(voff1), "s"(sbase), "s"(lds) : "memory");
}
template <int OFF> DEVI void dma1(u32 voff, const char* sbase, u32 lds) {
  asm volatile("s_mov_b32 m0, %2\n\ts_nop 0\n\tglobal_load_lds_dwordx4 %0, %1 offset:%3" ::"v"(voff), "s"(sbase), "s"(lds), "n"(OFF) : "memory");
}
template <int OFF> DEVI void dma1s(u32 voff, const char* sbase, u32 lds) {
  asm volatile("s_mov_b32 m0, %2\n\ts_nop 0\n\tglobal_load_lds_dwordx4 %0, %1 offset:%3 sc1" ::"v"(voff), "s"(sbase), "s"(lds), "n"(OFF) : "memory");
}
DEVI void dma1c(u32 voff, const char* sbase, u32 lds) {
  asm volatile("s_mov_b32 m0, %2\n\ts_nop 0\n\tglobal_load_lds_dword %0, %1 sc1" ::"v"(voff), "s"(sbase), "s"(lds) : "memory");
}
DEVI v2u ld8c_sync(u32 voff, const char* sb) { v2u d; asm volatile("global_load_dwordx2 %0, %1, %2 sc1\n\ts_waitcnt vmcnt(0)" : "=&v"(d) : "v"(voff), "s"(sb) : "memory"); return d; }
DEVI u32 ld4c_sync(u32 voff, const char* sb) { u32 d; asm volatile("global_load_dword %0, %1, %2 sc1\n\ts_waitcnt vmcnt(0)" : "=&v"(d) : "v"(voff), "s"(sb) : "memory"); return d; }
DEVI v4u ld16c_sync(u32 voff, const char* sb) { v4u d; asm volatile("global_load_dwordx4 %0, %1, %2 sc1\n\ts_waitcnt vmcnt(0)" : "=&v"(d) : "v"(voff), "s"(sb) : "memory"); return d; }
template <class T> DEVI void ld16c(T& d, u32 voff, const char* sb) { asm volatile("global_load_dwordx4 %0, %1, %2 sc1" : "=&v"(d) : "v"(voff), "s"(sb) : "memory"); }
template <class T> DEVI void ld8c(T& d, u32 voff, const char* sb) { asm volatile("global_load_dwordx2 %0, %1, %2 sc1" : "=&v"(d) : "v"(voff), "s"(sb) : "memory"); }
template <class T> DEVI void ld4c(T& d, u32 voff, const char* sb) { asm volatile("global_load_dword %0, %1, %2 sc1" : "=&v"(d) : "v"(voff), "s"(sb) : "memory"); }
template <class T> DEVI void ld16(T& d, u32 voff, const char* sb) { asm volatile("global_load_dwordx4 %0, %1, %2" : "=&v"(d) : "v"(voff), "s"(sb) : "memory"); }
DEVI void ld2(u32& d, u32 voff, const char* sb) { asm volatile("global_load_ushort %0, %1, %2" : "=&v"(d) : "v"(voff), "s"(sb) : "memory"); }
template <class T> DEVI void st16c(u32 voff, const char* sb, T v) { asm volatile("global_store_dwordx4 %0, %1, %2 sc1\n\ts_nop 1" ::"v"(voff), "v"(v), "s"(sb) : "memory"); }
template <class T> DEVI void st8c(u32 voff, const char* sb, T v) { asm volatile("global_store_dwordx2 %0, %1, %2 sc1" ::"v"(voff), "v"(v), "s"(sb) : "memory"); }
template <class T> DEVI void st4c(u32 voff, const char* sb, T v) { asm volatile("global_store_dword %0, %1, %2 sc1" ::"v"(voff), "v"(v), "s"(sb) : "memory"); }

DEVI v4s trread(LAS char* p) { return __builtin_amdgcn_ds_read_tr16_b64_v4i16((LAS v4s*)p); }
DEVI v8s cat8(v4s a, v4s b) { return __builtin_shufflevector(a, b, 0, 1, 2, 3, 4, 5, 6, 7); }
DEVI float bflo(u32 v) { return __builtin_bit_cast(float, v << 16); }
DEVI float bfhi(u32 v) { return __builtin_bit_cast(float, v & 0xffff0000u); }
DEVI float rbf(float v) { return (float)(__bf16)v; }
DEVI u32 pkbf(float a, float b) { v2b p = {(__bf16)a, (__bf16)b}; return __builtin_bit_cast(u32, p); }
DEVI u32 pkbf_o(float a, float b) { u32 r = pkbf(a, b); asm("" : "+v"(r)); return r; }
DEVI float cosr(float rev) { return __builtin_amdgcn_cosf(rev); }
DEVI float sinr(float rev) { return __builtin_amdgcn_sinf(rev); }
DEVI v4f mfma16h(v8h a, v8h b, v4f c) { return __builtin_amdgcn_mfma_f32_16x16x32_f16(a, b, c, 0, 0, 0); }
DEVI v4f mfma16b(v8s a, v4u b, v4f c) { return __builtin_amdgcn_mfma_f32_16x16x32_bf16(__builtin_bit_cast(v8b, a), __builtin_bit_cast(v8b, b), c, 0, 0, 0); }
DEVI v16f mfma32b(v8s a, v8s b, v16f c) { return __builtin_amdgcn_mfma_f32_32x32x16_bf16(__builtin_bit_cast(v8b, a), __builtin_bit_cast(v8b, b), c, 0, 0, 0); }
DEVI v16f mfma32h(v8h a, v8s b, v16f c) { return __builtin_amdgcn_mfma_f32_32x32x16_f16(a, __builtin_bit_cast(v8h, b), c, 0, 0, 0); }
DEVI v8h pack_ri(v4f re, v4f im) {
  v8h r;
  r[0] = (_Float16)re[0]; r[1] = (_Float16)re[1]; r[2] = (_Float16)re[2]; r[3] = (_Float16)re[3];
  r[4] = (_Float16)im[0]; r[5] = (_Float16)im[1]; r[6] = (_Float16)im[2]; r[7] = (_Float16)im[3];
  return r;
}
DEVI v4h pack4h(v4f a) { v4h r; r[0] = (_Float16)a[0]; r[1] = (_Float16)a[1]; r[2] = (_Float16)a[2]; r[3] = (_Float16)a[3]; return r; }
DEVI void twid(v4f& re, v4f& im, const v4f& c, const v4f& s) {
#pragma unroll
  for (int r = 0; r < 4; ++r) { float a = re[r], b = im[r]; re[r] = a * c[r] + b * s[r]; im[r] = b * c[r] - a * s[r]; }
}
template <int CTRL> DEVI float dppadd(float v) {
  return v + __builtin_bit_cast(float, __builtin_amdgcn_update_dpp(0, __builtin_bit_cast(int, v), CTRL, 0xf, 0xf, true));
}
DEVI float wave_sum(float v) {
  v = dppadd<0xB1>(v); v = dppadd<0x4E>(v); v = dppadd<0x141>(v); v = dppadd<0x140>(v);
  int bb = __builtin_bit_cast(int, v);
  float a0 = __builtin_bit_cast(float, __builtin_amdgcn_readlane(bb, 0)), a1 = __builtin_bit_cast(float, __builtin_amdgcn_readlane(bb, 16));
  float a2 = __builtin_bit_cast(float, __builtin_amdgcn_readlane(bb, 32)), a3 = __builtin_bit_cast(float, __builtin_amdgcn_readlane(bb, 48));
  return (a0 + a1) + (a2 + a3);
}
DEVI float dot2ones(u32 v, float acc) {
  const v2b ones = {(__bf16)1.0f, (__bf16)1.0f};
  return __builtin_amdgcn_fdot2_f32_bf16(__builtin_bit_cast(v2b, v), ones, acc, false);
}
template <int CTRL> DEVI u32 dppmax(u32 v) { return max(v, (u32)__builtin_amdgcn_update_dpp(0, (int)v, CTRL, 0xf, 0xf, true)); }
DEVI u32 wave_max(u32 v) {
  v = dppmax<0xB1>(v); v = dppmax<0x4E>(v); v = dppmax<0x141>(v); v = dppmax<0x140>(v);
  u32 a0 = __builtin_amdgcn_readlane(v, 0), a1 = __builtin_amdgcn_readlane(v, 16), a2 = __builtin_amdgcn_readlane(v, 32), a3 = __builtin_amdgcn_readlane(v, 48);
  return max(max(a0, a1), max(a2, a3));
}

struct Frag1 { v8s x0, x1, w1, w2; };

constexpr int L_SCR = 147456;
constexpr int L_CS = L_SCR, L_RED = L_SCR + 1024, L_MX = L_SCR + 1088, L_BC0 = L_SCR + 2048, L_BC1 = L_SCR + 2304;
constexpr int L_TAB = L_SCR + 3072;
constexpr int L_T = L_SCR + 7168, L_FA = L_T, L_E1 = L_T, L_E2 = 81920, L_ZX = 81920;
constexpr int L_C1 = 98304, L_IMG = L_C1 + 9216;
constexpr int L_FR = 131072;
constexpr int E1P = 544, E2P = 1024 + 72;
DEVI constexpr int wslot(int n) {
  constexpr int u[14] = {6, 7, 8, 0, 1, 2, 3, 4, 6, 7, 8, 0, 1, 2};
  return u[n] * 16384;
}
DEVI constexpr int aslot(int n) {
  constexpr int u[18] = {3, 4, 5, 9, 2, 6, 7, 8, 0, 1, 3, 4, 5, 9, 2, 6, 7, 8};
  return u[n] * 16384;
}

__global__ __launch_bounds__(512, 2) void kmain(const u16* __restrict__ x, const u16* __restrict__ wr, const u16* __restrict__ wi,
                                                const u16* __restrict__ Wg, const u16* __restrict__ Wu, const u16* __restrict__ Wd,
                                                char* __restrict__ ws, u16* __restrict__ out) {
  __shared__ __attribute__((aligned(16))) char smem_[163840];
  LAS char* smem = (LAS char*)smem_;
  const u32 lbase = (u32)(size_t)smem;
  const int tid = threadIdx.x, l = tid & 63;
  const int w = __builtin_amdgcn_readfirstlane(tid >> 6);
  const int b = blockIdx.x, xcd = b & 7, idx = b >> 3;
  const bool ffn = idx < 28;
  const int g = l >> 4, o = l & 15;
  const int h = l >> 5, blk = (l >> 4) & 1, q = (l & 15) >> 2, pp = l & 3;
  const char* FL = ws + OFF_FL;
  const char* BC = ws + OFF_BC;
  STAMP_DECL; STAMPT(0);

  const int pi = b * 8 + w;
  v4u xr;
  ld16(xr, (u32)(s_of_pi(pi) * 1024 + l * 16), (const char*)x);
  if (w == 0) dma1c((u32)((l & 1) * 4), BC, lbase + L_BC0);
  const int fkb = (b & 7) * 2 + (w >> 2), fcq = w & 3;
  {
    const u32 ks = (u32)((idx >> 1) + 16 * fkb + 256 * (4 * (idx & 1) + ((l >> 3) & 3)));
    const u32 base = ((ks * (DF_ * 2)) & ~15u) + fcq * 128;
    const u32 dlt = (u32)((const char*)wi - (const char*)wr);
    (void)dlt;
    const u32 c0 = ((l & 7) ^ (((l >> 4) & 1) << 2)) << 4;
    if (l < 32) dma1<0>(base + c0, (const char*)wr, lbase + L_FR + w * 2048);
    else dma1<0>(base + c0, (const char*)wi, lbase + L_FR + w * 2048);
    if (fcq == 3) {
      const u32 t0 = min(base + 128 + (c0 & 16), (u32)(2048 * DF_ * 2 - 16));
      if (l < 32) dma1<0>(t0, (const char*)wr, lbase + L_FR + w * 2048 + 1024);
      else dma1<0>(t0, (const char*)wi, lbase + L_FR + w * 2048 + 1024);
    }
  }
  const int p = (xcd >> 1) * 4 + idx / 7, j = (xcd & 1) * 7 + idx % 7;
  const int sh = w >> 2, fq = w & 3;
  u32 aoff[2];
#pragma unroll
  for (int i = 0; i < 2; ++i) {
    int row = (2 * w + i) * 8 + (l >> 3);
    aoff[i] = s_of_pi((p & 15) * 128 + row) * 1024 + (((l & 7) ^ ((row >> 1) & 7)) << 4) - i * 1024;
  }
  const u32 boff = (u32)(2 * w * 4 + (l >> 4)) * (F_ * 2) + j * 256 + (((l & 15) ^ ((l >> 4) << 2)) << 4);
  const u32 boff1 = boff + 4 * F_ * 2 - 1024;
  const int rot = p >> 1;
  auto issue = [&](int t, int slot) {
    const int kt = (t + rot) & 7;
    u32 dst = lbase + slot * 49152 + w * 2048;
    dma2(aoff[0], aoff[1], (const char*)x + kt * 128, dst);
    dma2(boff, boff1, (const char*)Wg + (size_t)kt * (64 * F_ * 2), dst + 16384);
    dma2(boff, boff1, (const char*)Wu + (size_t)kt * (64 * F_ * 2), dst + 32768);
  };
  const int sp = xcd * 4 + (idx - 28);
  if (ffn) issue(0, 0);
  else {
    u64 pc; u32 len;
    asm volatile("s_getpc_b64 %0\nkm_pf_a:\n\ts_mov_b32 %1, km_pf_b-km_pf_a" : "=s"(pc), "=s"(len));
    dma1<0>(min((u32)(((idx & 3) * 8 + w) * 1024 + l * 16), len) & ~15u, (const char*)(pc & ~15ull), lbase);
  }

  u32 E = 0, tick = 0;
  const bool coord = (b == 224) && (w == 0);
  auto do_tick = [&]() { if (coord) { ++tick; if (l == 0) st8c(0u, BC, v2u{E, tick}); } };
  auto get_epoch = [&]() {
    const v2u b0 = *(LAS v2u*)(smem + L_BC0);
    v2u b1 = *(LAS v2u*)(smem + L_BC1);
    int spins = 0;
    while (__builtin_amdgcn_readfirstlane((b0[0] ^ b1[0]) | (b0[1] ^ b1[1])) == 0 && ++spins < SPIN_CAP) {
      __builtin_amdgcn_s_sleep(4);
      b1 = ld8c_sync(0u, BC);
    }
    E = __builtin_amdgcn_readfirstlane(b1[0]);
  };
  auto signal_a = [&]() {
    v4u r0 = *(LAS v4u*)(smem + L_RED), r1 = *(LAS v4u*)(smem + L_RED + 16);
    u32 m = max(max(max(r0[0], r0[1]), max(r0[2], r0[3])), max(max(r1[0], r1[1]), max(r1[2], r1[3])));
    if (l == 0) { st4c((u32)(FL_A + b * 4), FL, E | (m >> 7)); *(LAS u32*)(smem + L_MX + 4) = E; }
  };
  auto poll4 = [&](v4u f, u32 voff) -> v4u {
    int spins = 0;
    while (!__all((((f[0] ^ E) | (f[1] ^ E) | (f[2] ^ E) | (f[3] ^ E)) >> 8) == 0) && ++spins < SPIN_CAP) {
      do_tick();
      __builtin_amdgcn_s_sleep(12);
      f = ld16c_sync(voff, FL);
    }
    return f;
  };
  auto check_a = [&](v4u f) {
    f = poll4(f, (u32)(FL_A + l * 16));
    u32 m = wave_max(max(max(f[0], f[1]), max(f[2], f[3]))) & 0xffu;
    if (l == 0) *(LAS u32*)(smem + L_MX) = m;
  };
  if (coord) {
    v4u f[10];
#pragma unroll
    for (int i = 0; i < 10; ++i) ld16c(f[i], (u32)(i * 1024 + l * 16), FL);
    WAIT_VM(0);
    u32 c = __builtin_amdgcn_readfirstlane(*(LAS u32*)(smem + L_BC0)) >> 8;
    bool hit;
    do {
      c = (c + 1) & 0xffffffu; hit = false;
#pragma unroll
      for (int i = 0; i < 10; ++i) { PIN(f[i]); hit |= ((f[i][0] >> 8) == c) | ((f[i][1] >> 8) == c) | ((f[i][2] >> 8) == c) | ((f[i][3] >> 8) == c); }
    } while (__any(hit));
    E = c << 8;
    do_tick();
  }

  {
    const float fo = (float)o, fg = (float)(4 * g);
    if (w < 2) {
      const float st = fo * (1.0f / 32.0f), a0 = fmaf(fg, st, w ? 0.25f : 0.f);
      v4u hi, lo;
#pragma unroll
      for (int e2 = 0; e2 < 4; ++e2) {
        float c0 = cosr(fmaf((float)(16 * (e2 >> 1) + 2 * (e2 & 1)), st, a0)), c1 = cosr(fmaf((float)(16 * (e2 >> 1) + 2 * (e2 & 1) + 1), st, a0));
        u32 ch = pkbf_o(c0, c1);
        hi[e2] = ch; lo[e2] = pkbf(c0 - bflo(ch), c1 - bfhi(ch));
      }
      *(LAS v4u*)(smem + L_C1 + w * 2048 + l * 16) = hi; *(LAS v4u*)(smem + L_C1 + w * 2048 + 1024 + l * 16) = lo;
    } else if (w < 5) {
      const float st = w == 2 ? (float)(o & 7) * (1.0f / 16.0f) : fo * (w == 3 ? 1.0f / 16.0f : 1.0f / 8.0f);
      const float a0 = (w == 2 && o >= 8) ? 0.25f : 0.f;
      const float sc = w == 2 ? 0.0625f : ((w == 4 && (g >> 1) != (o >> 3)) ? 0.f : 1.0f);
      v8h m;
#pragma unroll
      for (int e = 0; e < 4; ++e) { float a = fmaf(fg + (float)e, st, a0); m[e] = (_Float16)(sc * cosr(a)); m[4 + e] = (_Float16)(sc * sinr(a)); }
      *(LAS v8h*)(smem + (w == 2 ? L_C1 + 4096 : L_TAB + (w - 3) * 1024) + l * 16) = m;
    } else {
      const float st = w == 7 ? fo * (1.0f / 256.0f) : (fo + (w == 6 ? 16.f : 0.f)) * (1.0f / 512.0f);
      v4f c, sn;
#pragma unroll
      for (int e = 0; e < 4; ++e) { float a = (fg + (float)e) * st; c[e] = cosr(a); sn[e] = sinr(a); }
      LAS char* dst = smem + (w == 7 ? L_TAB + 2048 : L_C1 + 5120 + (w - 5) * 2048) + l * 16;
      *(LAS v4f*)dst = c; *(LAS v4f*)(dst + 1024) = sn;
    }
    BARRIER();
    LAS char* C1 = smem + L_C1 + l * 16;
    const v4u Fhr = *(LAS v4u*)C1, Flr = *(LAS v4u*)(C1 + 1024), Fhi = *(LAS v4u*)(C1 + 2048), Fli = *(LAS v4u*)(C1 + 3072);
    const v8h M2 = *(LAS v8h*)(C1 + 4096);
    v4f twc[2], tws[2];
#pragma unroll
    for (int u = 0; u < 2; ++u) { twc[u] = *(LAS v4f*)(C1 + 5120 + u * 2048); tws[u] = *(LAS v4f*)(C1 + 6144 + u * 2048); }
    if (ffn) WAIT_VM(8); else WAIT_VM(3);
    PIN(xr);
    STAMPT(3);
    const float c0 = bflo(__builtin_amdgcn_readfirstlane(xr[0]));
    v2f xv[4], s2 = {0.f, 0.f}, q2 = {0.f, 0.f};
#pragma unroll
    for (int e = 0; e < 4; ++e) {
      xv[e] = v2f{bflo(xr[e]), bfhi(xr[e])};
      v2f t = xv[e] - v2f{c0, c0};
      s2 += t; q2 = __builtin_elementwise_fma(t, t, q2);
    }
    const float dmean = wave_sum(s2[0] + s2[1]) * (1.0f / 512.0f), msq = wave_sum(q2[0] + q2[1]) * (1.0f / 512.0f);
    do_tick();
    const float mb = rbf(c0 + dmean);
    const float var = rbf(fmaxf(msq - dmean * dmean, 0.f));
    const float sd = rbf(__builtin_amdgcn_sqrtf(rbf(var + 1e-5f)));
    const float rs = __builtin_amdgcn_rcpf(sd);
#pragma unroll
    for (int e = 0; e < 4; ++e) {
      v2f t = xv[e] - v2f{mb, mb};
      u32 tb = pkbf_o(t[0], t[1]);
      t = v2f{bflo(tb), bfhi(tb)} * v2f{rs, rs};
      xr[e] = pkbf(t[0], t[1]);
    }
    *(LAS v4u*)(smem + L_IMG + w * 1024 + l * 16) = xr;
    if (l < 2) st4c((u32)(pi * 4 + l * 8192), ws + OFF_MB, l ? rs * rs : -mb);
    const v8s xf = cat8(trread(smem + L_IMG + w * 1024 + l * 8), trread(smem + L_IMG + w * 1024 + 512 + l * 8));
    const int kt1 = (1 + rot) & 7;
    const u32 dst1 = lbase + 49152 + w * 2048;
    if (ffn) dma2(aoff[0], aoff[1], (const char*)x + kt1 * 128, dst1);
    const v8s xg_ = __builtin_bit_cast(v8s, __builtin_bit_cast(v4u, xf) ^ v4u{0x80000000u, 0x80000000u, 0x80000000u, 0x80000000u});
    const v4f z = {0.f, 0.f, 0.f, 0.f};
    v4f dre[2], dim[2];
#pragma unroll
    for (int u = 0; u < 2; ++u) { dre[u] = mfma16b(u ? xg_ : xf, Flr, z); dim[u] = mfma16b(u ? xg_ : xf, Fli, z); }
#pragma unroll
    for (int u = 0; u < 2; ++u) { dre[u] = mfma16b(u ? xg_ : xf, Fhr, dre[u]); dim[u] = mfma16b(u ? xg_ : xf, Fhi, dim[u]); }
#pragma unroll
    for (int u = 0; u < 2; ++u) {
      twid(dre[u], dim[u], twc[u], tws[u]);
      const v8h bb = pack_ri(dre[u], dim[u]);
      v4f d2 = mfma16h(bb, M2, z);
      if (u == 0) {
        const u32 nv = o == 8 ? 0xac002c00u : 0u;
        v4f n = mfma16h(bb, __builtin_bit_cast(v8h, v4u{nv, nv, 0u, 0u}), z);
        if (l == 8) d2[0] = n[0];
      }
      *(LAS v4h*)(smem + L_T + ((o >> 3) * 8 + w) * 544 + (o & 7) * 64 + u * 32 + g * 8) = pack4h(d2);
    }
    if (ffn) {
      dma2(boff, boff1, (const char*)Wg + (size_t)kt1 * (64 * F_ * 2), dst1 + 16384);
      dma2(boff, boff1, (const char*)Wu + (size_t)kt1 * (64 * F_ * 2), dst1 + 32768);
    }
    STAMPT(4);
  }
  auto round1_out = [&]() {
    LAS char* rp = smem + L_T + (4 * (g & 1) + (o >> 2)) * 544 + (w * 32 + (g >> 1) * 16) * 2 + (o & 3) * 8;
    const v8s su = cat8(trread(rp), trread(rp + 8 * 544));
    LAS char* fp_ = smem + L_FR + w * 2048 + q * 128 + (((2 * g + (pp >> 1)) ^ ((q >> 1) << 2)) << 4) + (pp & 1) * 8;
    const v4u fu = __builtin_bit_cast(v4u, cat8(trread(fp_), trread(fp_ + 512)));
    st16c((u32)(b * 8192 + (w * 32 + (g >> 1) * 16 + o) * 32 + (g & 1) * 16), ws + OFF_S1, su);
    st16c((u32)((idx * 16 + fkb) * 4608 + (fcq * 64 + l) * 16), ws + OFF_WT, fu);
    typedef unsigned short v2us __attribute__((ext_vector_type(2)));
    const int dl = (idx >> 1) & 7;
    u32 msk = (fcq == 0 && l < dl) ? 0u : 0x7fff7fffu;
    v2us m2 = {0, 0};
#pragma unroll
    for (int e = 0; e < 4; ++e) { u32 t = fu[e] & msk; m2 = __builtin_elementwise_max(m2, __builtin_bit_cast(v2us, t)); }
    if (fcq == 3) {
      LAS char* tp = smem + L_FR + w * 2048 + 1024 + q * 128 + (pp >> 1) * 16 + (pp & 1) * 8;
      const v4u tu = __builtin_bit_cast(v4u, cat8(trread(tp), trread(tp + 512)));
      if (l < 16) st16c((u32)((idx * 16 + fkb) * 4608 + (256 + l) * 16), ws + OFF_WT, tu);
      msk = (o <= dl) ? 0x7fff7fffu : 0u;
#pragma unroll
      for (int e = 0; e < 4; ++e) { u32 t = tu[e] & msk; m2 = __builtin_elementwise_max(m2, __builtin_bit_cast(v2us, t)); }
    }
    const u32 mx = wave_max(max((u32)m2[0], (u32)m2[1]));
    if (l == 0) *(LAS u32*)(smem + L_RED + w * 4) = mx;
    if (w == 0 && !coord) dma1c((u32)((l & 1) * 4), BC, lbase + L_BC1);
  };

  const int RG = xcd * 4 + (idx >> 3), ct = idx & 7;
  const int kd = xcd * 32 + idx;
  const u32 swz = ((l & 7) ^ (((l >> 4) & 1) << 2)) << 4;
  const u32 ro0 = (u32)((l >> 3) * 1024) + swz, ro1 = ro0 + 8192 - 1024;
  const char* bsrc = (const char*)Wd + (size_t)(w * 16) * 1024 + ct * 128;
  auto issueW = [&](int n) { dma2(ro0, ro1, bsrc + (size_t)n * 131072, lbase + wslot(n) + w * 2048); };
  const int dh = w & 1, kq = w >> 1, ph = kq >> 1, kh = kq & 1;
  const int epi = RG * 64 + ph * 32 + (l & 31);
  const u32 eoff = (u32)(s_of_pi(epi) * D_ + ct * 64 + dh * 32 + kh * 16 + h * 8) * 2;
  v4u wt, s1v, xe; v4f pq[4]; float rs2;
  auto load_xe = [&]() { ld16(xe, eoff, (const char*)x); };
  auto load_s1 = [&]() { ld16c(s1v, (u32)((((4 * w + g) * 8 + (o >> 1)) * 256 + kd) * 32 + (o & 1) * 16), ws + OFF_S1); };
  auto load_wt = [&]() { ld16c(wt, (u32)((w * 64 + l) * 4608 + (kd + ((2 * w + (g >> 1)) & 7)) * 16), ws + OFF_WT); };
  auto load_rs = [&]() { ld4c(rs2, (u32)(epi * 4), ws + OFF_RS2); };
  v4f t2c, t2s, t3c, t3s;
  auto trig2 = [&]() {
    const float fw = (float)w, st2 = fw * (1.0f / 128.0f), a2 = fw * (float)(o + 64 * g) * (1.0f / 2048.0f);
#pragma unroll
    for (int e = 0; e < 4; ++e) { float b2 = fmaf((float)e, st2, a2); t2c[e] = cosr(b2); t2s[e] = sinr(b2); }
    PIN(t2c); PIN(t2s);
  };
  auto trig3 = [&]() {
    const float f7 = (float)(o & 7), st3 = f7 * (1.0f / 128.0f), a3 = f7 * (float)(2 * w + (o >> 3) + 64 * g) * (1.0f / 2048.0f);
#pragma unroll
    for (int e = 0; e < 4; ++e) { float b3 = fmaf((float)e, st3, a3); t3c[e] = cosr(b3); t3s[e] = sinr(b3); }
    PIN(t3c); PIN(t3s);
  };
  v4u f16; v4f t1c, t1s, re, im;
  const v4f z4 = {0.f, 0.f, 0.f, 0.f};
  auto imf = [](v4u f) { return __builtin_bit_cast(v8h, v4u{f[2] ^ 0x80008000u, f[3] ^ 0x80008000u, f[0], f[1]}); };
  auto segA0 = [&]() {
    f16 = *(LAS v4u*)(smem + L_TAB + l * 16);
    t1c = *(LAS v4f*)(smem + L_TAB + 2048 + l * 16); t1s = *(LAS v4f*)(smem + L_TAB + 3072 + l * 16);
  };
  auto segA1 = [&]() {
    PIN(s1v);
    const v8h a = __builtin_bit_cast(v8h, s1v);
    re = mfma16h(a, __builtin_bit_cast(v8h, f16), z4); im = mfma16h(a, imf(f16), z4);
  };
  auto segA2 = [&]() {
    twid(re, im, t1c, t1s);
    const v8h a = pack_ri(re, im);
    re = mfma16h(__builtin_bit_cast(v8h, f16), a, z4); im = mfma16h(imf(f16), a, z4);
  };
  auto segA3 = [&]() {
    twid(re, im, t2c, t2s);
    *(LAS v4h*)(smem + L_E1 + w * E1P + o * 32 + g * 8) = pack4h(re);
    *(LAS v4h*)(smem + L_E1 + (8 + w) * E1P + o * 32 + g * 8) = pack4h(im);
  };

  if (ffn) {
    v16f acc[2][2] = {};
    float mbr[2];
    const u32 wb0 = (u32)((8 * h + q) * 256 + ((fq ^ q) << 6) + blk * 32 + pp * 8);
    const u32 r1 = 16384 + sh * 16384, r2 = 32768 - sh * 16384;
    const int xrow = sh * 64 + (l & 31);
    const u32 xb0 = (u32)(xrow * 128 + ((h ^ ((xrow >> 1) & 7)) << 4));
    auto rd = [&](Frag1& f, LAS char* sb, int i) {
      LAS char* xa = sb + (xb0 ^ (i << 5));
      LAS char* wa_ = sb + wb0 + (i << 12);
      f.w1 = cat8(trread(wa_ + r1), trread(wa_ + r1 + 1024));
      f.x0 = *(LAS v8s*)xa; f.x1 = *(LAS v8s*)(xa + 4096);
      f.w2 = cat8(trread(wa_ + r2), trread(wa_ + r2 + 1024));
    };
    const u32 one = ((l & 15) == ((l >> 4) & 1)) ? 0x3f803f80u : 0u;
    const v8s sel = __builtin_bit_cast(v8s, v4u{one, one, one, one});
    v4f csa = {0.f, 0.f, 0.f, 0.f};
    Frag1 A, B;
    WAIT_VM(7);
    BARRIER();
    STAMPT(1); STAMPC(30);
    rd(A, smem, 0);
    dma2(aoff[0], aoff[1], (const char*)x + ((2 + rot) & 7) * 128, lbase + 2 * 49152 + w * 2048);
    v4u cs1, cs2;
    static_for<0, 8>([&](auto T) {
      constexpr int t = T, slot = t % 3, nslot = (t + 1) % 3, pslot = (t + 2) % 3;
      LAS char* sb = smem + slot * 49152;
      {
        const int kt = (t + 2 + rot) & 7;
        const char* s1 = t < 6 ? (const char*)Wg + (size_t)kt * (64 * F_ * 2) : bsrc + (size_t)((t - 6) * 3 + 1) * 131072;
        const char* s2 = t < 6 ? (const char*)Wu + (size_t)kt * (64 * F_ * 2) : bsrc + (size_t)((t - 6) * 3 + 2) * 131072;
        const u32 o2 = t < 6 ? boff : ro0, o3 = t < 6 ? boff1 : ro1;
        const u32 dst = lbase + pslot * 49152 + w * 2048;
        rd(B, sb, 1); SCHED();
        acc[0][0] = mfma32b(A.w1, A.x0, acc[0][0]); acc[0][1] = mfma32b(A.w1, A.x1, acc[0][1]);
        if constexpr (t == 0) csa = mfma16b(sel, __builtin_bit_cast(v4u, A.w1), csa);
        SCHED();
        dma1<0>(o2, s1, dst + 16384);
        if constexpr (t == 2) load_xe();
        if constexpr (t == 6) load_wt();
        acc[1][0] = mfma32b(A.w2, A.x0, acc[1][0]); acc[1][1] = mfma32b(A.w2, A.x1, acc[1][1]); SCHED();
        if constexpr (t == 0) { round1_out(); SCHED(); }
        if constexpr (t == 3) { trig2(); SCHED(); }
        if constexpr (t == 4) { trig3(); SCHED(); }
        rd(A, sb, 2); SCHED();
        acc[0][0] = mfma32b(B.w1, B.x0, acc[0][0]); SCHED();
        dma1<1024>(o3, s1, dst + 16384);
        acc[0][1] = mfma32b(B.w1, B.x1, acc[0][1]);
        if constexpr (t == 0) csa = mfma16b(sel, __builtin_bit_cast(v4u, B.w1), csa);
        acc[1][0] = mfma32b(B.w2, B.x0, acc[1][0]); acc[1][1] = mfma32b(B.w2, B.x1, acc[1][1]); SCHED();
        dma1<0>(o2, s2, dst + 32768);
        if constexpr (t == 6) {
          load_rs();
          if (kd == 0) {
#pragma unroll
            for (int r = 0; r < 4; ++r) ld16c(pq[r], (u32)(w * 4096 + r * 1024 + l * 16), ws + OFF_PQ);
          }
        }
        if constexpr (t == 7) { WAIT_VM(13); segA1(); SCHED(); }
        rd(B, sb, 3); SCHED();
        acc[0][0] = mfma32b(A.w1, A.x0, acc[0][0]); acc[0][1] = mfma32b(A.w1, A.x1, acc[0][1]);
        if constexpr (t == 0) csa = mfma16b(sel, __builtin_bit_cast(v4u, A.w1), csa);
        SCHED();
        dma1<1024>(o3, s2, dst + 32768);
        acc[1][0] = mfma32b(A.w2, A.x0, acc[1][0]); acc[1][1] = mfma32b(A.w2, A.x1, acc[1][1]); SCHED();
      }
      if constexpr (t < 7) {
        const char* s0 = t < 5 ? (const char*)x + ((t + 3 + rot) & 7) * 128 : bsrc + (size_t)((t - 5) * 3) * 131072;
        const u32 o0 = t < 5 ? aoff[0] : ro0, o1 = t < 5 ? aoff[1] : ro1;
        const u32 dst = lbase + slot * 49152 + w * 2048;
        if constexpr (t == 0) csa = mfma16b(sel, __builtin_bit_cast(v4u, B.w1), csa);
        WAIT_VM(t == 0 ? 8 : t == 2 ? 7 : t == 6 ? 13 : 6);
        if constexpr (t == 5) { if (w == 0) check_a(*(LAS v4u*)(smem + L_FA + l * 16)); }
        BARRIER();
        if constexpr (t == 0 || t == 5) STAMPT(22 + t);
        acc[0][0] = mfma32b(B.w1, B.x0, acc[0][0]); SCHED();
        rd(A, smem + nslot * 49152, 0);
        if constexpr (t == 6) segA0();
        SCHED();
        if constexpr (t == 0) {
          if (l < 16) {
#pragma unroll
            for (int i = 0; i < 2; ++i) {
              const float hi = rbf(csa[i]);
              st4c((u32)((((j * 2 + (p & 1)) * 256 + sh * 128 + fq * 32 + i * 16 + l) * 8 + rot) * 4), ws + OFF_CSP, pkbf(hi, csa[i] - hi));
            }
          }
        }
        if constexpr (t == 4) { if (w == 0) dma1s<0>((u32)(FL_A + l * 16), FL, lbase + L_FA); }
        if constexpr (t == 5) {
          const u32 co = (u32)(((j * 2 + (p & 1)) * 256 + fq * 32 + (l & 31)) * 32 + h * 16);
          ld16c(cs1, co + sh * 4096, ws + OFF_CSP); ld16c(cs2, co + (1 - sh) * 4096, ws + OFF_CSP);
          ld4c(mbr[0], (u32)((p * 128 + xrow) * 4), ws + OFF_MB); ld4c(mbr[1], (u32)((p * 128 + xrow + 32) * 4), ws + OFF_MB);
          load_s1();
        }
        dma1<0>(o0, s0, dst);
        acc[0][1] = mfma32b(B.w1, B.x1, acc[0][1]);
        acc[1][0] = mfma32b(B.w2, B.x0, acc[1][0]);
        acc[1][1] = mfma32b(B.w2, B.x1, acc[1][1]); SCHED();
        dma1<1024>(o1, s0, dst);
        if constexpr (t == 1) { if (w == 0) { get_epoch(); signal_a(); } }
        SCHED();
      } else {
        segA2(); SCHED();
        PIN(cs1); PIN(cs2); PIN(mbr[0]); PIN(mbr[1]);
        const u32 m0_ = pkbf(mbr[0], mbr[0]), m1_ = pkbf(mbr[1], mbr[1]);
        const v8s mb0 = __builtin_bit_cast(v8s, v4u{m0_, m0_, m0_, m0_}), mb1 = __builtin_bit_cast(v8s, v4u{m1_, m1_, m1_, m1_});
        acc[0][0] = mfma32b(B.w1, B.x0, acc[0][0]); acc[1][0] = mfma32b(B.w2, B.x0, acc[1][0]);
        acc[0][0] = mfma32b(__builtin_bit_cast(v8s, cs1), mb0, acc[0][0]); acc[1][0] = mfma32b(__builtin_bit_cast(v8s, cs2), mb0, acc[1][0]);
        acc[0][1] = mfma32b(B.w1, B.x1, acc[0][1]); acc[1][1] = mfma32b(B.w2, B.x1, acc[1][1]);
        acc[0][1] = mfma32b(__builtin_bit_cast(v8s, cs1), mb1, acc[0][1]); acc[1][1] = mfma32b(__builtin_bit_cast(v8s, cs2), mb1, acc[1][1]); SCHED();
      }
    });
    STAMPT(2); STAMPC(31);
    auto fin = [&](const v16f& ag, const v16f& au, int rt) {
#pragma unroll
      for (int s2i = 0; s2i < 2; ++s2i) {
        v4u ov;
#pragma unroll
        for (int e = 0; e < 4; ++e) {
          const int r = s2i * 8 + 2 * e;
          v2f gg = v2f{fmaxf(ag[r], 0.f), fmaxf(ag[r + 1], 0.f)} * v2f{au[r], au[r + 1]};
          ov[e] = pkbf(gg[0], gg[1]);
        }
        st16c((u32)(((p * 4 + sh * 2 + rt) * 112 + (j * 8 + fq * 2 + s2i)) * 1024 + l * 16), ws + OFF_H, ov);
      }
    };
    if (sh == 0) { fin(acc[0][0], acc[1][0], 0); fin(acc[0][1], acc[1][1], 1); }
    else { fin(acc[1][0], acc[0][0], 0); fin(acc[1][1], acc[0][1], 1); }
    STAMPT(6);
    segA3();
    WAIT_VM(4);
  } else {
    WAIT_VM(1);
    BARRIER();
    round1_out();
    do_tick();
    if (w == 7) {
      u32 sq[8];
      const int kss = (sp >> 1) + 16 * (l >> 2) + 256 * (4 * (sp & 1) + (l & 3)), km = (2048 - kss) & 2047;
      const u32 o1 = (u32)(kss * DF_ * 2), o2 = (u32)(km * DF_ * 2);
      ld2(sq[0], o1, (const char*)wr); ld2(sq[1], o1, (const char*)wi); ld2(sq[2], o2, (const char*)wr); ld2(sq[3], o2, (const char*)wi);
      ld2(sq[4], o1 + 512, (const char*)wr); ld2(sq[5], o1 + 512, (const char*)wi); ld2(sq[6], o2 + 512, (const char*)wr); ld2(sq[7], o2 + 512, (const char*)wi);
      WAIT_VM(0);
#pragma unroll
      for (int i = 0; i < 8; ++i) PIN(sq[i]);
      float war = bflo(sq[0]) + bflo(sq[2]), wai = bflo(sq[1]) - bflo(sq[3]);
      float wbr = bflo(sq[4]) + bflo(sq[6]), wbi = bflo(sq[5]) - bflo(sq[7]);
      v4f pv = {0.25f * (war + wbr), 0.25f * (wai + wbi), 0.25f * (war - wbr), 0.25f * (wai - wbi)};
      st16c((u32)((((sp >> 2) * 4 + (l & 3)) * 64 + (sp & 3) * 16 + (l >> 2)) * 16), ws + OFF_PQ, pv);
    }
    WAIT_VM(0);
    BARRIER();
    STAMPT(1); STAMPC(30);
    if (w == 0) { if (!coord) get_epoch(); signal_a(); }
    dma1c((u32)(sp * 57344 + (w * 112 + l) * 64), (const char*)Wd, lbase + 49152);
    dma1c((u32)(sp * 57344 + (w * 112 + 48 + l) * 64), (const char*)Wd, lbase + 49152);
    static_for<0, 6>([&](auto N) { issueW(N); });
#pragma unroll
    for (int i = 0; i < 8; ++i) dma1c((u32)(786432 + (idx & 3) * 262144 + ((i * 8 + w) * 64 + l) * 64), (const char*)Wd, lbase + 49152);
    load_xe();
    trig2(); trig3(); segA0();
    if (w == 0) check_a(v4u{~E, 0u, 0u, 0u});
    BARRIER();
    load_s1(); load_wt(); load_rs();
    WAIT_VM(0);
    segA1(); segA2(); segA3();
    STAMPT(2); STAMPC(31);
  }
  PIN(wt); PIN(xe); PIN(rs2);
  BARRIER();
  STAMPT(5);

  const u32 trG = (u32)(kq * 4096 + (4 * h + q) * 128 + ((dh ^ ((q >> 1) & 1)) << 6) + (blk ^ kh) * 32 + pp * 8);
  v8s Wf[14][2];
  auto readW = [&](auto N) {
    constexpr int n = N;
    LAS char* sb = smem + wslot(n) + trG;
    Wf[n][0] = cat8(trread(sb), trread(sb + 1024)); Wf[n][1] = cat8(trread(sb + 2048), trread(sb + 3072));
  };
  const char* hsrc = ws + OFF_H + ((size_t)(RG * 2 + (w >> 2)) * 112 + (w & 3) * 2) * 1024;
  const u32 so0 = (u32)((l >> 3) * 4096) + swz, so1 = so0 + 32768 - 1024;
  const char* ssrc = ws + OFF_S2 + (size_t)(w * 16) * 4096 + RG * 128;
  auto issueP = [&](int n, auto HALF) {
    constexpr int hf = HALF;
    if (n < 14) dma1s<hf * 1024>((u32)(l * 16), hsrc + n * 8192, lbase + aslot(n) + w * 2048);
    else dma1s<hf * 1024>(hf ? so1 : so0, ssrc + (size_t)(n - 14) * (128 * 4096), lbase + aslot(n) + w * 2048);
  };
  const int d = ct * 64 + dh * 32 + ((l & 31) ^ (kh << 4));
  const float gst = (float)d * (1.0f / 512.0f), ga0 = (float)(16 * kq + 2 * h) * gst;
  v8h G[8];
  auto gen = [&](auto FI) {
    constexpr int f = FI;
#pragma unroll
    for (int e2 = 0; e2 < 4; ++e2) {
      float a = fmaf((float)(64 * (f >> 1) + 8 * (f & 1) + 4 * (e2 >> 1) + (e2 & 1)), gst, ga0);
      float c = cosr(a), sn = sinr(a);
      if (f == 0 && e2 == 0) { if (kq == 0 && h == 0) { c = 0.5f; sn = (d & 1) ? 0.5f : -0.5f; } }
      G[f][2 * e2] = (_Float16)c; G[f][2 * e2 + 1] = (_Float16)sn;
    }
    PIN(G[f]);
  };

  float oscale;
  const u32 fhoff = (u32)(FL_H + (RG * 64 + min(l, 55)) * 4);
  u32 fp;
  {
    v8h a;
    {
      LAS char* rp = smem + L_E1 + (4 * (g & 1) + (o >> 2)) * E1P + (2 * w + (g >> 1)) * 32 + (o & 3) * 8;
      a = __builtin_bit_cast(v8h, cat8(trread(rp), trread(rp + 8 * E1P)));
    }
    const v4u f8 = *(LAS v4u*)(smem + L_TAB + 1024 + l * 16);
    const v2u me = *(LAS v2u*)(smem + L_MX);
    issueW(6);
    static_for<0, 3>(readW);
    const int Ex = (int)me[0] < 16 ? 16 : ((int)me[0] > 240 ? 240 : (int)me[0]);
    const float wscale = __builtin_bit_cast(float, (u32)(246 - Ex) << 23);
    oscale = __builtin_bit_cast(float, (u32)(Ex - 7) << 23);
    re = mfma16h(__builtin_bit_cast(v8h, f8), a, z4); im = mfma16h(imf(f8), a, z4);
    static_for<3, 6>(readW);
    WAIT_VM(2);
    STAMPT(7);
    if (ffn && l == 0) st4c((u32)(FL_H + ((p * 2 + sh) * 64 + j * 4 + fq) * 4), FL, me[1]);
    issueW(7);
    if (kd != 0) {
#pragma unroll
      for (int r = 0; r < 4; ++r) {
        float fr = ((r & 1) ? bfhi(wt[r >> 1]) : bflo(wt[r >> 1])) * wscale, fi = ((r & 1) ? bfhi(wt[2 + (r >> 1)]) : bflo(wt[2 + (r >> 1)])) * wscale;
        float zr = re[r], zi = im[r];
        re[r] = zr * fr - zi * fi;
        im[r] = -(zr * fi + zi * fr);
      }
    } else {
      const int ka = 2 * w + (g >> 1), kr = o + 64 * (g & 1);
#pragma unroll
      for (int r = 0; r < 4; ++r) { v2f t = {re[r], im[r]}; *(LAS v2f*)(smem + L_ZX + ka * 1024 + (kr + 16 * r) * 8) = t; }
      BARRIER();
#pragma unroll
      for (int r = 0; r < 4; ++r) {
        PIN(pq[r]);
        const int km = (2048 - (ka + 16 * (kr + 16 * r))) & 2047;
        v2f zm = *(LAS v2f*)(smem + L_ZX + (km & 15) * 1024 + (km >> 4) * 8);
        float zr = re[r], zi = im[r];
        re[r] = (zr * pq[r][0] - zi * pq[r][1] + zm[0] * pq[r][2] + zm[1] * pq[r][3]) * wscale;
        im[r] = -(zr * pq[r][1] + zi * pq[r][0] + zm[0] * pq[r][3] - zm[1] * pq[r][2]) * wscale;
      }
    }
    LAS char* E2 = smem + (kd == 0 ? L_E1 : L_E2);
    a = pack_ri(re, im);
    re = mfma16h(a, __builtin_bit_cast(v8h, f8), z4); im = mfma16h(a, imf(f8), z4);
    twid(re, im, t3c, t3s);
    *(LAS v4h*)(E2 + (o & 7) * E2P + (2 * w + (o >> 3)) * 32 + g * 8) = pack4h(re);
    *(LAS v4h*)(E2 + (o & 7) * E2P + 512 + (2 * w + (o >> 3)) * 32 + g * 8) = pack4h(im);
    BARRIER();
    STAMPT(8);
    {
      LAS char* rp = E2 + w * E2P + o * 32 + g * 8;
      a = __builtin_bit_cast(v8h, cat8(*(LAS v4s*)rp, *(LAS v4s*)(rp + 512)));
    }
    issueW(8);
    re = mfma16h(a, __builtin_bit_cast(v8h, f16), z4); im = mfma16h(a, imf(f16), z4);
    issueW(9);
    twid(re, im, t1c, t1s);
    a = pack_ri(re, im);
    re = mfma16h(__builtin_bit_cast(v8h, f16), a, z4); im = mfma16h(imf(f16), a, z4);
    issueW(10);
    st8c((u32)(2 * kd * 4096 + w * 512 + l * 8), ws + OFF_S2, pack4h(re));
    st8c((u32)((2 * kd + 1) * 4096 + w * 512 + l * 8), ws + OFF_S2, pack4h(im));
  }
  STAMPT(9);
  gen(IC<0>{}); issueW(11);
  WAIT_VM(8);
  BARRIER();
  static_for<6, 9>(readW);
  gen(IC<1>{}); issueW(12); gen(IC<2>{}); issueW(13);
  if (w == 0) ld4c(fp, fhoff, FL);
  gen(IC<3>{}); gen(IC<4>{});
  WAIT_VM(0);
  if (w == 0) {
    PIN(fp);
    u32 f = fp;
    int spins = 0;
    while (!__all(f == E) && ++spins < SPIN_CAP) {
      __builtin_amdgcn_s_sleep(2);
      f = ld4c_sync(fhoff, FL);
    }
  }
  BARRIER();
  STAMPT(10);
  if (tid == 0) st4c((u32)(FL_S + b * 4), FL, E);
  issueP(0, IC<0>{}); issueP(0, IC<1>{}); issueP(1, IC<0>{}); issueP(1, IC<1>{});
  static_for<9, 14>(readW);
  gen(IC<5>{});
  issueP(2, IC<0>{}); issueP(2, IC<1>{});
  gen(IC<6>{});
  issueP(3, IC<0>{}); issueP(3, IC<1>{});
  gen(IC<7>{});

  const u32 trS = (u32)(kq * 4096 + (4 * h + q) * 128 + ((ph ^ ((q >> 1) & 1)) << 6) + blk * 32 + pp * 8);
  const u32 hb = (u32)(ph * 8192 + kq * 2048 + l * 16);
  v16f accG[2] = {}, accS[2] = {};
  v8s fa[8], fb[8];
  v4u fs;
  float gv[8];
  const u32 xw = (u32)(w * 6144 + l * 16);
  auto xchg_out = [&](const v16f (&ac)[2], int base) {
    LAS char* X = smem + base + xw;
#pragma unroll
    for (int i = 0; i < 6; ++i) {
      const v16f& a = ac[(i + 2) >> 2]; const int r = ((i + 2) & 3) * 4;
      *(LAS v4f*)(X + i * 1024) = v4f{a[r], a[r + 1], a[r + 2], a[r + 3]};
    }
  };
  auto xchg_in = [&](const v16f (&ac)[2], int base, float (&o8)[8]) {
    v4f s0 = {ac[0][0], ac[0][1], ac[0][2], ac[0][3]}, s1 = {ac[0][4], ac[0][5], ac[0][6], ac[0][7]};
#pragma unroll
    for (int x_ = 1; x_ < 4; ++x_) {
      LAS char* X = smem + base + (w ^ (2 * x_)) * 6144 + (x_ - 1) * 2048 + l * 16;
      s0 += *(LAS v4f*)X; s1 += *(LAS v4f*)(X + 1024);
    }
#pragma unroll
    for (int e = 0; e < 4; ++e) { o8[e] = s0[e]; o8[4 + e] = s1[e]; }
  };
  v4f xg[6];
  float ov[8], ex[8];
  auto sadd = [](float a, float b) { float r; asm("v_add_f32 %0, %1, %2" : "=v"(r) : "v"(a), "v"(b)); return r; };
  auto smul = [](float a, float b) { float r; asm("v_mul_f32 %0, %1, %2" : "=v"(r) : "v"(a), "v"(b)); return r; };
  auto silu_step = [&](int i) {
    if (i < 3) {
#pragma unroll
      for (int e = 0; e < 8; ++e) { ov[e] = sadd(i == 0 ? accG[0][e] : ov[e], xg[2 * i + (e >> 2)][e & 3]); PIN(ov[e]); }
    } else if (i == 3) {
#pragma unroll
      for (int e = 0; e < 8; e += 2) { u32 t = pkbf_o(smul(ov[e], rs2), smul(ov[e + 1], rs2)); ov[e] = bflo(t); ov[e + 1] = bfhi(t); PIN(ov[e]); PIN(ov[e + 1]); }
    } else if (i == 4) {
#pragma unroll
      for (int e = 0; e < 8; ++e) { ex[e] = __builtin_amdgcn_exp2f(ov[e] * -1.44269504088896f); PIN(ex[e]); }
    } else if (i == 5) {
#pragma unroll
      for (int e = 0; e < 8; ++e) { ex[e] = __builtin_amdgcn_rcpf(1.0f + ex[e]); PIN(ex[e]); }
    } else if (i == 6) {
#pragma unroll
      for (int e = 0; e < 8; e += 2) { u32 t = pkbf_o(ov[e] * ex[e], ov[e + 1] * ex[e + 1]); gv[e] = bflo(t); gv[e + 1] = bfhi(t); PIN(gv[e]); PIN(gv[e + 1]); }
    }
  };
  static_for<0, 10>([&](auto V) {
    constexpr int v = V;
    v8s(&cur)[8] = (v & 1) ? fb : fa;
    v8s(&prv)[8] = (v & 1) ? fa : fb;
    auto mmp = [&](int s) {
      if constexpr (v >= 8) {
        accS[0] = mfma32h(G[(v - 8) * 4 + s], prv[2 * s], accS[0]); accS[1] = mfma32h(G[(v - 8) * 4 + s], prv[2 * s + 1], accS[1]);
        SCHED();
        if ((v - 8) * 4 + s >= 1) silu_step((v - 8) * 4 + s - 1);
      } else if constexpr (v >= 1) {
        accG[0] = mfma32b(Wf[2 * (v - 1) + (s >> 1)][s & 1], prv[2 * s], accG[0]);
        accG[1] = mfma32b(Wf[2 * (v - 1) + (s >> 1)][s & 1], prv[2 * s + 1], accG[1]);
      }
      SCHED();
    };
    if constexpr (v < 9) {
      WAIT_VM(v <= 7 ? 4 : 0);
      if constexpr (v == 5) { if (w == 0) { PIN(fs); poll4(fs, (u32)(FL_S + l * 16)); } }
      BARRIER();
      if constexpr (v == 0 || v == 8) STAMPT(11 + v);
      if constexpr (v == 4) { if (w == 0) ld16c(fs, (u32)(FL_S + l * 16), FL); }
      if constexpr (v == 8) {
#pragma unroll
        for (int x_ = 1; x_ < 4; ++x_) {
          LAS char* X = smem + 49152 + (w ^ (2 * x_)) * 6144 + (x_ - 1) * 2048 + l * 16;
          xg[2 * x_ - 2] = *(LAS v4f*)X; xg[2 * x_ - 1] = *(LAS v4f*)(X + 1024);
        }
        SCHED();
      }
      mmp(0);
#pragma unroll
      for (int s = 0; s < 4; ++s) {
        if constexpr (v >= 7) {
          LAS char* sb = smem + aslot(2 * v + (s >> 1)) + (s & 1) * 2048;
          cur[2 * s] = cat8(trread(sb + trS), trread(sb + trS + 1024));
          cur[2 * s + 1] = cat8(trread(sb + (trS ^ 64)), trread(sb + (trS ^ 64) + 1024));
        } else {
          LAS char* sb = smem + aslot(2 * v + (s >> 1)) + (s & 1) * 1024;
          cur[2 * s] = *(LAS v8s*)(sb + hb);
          cur[2 * s + 1] = *(LAS v8s*)(sb + (hb ^ 8192));
        }
      }
      SCHED();
      if constexpr (v <= 6) issueP(2 * v + 4, IC<0>{});
      mmp(1);
      if constexpr (v <= 6) issueP(2 * v + 4, IC<1>{});
      mmp(2);
      if constexpr (v <= 6) issueP(2 * v + 5, IC<0>{});
      mmp(3);
      if constexpr (v <= 6) issueP(2 * v + 5, IC<1>{});
    } else {
      mmp(0); mmp(1); mmp(2); mmp(3);
    }
    if constexpr (v == 7) xchg_out(accG, 49152);
    SCHED();
  });
  xchg_out(accS, 0);
  BARRIER();
  {
    float o8[8], pv[8];
    xchg_in(accS, 0, o8);
#pragma unroll
    for (int e = 0; e < 8; e += 2) { u32 t = pkbf_o(o8[e] * oscale, o8[e + 1] * oscale); pv[e] = bflo(t) * gv[e]; pv[e + 1] = bfhi(t) * gv[e + 1]; }
    u32 a0 = pkbf(pv[0], pv[1]), a1 = pkbf(pv[2], pv[3]), b0 = pkbf(pv[4], pv[5]), b1 = pkbf(pv[6], pv[7]);
    auto r0 = __builtin_amdgcn_permlane32_swap(a0, b0, false, false);
    auto r1 = __builtin_amdgcn_permlane32_swap(a1, b1, false, false);
    v4u pk = {r0[0], r1[0], r0[1], r1[1]};
    v4u ov4;
#pragma unroll
    for (int e = 0; e < 4; ++e) ov4[e] = pkbf(bflo(xe[e]) + bflo(pk[e]), bfhi(xe[e]) + bfhi(pk[e]));
    *(v4u*)((char*)out + eoff) = ov4;
  }
  asm volatile("km_pf_b:");
  STAMPT(20);
  STAMPT(21);
  STAMP_OUT(ws);
}

extern "C" void kernel_launch(void* const* d_in, const int* in_sizes, int n_in, void* d_out, int out_size, void* d_ws,
                              size_t ws_size, hipStream_t stream) {
  kmain<<<256, 512, 0, stream>>>((const u16*)d_in[0], (const u16*)d_in[1], (const u16*)d_in[2], (const u16*)d_in[4],
                                 (const u16*)d_in[5], (const u16*)d_in[6], (char*)d_ws + ((ws_size - ((size_t)16 << 20)) & ~(size_t)0x1fffff), (u16*)d_out);
}
```

```cpp
#include <hip/hip_runtime.h>
#include <stdint.h>

typedef unsigned short u16;
typedef unsigned int u32;
typedef unsigned long long u64;
typedef short v4s __attribute__((ext_vector_type(4)));
typedef short v8s __attribute__((ext_vector_type(8)));
typedef _Float16 v4h __attribute__((ext_vector_type(4)));
typedef _Float16 v8h __attribute__((ext_vector_type(8)));
typedef __bf16 v2b __attribute__((ext_vector_type(2)));
typedef __bf16 v8b __attribute__((ext_vector_type(8)));
typedef float v2f __attribute__((ext_vector_type(2)));
typedef float v4f __attribute__((ext_vector_type(4)));
typedef float v16f __attribute__((ext_vector_type(16)));
typedef u32 v2u __attribute__((ext_vector_type(2)));
typedef u32 v4u __attribute__((ext_vector_type(4)));

#define LAS __attribute__((address_space(3)))
#define GAS __attribute__((address_space(1)))
#define DEVI __device__ __forceinline__

constexpr int D_ = 512, F_ = 1792, DF_ = 257;
constexpr size_t OFF_H = (size_t)24 << 20;
constexpr size_t OFF_S1 = (size_t)32 << 20;
constexpr size_t OFF_WT = (size_t)34 << 20;
constexpr size_t OFF_S2 = (size_t)37 << 20;
constexpr size_t OFF_M = (size_t)39 << 20;
constexpr size_t OFF_FL = OFF_M;
constexpr u32 FL_S = 0, FL_A = 1024, FL_H = 2048;
constexpr size_t OFF_BC = OFF_M + 12288;
constexpr size_t OFF_MB = OFF_M + 16384;
constexpr size_t OFF_RS2 = OFF_M + 24576;
constexpr size_t OFF_PQ = OFF_M + 65536;
constexpr size_t OFF_CSP = OFF_M + 131072;
constexpr int SPIN_CAP = 1 << 16;

#define WS_BASE(n) ((((n) - ((size_t)40 << 20))) & ~(size_t)0x1fffff)
#define STAMP 1
#define PROBE 0
#define PROBE_B 5
constexpr size_t OFF_STAMP = 0;
#define STAMP_DECL u32 stamps_[32] = {}
#define STAMPT(id) do { u64 t_; asm volatile("s_memrealtime %0\n\ts_waitcnt lgkmcnt(0)" : "=s"(t_) :: "memory"); stamps_[id] = (u32)t_; } while (0)
#define STAMPC(id) do { u64 t_; asm volatile("s_memtime %0\n\ts_waitcnt lgkmcnt(0)" : "=s"(t_) :: "memory"); stamps_[id] = (u32)t_; } while (0)
#define STAMP_OUT(wsp) do { if (blockIdx.x == PROBE_B && threadIdx.x < 64) { u32 dl_ = 500u + (PROBE == 99 ? (stamps_[31] - stamps_[30]) / 20u : PROBE == 98 ? (stamps_[2] - stamps_[1]) : (stamps_[PROBE % 32] - stamps_[0])); u32 t1_; do { u64 t_; asm volatile("s_memrealtime %0\n\ts_waitcnt lgkmcnt(0)" : "=s"(t_) :: "memory"); t1_ = (u32)t_; __builtin_amdgcn_s_sleep(1); } while (t1_ - stamps_[21] < dl_); } } while (0)

template <int I> struct IC { static constexpr int v = I; constexpr operator int() const { return I; } };
template <int B, int E, class Fn> DEVI void static_for(Fn&& f) {
  if constexpr (B < E) { f(IC<B>{}); static_for<B + 1, E>(f); }
}
#define WAIT_VM(n) asm volatile("s_waitcnt vmcnt(%0)" ::"n"(n) : "memory")
#define BARRIER() asm volatile("s_waitcnt lgkmcnt(0)\n\ts_barrier" ::: "memory")
#define SCHED() __builtin_amdgcn_sched_barrier(0)
#define PIN(x) asm volatile("" : "+v"(x))

DEVI int s_of_pi(int pi) {
  int RG = pi >> 6, i = pi & 63;
  return 128 * (4 * (RG & 3) + (i & 3)) + 8 * (i >> 2) + (RG >> 2);
}
DEVI void dma2(u32 voff0, u32 voff1, const char* sbase, u32 lds) {
  asm volatile("s_mov_b32 m0, %3\n\ts_nop 0\n\tglobal_load_lds_dwordx4 %0, %2\n\tglobal_load_lds_dwordx4 %1, %2 offset:1024" ::"v"(voff0), "v"(voff1), "s"(sbase), "s"(lds) : "memory");
}
DEVI void dma2c(u32 voff0, u32 voff1, const char* sbase, u32 lds) {
  asm volatile("s_mov_b32 m0, %3\n\ts_nop 0\n\tglobal_load_lds_dwordx4 %0, %2 sc1\n\tglobal_load_lds_dwordx4 %1, %2 offset:1024 sc1" ::"v"(voff0), "v"(voff1), "s"(sbase), "s"(lds) : "memory");
}
template <int OFF> DEVI void dma1(u32 voff, const char* sbase, u32 lds) {
  asm volatile("s_mov_b32 m0, %2\n\ts_nop 0\n\tglobal_load_lds_dwordx4 %0, %1 offset:%3" ::"v"(voff), "s"(sbase), "s"(lds), "n"(OFF) : "memory");
}
template <int OFF> DEVI void dma1s(u32 voff, const char* sbase, u32 lds) {
  asm volatile("s_mov_b32 m0, %2\n\ts_nop 0\n\tglobal_load_lds_dwordx4 %0, %1 offset:%3 sc1" ::"v"(voff), "s"(sbase), "s"(lds), "n"(OFF) : "memory");
}
DEVI void dma1c(u32 voff, const char* sbase, u32 lds) {
  asm volatile("s_mov_b32 m0, %2\n\ts_nop 0\n\tglobal_load_lds_dword %0, %1 sc1" ::"v"(voff), "s"(sbase), "s"(lds) : "memory");
}
DEVI v2u ld8c_sync(u32 voff, const char* sb) { v2u d; asm volatile("global_load_dwordx2 %0, %1, %2 sc1\n\ts_waitcnt vmcnt(0)" : "=&v"(d) : "v"(voff), "s"(sb) : "memory"); return d; }
DEVI u32 ld4c_sync(u32 voff, const char* sb) { u32 d; asm volatile("global_load_dword %0, %1, %2 sc1\n\ts_waitcnt vmcnt(0)" : "=&v"(d) : "v"(voff), "s"(sb) : "memory"); return d; }
DEVI v4u ld16c_sync(u32 voff, const char* sb) { v4u d; asm volatile("global_load_dwordx4 %0, %1, %2 sc1\n\ts_waitcnt vmcnt(0)" : "=&v"(d) : "v"(voff), "s"(sb) : "memory"); return d; }
template <class T> DEVI void ld16c(T& d, u32 voff, const char* sb) { asm volatile("global_load_dwordx4 %0, %1, %2 sc1" : "=&v"(d) : "v"(voff), "s"(sb) : "memory"); }
template <class T> DEVI void ld8c(T& d, u32 voff, const char* sb) { asm volatile("global_load_dwordx2 %0, %1, %2 sc1" : "=&v"(d) : "v"(voff), "s"(sb) : "memory"); }
template <class T> DEVI void ld4c(T& d, u32 voff, const char* sb) { asm volatile("global_load_dword %0, %1, %2 sc1" : "=&v"(d) : "v"(voff), "s"(sb) : "memory"); }
template <class T> DEVI void ld16(T& d, u32 voff, const char* sb) { asm volatile("global_load_dwordx4 %0, %1, %2" : "=&v"(d) : "v"(voff), "s"(sb) : "memory"); }
DEVI void ld2(u32& d, u32 voff, const char* sb) { asm volatile("global_load_ushort %0, %1, %2" : "=&v"(d) : "v"(voff), "s"(sb) : "memory"); }
template <class T> DEVI void st16c(u32 voff, const char* sb, T v) { asm volatile("global_store_dwordx4 %0, %1, %2 sc1\n\ts_nop 1" ::"v"(voff), "v"(v), "s"(sb) : "memory"); }
template <class T> DEVI void st8c(u32 voff, const char* sb, T v) { asm volatile("global_store_dwordx2 %0, %1, %2 sc1" ::"v"(voff), "v"(v), "s"(sb) : "memory"); }
template <class T> DEVI void st4c(u32 voff, const char* sb, T v) { asm volatile("global_store_dword %0, %1, %2 sc1" ::"v"(voff), "v"(v), "s"(sb) : "memory"); }

DEVI v4s trread(LAS char* p) { return __builtin_amdgcn_ds_read_tr16_b64_v4i16((LAS v4s*)p); }
DEVI v8s cat8(v4s a, v4s b) { return __builtin_shufflevector(a, b, 0, 1, 2, 3, 4, 5, 6, 7); }
DEVI float bflo(u32 v) { return __builtin_bit_cast(float, v << 16); }
DEVI float bfhi(u32 v) { return __builtin_bit_cast(float, v & 0xffff0000u); }
DEVI float rbf(float v) { return (float)(__bf16)v; }
DEVI u32 pkbf(float a, float b) { v2b p = {(__bf16)a, (__bf16)b}; return __builtin_bit_cast(u32, p); }
DEVI u32 pkbf_o(float a, float b) { u32 r = pkbf(a, b); asm("" : "+v"(r)); return r; }
DEVI float cosr(float rev) { return __builtin_amdgcn_cosf(rev); }
DEVI float sinr(float rev) { return __builtin_amdgcn_sinf(rev); }
DEVI v4f mfma16h(v8h a, v8h b, v4f c) { return __builtin_amdgcn_mfma_f32_16x16x32_f16(a, b, c, 0, 0, 0); }
DEVI v4f mfma16b(v8s a, v4u b, v4f c) { return __builtin_amdgcn_mfma_f32_16x16x32_bf16(__builtin_bit_cast(v8b, a), __builtin_bit_cast(v8b, b), c, 0, 0, 0); }
DEVI v16f mfma32b(v8s a, v8s b, v16f c) { return __builtin_amdgcn_mfma_f32_32x32x16_bf16(__builtin_bit_cast(v8b, a), __builtin_bit_cast(v8b, b), c, 0, 0, 0); }
DEVI v16f mfma32h(v8h a, v8s b, v16f c) { return __builtin_amdgcn_mfma_f32_32x32x16_f16(a, __builtin_bit_cast(v8h, b), c, 0, 0, 0); }
DEVI v8h pack_ri(v4f re, v4f im) {
  v8h r;
  r[0] = (_Float16)re[0]; r[1] = (_Float16)re[1]; r[2] = (_Float16)re[2]; r[3] = (_Float16)re[3];
  r[4] = (_Float16)im[0]; r[5] = (_Float16)im[1]; r[6] = (_Float16)im[2]; r[7] = (_Float16)im[3];
  return r;
}
DEVI v4h pack4h(v4f a) { v4h r; r[0] = (_Float16)a[0]; r[1] = (_Float16)a[1]; r[2] = (_Float16)a[2]; r[3] = (_Float16)a[3]; return r; }
DEVI void twid(v4f& re, v4f& im, const v4f& c, const v4f& s) {
#pragma unroll
  for (int r = 0; r < 4; ++r) { float a = re[r], b = im[r]; re[r] = a * c[r] + b * s[r]; im[r] = b * c[r] - a * s[r]; }
}
template <int CTRL> DEVI float dppadd(float v) {
  return v + __builtin_bit_cast(float, __builtin_amdgcn_update_dpp(0, __builtin_bit_cast(int, v), CTRL, 0xf, 0xf, true));
}
DEVI float wave_sum(float v) {
  v = dppadd<0xB1>(v); v = dppadd<0x4E>(v); v = dppadd<0x141>(v); v = dppadd<0x140>(v);
  int bb = __builtin_bit_cast(int, v);
  float a0 = __builtin_bit_cast(float, __builtin_amdgcn_readlane(bb, 0)), a1 = __builtin_bit_cast(float, __builtin_amdgcn_readlane(bb, 16));
  float a2 = __builtin_bit_cast(float, __builtin_amdgcn_readlane(bb, 32)), a3 = __builtin_bit_cast(float, __builtin_amdgcn_readlane(bb, 48));
  return (a0 + a1) + (a2 + a3);
}
DEVI float dot2ones(u32 v, float acc) {
  const v2b ones = {(__bf16)1.0f, (__bf16)1.0f};
  return __builtin_amdgcn_fdot2_f32_bf16(__builtin_bit_cast(v2b, v), ones, acc, false);
}
template <int CTRL> DEVI u32 dppmax(u32 v) { return max(v, (u32)__builtin_amdgcn_update_dpp(0, (int)v, CTRL, 0xf, 0xf, true)); }
DEVI u32 wave_max(u32 v) {
  v = dppmax<0xB1>(v); v = dppmax<0x4E>(v); v = dppmax<0x141>(v); v = dppmax<0x140>(v);
  u32 a0 = __builtin_amdgcn_readlane(v, 0), a1 = __builtin_amdgcn_readlane(v, 16), a2 = __builtin_amdgcn_readlane(v, 32), a3 = __builtin_amdgcn_readlane(v, 48);
  return max(max(a0, a1), max(a2, a3));
}

struct Frag1 { v8s x0, x1, w1, w2; };

constexpr int L_SCR = 147456;
constexpr int L_CS = L_SCR, L_RED = L_SCR + 1024, L_MX = L_SCR + 1088, L_BC0 = L_SCR + 2048, L_BC1 = L_SCR + 2304;
constexpr int L_TAB = L_SCR + 3072;
constexpr int L_T = L_SCR + 7168, L_FA = L_T, L_E1 = L_T, L_E2 = 81920, L_ZX = 81920;
constexpr int L_C1 = 98304, L_IMG = L_C1 + 9216;
constexpr int L_FR = 131072;
constexpr int E1P = 544, E2P = 1024 + 72;
DEVI constexpr int wslot(int n) {
  constexpr int u[14] = {6, 7, 8, 0, 1, 2, 3, 4, 6, 7, 8, 0, 1, 2};
  return u[n] * 16384;
}
DEVI constexpr int aslot(int n) {
  constexpr int u[18] = {3, 4, 5, 9, 2, 6, 7, 8, 0, 1, 3, 4, 5, 9, 2, 6, 7, 8};
  return u[n] * 16384;
}

__global__ __launch_bounds__(512, 2) void kmain(const u16* __restrict__ x, const u16* __restrict__ wr, const u16* __restrict__ wi,
                                                const u16* __restrict__ Wg, const u16* __restrict__ Wu, const u16* __restrict__ Wd,
                                                char* __restrict__ ws, u16* __restrict__ out) {
  __shared__ __attribute__((aligned(16))) char smem_[163840];
  LAS char* smem = (LAS char*)smem_;
  const u32 lbase = (u32)(size_t)smem;
  const int tid = threadIdx.x, l = tid & 63;
  const int w = __builtin_amdgcn_readfirstlane(tid >> 6);
  const int b = blockIdx.x, xcd = b & 7, idx = b >> 3;
  const bool ffn = idx < 28;
  const int g = l >> 4, o = l & 15;
  const int h = l >> 5, blk = (l >> 4) & 1, q = (l & 15) >> 2, pp = l & 3;
  const char* FL = ws + OFF_FL;
  const char* BC = ws + OFF_BC;
  STAMP_DECL; STAMPT(0);

  const int pi = b * 8 + w;
  v4u xr;
  {
    u64 pc; u32 len;
    asm volatile("s_getpc_b64 %0\nkm_pf_a:\n\ts_mov_b32 %1, km_pf_b-km_pf_a" : "=s"(pc), "=s"(len));
    dma1<0>(min((u32)(((idx & 3) * 8 + w) * 1024 + l * 16), len) & ~15u, (const char*)(pc & ~15ull), lbase + L_IMG + w * 1024);
  }
  ld16(xr, (u32)(s_of_pi(pi) * 1024 + l * 16), (const char*)x);
  if (w == 0) dma1c((u32)((l & 1) * 4), BC, lbase + L_BC0);
  const int fkb = (b & 7) * 2 + (w >> 2), fcq = w & 3;
  {
    const u32 ks = (u32)((idx >> 1) + 16 * fkb + 256 * (4 * (idx & 1) + ((l >> 3) & 3)));
    const u32 base = ((ks * (DF_ * 2)) & ~15u) + fcq * 128;
    const u32 dlt = (u32)((const char*)wi - (const char*)wr);
    (void)dlt;
    const u32 c0 = ((l & 7) ^ (((l >> 4) & 1) << 2)) << 4;
    if (l < 32) dma1<0>(base + c0, (const char*)wr, lbase + L_FR + w * 2048);
    else dma1<0>(base + c0, (const char*)wi, lbase + L_FR + w * 2048);
    if (fcq == 3) {
      const u32 t0 = min(base + 128 + (c0 & 16), (u32)(2048 * DF_ * 2 - 16));
      if (l < 32) dma1<0>(t0, (const char*)wr, lbase + L_FR + w * 2048 + 1024);
      else dma1<0>(t0, (const char*)wi, lbase + L_FR + w * 2048 + 1024);
    }
  }
  const int p = (xcd >> 1) * 4 + idx / 7, j = (xcd & 1) * 7 + idx % 7;
  const int sh = w >> 2, fq = w & 3;
  u32 aoff[2];
#pragma unroll
  for (int i = 0; i < 2; ++i) {
    int row = (2 * w + i) * 8 + (l >> 3);
    aoff[i] = s_of_pi((p & 15) * 128 + row) * 1024 + (((l & 7) ^ ((row >> 1) & 7)) << 4) - i * 1024;
  }
  const u32 boff = (u32)(2 * w * 4 + (l >> 4)) * (F_ * 2) + j * 256 + (((l & 15) ^ ((l >> 4) << 2)) << 4);
  const u32 boff1 = boff + 4 * F_ * 2 - 1024;
  const int rot = p >> 1;
  auto issue = [&](int t, int slot) {
    const int kt = (t + rot) & 7;
    u32 dst = lbase + slot * 49152 + w * 2048;
    dma2(aoff[0], aoff[1], (const char*)x + kt * 128, dst);
    dma2(boff, boff1, (const char*)Wg + (size_t)kt * (64 * F_ * 2), dst + 16384);
    dma2(boff, boff1, (const char*)Wu + (size_t)kt * (64 * F_ * 2), dst + 32768);
  };
  const int sp = xcd * 4 + (idx - 28);
  if (ffn) issue(0, 0);

  u32 E = 0, tick = 0;
  const bool coord = (b == 224) && (w == 0);
  auto do_tick = [&]() { if (coord) { ++tick; if (l == 0) st8c(0u, BC, v2u{E, tick}); } };
  auto get_epoch = [&]() {
    const v2u b0 = *(LAS v2u*)(smem + L_BC0);
    v2u b1 = *(LAS v2u*)(smem + L_BC1);
    int spins = 0;
    while (__builtin_amdgcn_readfirstlane((b0[0] ^ b1[0]) | (b0[1] ^ b1[1])) == 0 && ++spins < SPIN_CAP) {
      __builtin_amdgcn_s_sleep(4);
      b1 = ld8c_sync(0u, BC);
    }
    E = __builtin_amdgcn_readfirstlane(b1[0]);
  };
  auto signal_a = [&]() {
    v4u r0 = *(LAS v4u*)(smem + L_RED), r1 = *(LAS v4u*)(smem + L_RED + 16);
    u32 m = max(max(max(r0[0], r0[1]), max(r0[2], r0[3])), max(max(r1[0], r1[1]), max(r1[2], r1[3])));
    if (l == 0) { st4c((u32)(FL_A + b * 4), FL, E | (m >> 7)); *(LAS u32*)(smem + L_MX + 4) = E; }
  };
  auto poll4 = [&](v4u f, u32 voff) -> v4u {
    int spins = 0;
    while (!__all((((f[0] ^ E) | (f[1] ^ E) | (f[2] ^ E) | (f[3] ^ E)) >> 8) == 0) && ++spins < SPIN_CAP) {
      do_tick();
      __builtin_amdgcn_s_sleep(12);
      f = ld16c_sync(voff, FL);
    }
    return f;
  };
  auto check_a = [&](v4u f) {
    f = poll4(f, (u32)(FL_A + l * 16));
    u32 m = wave_max(max(max(f[0], f[1]), max(f[2], f[3]))) & 0xffu;
    if (l == 0) *(LAS u32*)(smem + L_MX) = m;
  };
  if (coord) {
    v4u f[10];
#pragma unroll
    for (int i = 0; i < 10; ++i) ld16c(f[i], (u32)(i * 1024 + l * 16), FL);
    WAIT_VM(0);
    u32 c = __builtin_amdgcn_readfirstlane(*(LAS u32*)(smem + L_BC0)) >> 8;
    bool hit;
    do {
      c = (c + 1) & 0xffffffu; hit = false;
#pragma unroll
      for (int i = 0; i < 10; ++i) { PIN(f[i]); hit |= ((f[i][0] >> 8) == c) | ((f[i][1] >> 8) == c) | ((f[i][2] >> 8) == c) | ((f[i][3] >> 8) == c); }
    } while (__any(hit));
    E = c << 8;
    do_tick();
  }

  {
    const float fo = (float)o, fg = (float)(4 * g);
    if (w < 2) {
      const float st = fo * (1.0f / 32.0f), a0 = fmaf(fg, st, w ? 0.25f : 0.f);
      v4u hi, lo;
#pragma unroll
      for (int e2 = 0; e2 < 4; ++e2) {
        float c0 = cosr(fmaf((float)(16 * (e2 >> 1) + 2 * (e2 & 1)), st, a0)), c1 = cosr(fmaf((float)(16 * (e2 >> 1) + 2 * (e2 & 1) + 1), st, a0));
        u32 ch = pkbf_o(c0, c1);
        hi[e2] = ch; lo[e2] = pkbf(c0 - bflo(ch), c1 - bfhi(ch));
      }
      *(LAS v4u*)(smem + L_C1 + w * 2048 + l * 16) = hi; *(LAS v4u*)(smem + L_C1 + w * 2048 + 1024 + l * 16) = lo;
    } else if (w < 5) {
      const float st = w == 2 ? (float)(o & 7) * (1.0f / 16.0f) : fo * (w == 3 ? 1.0f / 16.0f : 1.0f / 8.0f);
      const float a0 = (w == 2 && o >= 8) ? 0.25f : 0.f;
      const float sc = w == 2 ? 0.0625f : ((w == 4 && (g >> 1) != (o >> 3)) ? 0.f : 1.0f);
      v8h m;
#pragma unroll
      for (int e = 0; e < 4; ++e) { float a = fmaf(fg + (float)e, st, a0); m[e] = (_Float16)(sc * cosr(a)); m[4 + e] = (_Float16)(sc * sinr(a)); }
      *(LAS v8h*)(smem + (w == 2 ? L_C1 + 4096 : L_TAB + (w - 3) * 1024) + l * 16) = m;
    } else {
      const float st = w == 7 ? fo * (1.0f / 256.0f) : (fo + (w == 6 ? 16.f : 0.f)) * (1.0f / 512.0f);
      v4f c, sn;
#pragma unroll
      for (int e = 0; e < 4; ++e) { float a = (fg + (float)e) * st; c[e] = cosr(a); sn[e] = sinr(a); }
      LAS char* dst = smem + (w == 7 ? L_TAB + 2048 : L_C1 + 5120 + (w - 5) * 2048) + l * 16;
      *(LAS v4f*)dst = c; *(LAS v4f*)(dst + 1024) = sn;
    }
    BARRIER();
    LAS char* C1 = smem + L_C1 + l * 16;
    const v4u Fhr = *(LAS v4u*)C1, Flr = *(LAS v4u*)(C1 + 1024), Fhi = *(LAS v4u*)(C1 + 2048), Fli = *(LAS v4u*)(C1 + 3072);
    const v8h M2 = *(LAS v8h*)(C1 + 4096);
    v4f twc[2], tws[2];
#pragma unroll
    for (int u = 0; u < 2; ++u) { twc[u] = *(LAS v4f*)(C1 + 5120 + u * 2048); tws[u] = *(LAS v4f*)(C1 + 6144 + u * 2048); }
    if (ffn) WAIT_VM(8); else WAIT_VM(2);
    PIN(xr);
    STAMPT(3);
    const float c0 = bflo(__builtin_amdgcn_readfirstlane(xr[0]));
    v2f xv[4], s2 = {0.f, 0.f}, q2 = {0.f, 0.f};
#pragma unroll
    for (int e = 0; e < 4; ++e) {
      xv[e] = v2f{bflo(xr[e]), bfhi(xr[e])};
      v2f t = xv[e] - v2f{c0, c0};
      s2 += t; q2 = __builtin_elementwise_fma(t, t, q2);
    }
    const float dmean = wave_sum(s2[0] + s2[1]) * (1.0f / 512.0f), msq = wave_sum(q2[0] + q2[1]) * (1.0f / 512.0f);
    do_tick();
    const float mb = rbf(c0 + dmean);
    const float var = rbf(fmaxf(msq - dmean * dmean, 0.f));
    const float sd = rbf(__builtin_amdgcn_sqrtf(rbf(var + 1e-5f)));
    const float rs = __builtin_amdgcn_rcpf(sd);
#pragma unroll
    for (int e = 0; e < 4; ++e) {
      v2f t = xv[e] - v2f{mb, mb};
      u32 tb = pkbf_o(t[0], t[1]);
      t = v2f{bflo(tb), bfhi(tb)} * v2f{rs, rs};
      xr[e] = pkbf(t[0], t[1]);
    }
    *(LAS v4u*)(smem + L_IMG + w * 1024 + l * 16) = xr;
    if (l < 2) st4c((u32)(pi * 4 + l * 8192), ws + OFF_MB, l ? rs * rs : -mb);
    const v8s xf = cat8(trread(smem + L_IMG + w * 1024 + l * 8), trread(smem + L_IMG + w * 1024 + 512 + l * 8));
    const int kt1 = (1 + rot) & 7;
    const u32 dst1 = lbase + 49152 + w * 2048;
    if (ffn) dma2(aoff[0], aoff[1], (const char*)x + kt1 * 128, dst1);
    const v8s xg_ = __builtin_bit_cast(v8s, __builtin_bit_cast(v4u, xf) ^ v4u{0x80000000u, 0x80000000u, 0x80000000u, 0x80000000u});
    const v4f z = {0.f, 0.f, 0.f, 0.f};
    v4f dre[2], dim[2];
#pragma unroll
    for (int u = 0; u < 2; ++u) { dre[u] = mfma16b(u ? xg_ : xf, Flr, z); dim[u] = mfma16b(u ? xg_ : xf, Fli, z); }
#pragma unroll
    for (int u = 0; u < 2; ++u) { dre[u] = mfma16b(u ? xg_ : xf, Fhr, dre[u]); dim[u] = mfma16b(u ? xg_ : xf, Fhi, dim[u]); }
#pragma unroll
    for (int u = 0; u < 2; ++u) {
      twid(dre[u], dim[u], twc[u], tws[u]);
      const v8h bb = pack_ri(dre[u], dim[u]);
      v4f d2 = mfma16h(bb, M2, z);
      if (u == 0) {
        const u32 nv = o == 8 ? 0xac002c00u : 0u;
        v4f n = mfma16h(bb, __builtin_bit_cast(v8h, v4u{nv, nv, 0u, 0u}), z);
        if (l == 8) d2[0] = n[0];
      }
      *(LAS v4h*)(smem + L_T + ((o >> 3) * 8 + w) * 544 + (o & 7) * 64 + u * 32 + g * 8) = pack4h(d2);
    }
    if (ffn) {
      dma2(boff, boff1, (const char*)Wg + (size_t)kt1 * (64 * F_ * 2), dst1 + 16384);
      dma2(boff, boff1, (const char*)Wu + (size_t)kt1 * (64 * F_ * 2), dst1 + 32768);
    }
    STAMPT(4);
  }
  auto round1_out = [&]() {
    LAS char* rp = smem + L_T + (4 * (g & 1) + (o >> 2)) * 544 + (w * 32 + (g >> 1) * 16) * 2 + (o & 3) * 8;
    const v8s su = cat8(trread(rp), trread(rp + 8 * 544));
    LAS char* fp_ = smem + L_FR + w * 2048 + q * 128 + (((2 * g + (pp >> 1)) ^ ((q >> 1) << 2)) << 4) + (pp & 1) * 8;
    const v4u fu = __builtin_bit_cast(v4u, cat8(trread(fp_), trread(fp_ + 512)));
    st16c((u32)(b * 8192 + (w * 32 + (g >> 1) * 16 + o) * 32 + (g & 1) * 16), ws + OFF_S1, su);
    st16c((u32)((idx * 16 + fkb) * 4608 + (fcq * 64 + l) * 16), ws + OFF_WT, fu);
    typedef unsigned short v2us __attribute__((ext_vector_type(2)));
    const int dl = (idx >> 1) & 7;
    u32 msk = (fcq == 0 && l < dl) ? 0u : 0x7fff7fffu;
    v2us m2 = {0, 0};
#pragma unroll
    for (int e = 0; e < 4; ++e) { u32 t = fu[e] & msk; m2 = __builtin_elementwise_max(m2, __builtin_bit_cast(v2us, t)); }
    if (fcq == 3) {
      LAS char* tp = smem + L_FR + w * 2048 + 1024 + q * 128 + (pp >> 1) * 16 + (pp & 1) * 8;
      const v4u tu = __builtin_bit_cast(v4u, cat8(trread(tp), trread(tp + 512)));
      if (l < 16) st16c((u32)((idx * 16 + fkb) * 4608 + (256 + l) * 16), ws + OFF_WT, tu);
      msk = (o <= dl) ? 0x7fff7fffu : 0u;
#pragma unroll
      for (int e = 0; e < 4; ++e) { u32 t = tu[e] & msk; m2 = __builtin_elementwise_max(m2, __builtin_bit_cast(v2us, t)); }
    }
    const u32 mx = wave_max(max((u32)m2[0], (u32)m2[1]));
    if (l == 0) *(LAS u32*)(smem + L_RED + w * 4) = mx;
    if (w == 0 && !coord) dma1c((u32)((l & 1) * 4), BC, lbase + L_BC1);
  };

  const int RG = xcd * 4 + (idx >> 3), ct = idx & 7;
  const int kd = xcd * 32 + idx;
  const u32 swz = ((l & 7) ^ (((l >> 4) & 1) << 2)) << 4;
  const u32 ro0 = (u32)((l >> 3) * 1024) + swz, ro1 = ro0 + 8192 - 1024;
  const char* bsrc = (const char*)Wd + (size_t)(w * 16) * 1024 + ct * 128;
  auto issueW = [&](int n) { dma2(ro0, ro1, bsrc + (size_t)n * 131072, lbase + wslot(n) + w * 2048); };
  const int dh = w & 1, kq = w >> 1, ph = kq >> 1, kh = kq & 1;
  const int epi = RG * 64 + ph * 32 + (l & 31);
  const u32 eoff = (u32)(s_of_pi(epi) * D_ + ct * 64 + dh * 32 + kh * 16 + h * 8) * 2;
  v4u wt, s1v, xe; v4f pq[4]; float rs2;
  auto load_xe = [&]() { ld16(xe, eoff, (const char*)x); };
  auto load_s1 = [&]() { ld16c(s1v, (u32)((((4 * w + g) * 8 + (o >> 1)) * 256 + kd) * 32 + (o & 1) * 16), ws + OFF_S1); };
  auto load_wt = [&]() { ld16c(wt, (u32)((w * 64 + l) * 4608 + (kd + ((2 * w + (g >> 1)) & 7)) * 16), ws + OFF_WT); };
  auto load_rs = [&]() { ld4c(rs2, (u32)(epi * 4), ws + OFF_RS2); };
  v4f t2c, t2s, t3c, t3s;
  auto trig2 = [&]() {
    const float fw = (float)w, st2 = fw * (1.0f / 128.0f), a2 = fw * (float)(o + 64 * g) * (1.0f / 2048.0f);
#pragma unroll
    for (int e = 0; e < 4; ++e) { float b2 = fmaf((float)e, st2, a2); t2c[e] = cosr(b2); t2s[e] = sinr(b2); }
    PIN(t2c); PIN(t2s);
  };
  auto trig3 = [&]() {
    const float f7 = (float)(o & 7), st3 = f7 * (1.0f / 128.0f), a3 = f7 * (float)(2 * w + (o >> 3) + 64 * g) * (1.0f / 2048.0f);
#pragma unroll
    for (int e = 0; e < 4; ++e) { float b3 = fmaf((float)e, st3, a3); t3c[e] = cosr(b3); t3s[e] = sinr(b3); }
    PIN(t3c); PIN(t3s);
  };
  v4u f16; v4f t1c, t1s, re, im;
  const v4f z4 = {0.f, 0.f, 0.f, 0.f};
  auto imf = [](v4u f) { return __builtin_bit_cast(v8h, v4u{f[2] ^ 0x80008000u, f[3] ^ 0x80008000u, f[0], f[1]}); };
  auto segA0 = [&]() {
    f16 = *(LAS v4u*)(smem + L_TAB + l * 16);
    t1c = *(LAS v4f*)(smem + L_TAB + 2048 + l * 16); t1s = *(LAS v4f*)(smem + L_TAB + 3072 + l * 16);
  };
  auto segA1 = [&]() {
    PIN(s1v);
    const v8h a = __builtin_bit_cast(v8h, s1v);
    re = mfma16h(a, __builtin_bit_cast(v8h, f16), z4); im = mfma16h(a, imf(f16), z4);
  };
  auto segA2 = [&]() {
    twid(re, im, t1c, t1s);
    const v8h a = pack_ri(re, im);
    re = mfma16h(__builtin_bit_cast(v8h, f16), a, z4); im = mfma16h(imf(f16), a, z4);
  };
  auto segA3 = [&]() {
    twid(re, im, t2c, t2s);
    *(LAS v4h*)(smem + L_E1 + w * E1P + o * 32 + g * 8) = pack4h(re);
    *(LAS v4h*)(smem + L_E1 + (8 + w) * E1P + o * 32 + g * 8) = pack4h(im);
  };

  if (ffn) {
    v16f acc[2][2] = {};
    float mbr[2];
    const u32 wb0 = (u32)((8 * h + q) * 256 + ((fq ^ q) << 6) + blk * 32 + pp * 8);
    const u32 r1 = 16384 + sh * 16384, r2 = 32768 - sh * 16384;
    const int xrow = sh * 64 + (l & 31);
    const u32 xb0 = (u32)(xrow * 128 + ((h ^ ((xrow >> 1) & 7)) << 4));
    auto rd = [&](Frag1& f, LAS char* sb, int i) {
      LAS char* xa = sb + (xb0 ^ (i << 5));
      LAS char* wa_ = sb + wb0 + (i << 12);
      f.w1 = cat8(trread(wa_ + r1), trread(wa_ + r1 + 1024));
      f.x0 = *(LAS v8s*)xa; f.x1 = *(LAS v8s*)(xa + 4096);
      f.w2 = cat8(trread(wa_ + r2), trread(wa_ + r2 + 1024));
    };
    const u32 one = ((l & 15) == ((l >> 4) & 1)) ? 0x3f803f80u : 0u;
    const v8s sel = __builtin_bit_cast(v8s, v4u{one, one, one, one});
    v4f csa = {0.f, 0.f, 0.f, 0.f};
    Frag1 A, B;
    WAIT_VM(7);
    BARRIER();
    STAMPT(1); STAMPC(30);
    rd(A, smem, 0);
    dma2(aoff[0], aoff[1], (const char*)x + ((2 + rot) & 7) * 128, lbase + 2 * 49152 + w * 2048);
    v4u cs1, cs2;
    static_for<0, 8>([&](auto T) {
      constexpr int t = T, slot = t % 3, nslot = (t + 1) % 3, pslot = (t + 2) % 3;
      LAS char* sb = smem + slot * 49152;
      {
        const int kt = (t + 2 + rot) & 7;
        const char* s1 = t < 6 ? (const char*)Wg + (size_t)kt * (64 * F_ * 2) : bsrc + (size_t)((t - 6) * 3 + 1) * 131072;
        const char* s2 = t < 6 ? (const char*)Wu + (size_t)kt * (64 * F_ * 2) : bsrc + (size_t)((t - 6) * 3 + 2) * 131072;
        const u32 o2 = t < 6 ? boff : ro0, o3 = t < 6 ? boff1 : ro1;
        const u32 dst = lbase + pslot * 49152 + w * 2048;
        rd(B, sb, 1); SCHED();
        acc[0][0] = mfma32b(A.w1, A.x0, acc[0][0]); acc[0][1] = mfma32b(A.w1, A.x1, acc[0][1]);
        if constexpr (t == 0) csa = mfma16b(sel, __builtin_bit_cast(v4u, A.w1), csa);
        SCHED();
        dma1<0>(o2, s1, dst + 16384);
        if constexpr (t == 2) load_xe();
        if constexpr (t == 6) load_wt();
        acc[1][0] = mfma32b(A.w2, A.x0, acc[1][0]); acc[1][1] = mfma32b(A.w2, A.x1, acc[1][1]); SCHED();
        if constexpr (t == 0) { round1_out(); SCHED(); }
        if constexpr (t == 3) { trig2(); SCHED(); }
        if constexpr (t == 4) { trig3(); SCHED(); }
        rd(A, sb, 2); SCHED();
        acc[0][0] = mfma32b(B.w1, B.x0, acc[0][0]); SCHED();
        dma1<1024>(o3, s1, dst + 16384);
        acc[0][1] = mfma32b(B.w1, B.x1, acc[0][1]);
        if constexpr (t == 0) csa = mfma16b(sel, __builtin_bit_cast(v4u, B.w1), csa);
        acc[1][0] = mfma32b(B.w2, B.x0, acc[1][0]); acc[1][1] = mfma32b(B.w2, B.x1, acc[1][1]); SCHED();
        dma1<0>(o2, s2, dst + 32768);
        if constexpr (t == 6) {
          load_rs();
          if (kd == 0) {
#pragma unroll
            for (int r = 0; r < 4; ++r) ld16c(pq[r], (u32)(w * 4096 + r * 1024 + l * 16), ws + OFF_PQ);
          }
        }
        if constexpr (t == 7) { WAIT_VM(13); segA1(); SCHED(); }
        rd(B, sb, 3); SCHED();
        acc[0][0] = mfma32b(A.w1, A.x0, acc[0][0]); acc[0][1] = mfma32b(A.w1, A.x1, acc[0][1]);
        if constexpr (t == 0) csa = mfma16b(sel, __builtin_bit_cast(v4u, A.w1), csa);
        SCHED();
        dma1<1024>(o3, s2, dst + 32768);
        acc[1][0] = mfma32b(A.w2, A.x0, acc[1][0]); acc[1][1] = mfma32b(A.w2, A.x1, acc[1][1]); SCHED();
      }
      if constexpr (t < 7) {
        const char* s0 = t < 5 ? (const char*)x + ((t + 3 + rot) & 7) * 128 : bsrc + (size_t)((t - 5) * 3) * 131072;
        const u32 o0 = t < 5 ? aoff[0] : ro0, o1 = t < 5 ? aoff[1] : ro1;
        const u32 dst = lbase + slot * 49152 + w * 2048;
        if constexpr (t == 0) csa = mfma16b(sel, __builtin_bit_cast(v4u, B.w1), csa);
        WAIT_VM(t == 0 ? 8 : t == 2 ? 7 : t == 6 ? 13 : 6);
        if constexpr (t == 5) { if (w == 0) check_a(*(LAS v4u*)(smem + L_FA + l * 16)); }
        BARRIER();
        if constexpr (t == 0 || t == 5) STAMPT(22 + t);
        acc[0][0] = mfma32b(B.w1, B.x0, acc[0][0]); SCHED();
        rd(A, smem + nslot * 49152, 0);
        if constexpr (t == 6) segA0();
        SCHED();
        if constexpr (t == 0) {
          if (l < 16) {
#pragma unroll
            for (int i = 0; i < 2; ++i) {
              const float hi = rbf(csa[i]);
              st4c((u32)((((j * 2 + (p & 1)) * 256 + sh * 128 + fq * 32 + i * 16 + l) * 8 + rot) * 4), ws + OFF_CSP, pkbf(hi, csa[i] - hi));
            }
          }
        }
        if constexpr (t == 4) { if (w == 0) dma1s<0>((u32)(FL_A + l * 16), FL, lbase + L_FA); }
        if constexpr (t == 5) {
          const u32 co = (u32)(((j * 2 + (p & 1)) * 256 + fq * 32 + (l & 31)) * 32 + h * 16);
          ld16c(cs1, co + sh * 4096, ws + OFF_CSP); ld16c(cs2, co + (1 - sh) * 4096, ws + OFF_CSP);
          ld4c(mbr[0], (u32)((p * 128 + xrow) * 4), ws + OFF_MB); ld4c(mbr[1], (u32)((p * 128 + xrow + 32) * 4), ws + OFF_MB);
          load_s1();
        }
        dma1<0>(o0, s0, dst);
        acc[0][1] = mfma32b(B.w1, B.x1, acc[0][1]);
        acc[1][0] = mfma32b(B.w2, B.x0, acc[1][0]);
        acc[1][1] = mfma32b(B.w2, B.x1, acc[1][1]); SCHED();
        dma1<1024>(o1, s0, dst);
        if constexpr (t == 1) { if (w == 0) { get_epoch(); signal_a(); } }
        SCHED();
      } else {
        segA2(); SCHED();
        PIN(cs1); PIN(cs2); PIN(mbr[0]); PIN(mbr[1]);
        const u32 m0_ = pkbf(mbr[0], mbr[0]), m1_ = pkbf(mbr[1], mbr[1]);
        const v8s mb0 = __builtin_bit_cast(v8s, v4u{m0_, m0_, m0_, m0_}), mb1 = __builtin_bit_cast(v8s, v4u{m1_, m1_, m1_, m1_});
        acc[0][0] = mfma32b(B.w1, B.x0, acc[0][0]); acc[1][0] = mfma32b(B.w2, B.x0, acc[1][0]);
        acc[0][0] = mfma32b(__builtin_bit_cast(v8s, cs1), mb0, acc[0][0]); acc[1][0] = mfma32b(__builtin_bit_cast(v8s, cs2), mb0, acc[1][0]);
        acc[0][1] = mfma32b(B.w1, B.x1, acc[0][1]); acc[1][1] = mfma32b(B.w2, B.x1, acc[1][1]);
        acc[0][1] = mfma32b(__builtin_bit_cast(v8s, cs1), mb1, acc[0][1]); acc[1][1] = mfma32b(__builtin_bit_cast(v8s, cs2), mb1, acc[1][1]); SCHED();
      }
    });
    STAMPT(2); STAMPC(31);
    auto fin = [&](const v16f& ag, const v16f& au, int rt) {
#pragma unroll
      for (int s2i = 0; s2i < 2; ++s2i) {
        v4u ov;
#pragma unroll
        for (int e = 0; e < 4; ++e) {
          const int r = s2i * 8 + 2 * e;
          v2f gg = v2f{fmaxf(ag[r], 0.f), fmaxf(ag[r + 1], 0.f)} * v2f{au[r], au[r + 1]};
          ov[e] = pkbf(gg[0], gg[1]);
        }
        st16c((u32)(((p * 4 + sh * 2 + rt) * 112 + (j * 8 + fq * 2 + s2i)) * 1024 + l * 16), ws + OFF_H, ov);
      }
    };
    if (sh == 0) { fin(acc[0][0], acc[1][0], 0); fin(acc[0][1], acc[1][1], 1); }
    else { fin(acc[1][0], acc[0][0], 0); fin(acc[1][1], acc[0][1], 1); }
    STAMPT(6);
    segA3();
    WAIT_VM(4);
  } else {
    WAIT_VM(1);
    BARRIER();
    round1_out();
    do_tick();
    if (w == 7) {
      u32 sq[8];
      const int kss = (sp >> 1) + 16 * (l >> 2) + 256 * (4 * (sp & 1) + (l & 3)), km = (2048 - kss) & 2047;
      const u32 o1 = (u32)(kss * DF_ * 2), o2 = (u32)(km * DF_ * 2);
      ld2(sq[0], o1, (const char*)wr); ld2(sq[1], o1, (const char*)wi); ld2(sq[2], o2, (const char*)wr); ld2(sq[3], o2, (const char*)wi);
      ld2(sq[4], o1 + 512, (const char*)wr); ld2(sq[5], o1 + 512, (const char*)wi); ld2(sq[6], o2 + 512, (const char*)wr); ld2(sq[7], o2 + 512, (const char*)wi);
      WAIT_VM(0);
#pragma unroll
      for (int i = 0; i < 8; ++i) PIN(sq[i]);
      float war = bflo(sq[0]) + bflo(sq[2]), wai = bflo(sq[1]) - bflo(sq[3]);
      float wbr = bflo(sq[4]) + bflo(sq[6]), wbi = bflo(sq[5]) - bflo(sq[7]);
      v4f pv = {0.25f * (war + wbr), 0.25f * (wai + wbi), 0.25f * (war - wbr), 0.25f * (wai - wbi)};
      st16c((u32)((((sp >> 2) * 4 + (l & 3)) * 64 + (sp & 3) * 16 + (l >> 2)) * 16), ws + OFF_PQ, pv);
    }
    WAIT_VM(0);
    BARRIER();
    STAMPT(1); STAMPC(30);
    if (w == 0) { if (!coord) get_epoch(); signal_a(); }
    dma1c((u32)(sp * 57344 + (w * 112 + l) * 64), (const char*)Wd, lbase + 49152);
    dma1c((u32)(sp * 57344 + (w * 112 + 48 + l) * 64), (const char*)Wd, lbase + 49152);
    static_for<0, 6>([&](auto N) { issueW(N); });
#pragma unroll
    for (int i = 0; i < 8; ++i) dma1c((u32)(786432 + (idx & 3) * 262144 + ((i * 8 + w) * 64 + l) * 64), (const char*)Wd, lbase + 49152);
    load_xe();
    trig2(); trig3(); segA0();
    if (w == 0) check_a(v4u{~E, 0u, 0u, 0u});
    BARRIER();
    load_s1(); load_wt(); load_rs();
    WAIT_VM(0);
    segA1(); segA2(); segA3();
    STAMPT(2); STAMPC(31);
  }
  PIN(wt); PIN(xe); PIN(rs2);
  BARRIER();
  STAMPT(5);

  const u32 trG = (u32)(kq * 4096 + (4 * h + q) * 128 + ((dh ^ ((q >> 1) & 1)) << 6) + (blk ^ kh) * 32 + pp * 8);
  v8s Wf[14][2];
  auto readW = [&](auto N) {
    constexpr int n = N;
    LAS char* sb = smem + wslot(n) + trG;
    Wf[n][0] = cat8(trread(sb), trread(sb + 1024)); Wf[n][1] = cat8(trread(sb + 2048), trread(sb + 3072));
  };
  const char* hsrc = ws + OFF_H + ((size_t)(RG * 2 + (w >> 2)) * 112 + (w & 3) * 2) * 1024;
  const u32 so0 = (u32)((l >> 3) * 4096) + swz, so1 = so0 + 32768 - 1024;
  const char* ssrc = ws + OFF_S2 + (size_t)(w * 16) * 4096 + RG * 128;
  auto issueP = [&](int n, auto HALF) {
    constexpr int hf = HALF;
    if (n < 14) dma1s<hf * 1024>((u32)(l * 16), hsrc + n * 8192, lbase + aslot(n) + w * 2048);
    else dma1s<hf * 1024>(hf ? so1 : so0, ssrc + (size_t)(n - 14) * (128 * 4096), lbase + aslot(n) + w * 2048);
  };
  const int d = ct * 64 + dh * 32 + ((l & 31) ^ (kh << 4));
  const float gst = (float)d * (1.0f / 512.0f), ga0 = (float)(16 * kq + 2 * h) * gst;
  v8h G[8];
  auto gen = [&](auto FI) {
    constexpr int f = FI;
#pragma unroll
    for (int e2 = 0; e2 < 4; ++e2) {
      float a = fmaf((float)(64 * (f >> 1) + 8 * (f & 1) + 4 * (e2 >> 1) + (e2 & 1)), gst, ga0);
      float c = cosr(a), sn = sinr(a);
      if (f == 0 && e2 == 0) { if (kq == 0 && h == 0) { c = 0.5f; sn = (d & 1) ? 0.5f : -0.5f; } }
      G[f][2 * e2] = (_Float16)c; G[f][2 * e2 + 1] = (_Float16)sn;
    }
    PIN(G[f]);
  };

  float oscale;
  const u32 fhoff = (u32)(FL_H + (RG * 64 + min(l, 55)) * 4);
  u32 fp;
  {
    v8h a;
    {
      LAS char* rp = smem + L_E1 + (4 * (g & 1) + (o >> 2)) * E1P + (2 * w + (g >> 1)) * 32 + (o & 3) * 8;
      a = __builtin_bit_cast(v8h, cat8(trread(rp), trread(rp + 8 * E1P)));
    }
    const v4u f8 = *(LAS v4u*)(smem + L_TAB + 1024 + l * 16);
    const v2u me = *(LAS v2u*)(smem + L_MX);
    issueW(6);
    static_for<0, 3>(readW);
    const int Ex = (int)me[0] < 16 ? 16 : ((int)me[0] > 240 ? 240 : (int)me[0]);
    const float wscale = __builtin_bit_cast(float, (u32)(246 - Ex) << 23);
    oscale = __builtin_bit_cast(float, (u32)(Ex - 7) << 23);
    re = mfma16h(__builtin_bit_cast(v8h, f8), a, z4); im = mfma16h(imf(f8), a, z4);
    static_for<3, 6>(readW);
    WAIT_VM(2);
    STAMPT(7);
    if (ffn && l == 0) st4c((u32)(FL_H + ((p * 2 + sh) * 64 + j * 4 + fq) * 4), FL, me[1]);
    issueW(7);
    if (kd != 0) {
#pragma unroll
      for (int r = 0; r < 4; ++r) {
        float fr = ((r & 1) ? bfhi(wt[r >> 1]) : bflo(wt[r >> 1])) * wscale, fi = ((r & 1) ? bfhi(wt[2 + (r >> 1)]) : bflo(wt[2 + (r >> 1)])) * wscale;
        float zr = re[r], zi = im[r];
        re[r] = zr * fr - zi * fi;
        im[r] = -(zr * fi + zi * fr);
      }
    } else {
      const int ka = 2 * w + (g >> 1), kr = o + 64 * (g & 1);
#pragma unroll
      for (int r = 0; r < 4; ++r) { v2f t = {re[r], im[r]}; *(LAS v2f*)(smem + L_ZX + ka * 1024 + (kr + 16 * r) * 8) = t; }
      BARRIER();
#pragma unroll
      for (int r = 0; r < 4; ++r) {
        PIN(pq[r]);
        const int km = (2048 - (ka + 16 * (kr + 16 * r))) & 2047;
        v2f zm = *(LAS v2f*)(smem + L_ZX + (km & 15) * 1024 + (km >> 4) * 8);
        float zr = re[r], zi = im[r];
        re[r] = (zr * pq[r][0] - zi * pq[r][1] + zm[0] * pq[r][2] + zm[1] * pq[r][3]) * wscale;
        im[r] = -(zr * pq[r][1] + zi * pq[r][0] + zm[0] * pq[r][3] - zm[1] * pq[r][2]) * wscale;
      }
    }
    LAS char* E2 = smem + (kd == 0 ? L_E1 : L_E2);
    a = pack_ri(re, im);
    re = mfma16h(a, __builtin_bit_cast(v8h, f8), z4); im = mfma16h(a, imf(f8), z4);
    twid(re, im, t3c, t3s);
    *(LAS v4h*)(E2 + (o & 7) * E2P + (2 * w + (o >> 3)) * 32 + g * 8) = pack4h(re);
    *(LAS v4h*)(E2 + (o & 7) * E2P + 512 + (2 * w + (o >> 3)) * 32 + g * 8) = pack4h(im);
    BARRIER();
    STAMPT(8);
    {
      LAS char* rp = E2 + w * E2P + o * 32 + g * 8;
      a = __builtin_bit_cast(v8h, cat8(*(LAS v4s*)rp, *(LAS v4s*)(rp + 512)));
    }
    issueW(8);
    re = mfma16h(a, __builtin_bit_cast(v8h, f16), z4); im = mfma16h(a, imf(f16), z4);
    issueW(9);
    twid(re, im, t1c, t1s);
    a = pack_ri(re, im);
    re = mfma16h(__builtin_bit_cast(v8h, f16), a, z4); im = mfma16h(imf(f16), a, z4);
    issueW(10);
    st8c((u32)(2 * kd * 4096 + w * 512 + l * 8), ws + OFF_S2, pack4h(re));
    st8c((u32)((2 * kd + 1) * 4096 + w * 512 + l * 8), ws + OFF_S2, pack4h(im));
  }
  STAMPT(9);
  gen(IC<0>{}); issueW(11);
  WAIT_VM(8);
  BARRIER();
  static_for<6, 9>(readW);
  gen(IC<1>{}); issueW(12); gen(IC<2>{}); issueW(13);
  if (w == 0) ld4c(fp, fhoff, FL);
  gen(IC<3>{}); gen(IC<4>{});
  WAIT_VM(0);
  if (w == 0) {
    PIN(fp);
    u32 f = fp;
    int spins = 0;
    while (!__all(f == E) && ++spins < SPIN_CAP) {
      __builtin_amdgcn_s_sleep(2);
      f = ld4c_sync(fhoff, FL);
    }
  }
  BARRIER();
  STAMPT(10);
  if (tid == 0) st4c((u32)(FL_S + b * 4), FL, E);
  issueP(0, IC<0>{}); issueP(0, IC<1>{}); issueP(1, IC<0>{}); issueP(1, IC<1>{});
  static_for<9, 14>(readW);
  gen(IC<5>{});
  issueP(2, IC<0>{}); issueP(2, IC<1>{});
  gen(IC<6>{});
  issueP(3, IC<0>{}); issueP(3, IC<1>{});
  gen(IC<7>{});

  const u32 trS = (u32)(kq * 4096 + (4 * h + q) * 128 + ((ph ^ ((q >> 1) & 1)) << 6) + blk * 32 + pp * 8);
  const u32 hb = (u32)(ph * 8192 + kq * 2048 + l * 16);
  v16f accG[2] = {}, accS[2] = {};
  v8s fa[8], fb[8];
  v4u fs;
  float gv[8];
  const u32 xw = (u32)(w * 6144 + l * 16);
  auto xchg_out = [&](const v16f (&ac)[2], int base) {
    LAS char* X = smem + base + xw;
#pragma unroll
    for (int i = 0; i < 6; ++i) {
      const v16f& a = ac[(i + 2) >> 2]; const int r = ((i + 2) & 3) * 4;
      *(LAS v4f*)(X + i * 1024) = v4f{a[r], a[r + 1], a[r + 2], a[r + 3]};
    }
  };
  auto xchg_in = [&](const v16f (&ac)[2], int base, float (&o8)[8]) {
    v4f s0 = {ac[0][0], ac[0][1], ac[0][2], ac[0][3]}, s1 = {ac[0][4], ac[0][5], ac[0][6], ac[0][7]};
#pragma unroll
    for (int x_ = 1; x_ < 4; ++x_) {
      LAS char* X = smem + base + (w ^ (2 * x_)) * 6144 + (x_ - 1) * 2048 + l * 16;
      s0 += *(LAS v4f*)X; s1 += *(LAS v4f*)(X + 1024);
    }
#pragma unroll
    for (int e = 0; e < 4; ++e) { o8[e] = s0[e]; o8[4 + e] = s1[e]; }
  };
  v4f xg[6];
  float ov[8], ex[8];
  auto sadd = [](float a, float b) { float r; asm("v_add_f32 %0, %1, %2" : "=v"(r) : "v"(a), "v"(b)); return r; };
  auto smul = [](float a, float b) { float r; asm("v_mul_f32 %0, %1, %2" : "=v"(r) : "v"(a), "v"(b)); return r; };
  auto silu_step = [&](int i) {
    if (i < 3) {
#pragma unroll
      for (int e = 0; e < 8; ++e) { ov[e] = sadd(i == 0 ? accG[0][e] : ov[e], xg[2 * i + (e >> 2)][e & 3]); PIN(ov[e]); }
    } else if (i == 3) {
#pragma unroll
      for (int e = 0; e < 8; e += 2) { u32 t = pkbf_o(smul(ov[e], rs2), smul(ov[e + 1], rs2)); ov[e] = bflo(t); ov[e + 1] = bfhi(t); PIN(ov[e]); PIN(ov[e + 1]); }
    } else if (i == 4) {
#pragma unroll
      for (int e = 0; e < 8; ++e) { ex[e] = __builtin_amdgcn_exp2f(ov[e] * -1.44269504088896f); PIN(ex[e]); }
    } else if (i == 5) {
#pragma unroll
      for (int e = 0; e < 8; ++e) { ex[e] = __builtin_amdgcn_rcpf(1.0f + ex[e]); PIN(ex[e]); }
    } else if (i == 6) {
#pragma unroll
      for (int e = 0; e < 8; e += 2) { u32 t = pkbf_o(ov[e] * ex[e], ov[e + 1] * ex[e + 1]); gv[e] = bflo(t); gv[e + 1] = bfhi(t); PIN(gv[e]); PIN(gv[e + 1]); }
    }
  };
  static_for<0, 10>([&](auto V) {
    constexpr int v = V;
    v8s(&cur)[8] = (v & 1) ? fb : fa;
    v8s(&prv)[8] = (v & 1) ? fa : fb;
    auto mmp = [&](int s) {
      if constexpr (v >= 8) {
        accS[0] = mfma32h(G[(v - 8) * 4 + s], prv[2 * s], accS[0]); accS[1] = mfma32h(G[(v - 8) * 4 + s], prv[2 * s + 1], accS[1]);
        SCHED();
        if ((v - 8) * 4 + s >= 1) silu_step((v - 8) * 4 + s - 1);
      } else if constexpr (v >= 1) {
        accG[0] = mfma32b(Wf[2 * (v - 1) + (s >> 1)][s & 1], prv[2 * s], accG[0]);
        accG[1] = mfma32b(Wf[2 * (v - 1) + (s >> 1)][s & 1], prv[2 * s + 1], accG[1]);
      }
      SCHED();
    };
    if constexpr (v < 9) {
      WAIT_VM(v <= 7 ? 4 : 0);
      if constexpr (v == 5) { if (w == 0) { PIN(fs); poll4(fs, (u32)(FL_S + l * 16)); } }
      BARRIER();
      if constexpr (v == 0 || v == 8) STAMPT(11 + v);
      if constexpr (v == 4) { if (w == 0) ld16c(fs, (u32)(FL_S + l * 16), FL); }
      if constexpr (v == 8) {
#pragma unroll
        for (int x_ = 1; x_ < 4; ++x_) {
          LAS char* X = smem + 49152 + (w ^ (2 * x_)) * 6144 + (x_ - 1) * 2048 + l * 16;
          xg[2 * x_ - 2] = *(LAS v4f*)X; xg[2 * x_ - 1] = *(LAS v4f*)(X + 1024);
        }
        SCHED();
      }
      mmp(0);
#pragma unroll
      for (int s = 0; s < 4; ++s) {
        if constexpr (v >= 7) {
          LAS char* sb = smem + aslot(2 * v + (s >> 1)) + (s & 1) * 2048;
          cur[2 * s] = cat8(trread(sb + trS), trread(sb + trS + 1024));
          cur[2 * s + 1] = cat8(trread(sb + (trS ^ 64)), trread(sb + (trS ^ 64) + 1024));
        } else {
          LAS char* sb = smem + aslot(2 * v + (s >> 1)) + (s & 1) * 1024;
          cur[2 * s] = *(LAS v8s*)(sb + hb);
          cur[2 * s + 1] = *(LAS v8s*)(sb + (hb ^ 8192));
        }
      }
      SCHED();
      if constexpr (v <= 6) issueP(2 * v + 4, IC<0>{});
      mmp(1);
      if constexpr (v <= 6) issueP(2 * v + 4, IC<1>{});
      mmp(2);
      if constexpr (v <= 6) issueP(2 * v + 5, IC<0>{});
      mmp(3);
      if constexpr (v <= 6) issueP(2 * v + 5, IC<1>{});
    } else {
      mmp(0); mmp(1); mmp(2); mmp(3);
    }
    if constexpr (v == 7) xchg_out(accG, 49152);
    SCHED();
  });
  xchg_out(accS, 0);
  BARRIER();
  {
    float o8[8], pv[8];
    xchg_in(accS, 0, o8);
#pragma unroll
    for (int e = 0; e < 8; e += 2) { u32 t = pkbf_o(o8[e] * oscale, o8[e + 1] * oscale); pv[e] = bflo(t) * gv[e]; pv[e + 1] = bfhi(t) * gv[e + 1]; }
    u32 a0 = pkbf(pv[0], pv[1]), a1 = pkbf(pv[2], pv[3]), b0 = pkbf(pv[4], pv[5]), b1 = pkbf(pv[6], pv[7]);
    auto r0 = __builtin_amdgcn_permlane32_swap(a0, b0, false, false);
    auto r1 = __builtin_amdgcn_permlane32_swap(a1, b1, false, false);
    v4u pk = {r0[0], r1[0], r0[1], r1[1]};
    v4u ov4;
#pragma unroll
    for (int e = 0; e < 4; ++e) ov4[e] = pkbf(bflo(xe[e]) + bflo(pk[e]), bfhi(xe[e]) + bfhi(pk[e]));
    *(v4u*)((char*)out + eoff) = ov4;
  }
  asm volatile("km_pf_b:");
  STAMPT(20);
  WAIT_VM(0);
  STAMPT(21);
  STAMP_OUT(ws);
}

extern "C" void kernel_launch(void* const* d_in, const int* in_sizes, int n_in, void* d_out, int out_size, void* d_ws,
                              size_t ws_size, hipStream_t stream) {
  kmain<<<256, 512, 0, stream>>>((const u16*)d_in[0], (const u16*)d_in[1], (const u16*)d_in[2], (const u16*)d_in[4],
                                 (const u16*)d_in[5], (const u16*)d_in[6], (char*)d_ws + WS_BASE(ws_size), (u16*)d_out);
}
```

```cpp
#include <hip/hip_runtime.h>
#include <stdint.h>

typedef unsigned short u16;
typedef unsigned int u32;
typedef unsigned long long u64;
typedef short v4s __attribute__((ext_vector_type(4)));
typedef short v8s __attribute__((ext_vector_type(8)));
typedef _Float16 v4h __attribute__((ext_vector_type(4)));
typedef _Float16 v8h __attribute__((ext_vector_type(8)));
typedef __bf16 v2b __attribute__((ext_vector_type(2)));
typedef __bf16 v8b __attribute__((ext_vector_type(8)));
typedef float v2f __attribute__((ext_vector_type(2)));
typedef float v4f __attribute__((ext_vector_type(4)));
typedef float v16f __attribute__((ext_vector_type(16)));
typedef u32 v2u __attribute__((ext_vector_type(2)));
typedef u32 v4u __attribute__((ext_vector_type(4)));

#define LAS __attribute__((address_space(3)))
#define GAS __attribute__((address_space(1)))
#define DEVI __device__ __forceinline__

constexpr int D_ = 512, F_ = 1792, DF_ = 257;
constexpr size_t OFF_H = (size_t)24 << 20;
constexpr size_t OFF_S1 = (size_t)32 << 20;
constexpr size_t OFF_WT = (size_t)34 << 20;
constexpr size_t OFF_S2 = (size_t)37 << 20;
constexpr size_t OFF_M = (size_t)39 << 20;
constexpr size_t OFF_FL = OFF_M;
constexpr u32 FL_S = 0, FL_A = 1024, FL_H = 2048;
constexpr size_t OFF_BC = OFF_M + 12288;
constexpr size_t OFF_MB = OFF_M + 16384;
constexpr size_t OFF_RS2 = OFF_M + 24576;
constexpr size_t OFF_PQ = OFF_M + 65536;
constexpr size_t OFF_CSP = OFF_M + 131072;
constexpr int SPIN_CAP = 1 << 16;

#define WS_BASE(n) ((((n) - ((size_t)40 << 20))) & ~(size_t)0x1fffff)
#define STAMP_DECL
#define STAMPT(id)
#define STAMPC(id)
#define STAMP_OUT(wsp)

template <int I> struct IC { static constexpr int v = I; constexpr operator int() const { return I; } };
template <int B, int E, class Fn> DEVI void static_for(Fn&& f) {
  if constexpr (B < E) { f(IC<B>{}); static_for<B + 1, E>(f); }
}
#define WAIT_VM(n) asm volatile("s_waitcnt vmcnt(%0)" ::"n"(n) : "memory")
#define BARRIER() asm volatile("s_waitcnt lgkmcnt(0)\n\ts_barrier" ::: "memory")
#define SCHED() __builtin_amdgcn_sched_barrier(0)
#define PIN(x) asm volatile("" : "+v"(x))

DEVI int s_of_pi(int pi) {
  int RG = pi >> 6, i = pi & 63;
  return 128 * (4 * (RG & 3) + (i & 3)) + 8 * (i >> 2) + (RG >> 2);
}
DEVI void dma2(u32 voff0, u32 voff1, const char* sbase, u32 lds) {
  asm volatile("s_mov_b32 m0, %3\n\ts_nop 0\n\tglobal_load_lds_dwordx4 %0, %2\n\tglobal_load_lds_dwordx4 %1, %2 offset:1024" ::"v"(voff0), "v"(voff1), "s"(sbase), "s"(lds) : "memory");
}
DEVI void dma2c(u32 voff0, u32 voff1, const char* sbase, u32 lds) {
  asm volatile("s_mov_b32 m0, %3\n\ts_nop 0\n\tglobal_load_lds_dwordx4 %0, %2 sc1\n\tglobal_load_lds_dwordx4 %1, %2 offset:1024 sc1" ::"v"(voff0), "v"(voff1), "s"(sbase), "s"(lds) : "memory");
}
template <int OFF> DEVI void dma1(u32 voff, const char* sbase, u32 lds) {
  asm volatile("s_mov_b32 m0, %2\n\ts_nop 0\n\tglobal_load_lds_dwordx4 %0, %1 offset:%3" ::"v"(voff), "s"(sbase), "s"(lds), "n"(OFF) : "memory");
}
template <int OFF> DEVI void dma1s(u32 voff, const char* sbase, u32 lds) {
  asm volatile("s_mov_b32 m0, %2\n\ts_nop 0\n\tglobal_load_lds_dwordx4 %0, %1 offset:%3 sc1" ::"v"(voff), "s"(sbase), "s"(lds), "n"(OFF) : "memory");
}
DEVI void dma1c(u32 voff, const char* sbase, u32 lds) {
  asm volatile("s_mov_b32 m0, %2\n\ts_nop 0\n\tglobal_load_lds_dword %0, %1 sc1" ::"v"(voff), "s"(sbase), "s"(lds) : "memory");
}
DEVI v2u ld8c_sync(u32 voff, const char* sb) { v2u d; asm volatile("global_load_dwordx2 %0, %1, %2 sc1\n\ts_waitcnt vmcnt(0)" : "=&v"(d) : "v"(voff), "s"(sb) : "memory"); return d; }
DEVI u32 ld4c_sync(u32 voff, const char* sb) { u32 d; asm volatile("global_load_dword %0, %1, %2 sc1\n\ts_waitcnt vmcnt(0)" : "=&v"(d) : "v"(voff), "s"(sb) : "memory"); return d; }
DEVI v4u ld16c_sync(u32 voff, const char* sb) { v4u d; asm volatile("global_load_dwordx4 %0, %1, %2 sc1\n\ts_waitcnt vmcnt(0)" : "=&v"(d) : "v"(voff), "s"(sb) : "memory"); return d; }
template <class T> DEVI void ld16c(T& d, u32 voff, const char* sb) { asm volatile("global_load_dwordx4 %0, %1, %2 sc1" : "=&v"(d) : "v"(voff), "s"(sb) : "memory"); }
template <class T> DEVI void ld8c(T& d, u32 voff, const char* sb) { asm volatile("global_load_dwordx2 %0, %1, %2 sc1" : "=&v"(d) : "v"(voff), "s"(sb) : "memory"); }
template <class T> DEVI void ld4c(T& d, u32 voff, const char* sb) { asm volatile("global_load_dword %0, %1, %2 sc1" : "=&v"(d) : "v"(voff), "s"(sb) : "memory"); }
template <class T> DEVI void ld16(T& d, u32 voff, const char* sb) { asm volatile("global_load_dwordx4 %0, %1, %2" : "=&v"(d) : "v"(voff), "s"(sb) : "memory"); }
DEVI void ld2(u32& d, u32 voff, const char* sb) { asm volatile("global_load_ushort %0, %1, %2" : "=&v"(d) : "v"(voff), "s"(sb) : "memory"); }
template <class T> DEVI void st16c(u32 voff, const char* sb, T v) { asm volatile("global_store_dwordx4 %0, %1, %2 sc1\n\ts_nop 1" ::"v"(voff), "v"(v), "s"(sb) : "memory"); }
template <class T> DEVI void st8c(u32 voff, const char* sb, T v) { asm volatile("global_store_dwordx2 %0, %1, %2 sc1" ::"v"(voff), "v"(v), "s"(sb) : "memory"); }
template <class T> DEVI void st4c(u32 voff, const char* sb, T v) { asm volatile("global_store_dword %0, %1, %2 sc1" ::"v"(voff), "v"(v), "s"(sb) : "memory"); }

DEVI v4s trread(LAS char* p) { return __builtin_amdgcn_ds_read_tr16_b64_v4i16((LAS v4s*)p); }
DEVI v8s cat8(v4s a, v4s b) { return __builtin_shufflevector(a, b, 0, 1, 2, 3, 4, 5, 6, 7); }
DEVI float bflo(u32 v) { return __builtin_bit_cast(float, v << 16); }
DEVI float bfhi(u32 v) { return __builtin_bit_cast(float, v & 0xffff0000u); }
DEVI float rbf(float v) { return (float)(__bf16)v; }
DEVI u32 pkbf(float a, float b) { v2b p = {(__bf16)a, (__bf16)b}; return __builtin_bit_cast(u32, p); }
DEVI u32 pkbf_o(float a, float b) { u32 r = pkbf(a, b); asm("" : "+v"(r)); return r; }
DEVI float cosr(float rev) { return __builtin_amdgcn_cosf(rev); }
DEVI float sinr(float rev) { return __builtin_amdgcn_sinf(rev); }
DEVI v4f mfma16h(v8h a, v8h b, v4f c) { return __builtin_amdgcn_mfma_f32_16x16x32_f16(a, b, c, 0, 0, 0); }
DEVI v4f mfma16b(v8s a, v4u b, v4f c) { return __builtin_amdgcn_mfma_f32_16x16x32_bf16(__builtin_bit_cast(v8b, a), __builtin_bit_cast(v8b, b), c, 0, 0, 0); }
DEVI v16f mfma32b(v8s a, v8s b, v16f c) { return __builtin_amdgcn_mfma_f32_32x32x16_bf16(__builtin_bit_cast(v8b, a), __builtin_bit_cast(v8b, b), c, 0, 0, 0); }
DEVI v16f mfma32h(v8h a, v8s b, v16f c) { return __builtin_amdgcn_mfma_f32_32x32x16_f16(a, __builtin_bit_cast(v8h, b), c, 0, 0, 0); }
DEVI v8h pack_ri(v4f re, v4f im) {
  v8h r;
  r[0] = (_Float16)re[0]; r[1] = (_Float16)re[1]; r[2] = (_Float16)re[2]; r[3] = (_Float16)re[3];
  r[4] = (_Float16)im[0]; r[5] = (_Float16)im[1]; r[6] = (_Float16)im[2]; r[7] = (_Float16)im[3];
  return r;
}
DEVI v4h pack4h(v4f a) { v4h r; r[0] = (_Float16)a[0]; r[1] = (_Float16)a[1]; r[2] = (_Float16)a[2]; r[3] = (_Float16)a[3]; return r; }
DEVI void twid(v4f& re, v4f& im, const v4f& c, const v4f& s) {
#pragma unroll
  for (int r = 0; r < 4; ++r) { float a = re[r], b = im[r]; re[r] = a * c[r] + b * s[r]; im[r] = b * c[r] - a * s[r]; }
}
template <int CTRL> DEVI float dppadd(float v) {
  return v + __builtin_bit_cast(float, __builtin_amdgcn_update_dpp(0, __builtin_bit_cast(int, v), CTRL, 0xf, 0xf, true));
}
DEVI float wave_sum(float v) {
  v = dppadd<0xB1>(v); v = dppadd<0x4E>(v); v = dppadd<0x141>(v); v = dppadd<0x140>(v);
  int bb = __builtin_bit_cast(int, v);
  float a0 = __builtin_bit_cast(float, __builtin_amdgcn_readlane(bb, 0)), a1 = __builtin_bit_cast(float, __builtin_amdgcn_readlane(bb, 16));
  float a2 = __builtin_bit_cast(float, __builtin_amdgcn_readlane(bb, 32)), a3 = __builtin_bit_cast(float, __builtin_amdgcn_readlane(bb, 48));
  return (a0 + a1) + (a2 + a3);
}
DEVI float dot2ones(u32 v, float acc) {
  const v2b ones = {(__bf16)1.0f, (__bf16)1.0f};
  return __builtin_amdgcn_fdot2_f32_bf16(__builtin_bit_cast(v2b, v), ones, acc, false);
}
template <int CTRL> DEVI u32 dppmax(u32 v) { return max(v, (u32)__builtin_amdgcn_update_dpp(0, (int)v, CTRL, 0xf, 0xf, true)); }
DEVI u32 wave_max(u32 v) {
  v = dppmax<0xB1>(v); v = dppmax<0x4E>(v); v = dppmax<0x141>(v); v = dppmax<0x140>(v);
  u32 a0 = __builtin_amdgcn_readlane(v, 0), a1 = __builtin_amdgcn_readlane(v, 16), a2 = __builtin_amdgcn_readlane(v, 32), a3 = __builtin_amdgcn_readlane(v, 48);
  return max(max(a0, a1), max(a2, a3));
}

struct Frag1 { v8s x0, x1, w1, w2; };

constexpr int L_SCR = 147456;
constexpr int L_CS = L_SCR, L_RED = L_SCR + 1024, L_MX = L_SCR + 1088, L_BC0 = L_SCR + 2048, L_BC1 = L_SCR + 2304;
constexpr int L_TAB = L_SCR + 3072;
constexpr int L_T = L_SCR + 7168, L_FA = L_T, L_E1 = L_T, L_E2 = 81920, L_ZX = 81920;
constexpr int L_C1 = 98304, L_IMG = L_C1 + 9216;
constexpr int L_FR = 131072;
constexpr int E1P = 544, E2P = 1024 + 72;
DEVI constexpr int wslot(int n) {
  constexpr int u[14] = {6, 7, 8, 0, 1, 2, 3, 4, 6, 7, 8, 0, 1, 2};
  return u[n] * 16384;
}
DEVI constexpr int aslot(int n) {
  constexpr int u[18] = {3, 4, 5, 9, 2, 6, 7, 8, 0, 1, 3, 4, 5, 9, 2, 6, 7, 8};
  return u[n] * 16384;
}

__global__ __launch_bounds__(512, 2) void kmain(const u16* __restrict__ x, const u16* __restrict__ wr, const u16* __restrict__ wi,
                                                const u16* __restrict__ Wg, const u16* __restrict__ Wu, const u16* __restrict__ Wd,
                                                char* __restrict__ ws, u16* __restrict__ out) {
  __shared__ __attribute__((aligned(16))) char smem_[163840];
  LAS char* smem = (LAS char*)smem_;
  const u32 lbase = (u32)(size_t)smem;
  const int tid = threadIdx.x, l = tid & 63;
  const int w = __builtin_amdgcn_readfirstlane(tid >> 6);
  const int b = blockIdx.x, xcd = b & 7, idx = b >> 3;
  const bool ffn = idx < 28;
  const int g = l >> 4, o = l & 15;
  const int h = l >> 5, blk = (l >> 4) & 1, q = (l & 15) >> 2, pp = l & 3;
  const char* FL = ws + OFF_FL;
  const char* BC = ws + OFF_BC;
  STAMP_DECL; STAMPT(0);

  const int pi = b * 8 + w;
  v4u xr;
  {
    u64 pc; u32 len;
    asm volatile("s_getpc_b64 %0\nkm_pf_a:\n\ts_mov_b32 %1, km_pf_b-km_pf_a" : "=s"(pc), "=s"(len));
    dma1<0>(min((u32)(((idx & 3) * 8 + w) * 1024 + l * 16), len) & ~15u, (const char*)(pc & ~15ull), lbase + L_IMG + w * 1024);
  }
  ld16(xr, (u32)(s_of_pi(pi) * 1024 + l * 16), (const char*)x);
  if (w == 0) dma1c((u32)((l & 1) * 4), BC, lbase + L_BC0);
  const int fkb = (b & 7) * 2 + (w >> 2), fcq = w & 3;
  {
    const u32 ks = (u32)((idx >> 1) + 16 * fkb + 256 * (4 * (idx & 1) + ((l >> 3) & 3)));
    const u32 base = ((ks * (DF_ * 2)) & ~15u) + fcq * 128;
    const u32 dlt = (u32)((const char*)wi - (const char*)wr);
    (void)dlt;
    const u32 c0 = ((l & 7) ^ (((l >> 4) & 1) << 2)) << 4;
    if (l < 32) dma1<0>(base + c0, (const char*)wr, lbase + L_FR + w * 2048);
    else dma1<0>(base + c0, (const char*)wi, lbase + L_FR + w * 2048);
    if (fcq == 3) {
      const u32 t0 = min(base + 128 + (c0 & 16), (u32)(2048 * DF_ * 2 - 16));
      if (l < 32) dma1<0>(t0, (const char*)wr, lbase + L_FR + w * 2048 + 1024);
      else dma1<0>(t0, (const char*)wi, lbase + L_FR + w * 2048 + 1024);
    }
  }
  const int p = (xcd >> 1) * 4 + idx / 7, j = (xcd & 1) * 7 + idx % 7;
  const int sh = w >> 2, fq = w & 3;
  u32 aoff[2];
#pragma unroll
  for (int i = 0; i < 2; ++i) {
    int row = (2 * w + i) * 8 + (l >> 3);
    aoff[i] = s_of_pi((p & 15) * 128 + row) * 1024 + (((l & 7) ^ ((row >> 1) & 7)) << 4) - i * 1024;
  }
  const u32 boff = (u32)(2 * w * 4 + (l >> 4)) * (F_ * 2) + j * 256 + (((l & 15) ^ ((l >> 4) << 2)) << 4);
  const u32 boff1 = boff + 4 * F_ * 2 - 1024;
  const int rot = p >> 1;
  auto issue = [&](int t, int slot) {
    const int kt = (t + rot) & 7;
    u32 dst = lbase + slot * 49152 + w * 2048;
    dma2(aoff[0], aoff[1], (const char*)x + kt * 128, dst);
    dma2(boff, boff1, (const char*)Wg + (size_t)kt * (64 * F_ * 2), dst + 16384);
    dma2(boff, boff1, (const char*)Wu + (size_t)kt * (64 * F_ * 2), dst + 32768);
  };
  const int sp = xcd * 4 + (idx - 28);
  if (ffn) issue(0, 0);

  u32 E = 0, tick = 0;
  const bool coord = (b == 224) && (w == 0);
  auto do_tick = [&]() { if (coord) { ++tick; if (l == 0) st8c(0u, BC, v2u{E, tick}); } };
  auto get_epoch = [&]() {
    const v2u b0 = *(LAS v2u*)(smem + L_BC0);
    v2u b1 = *(LAS v2u*)(smem + L_BC1);
    int spins = 0;
    while (__builtin_amdgcn_readfirstlane((b0[0] ^ b1[0]) | (b0[1] ^ b1[1])) == 0 && ++spins < SPIN_CAP) {
      __builtin_amdgcn_s_sleep(4);
      b1 = ld8c_sync(0u, BC);
    }
    E = __builtin_amdgcn_readfirstlane(b1[0]);
  };
  auto signal_a = [&]() {
    v4u r0 = *(LAS v4u*)(smem + L_RED), r1 = *(LAS v4u*)(smem + L_RED + 16);
    u32 m = max(max(max(r0[0], r0[1]), max(r0[2], r0[3])), max(max(r1[0], r1[1]), max(r1[2], r1[3])));
    if (l == 0) { st4c((u32)(FL_A + b * 4), FL, E | (m >> 7)); *(LAS u32*)(smem + L_MX + 4) = E; }
  };
  auto poll4 = [&](v4u f, u32 voff) -> v4u {
    int spins = 0;
    while (!__all((((f[0] ^ E) | (f[1] ^ E) | (f[2] ^ E) | (f[3] ^ E)) >> 8) == 0) && ++spins < SPIN_CAP) {
      do_tick();
      __builtin_amdgcn_s_sleep(12);
      f = ld16c_sync(voff, FL);
    }
    return f;
  };
  auto check_a = [&](v4u f) {
    f = poll4(f, (u32)(FL_A + l * 16));
    u32 m = wave_max(max(max(f[0], f[1]), max(f[2], f[3]))) & 0xffu;
    if (l == 0) *(LAS u32*)(smem + L_MX) = m;
  };
  if (coord) {
    v4u f[10];
#pragma unroll
    for (int i = 0; i < 10; ++i) ld16c(f[i], (u32)(i * 1024 + l * 16), FL);
    WAIT_VM(0);
    u32 c = __builtin_amdgcn_readfirstlane(*(LAS u32*)(smem + L_BC0)) >> 8;
    bool hit;
    do {
      c = (c + 1) & 0xffffffu; hit = false;
#pragma unroll
      for (int i = 0; i < 10; ++i) { PIN(f[i]); hit |= ((f[i][0] >> 8) == c) | ((f[i][1] >> 8) == c) | ((f[i][2] >> 8) == c) | ((f[i][3] >> 8) == c); }
    } while (__any(hit));
    E = c << 8;
    do_tick();
  }

  {
    const float fo = (float)o, fg = (float)(4 * g);
    if (w < 2) {
      const float st = fo * (1.0f / 32.0f), a0 = fmaf(fg, st, w ? 0.25f : 0.f);
      v4u hi, lo;
#pragma unroll
      for (int e2 = 0; e2 < 4; ++e2) {
        float c0 = cosr(fmaf((float)(16 * (e2 >> 1) + 2 * (e2 & 1)), st, a0)), c1 = cosr(fmaf((float)(16 * (e2 >> 1) + 2 * (e2 & 1) + 1), st, a0));
        u32 ch = pkbf_o(c0, c1);
        hi[e2] = ch; lo[e2] = pkbf(c0 - bflo(ch), c1 - bfhi(ch));
      }
      *(LAS v4u*)(smem + L_C1 + w * 2048 + l * 16) = hi; *(LAS v4u*)(smem + L_C1 + w * 2048 + 1024 + l * 16) = lo;
    } else if (w < 5) {
      const float st = w == 2 ? (float)(o & 7) * (1.0f / 16.0f) : fo * (w == 3 ? 1.0f / 16.0f : 1.0f / 8.0f);
      const float a0 = (w == 2 && o >= 8) ? 0.25f : 0.f;
      const float sc = w == 2 ? 0.0625f : ((w == 4 && (g >> 1) != (o >> 3)) ? 0.f : 1.0f);
      v8h m;
#pragma unroll
      for (int e = 0; e < 4; ++e) { float a = fmaf(fg + (float)e, st, a0); m[e] = (_Float16)(sc * cosr(a)); m[4 + e] = (_Float16)(sc * sinr(a)); }
      *(LAS v8h*)(smem + (w == 2 ? L_C1 + 4096 : L_TAB + (w - 3) * 1024) + l * 16) = m;
    } else {
      const float st = w == 7 ? fo * (1.0f / 256.0f) : (fo + (w == 6 ? 16.f : 0.f)) * (1.0f / 512.0f);
      v4f c, sn;
#pragma unroll
      for (int e = 0; e < 4; ++e) { float a = (fg + (float)e) * st; c[e] = cosr(a); sn[e] = sinr(a); }
      LAS char* dst = smem + (w == 7 ? L_TAB + 2048 : L_C1 + 5120 + (w - 5) * 2048) + l * 16;
      *(LAS v4f*)dst = c; *(LAS v4f*)(dst + 1024) = sn;
    }
    BARRIER();
    LAS char* C1 = smem + L_C1 + l * 16;
    const v4u Fhr = *(LAS v4u*)C1, Flr = *(LAS v4u*)(C1 + 1024), Fhi = *(LAS v4u*)(C1 + 2048), Fli = *(LAS v4u*)(C1 + 3072);
    const v8h M2 = *(LAS v8h*)(C1 + 4096);
    v4f twc[2], tws[2];
#pragma unroll
    for (int u = 0; u < 2; ++u) { twc[u] = *(LAS v4f*)(C1 + 5120 + u * 2048); tws[u] = *(LAS v4f*)(C1 + 6144 + u * 2048); }
    if (ffn) WAIT_VM(8); else WAIT_VM(2);
    PIN(xr);
    STAMPT(3);
    const float c0 = bflo(__builtin_amdgcn_readfirstlane(xr[0]));
    v2f xv[4], s2 = {0.f, 0.f}, q2 = {0.f, 0.f};
#pragma unroll
    for (int e = 0; e < 4; ++e) {
      xv[e] = v2f{bflo(xr[e]), bfhi(xr[e])};
      v2f t = xv[e] - v2f{c0, c0};
      s2 += t; q2 = __builtin_elementwise_fma(t, t, q2);
    }
    const float dmean = wave_sum(s2[0] + s2[1]) * (1.0f / 512.0f), msq = wave_sum(q2[0] + q2[1]) * (1.0f / 512.0f);
    do_tick();
    const float mb = rbf(c0 + dmean);
    const float var = rbf(fmaxf(msq - dmean * dmean, 0.f));
    const float sd = rbf(__builtin_amdgcn_sqrtf(rbf(var + 1e-5f)));
    const float rs = __builtin_amdgcn_rcpf(sd);
#pragma unroll
    for (int e = 0; e < 4; ++e) {
      v2f t = xv[e] - v2f{mb, mb};
      u32 tb = pkbf_o(t[0], t[1]);
      t = v2f{bflo(tb), bfhi(tb)} * v2f{rs, rs};
      xr[e] = pkbf(t[0], t[1]);
    }
    *(LAS v4u*)(smem + L_IMG + w * 1024 + l * 16) = xr;
    if (l < 2) st4c((u32)(pi * 4 + l * 8192), ws + OFF_MB, l ? rs * rs : -mb);
    const v8s xf = cat8(trread(smem + L_IMG + w * 1024 + l * 8), trread(smem + L_IMG + w * 1024 + 512 + l * 8));
    const int kt1 = (1 + rot) & 7;
    const u32 dst1 = lbase + 49152 + w * 2048;
    if (ffn) dma2(aoff[0], aoff[1], (const char*)x + kt1 * 128, dst1);
    const v8s xg_ = __builtin_bit_cast(v8s, __builtin_bit_cast(v4u, xf) ^ v4u{0x80000000u, 0x80000000u, 0x80000000u, 0x80000000u});
    const v4f z = {0.f, 0.f, 0.f, 0.f};
    v4f dre[2], dim[2];
#pragma unroll
    for (int u = 0; u < 2; ++u) { dre[u] = mfma16b(u ? xg_ : xf, Flr, z); dim[u] = mfma16b(u ? xg_ : xf, Fli, z); }
#pragma unroll
    for (int u = 0; u < 2; ++u) { dre[u] = mfma16b(u ? xg_ : xf, Fhr, dre[u]); dim[u] = mfma16b(u ? xg_ : xf, Fhi, dim[u]); }
#pragma unroll
    for (int u = 0; u < 2; ++u) {
      twid(dre[u], dim[u], twc[u], tws[u]);
      const v8h bb = pack_ri(dre[u], dim[u]);
      v4f d2 = mfma16h(bb, M2, z);
      if (u == 0) {
        const u32 nv = o == 8 ? 0xac002c00u : 0u;
        v4f n = mfma16h(bb, __builtin_bit_cast(v8h, v4u{nv, nv, 0u, 0u}), z);
        if (l == 8) d2[0] = n[0];
      }
      *(LAS v4h*)(smem + L_T + ((o >> 3) * 8 + w) * 544 + (o & 7) * 64 + u * 32 + g * 8) = pack4h(d2);
    }
    if (ffn) {
      dma2(boff, boff1, (const char*)Wg + (size_t)kt1 * (64 * F_ * 2), dst1 + 16384);
      dma2(boff, boff1, (const char*)Wu + (size_t)kt1 * (64 * F_ * 2), dst1 + 32768);
    }
    STAMPT(4);
  }
  auto round1_out = [&]() {
    LAS char* rp = smem + L_T + (4 * (g & 1) + (o >> 2)) * 544 + (w * 32 + (g >> 1) * 16) * 2 + (o & 3) * 8;
    const v8s su = cat8(trread(rp), trread(rp + 8 * 544));
    LAS char* fp_ = smem + L_FR + w * 2048 + q * 128 + (((2 * g + (pp >> 1)) ^ ((q >> 1) << 2)) << 4) + (pp & 1) * 8;
    const v4u fu = __builtin_bit_cast(v4u, cat8(trread(fp_), trread(fp_ + 512)));
    st16c((u32)(b * 8192 + (w * 32 + (g >> 1) * 16 + o) * 32 + (g & 1) * 16), ws + OFF_S1, su);
    st16c((u32)((idx * 16 + fkb) * 4608 + (fcq * 64 + l) * 16), ws + OFF_WT, fu);
    typedef unsigned short v2us __attribute__((ext_vector_type(2)));
    const int dl = (idx >> 1) & 7;
    u32 msk = (fcq == 0 && l < dl) ? 0u : 0x7fff7fffu;
    v2us m2 = {0, 0};
#pragma unroll
    for (int e = 0; e < 4; ++e) { u32 t = fu[e] & msk; m2 = __builtin_elementwise_max(m2, __builtin_bit_cast(v2us, t)); }
    if (fcq == 3) {
      LAS char* tp = smem + L_FR + w * 2048 + 1024 + q * 128 + (pp >> 1) * 16 + (pp & 1) * 8;
      const v4u tu = __builtin_bit_cast(v4u, cat8(trread(tp), trread(tp + 512)));
      if (l < 16) st16c((u32)((idx * 16 + fkb) * 4608 + (256 + l) * 16), ws + OFF_WT, tu);
      msk = (o <= dl) ? 0x7fff7fffu : 0u;
#pragma unroll
      for (int e = 0; e < 4; ++e) { u32 t = tu[e] & msk; m2 = __builtin_elementwise_max(m2, __builtin_bit_cast(v2us, t)); }
    }
    const u32 mx = wave_max(max((u32)m2[0], (u32)m2[1]));
    if (l == 0) *(LAS u32*)(smem + L_RED + w * 4) = mx;
    if (w == 0 && !coord) dma1c((u32)((l & 1) * 4), BC, lbase + L_BC1);
  };

  const int RG = xcd * 4 + (idx >> 3), ct = idx & 7;
  const int kd = xcd * 32 + idx;
  const u32 swz = ((l & 7) ^ (((l >> 4) & 1) << 2)) << 4;
  const u32 ro0 = (u32)((l >> 3) * 1024) + swz, ro1 = ro0 + 8192 - 1024;
  const char* bsrc = (const char*)Wd + (size_t)(w * 16) * 1024 + ct * 128;
  auto issueW = [&](int n) { dma2(ro0, ro1, bsrc + (size_t)n * 131072, lbase + wslot(n) + w * 2048); };
  const int dh = w & 1, kq = w >> 1, ph = kq >> 1, kh = kq & 1;
  const int epi = RG * 64 + ph * 32 + (l & 31);
  const u32 eoff = (u32)(s_of_pi(epi) * D_ + ct * 64 + dh * 32 + kh * 16 + h * 8) * 2;
  v4u wt, s1v, xe; v4f pq[4]; float rs2;
  auto load_xe = [&]() { ld16(xe, eoff, (const char*)x); };
  auto load_s1 = [&]() { ld16c(s1v, (u32)((((4 * w + g) * 8 + (o >> 1)) * 256 + kd) * 32 + (o & 1) * 16), ws + OFF_S1); };
  auto load_wt = [&]() { ld16c(wt, (u32)((w * 64 + l) * 4608 + (kd + ((2 * w + (g >> 1)) & 7)) * 16), ws + OFF_WT); };
  auto load_rs = [&]() { ld4c(rs2, (u32)(epi * 4), ws + OFF_RS2); };
  v4f t2c, t2s, t3c, t3s;
  auto trig2 = [&]() {
    const float fw = (float)w, st2 = fw * (1.0f / 128.0f), a2 = fw * (float)(o + 64 * g) * (1.0f / 2048.0f);
#pragma unroll
    for (int e = 0; e < 4; ++e) { float b2 = fmaf((float)e, st2, a2); t2c[e] = cosr(b2); t2s[e] = sinr(b2); }
    PIN(t2c); PIN(t2s);
  };
  auto trig3 = [&]() {
    const float f7 = (float)(o & 7), st3 = f7 * (1.0f / 128.0f), a3 = f7 * (float)(2 * w + (o >> 3) + 64 * g) * (1.0f / 2048.0f);
#pragma unroll
    for (int e = 0; e < 4; ++e) { float b3 = fmaf((float)e, st3, a3); t3c[e] = cosr(b3); t3s[e] = sinr(b3); }
    PIN(t3c); PIN(t3s);
  };
  v4u f16; v4f t1c, t1s, re, im;
  const v4f z4 = {0.f, 0.f, 0.f, 0.f};
  auto imf = [](v4u f) { return __builtin_bit_cast(v8h, v4u{f[2] ^ 0x80008000u, f[3] ^ 0x80008000u, f[0], f[1]}); };
  auto segA0 = [&]() {
    f16 = *(LAS v4u*)(smem + L_TAB + l * 16);
    t1c = *(LAS v4f*)(smem + L_TAB + 2048 + l * 16); t1s = *(LAS v4f*)(smem + L_TAB + 3072 + l * 16);
  };
  auto segA1 = [&]() {
    PIN(s1v);
    const v8h a = __builtin_bit_cast(v8h, s1v);
    re = mfma16h(a, __builtin_bit_cast(v8h, f16), z4); im = mfma16h(a, imf(f16), z4);
  };
  auto segA2 = [&]() {
    twid(re, im, t1c, t1s);
    const v8h a = pack_ri(re, im);
    re = mfma16h(__builtin_bit_cast(v8h, f16), a, z4); im = mfma16h(imf(f16), a, z4);
  };
  auto segA3 = [&]() {
    twid(re, im, t2c, t2s);
    *(LAS v4h*)(smem + L_E1 + w * E1P + o * 32 + g * 8) = pack4h(re);
    *(LAS v4h*)(smem + L_E1 + (8 + w) * E1P + o * 32 + g * 8) = pack4h(im);
  };

  if (ffn) {
    v16f acc[2][2] = {};
    float mbr[2];
    const u32 wb0 = (u32)((8 * h + q) * 256 + ((fq ^ q) << 6) + blk * 32 + pp * 8);
    const u32 r1 = 16384 + sh * 16384, r2 = 32768 - sh * 16384;
    const int xrow = sh * 64 + (l & 31);
    const u32 xb0 = (u32)(xrow * 128 + ((h ^ ((xrow >> 1) & 7)) << 4));
    auto rd = [&](Frag1& f, LAS char* sb, int i) {
      LAS char* xa = sb + (xb0 ^ (i << 5));
      LAS char* wa_ = sb + wb0 + (i << 12);
      f.w1 = cat8(trread(wa_ + r1), trread(wa_ + r1 + 1024));
      f.x0 = *(LAS v8s*)xa; f.x1 = *(LAS v8s*)(xa + 4096);
      f.w2 = cat8(trread(wa_ + r2), trread(wa_ + r2 + 1024));
    };
    const u32 one = ((l & 15) == ((l >> 4) & 1)) ? 0x3f803f80u : 0u;
    const v8s sel = __builtin_bit_cast(v8s, v4u{one, one, one, one});
    v4f csa = {0.f, 0.f, 0.f, 0.f};
    Frag1 A, B;
    WAIT_VM(7);
    BARRIER();
    STAMPT(1); STAMPC(30);
    rd(A, smem, 0);
    dma2(aoff[0], aoff[1], (const char*)x + ((2 + rot) & 7) * 128, lbase + 2 * 49152 + w * 2048);
    v4u cs1, cs2;
    static_for<0, 8>([&](auto T) {
      constexpr int t = T, slot = t % 3, nslot = (t + 1) % 3, pslot = (t + 2) % 3;
      LAS char* sb = smem + slot * 49152;
      {
        const int kt = (t + 2 + rot) & 7;
        const char* s1 = t < 6 ? (const char*)Wg + (size_t)kt * (64 * F_ * 2) : bsrc + (size_t)((t - 6) * 3 + 1) * 131072;
        const char* s2 = t < 6 ? (const char*)Wu + (size_t)kt * (64 * F_ * 2) : bsrc + (size_t)((t - 6) * 3 + 2) * 131072;
        const u32 o2 = t < 6 ? boff : ro0, o3 = t < 6 ? boff1 : ro1;
        const u32 dst = lbase + pslot * 49152 + w * 2048;
        rd(B, sb, 1); SCHED();
        acc[0][0] = mfma32b(A.w1, A.x0, acc[0][0]); acc[0][1] = mfma32b(A.w1, A.x1, acc[0][1]);
        if constexpr (t == 0) csa = mfma16b(sel, __builtin_bit_cast(v4u, A.w1), csa);
        SCHED();
        dma1<0>(o2, s1, dst + 16384);
        if constexpr (t == 2) load_xe();
        if constexpr (t == 6) load_wt();
        acc[1][0] = mfma32b(A.w2, A.x0, acc[1][0]); acc[1][1] = mfma32b(A.w2, A.x1, acc[1][1]); SCHED();
        if constexpr (t == 0) { round1_out(); SCHED(); }
        if constexpr (t == 3) { trig2(); SCHED(); }
        if constexpr (t == 4) { trig3(); SCHED(); }
        rd(A, sb, 2); SCHED();
        acc[0][0] = mfma32b(B.w1, B.x0, acc[0][0]); SCHED();
        dma1<1024>(o3, s1, dst + 16384);
        acc[0][1] = mfma32b(B.w1, B.x1, acc[0][1]);
        if constexpr (t == 0) csa = mfma16b(sel, __builtin_bit_cast(v4u, B.w1), csa);
        acc[1][0] = mfma32b(B.w2, B.x0, acc[1][0]); acc[1][1] = mfma32b(B.w2, B.x1, acc[1][1]); SCHED();
        dma1<0>(o2, s2, dst + 32768);
        if constexpr (t == 6) {
          load_rs();
          if (kd == 0) {
#pragma unroll
            for (int r = 0; r < 4; ++r) ld16c(pq[r], (u32)(w * 4096 + r * 1024 + l * 16), ws + OFF_PQ);
          }
        }
        if constexpr (t == 7) { WAIT_VM(13); segA1(); SCHED(); }
        rd(B, sb, 3); SCHED();
        acc[0][0] = mfma32b(A.w1, A.x0, acc[0][0]); acc[0][1] = mfma32b(A.w1, A.x1, acc[0][1]);
        if constexpr (t == 0) csa = mfma16b(sel, __builtin_bit_cast(v4u, A.w1), csa);
        SCHED();
        dma1<1024>(o3, s2, dst + 32768);
        acc[1][0] = mfma32b(A.w2, A.x0, acc[1][0]); acc[1][1] = mfma32b(A.w2, A.x1, acc[1][1]); SCHED();
      }
      if constexpr (t < 7) {
        const char* s0 = t < 5 ? (const char*)x + ((t + 3 + rot) & 7) * 128 : bsrc + (size_t)((t - 5) * 3) * 131072;
        const u32 o0 = t < 5 ? aoff[0] : ro0, o1 = t < 5 ? aoff[1] : ro1;
        const u32 dst = lbase + slot * 49152 + w * 2048;
        if constexpr (t == 0) csa = mfma16b(sel, __builtin_bit_cast(v4u, B.w1), csa);
        WAIT_VM(t == 0 ? 8 : t == 2 ? 7 : t == 6 ? 13 : 6);
        if constexpr (t == 5) { if (w == 0) check_a(*(LAS v4u*)(smem + L_FA + l * 16)); }
        BARRIER();
        if constexpr (t == 0 || t == 5) STAMPT(22 + t);
        acc[0][0] = mfma32b(B.w1, B.x0, acc[0][0]); SCHED();
        rd(A, smem + nslot * 49152, 0);
        if constexpr (t == 6) segA0();
        SCHED();
        if constexpr (t == 0) {
          if (l < 16) {
#pragma unroll
            for (int i = 0; i < 2; ++i) {
              const float hi = rbf(csa[i]);
              st4c((u32)((((j * 2 + (p & 1)) * 256 + sh * 128 + fq * 32 + i * 16 + l) * 8 + rot) * 4), ws + OFF_CSP, pkbf(hi, csa[i] - hi));
            }
          }
        }
        if constexpr (t == 4) { if (w == 0) dma1s<0>((u32)(FL_A + l * 16), FL, lbase + L_FA); }
        if constexpr (t == 5) {
          const u32 co = (u32)(((j * 2 + (p & 1)) * 256 + fq * 32 + (l & 31)) * 32 + h * 16);
          ld16c(cs1, co + sh * 4096, ws + OFF_CSP); ld16c(cs2, co + (1 - sh) * 4096, ws + OFF_CSP);
          ld4c(mbr[0], (u32)((p * 128 + xrow) * 4), ws + OFF_MB); ld4c(mbr[1], (u32)((p * 128 + xrow + 32) * 4), ws + OFF_MB);
          load_s1();
        }
        dma1<0>(o0, s0, dst);
        acc[0][1] = mfma32b(B.w1, B.x1, acc[0][1]);
        acc[1][0] = mfma32b(B.w2, B.x0, acc[1][0]);
        acc[1][1] = mfma32b(B.w2, B.x1, acc[1][1]); SCHED();
        dma1<1024>(o1, s0, dst);
        if constexpr (t == 1) { if (w == 0) { get_epoch(); signal_a(); } }
        SCHED();
      } else {
        segA2(); SCHED();
        PIN(cs1); PIN(cs2); PIN(mbr[0]); PIN(mbr[1]);
        const u32 m0_ = pkbf(mbr[0], mbr[0]), m1_ = pkbf(mbr[1], mbr[1]);
        const v8s mb0 = __builtin_bit_cast(v8s, v4u{m0_, m0_, m0_, m0_}), mb1 = __builtin_bit_cast(v8s, v4u{m1_, m1_, m1_, m1_});
        acc[0][0] = mfma32b(B.w1, B.x0, acc[0][0]); acc[1][0] = mfma32b(B.w2, B.x0, acc[1][0]);
        acc[0][0] = mfma32b(__builtin_bit_cast(v8s, cs1), mb0, acc[0][0]); acc[1][0] = mfma32b(__builtin_bit_cast(v8s, cs2), mb0, acc[1][0]);
        acc[0][1] = mfma32b(B.w1, B.x1, acc[0][1]); acc[1][1] = mfma32b(B.w2, B.x1, acc[1][1]);
        acc[0][1] = mfma32b(__builtin_bit_cast(v8s, cs1), mb1, acc[0][1]); acc[1][1] = mfma32b(__builtin_bit_cast(v8s, cs2), mb1, acc[1][1]); SCHED();
      }
    });
    STAMPT(2); STAMPC(31);
    auto fin = [&](const v16f& ag, const v16f& au, int rt) {
#pragma unroll
      for (int s2i = 0; s2i < 2; ++s2i) {
        v4u ov;
#pragma unroll
        for (int e = 0; e < 4; ++e) {
          const int r = s2i * 8 + 2 * e;
          v2f gg = v2f{fmaxf(ag[r], 0.f), fmaxf(ag[r + 1], 0.f)} * v2f{au[r], au[r + 1]};
          ov[e] = pkbf(gg[0], gg[1]);
        }
        st16c((u32)(((p * 4 + sh * 2 + rt) * 112 + (j * 8 + fq * 2 + s2i)) * 1024 + l * 16), ws + OFF_H, ov);
      }
    };
    if (sh == 0) { fin(acc[0][0], acc[1][0], 0); fin(acc[0][1], acc[1][1], 1); }
    else { fin(acc[1][0], acc[0][0], 0); fin(acc[1][1], acc[0][1], 1); }
    STAMPT(6);
    segA3();
    WAIT_VM(4);
  } else {
    WAIT_VM(1);
    BARRIER();
    round1_out();
    do_tick();
    if (w == 7) {
      u32 sq[8];
      const int kss = (sp >> 1) + 16 * (l >> 2) + 256 * (4 * (sp & 1) + (l & 3)), km = (2048 - kss) & 2047;
      const u32 o1 = (u32)(kss * DF_ * 2), o2 = (u32)(km * DF_ * 2);
      ld2(sq[0], o1, (const char*)wr); ld2(sq[1], o1, (const char*)wi); ld2(sq[2], o2, (const char*)wr); ld2(sq[3], o2, (const char*)wi);
      ld2(sq[4], o1 + 512, (const char*)wr); ld2(sq[5], o1 + 512, (const char*)wi); ld2(sq[6], o2 + 512, (const char*)wr); ld2(sq[7], o2 + 512, (const char*)wi);
      WAIT_VM(0);
#pragma unroll
      for (int i = 0; i < 8; ++i) PIN(sq[i]);
      float war = bflo(sq[0]) + bflo(sq[2]), wai = bflo(sq[1]) - bflo(sq[3]);
      float wbr = bflo(sq[4]) + bflo(sq[6]), wbi = bflo(sq[5]) - bflo(sq[7]);
      v4f pv = {0.25f * (war + wbr), 0.25f * (wai + wbi), 0.25f * (war - wbr), 0.25f * (wai - wbi)};
      st16c((u32)((((sp >> 2) * 4 + (l & 3)) * 64 + (sp & 3) * 16 + (l >> 2)) * 16), ws + OFF_PQ, pv);
    }
    WAIT_VM(0);
    BARRIER();
    STAMPT(1); STAMPC(30);
    if (w == 0) { if (!coord) get_epoch(); signal_a(); }
    dma1c((u32)(sp * 57344 + (w * 112 + l) * 64), (const char*)Wd, lbase + 49152);
    dma1c((u32)(sp * 57344 + (w * 112 + 48 + l) * 64), (const char*)Wd, lbase + 49152);
    static_for<0, 6>([&](auto N) { issueW(N); });
#pragma unroll
    for (int i = 0; i < 8; ++i) dma1c((u32)(786432 + (idx & 3) * 262144 + ((i * 8 + w) * 64 + l) * 64), (const char*)Wd, lbase + 49152);
    load_xe();
    trig2(); trig3(); segA0();
    if (w == 0) check_a(v4u{~E, 0u, 0u, 0u});
    BARRIER();
    load_s1(); load_wt(); load_rs();
    WAIT_VM(0);
    segA1(); segA2(); segA3();
    STAMPT(2); STAMPC(31);
  }
  PIN(wt); PIN(xe); PIN(rs2);
  BARRIER();
  STAMPT(5);

  const u32 trG = (u32)(kq * 4096 + (4 * h + q) * 128 + ((dh ^ ((q >> 1) & 1)) << 6) + (blk ^ kh) * 32 + pp * 8);
  v8s Wf[14][2];
  auto readW = [&](auto N) {
    constexpr int n = N;
    LAS char* sb = smem + wslot(n) + trG;
    Wf[n][0] = cat8(trread(sb), trread(sb + 1024)); Wf[n][1] = cat8(trread(sb + 2048), trread(sb + 3072));
  };
  const char* hsrc = ws + OFF_H + ((size_t)(RG * 2 + (w >> 2)) * 112 + (w & 3) * 2) * 1024;
  const u32 so0 = (u32)((l >> 3) * 4096) + swz, so1 = so0 + 32768 - 1024;
  const char* ssrc = ws + OFF_S2 + (size_t)(w * 16) * 4096 + RG * 128;
  auto issueP = [&](int n, auto HALF) {
    constexpr int hf = HALF;
    if (n < 14) dma1s<hf * 1024>((u32)(l * 16), hsrc + n * 8192, lbase + aslot(n) + w * 2048);
    else dma1s<hf * 1024>(hf ? so1 : so0, ssrc + (size_t)(n - 14) * (128 * 4096), lbase + aslot(n) + w * 2048);
  };
  const int d = ct * 64 + dh * 32 + ((l & 31) ^ (kh << 4));
  const float gst = (float)d * (1.0f / 512.0f), ga0 = (float)(16 * kq + 2 * h) * gst;
  v8h G[8];
  auto gen = [&](auto FI) {
    constexpr int f = FI;
#pragma unroll
    for (int e2 = 0; e2 < 4; ++e2) {
      float a = fmaf((float)(64 * (f >> 1) + 8 * (f & 1) + 4 * (e2 >> 1) + (e2 & 1)), gst, ga0);
      float c = cosr(a), sn = sinr(a);
      if (f == 0 && e2 == 0) { if (kq == 0 && h == 0) { c = 0.5f; sn = (d & 1) ? 0.5f : -0.5f; } }
      G[f][2 * e2] = (_Float16)c; G[f][2 * e2 + 1] = (_Float16)sn;
    }
    PIN(G[f]);
  };

  float oscale;
  const u32 fhoff = (u32)(FL_H + (RG * 64 + min(l, 55)) * 4);
  u32 fp;
  {
    v8h a;
    {
      LAS char* rp = smem + L_E1 + (4 * (g & 1) + (o >> 2)) * E1P + (2 * w + (g >> 1)) * 32 + (o & 3) * 8;
      a = __builtin_bit_cast(v8h, cat8(trread(rp), trread(rp + 8 * E1P)));
    }
    const v4u f8 = *(LAS v4u*)(smem + L_TAB + 1024 + l * 16);
    const v2u me = *(LAS v2u*)(smem + L_MX);
    issueW(6);
    static_for<0, 3>(readW);
    const int Ex = (int)me[0] < 16 ? 16 : ((int)me[0] > 240 ? 240 : (int)me[0]);
    const float wscale = __builtin_bit_cast(float, (u32)(246 - Ex) << 23);
    oscale = __builtin_bit_cast(float, (u32)(Ex - 7) << 23);
    re = mfma16h(__builtin_bit_cast(v8h, f8), a, z4); im = mfma16h(imf(f8), a, z4);
    static_for<3, 6>(readW);
    WAIT_VM(2);
    STAMPT(7);
    if (ffn && l == 0) st4c((u32)(FL_H + ((p * 2 + sh) * 64 + j * 4 + fq) * 4), FL, me[1]);
    issueW(7);
    if (kd != 0) {
#pragma unroll
      for (int r = 0; r < 4; ++r) {
        float fr = ((r & 1) ? bfhi(wt[r >> 1]) : bflo(wt[r >> 1])) * wscale, fi = ((r & 1) ? bfhi(wt[2 + (r >> 1)]) : bflo(wt[2 + (r >> 1)])) * wscale;
        float zr = re[r], zi = im[r];
        re[r] = zr * fr - zi * fi;
        im[r] = -(zr * fi + zi * fr);
      }
    } else {
      const int ka = 2 * w + (g >> 1), kr = o + 64 * (g & 1);
#pragma unroll
      for (int r = 0; r < 4; ++r) { v2f t = {re[r], im[r]}; *(LAS v2f*)(smem + L_ZX + ka * 1024 + (kr + 16 * r) * 8) = t; }
      BARRIER();
#pragma unroll
      for (int r = 0; r < 4; ++r) {
        PIN(pq[r]);
        const int km = (2048 - (ka + 16 * (kr + 16 * r))) & 2047;
        v2f zm = *(LAS v2f*)(smem + L_ZX + (km & 15) * 1024 + (km >> 4) * 8);
        float zr = re[r], zi = im[r];
        re[r] = (zr * pq[r][0] - zi * pq[r][1] + zm[0] * pq[r][2] + zm[1] * pq[r][3]) * wscale;
        im[r] = -(zr * pq[r][1] + zi * pq[r][0] + zm[0] * pq[r][3] - zm[1] * pq[r][2]) * wscale;
      }
    }
    LAS char* E2 = smem + (kd == 0 ? L_E1 : L_E2);
    a = pack_ri(re, im);
    re = mfma16h(a, __builtin_bit_cast(v8h, f8), z4); im = mfma16h(a, imf(f8), z4);
    twid(re, im, t3c, t3s);
    *(LAS v4h*)(E2 + (o & 7) * E2P + (2 * w + (o >> 3)) * 32 + g * 8) = pack4h(re);
    *(LAS v4h*)(E2 + (o & 7) * E2P + 512 + (2 * w + (o >> 3)) * 32 + g * 8) = pack4h(im);
    BARRIER();
    STAMPT(8);
    {
      LAS char* rp = E2 + w * E2P + o * 32 + g * 8;
      a = __builtin_bit_cast(v8h, cat8(*(LAS v4s*)rp, *(LAS v4s*)(rp + 512)));
    }
    issueW(8);
    re = mfma16h(a, __builtin_bit_cast(v8h, f16), z4); im = mfma16h(a, imf(f16), z4);
    issueW(9);
    twid(re, im, t1c, t1s);
    a = pack_ri(re, im);
    re = mfma16h(__builtin_bit_cast(v8h, f16), a, z4); im = mfma16h(imf(f16), a, z4);
    issueW(10);
    st8c((u32)(2 * kd * 4096 + w * 512 + l * 8), ws + OFF_S2, pack4h(re));
    st8c((u32)((2 * kd + 1) * 4096 + w * 512 + l * 8), ws + OFF_S2, pack4h(im));
  }
  STAMPT(9);
  gen(IC<0>{}); issueW(11);
  WAIT_VM(8);
  BARRIER();
  static_for<6, 9>(readW);
  gen(IC<1>{}); issueW(12); gen(IC<2>{}); issueW(13);
  if (w == 0) ld4c(fp, fhoff, FL);
  gen(IC<3>{}); gen(IC<4>{});
  WAIT_VM(0);
  if (w == 0) {
    PIN(fp);
    u32 f = fp;
    int spins = 0;
    while (!__all(f == E) && ++spins < SPIN_CAP) {
      __builtin_amdgcn_s_sleep(2);
      f = ld4c_sync(fhoff, FL);
    }
  }
  BARRIER();
  STAMPT(10);
  if (tid == 0) st4c((u32)(FL_S + b * 4), FL, E);
  issueP(0, IC<0>{}); issueP(0, IC<1>{}); issueP(1, IC<0>{}); issueP(1, IC<1>{});
  static_for<9, 14>(readW);
  gen(IC<5>{});
  issueP(2, IC<0>{}); issueP(2, IC<1>{});
  gen(IC<6>{});
  issueP(3, IC<0>{}); issueP(3, IC<1>{});
  gen(IC<7>{});

  const u32 trS = (u32)(kq * 4096 + (4 * h + q) * 128 + ((ph ^ ((q >> 1) & 1)) << 6) + blk * 32 + pp * 8);
  const u32 hb = (u32)(ph * 8192 + kq * 2048 + l * 16);
  v16f accG[2] = {}, accS[2] = {};
  v8s fa[8], fb[8];
  v4u fs;
  float gv[8];
  const u32 xw = (u32)(w * 6144 + l * 16);
  auto xchg_out = [&](const v16f (&ac)[2], int base) {
    LAS char* X = smem + base + xw;
#pragma unroll
    for (int i = 0; i < 6; ++i) {
      const v16f& a = ac[(i + 2) >> 2]; const int r = ((i + 2) & 3) * 4;
      *(LAS v4f*)(X + i * 1024) = v4f{a[r], a[r + 1], a[r + 2], a[r + 3]};
    }
  };
  auto xchg_in = [&](const v16f (&ac)[2], int base, float (&o8)[8]) {
    v4f s0 = {ac[0][0], ac[0][1], ac[0][2], ac[0][3]}, s1 = {ac[0][4], ac[0][5], ac[0][6], ac[0][7]};
#pragma unroll
    for (int x_ = 1; x_ < 4; ++x_) {
      LAS char* X = smem + base + (w ^ (2 * x_)) * 6144 + (x_ - 1) * 2048 + l * 16;
      s0 += *(LAS v4f*)X; s1 += *(LAS v4f*)(X + 1024);
    }
#pragma unroll
    for (int e = 0; e < 4; ++e) { o8[e] = s0[e]; o8[4 + e] = s1[e]; }
  };
  v4f xg[6];
  float ov[8], ex[8];
  auto sadd = [](float a, float b) { float r; asm("v_add_f32 %0, %1, %2" : "=v"(r) : "v"(a), "v"(b)); return r; };
  auto smul = [](float a, float b) { float r; asm("v_mul_f32 %0, %1, %2" : "=v"(r) : "v"(a), "v"(b)); return r; };
  auto silu_step = [&](int i) {
    if (i < 3) {
#pragma unroll
      for (int e = 0; e < 8; ++e) { ov[e] = sadd(i == 0 ? accG[0][e] : ov[e], xg[2 * i + (e >> 2)][e & 3]); PIN(ov[e]); }
    } else if (i == 3) {
#pragma unroll
      for (int e = 0; e < 8; e += 2) { u32 t = pkbf_o(smul(ov[e], rs2), smul(ov[e + 1], rs2)); ov[e] = bflo(t); ov[e + 1] = bfhi(t); PIN(ov[e]); PIN(ov[e + 1]); }
    } else if (i == 4) {
#pragma unroll
      for (int e = 0; e < 8; ++e) { ex[e] = __builtin_amdgcn_exp2f(ov[e] * -1.44269504088896f); PIN(ex[e]); }
    } else if (i == 5) {
#pragma unroll
      for (int e = 0; e < 8; ++e) { ex[e] = __builtin_amdgcn_rcpf(1.0f + ex[e]); PIN(ex[e]); }
    } else if (i == 6) {
#pragma unroll
      for (int e = 0; e < 8; e += 2) { u32 t = pkbf_o(ov[e] * ex[e], ov[e + 1] * ex[e + 1]); gv[e] = bflo(t); gv[e + 1] = bfhi(t); PIN(gv[e]); PIN(gv[e + 1]); }
    }
  };
  static_for<0, 10>([&](auto V) {
    constexpr int v = V;
    v8s(&cur)[8] = (v & 1) ? fb : fa;
    v8s(&prv)[8] = (v & 1) ? fa : fb;
    auto mmp = [&](int s) {
      if constexpr (v >= 8) {
        accS[0] = mfma32h(G[(v - 8) * 4 + s], prv[2 * s], accS[0]); accS[1] = mfma32h(G[(v - 8) * 4 + s], prv[2 * s + 1], accS[1]);
        SCHED();
        if ((v - 8) * 4 + s >= 1) silu_step((v - 8) * 4 + s - 1);
      } else if constexpr (v >= 1) {
        accG[0] = mfma32b(Wf[2 * (v - 1) + (s >> 1)][s & 1], prv[2 * s], accG[0]);
        accG[1] = mfma32b(Wf[2 * (v - 1) + (s >> 1)][s & 1], prv[2 * s + 1], accG[1]);
      }
      SCHED();
    };
    if constexpr (v < 9) {
      WAIT_VM(v <= 7 ? 4 : 0);
      if constexpr (v == 5) { if (w == 0) { PIN(fs); poll4(fs, (u32)(FL_S + l * 16)); } }
      BARRIER();
      if constexpr (v == 0 || v == 8) STAMPT(11 + v);
      if constexpr (v == 4) { if (w == 0) ld16c(fs, (u32)(FL_S + l * 16), FL); }
      if constexpr (v == 8) {
#pragma unroll
        for (int x_ = 1; x_ < 4; ++x_) {
          LAS char* X = smem + 49152 + (w ^ (2 * x_)) * 6144 + (x_ - 1) * 2048 + l * 16;
          xg[2 * x_ - 2] = *(LAS v4f*)X; xg[2 * x_ - 1] = *(LAS v4f*)(X + 1024);
        }
        SCHED();
      }
      mmp(0);
#pragma unroll
      for (int s = 0; s < 4; ++s) {
        if constexpr (v >= 7) {
          LAS char* sb = smem + aslot(2 * v + (s >> 1)) + (s & 1) * 2048;
          cur[2 * s] = cat8(trread(sb + trS), trread(sb + trS + 1024));
          cur[2 * s + 1] = cat8(trread(sb + (trS ^ 64)), trread(sb + (trS ^ 64) + 1024));
        } else {
          LAS char* sb = smem + aslot(2 * v + (s >> 1)) + (s & 1) * 1024;
          cur[2 * s] = *(LAS v8s*)(sb + hb);
          cur[2 * s + 1] = *(LAS v8s*)(sb + (hb ^ 8192));
        }
      }
      SCHED();
      if constexpr (v <= 6) issueP(2 * v + 4, IC<0>{});
      mmp(1);
      if constexpr (v <= 6) issueP(2 * v + 4, IC<1>{});
      mmp(2);
      if constexpr (v <= 6) issueP(2 * v + 5, IC<0>{});
      mmp(3);
      if constexpr (v <= 6) issueP(2 * v + 5, IC<1>{});
    } else {
      mmp(0); mmp(1); mmp(2); mmp(3);
    }
    if constexpr (v == 7) xchg_out(accG, 49152);
    SCHED();
  });
  xchg_out(accS, 0);
  BARRIER();
  {
    float o8[8], pv[8];
    xchg_in(accS, 0, o8);
#pragma unroll
    for (int e = 0; e < 8; e += 2) { u32 t = pkbf_o(o8[e] * oscale, o8[e + 1] * oscale); pv[e] = bflo(t) * gv[e]; pv[e + 1] = bfhi(t) * gv[e + 1]; }
    u32 a0 = pkbf(pv[0], pv[1]), a1 = pkbf(pv[2], pv[3]), b0 = pkbf(pv[4], pv[5]), b1 = pkbf(pv[6], pv[7]);
    auto r0 = __builtin_amdgcn_permlane32_swap(a0, b0, false, false);
    auto r1 = __builtin_amdgcn_permlane32_swap(a1, b1, false, false);
    v4u pk = {r0[0], r1[0], r0[1], r1[1]};
    v4u ov4;
#pragma unroll
    for (int e = 0; e < 4; ++e) ov4[e] = pkbf(bflo(xe[e]) + bflo(pk[e]), bfhi(xe[e]) + bfhi(pk[e]));
    *(v4u*)((char*)out + eoff) = ov4;
  }
  asm volatile("km_pf_b:");
  STAMPT(20);
  STAMPT(21);
  STAMP_OUT(ws);
}

extern "C" void kernel_launch(void* const* d_in, const int* in_sizes, int n_in, void* d_out, int out_size, void* d_ws,
                              size_t ws_size, hipStream_t stream) {
  kmain<<<256, 512, 0, stream>>>((const u16*)d_in[0], (const u16*)d_in[1], (const u16*)d_in[2], (const u16*)d_in[4],
                                 (const u16*)d_in[5], (const u16*)d_in[6], (char*)d_ws + WS_BASE(ws_size), (u16*)d_out);
}
```

```cpp
#include <hip/hip_runtime.h>
#include <stdint.h>

typedef unsigned short u16;
typedef unsigned int u32;
typedef unsigned long long u64;
typedef short v4s __attribute__((ext_vector_type(4)));
typedef short v8s __attribute__((ext_vector_type(8)));
typedef _Float16 v4h __attribute__((ext_vector_type(4)));
typedef _Float16 v8h __attribute__((ext_vector_type(8)));
typedef __bf16 v2b __attribute__((ext_vector_type(2)));
typedef __bf16 v8b __attribute__((ext_vector_type(8)));
typedef float v2f __attribute__((ext_vector_type(2)));
typedef float v4f __attribute__((ext_vector_type(4)));
typedef float v16f __attribute__((ext_vector_type(16)));
typedef u32 v2u __attribute__((ext_vector_type(2)));
typedef u32 v4u __attribute__((ext_vector_type(4)));

#define LAS __attribute__((address_space(3)))
#define GAS __attribute__((address_space(1)))
#define DEVI __device__ __forceinline__

constexpr int D_ = 512, F_ = 1792, DF_ = 257;
constexpr size_t OFF_H = (size_t)24 << 20;
constexpr size_t OFF_S1 = (size_t)32 << 20;
constexpr size_t OFF_WT = (size_t)34 << 20;
constexpr size_t OFF_S2 = (size_t)37 << 20;
constexpr size_t OFF_M = (size_t)39 << 20;
constexpr size_t OFF_FL = OFF_M;
constexpr u32 FL_S = 0, FL_A = 1024, FL_H = 2048;
constexpr size_t OFF_BC = OFF_M + 49152;
constexpr size_t OFF_MB = OFF_M + 98304;
constexpr size_t OFF_RS2 = OFF_M + 106496;
constexpr size_t OFF_PQ = OFF_M + 65536;
constexpr size_t OFF_CSP = OFF_M + 131072;
constexpr int SPIN_CAP = 1 << 16;
#define SLEEP_A 48

#define WS_BASE(n) ((((n) - ((size_t)40 << 20))) & ~(size_t)0x1fffff)
#define STAMP_DECL
#define STAMPT(id)
#define STAMPC(id)
#define STAMP_OUT(wsp)

template <int I> struct IC { static constexpr int v = I; constexpr operator int() const { return I; } };
template <int B, int E, class Fn> DEVI void static_for(Fn&& f) {
  if constexpr (B < E) { f(IC<B>{}); static_for<B + 1, E>(f); }
}
#define WAIT_VM(n) asm volatile("s_waitcnt vmcnt(%0)" ::"n"(n) : "memory")
#define BARRIER() asm volatile("s_waitcnt lgkmcnt(0)\n\ts_barrier" ::: "memory")
#define SCHED() __builtin_amdgcn_sched_barrier(0)
#define PIN(x) asm volatile("" : "+v"(x))

DEVI int s_of_pi(int pi) {
  int RG = pi >> 6, i = pi & 63;
  return 128 * (4 * (RG & 3) + (i & 3)) + 8 * (i >> 2) + (RG >> 2);
}
DEVI void dma2(u32 voff0, u32 voff1, const char* sbase, u32 lds) {
  asm volatile("s_mov_b32 m0, %3\n\ts_nop 0\n\tglobal_load_lds_dwordx4 %0, %2\n\tglobal_load_lds_dwordx4 %1, %2 offset:1024" ::"v"(voff0), "v"(voff1), "s"(sbase), "s"(lds) : "memory");
}
DEVI void dma2c(u32 voff0, u32 voff1, const char* sbase, u32 lds) {
  asm volatile("s_mov_b32 m0, %3\n\ts_nop 0\n\tglobal_load_lds_dwordx4 %0, %2 sc1\n\tglobal_load_lds_dwordx4 %1, %2 offset:1024 sc1" ::"v"(voff0), "v"(voff1), "s"(sbase), "s"(lds) : "memory");
}
template <int OFF> DEVI void dma1(u32 voff, const char* sbase, u32 lds) {
  asm volatile("s_mov_b32 m0, %2\n\ts_nop 0\n\tglobal_load_lds_dwordx4 %0, %1 offset:%3" ::"v"(voff), "s"(sbase), "s"(lds), "n"(OFF) : "memory");
}
template <int OFF> DEVI void dma1s(u32 voff, const char* sbase, u32 lds) {
  asm volatile("s_mov_b32 m0, %2\n\ts_nop 0\n\tglobal_load_lds_dwordx4 %0, %1 offset:%3 sc1" ::"v"(voff), "s"(sbase), "s"(lds), "n"(OFF) : "memory");
}
DEVI void dma1c(u32 voff, const char* sbase, u32 lds) {
  asm volatile("s_mov_b32 m0, %2\n\ts_nop 0\n\tglobal_load_lds_dword %0, %1 sc1" ::"v"(voff), "s"(sbase), "s"(lds) : "memory");
}
DEVI v2u ld8c_sync(u32 voff, const char* sb) { v2u d; asm volatile("global_load_dwordx2 %0, %1, %2 sc1\n\ts_waitcnt vmcnt(0)" : "=&v"(d) : "v"(voff), "s"(sb) : "memory"); return d; }
DEVI u32 ld4c_sync(u32 voff, const char* sb) { u32 d; asm volatile("global_load_dword %0, %1, %2 sc1\n\ts_waitcnt vmcnt(0)" : "=&v"(d) : "v"(voff), "s"(sb) : "memory"); return d; }
DEVI v4u ld16c_sync(u32 voff, const char* sb) { v4u d; asm volatile("global_load_dwordx4 %0, %1, %2 sc1\n\ts_waitcnt vmcnt(0)" : "=&v"(d) : "v"(voff), "s"(sb) : "memory"); return d; }
template <class T> DEVI void ld16c(T& d, u32 voff, const char* sb) { asm volatile("global_load_dwordx4 %0, %1, %2 sc1" : "=&v"(d) : "v"(voff), "s"(sb) : "memory"); }
template <class T> DEVI void ld8c(T& d, u32 voff, const char* sb) { asm volatile("global_load_dwordx2 %0, %1, %2 sc1" : "=&v"(d) : "v"(voff), "s"(sb) : "memory"); }
template <class T> DEVI void ld4c(T& d, u32 voff, const char* sb) { asm volatile("global_load_dword %0, %1, %2 sc1" : "=&v"(d) : "v"(voff), "s"(sb) : "memory"); }
template <class T> DEVI void ld16(T& d, u32 voff, const char* sb) { asm volatile("global_load_dwordx4 %0, %1, %2" : "=&v"(d) : "v"(voff), "s"(sb) : "memory"); }
DEVI void ld2(u32& d, u32 voff, const char* sb) { asm volatile("global_load_ushort %0, %1, %2" : "=&v"(d) : "v"(voff), "s"(sb) : "memory"); }
template <class T> DEVI void st16c(u32 voff, const char* sb, T v) { asm volatile("global_store_dwordx4 %0, %1, %2 sc1\n\ts_nop 1" ::"v"(voff), "v"(v), "s"(sb) : "memory"); }
template <class T> DEVI void st8c(u32 voff, const char* sb, T v) { asm volatile("global_store_dwordx2 %0, %1, %2 sc1" ::"v"(voff), "v"(v), "s"(sb) : "memory"); }
template <class T> DEVI void st4c(u32 voff, const char* sb, T v) { asm volatile("global_store_dword %0, %1, %2 sc1" ::"v"(voff), "v"(v), "s"(sb) : "memory"); }

DEVI v4s trread(LAS char* p) { return __builtin_amdgcn_ds_read_tr16_b64_v4i16((LAS v4s*)p); }
DEVI v8s cat8(v4s a, v4s b) { return __builtin_shufflevector(a, b, 0, 1, 2, 3, 4, 5, 6, 7); }
DEVI float bflo(u32 v) { return __builtin_bit_cast(float, v << 16); }
DEVI float bfhi(u32 v) { return __builtin_bit_cast(float, v & 0xffff0000u); }
DEVI float relu(float v) { return __builtin_bit_cast(float, max(__builtin_bit_cast(int, v), 0)); }
DEVI float rbf(float v) { return (float)(__bf16)v; }
DEVI u32 pkbf(float a, float b) { v2b p = {(__bf16)a, (__bf16)b}; return __builtin_bit_cast(u32, p); }
DEVI u32 pkbf_o(float a, float b) { u32 r = pkbf(a, b); asm("" : "+v"(r)); return r; }
DEVI float cosr(float rev) { return __builtin_amdgcn_cosf(rev); }
DEVI float sinr(float rev) { return __builtin_amdgcn_sinf(rev); }
DEVI v4f mfma16h(v8h a, v8h b, v4f c) { return __builtin_amdgcn_mfma_f32_16x16x32_f16(a, b, c, 0, 0, 0); }
DEVI v4f mfma16b(v8s a, v4u b, v4f c) { return __builtin_amdgcn_mfma_f32_16x16x32_bf16(__builtin_bit_cast(v8b, a), __builtin_bit_cast(v8b, b), c, 0, 0, 0); }
DEVI v16f mfma32b(v8s a, v8s b, v16f c) { return __builtin_amdgcn_mfma_f32_32x32x16_bf16(__builtin_bit_cast(v8b, a), __builtin_bit_cast(v8b, b), c, 0, 0, 0); }
DEVI v16f mfma32h(v8h a, v8s b, v16f c) { return __builtin_amdgcn_mfma_f32_32x32x16_f16(a, __builtin_bit_cast(v8h, b), c, 0, 0, 0); }
DEVI v8h pack_ri(v4f re, v4f im) {
  v8h r;
  r[0] = (_Float16)re[0]; r[1] = (_Float16)re[1]; r[2] = (_Float16)re[2]; r[3] = (_Float16)re[3];
  r[4] = (_Float16)im[0]; r[5] = (_Float16)im[1]; r[6] = (_Float16)im[2]; r[7] = (_Float16)im[3];
  return r;
}
DEVI v4h pack4h(v4f a) { v4h r; r[0] = (_Float16)a[0]; r[1] = (_Float16)a[1]; r[2] = (_Float16)a[2]; r[3] = (_Float16)a[3]; return r; }
DEVI void twid(v4f& re, v4f& im, const v4f& c, const v4f& s) {
#pragma unroll
  for (int r = 0; r < 4; ++r) { float a = re[r], b = im[r]; re[r] = a * c[r] + b * s[r]; im[r] = b * c[r] - a * s[r]; }
}
template <int CTRL> DEVI float dppadd(float v) {
  return v + __builtin_bit_cast(float, __builtin_amdgcn_update_dpp(0, __builtin_bit_cast(int, v), CTRL, 0xf, 0xf, true));
}
DEVI float wave_sum(float v) {
  v = dppadd<0xB1>(v); v = dppadd<0x4E>(v); v = dppadd<0x141>(v); v = dppadd<0x140>(v);
  int bb = __builtin_bit_cast(int, v);
  float a0 = __builtin_bit_cast(float, __builtin_amdgcn_readlane(bb, 0)), a1 = __builtin_bit_cast(float, __builtin_amdgcn_readlane(bb, 16));
  float a2 = __builtin_bit_cast(float, __builtin_amdgcn_readlane(bb, 32)), a3 = __builtin_bit_cast(float, __builtin_amdgcn_readlane(bb, 48));
  return (a0 + a1) + (a2 + a3);
}
DEVI float dot2ones(u32 v, float acc) {
  const v2b ones = {(__bf16)1.0f, (__bf16)1.0f};
  return __builtin_amdgcn_fdot2_f32_bf16(__builtin_bit_cast(v2b, v), ones, acc, false);
}
template <int CTRL> DEVI u32 dppmax(u32 v) { return max(v, (u32)__builtin_amdgcn_update_dpp(0, (int)v, CTRL, 0xf, 0xf, true)); }
DEVI u32 wave_max(u32 v) {
  v = dppmax<0xB1>(v); v = dppmax<0x4E>(v); v = dppmax<0x141>(v); v = dppmax<0x140>(v);
  u32 a0 = __builtin_amdgcn_readlane(v, 0), a1 = __builtin_amdgcn_readlane(v, 16), a2 = __builtin_amdgcn_readlane(v, 32), a3 = __builtin_amdgcn_readlane(v, 48);
  return max(max(a0, a1), max(a2, a3));
}

struct Frag1 { v8s x0, x1, w1, w2; };

constexpr int L_SCR = 147456;
constexpr int L_CS = L_SCR, L_RED = L_SCR + 1024, L_MX = L_SCR + 1088, L_BC0 = L_SCR + 2048, L_BC1 = L_SCR + 2304;
constexpr int L_TAB = L_SCR + 3072;
constexpr int L_T = L_SCR + 7168, L_FA = L_SCR, L_E1 = L_T, L_E2 = 81920, L_ZX = 81920;
constexpr int L_C1 = 98304, L_IMG = L_C1 + 9216;
constexpr int L_FR = 131072;
constexpr int E1P = 544, E2P = 1024 + 72;
DEVI constexpr int wslot(int n) {
  constexpr int u[14] = {6, 7, 8, 0, 1, 2, 3, 4, 6, 7, 8, 0, 1, 2};
  return u[n] * 16384;
}
DEVI constexpr int aslot(int n) {
  constexpr int u[18] = {3, 4, 5, 9, 2, 6, 7, 8, 0, 1, 3, 4, 5, 9, 2, 6, 7, 8};
  return u[n] * 16384;
}

__global__ __launch_bounds__(512, 2) void kmain(const u16* __restrict__ x, const u16* __restrict__ wr, const u16* __restrict__ wi,
                                                const u16* __restrict__ Wg, const u16* __restrict__ Wu, const u16* __restrict__ Wd,
                                                char* __restrict__ ws, u16* __restrict__ out) {
  __shared__ __attribute__((aligned(16))) char smem_[163840];
  LAS char* smem = (LAS char*)smem_;
  const u32 lbase = (u32)(size_t)smem;
  const int tid = threadIdx.x, l = tid & 63;
  const int w = __builtin_amdgcn_readfirstlane(tid >> 6);
  const int b = blockIdx.x, xcd = b & 7, idx = b >> 3;
  const bool ffn = idx < 28;
  const int g = l >> 4, o = l & 15;
  const int h = l >> 5, blk = (l >> 4) & 1, q = (l & 15) >> 2, pp = l & 3;
  const char* FL = ws + OFF_FL;
  const char* BC = ws + OFF_BC;
  STAMP_DECL; STAMPT(0);

  const int pi = b * 8 + w;
  v4u xr;
  {
    u64 pc; u32 len;
    asm volatile("s_getpc_b64 %0\nkm_pf_a:\n\ts_mov_b32 %1, km_pf_b-km_pf_a" : "=s"(pc), "=s"(len));
    dma1<0>(min((u32)(((idx & 3) * 8 + w) * 1024 + l * 16), len) & ~15u, (const char*)(pc & ~15ull), lbase + L_IMG + w * 1024);
  }
  ld16(xr, (u32)(s_of_pi(pi) * 1024 + l * 16), (const char*)x);
  if (w == 0) dma1c((u32)((l & 1) * 4), BC, lbase + L_BC0);
  const int fkb = (b & 7) * 2 + (w >> 2), fcq = w & 3;
  {
    const u32 ks = (u32)((idx >> 1) + 16 * fkb + 256 * (4 * (idx & 1) + ((l >> 3) & 3)));
    const u32 base = ((ks * (DF_ * 2)) & ~15u) + fcq * 128;
    const u32 dlt = (u32)((const char*)wi - (const char*)wr);
    (void)dlt;
    const u32 c0 = ((l & 7) ^ (((l >> 4) & 1) << 2)) << 4;
    if (l < 32) dma1<0>(base + c0, (const char*)wr, lbase + L_FR + w * 2048);
    else dma1<0>(base + c0, (const char*)wi, lbase + L_FR + w * 2048);
    if (fcq == 3) {
      const u32 t0 = min(base + 128 + (c0 & 16), (u32)(2048 * DF_ * 2 - 16));
      if (l < 32) dma1<0>(t0, (const char*)wr, lbase + L_FR + w * 2048 + 1024);
      else dma1<0>(t0, (const char*)wi, lbase + L_FR + w * 2048 + 1024);
    }
  }
  const int p = (xcd >> 1) * 4 + idx / 7, j = (xcd & 1) * 7 + idx % 7;
  const int sh = w >> 2, fq = w & 3;
  u32 aoff[2];
#pragma unroll
  for (int i = 0; i < 2; ++i) {
    int row = (2 * w + i) * 8 + (l >> 3);
    aoff[i] = s_of_pi((p & 15) * 128 + row) * 1024 + (((l & 7) ^ ((row >> 1) & 7)) << 4) - i * 1024;
  }
  const u32 boff = (u32)(2 * w * 4 + (l >> 4)) * (F_ * 2) + j * 256 + (((l & 15) ^ ((l >> 4) << 2)) << 4);
  const u32 boff1 = boff + 4 * F_ * 2 - 1024;
  const int rot = p >> 1;
  auto issue = [&](int t, int slot) {
    const int kt = (t + rot) & 7;
    u32 dst = lbase + slot * 49152 + w * 2048;
    dma2(aoff[0], aoff[1], (const char*)x + kt * 128, dst);
    dma2(boff, boff1, (const char*)Wg + (size_t)kt * (64 * F_ * 2), dst + 16384);
    dma2(boff, boff1, (const char*)Wu + (size_t)kt * (64 * F_ * 2), dst + 32768);
  };
  const int sp = xcd * 4 + (idx - 28);
  if (ffn) issue(0, 0);

  u32 E = 0, tick = 0;
  const bool coord = (b == 224) && (w == 0);
  auto do_tick = [&]() { if (coord) { ++tick; if (l == 0) st8c(0u, BC, v2u{E, tick}); } };
  auto get_epoch = [&]() {
    const v2u b0 = *(LAS v2u*)(smem + L_BC0);
    v2u b1 = *(LAS v2u*)(smem + L_BC1);
    int spins = 0;
    while (__builtin_amdgcn_readfirstlane((b0[0] ^ b1[0]) | (b0[1] ^ b1[1])) == 0 && ++spins < SPIN_CAP) {
      __builtin_amdgcn_s_sleep(4);
      b1 = ld8c_sync(0u, BC);
    }
    E = __builtin_amdgcn_readfirstlane(b1[0]);
  };
  auto signal_a = [&]() {
    v4u r0 = *(LAS v4u*)(smem + L_RED), r1 = *(LAS v4u*)(smem + L_RED + 16);
    u32 m = max(max(max(r0[0], r0[1]), max(r0[2], r0[3])), max(max(r1[0], r1[1]), max(r1[2], r1[3])));
    if (l == 0) { st4c((u32)(FL_A + b * 4), FL, E | (m >> 7)); *(LAS u32*)(smem + L_MX + 4) = E; }
  };
  auto poll4 = [&](v4u f, u32 voff) -> v4u {
    int spins = 0;
    while (!__all((((f[0] ^ E) | (f[1] ^ E) | (f[2] ^ E) | (f[3] ^ E)) >> 8) == 0) && ++spins < SPIN_CAP) {
      do_tick();
      __builtin_amdgcn_s_sleep(12);
      f = ld16c_sync(voff, FL);
    }
    return f;
  };
  auto check_a = [&](v4u f) {
    f = poll4(f, (u32)(FL_A + l * 16));
    u32 m = wave_max(max(max(f[0], f[1]), max(f[2], f[3]))) & 0xffu;
    if (l == 0) *(LAS u32*)(smem + L_MX) = m;
  };
  if (coord) {
#pragma unroll
    for (int i = 0; i < 5; ++i) dma2c(i ? (u32)(FL_H + ((i - 1) * 128 + l) * 64) : (u32)(l * 16), i ? (u32)(FL_H + ((i - 1) * 128 + 64 + l) * 64 - 1024) : (u32)(l * 16), FL, lbase + i * 2048);
  }
  u32 sq[8];
  if (!ffn && w == 7) {
    const int kss = (sp >> 1) + 16 * (l >> 2) + 256 * (4 * (sp & 1) + (l & 3)), km = (2048 - kss) & 2047;
    const u32 o1 = (u32)(kss * DF_ * 2), o2 = (u32)(km * DF_ * 2);
    ld2(sq[0], o1, (const char*)wr); ld2(sq[1], o1, (const char*)wi); ld2(sq[2], o2, (const char*)wr); ld2(sq[3], o2, (const char*)wi);
    ld2(sq[4], o1 + 512, (const char*)wr); ld2(sq[5], o1 + 512, (const char*)wi); ld2(sq[6], o2 + 512, (const char*)wr); ld2(sq[7], o2 + 512, (const char*)wi);
  }

  {
    const float fo = (float)o, fg = (float)(4 * g);
    if (w < 2) {
      const float st = fo * (1.0f / 32.0f), a0 = fmaf(fg, st, w ? 0.25f : 0.f);
      v4u hi, lo;
#pragma unroll
      for (int e2 = 0; e2 < 4; ++e2) {
        float c0 = cosr(fmaf((float)(16 * (e2 >> 1) + 2 * (e2 & 1)), st, a0)), c1 = cosr(fmaf((float)(16 * (e2 >> 1) + 2 * (e2 & 1) + 1), st, a0));
        u32 ch = pkbf_o(c0, c1);
        hi[e2] = ch; lo[e2] = pkbf(c0 - bflo(ch), c1 - bfhi(ch));
      }
      *(LAS v4u*)(smem + L_C1 + w * 2048 + l * 16) = hi; *(LAS v4u*)(smem + L_C1 + w * 2048 + 1024 + l * 16) = lo;
    } else if (w < 5) {
      const float st = w == 2 ? (float)(o & 7) * (1.0f / 16.0f) : fo * (w == 3 ? 1.0f / 16.0f : 1.0f / 8.0f);
      const float a0 = (w == 2 && o >= 8) ? 0.25f : 0.f;
      const float sc = w == 2 ? 0.0625f : ((w == 4 && (g >> 1) != (o >> 3)) ? 0.f : 1.0f);
      v8h m;
#pragma unroll
      for (int e = 0; e < 4; ++e) { float a = fmaf(fg + (float)e, st, a0); m[e] = (_Float16)(sc * cosr(a)); m[4 + e] = (_Float16)(sc * sinr(a)); }
      *(LAS v8h*)(smem + (w == 2 ? L_C1 + 4096 : L_TAB + (w - 3) * 1024) + l * 16) = m;
    } else {
      const float st = w == 7 ? fo * (1.0f / 256.0f) : (fo + (w == 6 ? 16.f : 0.f)) * (1.0f / 512.0f);
      v4f c, sn;
#pragma unroll
      for (int e = 0; e < 4; ++e) { float a = (fg + (float)e) * st; c[e] = cosr(a); sn[e] = sinr(a); }
      LAS char* dst = smem + (w == 7 ? L_TAB + 2048 : L_C1 + 5120 + (w - 5) * 2048) + l * 16;
      *(LAS v4f*)dst = c; *(LAS v4f*)(dst + 1024) = sn;
    }
    BARRIER();
    LAS char* C1 = smem + L_C1 + l * 16;
    const v4u Fhr = *(LAS v4u*)C1, Flr = *(LAS v4u*)(C1 + 1024), Fhi = *(LAS v4u*)(C1 + 2048), Fli = *(LAS v4u*)(C1 + 3072);
    const v8h M2 = *(LAS v8h*)(C1 + 4096);
    v4f twc[2], tws[2];
#pragma unroll
    for (int u = 0; u < 2; ++u) { twc[u] = *(LAS v4f*)(C1 + 5120 + u * 2048); tws[u] = *(LAS v4f*)(C1 + 6144 + u * 2048); }
    if (ffn) WAIT_VM(8); else if (w == 7) WAIT_VM(10); else WAIT_VM(2);
    if (coord) {
      WAIT_VM(0);
      v4u f[10];
#pragma unroll
      for (int i = 0; i < 10; ++i) f[i] = *(LAS v4u*)(smem + i * 1024 + l * 16);
      u32 c = __builtin_amdgcn_readfirstlane(*(LAS u32*)(smem + L_BC0)) >> 8;
      bool hit;
      do {
        c = (c + 1) & 0xffffffu; hit = false;
#pragma unroll
        for (int i = 0; i < 10; ++i) hit |= ((f[i][0] >> 8) == c) | ((f[i][1] >> 8) == c) | ((f[i][2] >> 8) == c) | ((f[i][3] >> 8) == c);
      } while (__any(hit));
      E = c << 8;
      do_tick();
    }
    PIN(xr);
    STAMPT(3);
    const float c0 = bflo(__builtin_amdgcn_readfirstlane(xr[0]));
    v2f xv[4], s2 = {0.f, 0.f}, q2 = {0.f, 0.f};
#pragma unroll
    for (int e = 0; e < 4; ++e) {
      xv[e] = v2f{bflo(xr[e]), bfhi(xr[e])};
      v2f t = xv[e] - v2f{c0, c0};
      s2 += t; q2 = __builtin_elementwise_fma(t, t, q2);
    }
    const float dmean = wave_sum(s2[0] + s2[1]) * (1.0f / 512.0f), msq = wave_sum(q2[0] + q2[1]) * (1.0f / 512.0f);
    do_tick();
    const float mb = rbf(c0 + dmean);
    const float var = rbf(fmaxf(msq - dmean * dmean, 0.f));
    const float sd = rbf(__builtin_amdgcn_sqrtf(rbf(var + 1e-5f)));
    const float rs = __builtin_amdgcn_rcpf(sd);
#pragma unroll
    for (int e = 0; e < 4; ++e) {
      v2f t = xv[e] - v2f{mb, mb};
      u32 tb = pkbf_o(t[0], t[1]);
      t = v2f{bflo(tb), bfhi(tb)} * v2f{rs, rs};
      xr[e] = pkbf(t[0], t[1]);
    }
    *(LAS v4u*)(smem + L_IMG + w * 1024 + l * 16) = xr;
    if (l < 2) st4c((u32)(pi * 4 + l * 8192), ws + OFF_MB, l ? rs * rs : -mb);
    const v8s xf = cat8(trread(smem + L_IMG + w * 1024 + l * 8), trread(smem + L_IMG + w * 1024 + 512 + l * 8));
    const int kt1 = (1 + rot) & 7;
    const u32 dst1 = lbase + 49152 + w * 2048;
    if (ffn) dma2(aoff[0], aoff[1], (const char*)x + kt1 * 128, dst1);
    const v8s xg_ = __builtin_bit_cast(v8s, __builtin_bit_cast(v4u, xf) ^ v4u{0x80000000u, 0x80000000u, 0x80000000u, 0x80000000u});
    const v4f z = {0.f, 0.f, 0.f, 0.f};
    v4f dre[2], dim[2];
#pragma unroll
    for (int u = 0; u < 2; ++u) { dre[u] = mfma16b(u ? xg_ : xf, Flr, z); dim[u] = mfma16b(u ? xg_ : xf, Fli, z); }
#pragma unroll
    for (int u = 0; u < 2; ++u) { dre[u] = mfma16b(u ? xg_ : xf, Fhr, dre[u]); dim[u] = mfma16b(u ? xg_ : xf, Fhi, dim[u]); }
#pragma unroll
    for (int u = 0; u < 2; ++u) {
      twid(dre[u], dim[u], twc[u], tws[u]);
      const v8h bb = pack_ri(dre[u], dim[u]);
      v4f d2 = mfma16h(bb, M2, z);
      if (u == 0) {
        const u32 nv = o == 8 ? 0xac002c00u : 0u;
        v4f n = mfma16h(bb, __builtin_bit_cast(v8h, v4u{nv, nv, 0u, 0u}), z);
        if (l == 8) d2[0] = n[0];
      }
      *(LAS v4h*)(smem + L_T + ((o >> 3) * 8 + w) * 544 + (o & 7) * 64 + u * 32 + g * 8) = pack4h(d2);
    }
    if (ffn) {
      dma2(boff, boff1, (const char*)Wg + (size_t)kt1 * (64 * F_ * 2), dst1 + 16384);
      dma2(boff, boff1, (const char*)Wu + (size_t)kt1 * (64 * F_ * 2), dst1 + 32768);
    }
    STAMPT(4);
  }
  auto round1_out = [&]() {
    LAS char* rp = smem + L_T + (4 * (g & 1) + (o >> 2)) * 544 + (w * 32 + (g >> 1) * 16) * 2 + (o & 3) * 8;
    const v8s su = cat8(trread(rp), trread(rp + 8 * 544));
    LAS char* fp_ = smem + L_FR + w * 2048 + q * 128 + (((2 * g + (pp >> 1)) ^ ((q >> 1) << 2)) << 4) + (pp & 1) * 8;
    const v4u fu = __builtin_bit_cast(v4u, cat8(trread(fp_), trread(fp_ + 512)));
    st16c((u32)(b * 8192 + (w * 32 + (g >> 1) * 16 + o) * 32 + (g & 1) * 16), ws + OFF_S1, su);
    st16c((u32)((idx * 16 + fkb) * 4608 + (fcq * 64 + l) * 16), ws + OFF_WT, fu);
    typedef unsigned short v2us __attribute__((ext_vector_type(2)));
    const int dl = (idx >> 1) & 7;
    u32 msk = (fcq == 0 && l < dl) ? 0u : 0x7fff7fffu;
    v2us m2 = {0, 0};
#pragma unroll
    for (int e = 0; e < 4; ++e) { u32 t = fu[e] & msk; m2 = __builtin_elementwise_max(m2, __builtin_bit_cast(v2us, t)); }
    if (fcq == 3) {
      LAS char* tp = smem + L_FR + w * 2048 + 1024 + q * 128 + (pp >> 1) * 16 + (pp & 1) * 8;
      const v4u tu = __builtin_bit_cast(v4u, cat8(trread(tp), trread(tp + 512)));
      if (l < 16) st16c((u32)((idx * 16 + fkb) * 4608 + (256 + l) * 16), ws + OFF_WT, tu);
      msk = (o <= dl) ? 0x7fff7fffu : 0u;
#pragma unroll
      for (int e = 0; e < 4; ++e) { u32 t = tu[e] & msk; m2 = __builtin_elementwise_max(m2, __builtin_bit_cast(v2us, t)); }
    }
    const u32 mx = wave_max(max((u32)m2[0], (u32)m2[1]));
    if (l == 0) *(LAS u32*)(smem + L_RED + w * 4) = mx;
    if (w == 0 && !coord) dma1c((u32)((l & 1) * 4), BC, lbase + L_BC1);
  };

  const int RG = xcd * 4 + (idx >> 3), ct = idx & 7;
  const int kd = xcd * 32 + idx;
  const u32 swz = ((l & 7) ^ (((l >> 4) & 1) << 2)) << 4;
  const u32 ro0 = (u32)((l >> 3) * 1024) + swz, ro1 = ro0 + 8192 - 1024;
  const char* bsrc = (const char*)Wd + (size_t)(w * 16) * 1024 + ct * 128;
  auto issueW = [&](int n) { dma2(ro0, ro1, bsrc + (size_t)n * 131072, lbase + wslot(n) + w * 2048); };
  auto pfw = [&](int i, u32 zone) { dma1<0>((u32)(l * 16), (const char*)Wd + 786432 + idx * 32768 + (i * 8 + w) * 1024, lbase + zone + w * 1024); };
  const int dh = w & 1, kq = w >> 1, ph = kq >> 1, kh = kq & 1;
  const int epi = RG * 64 + ph * 32 + (l & 31);
  const u32 eoff = (u32)(s_of_pi(epi) * D_ + ct * 64 + dh * 32 + kh * 16 + h * 8) * 2;
  v4u wt, s1v, xe; v4f pq[4]; float rs2;
  auto load_xe = [&]() { ld16(xe, eoff, (const char*)x); };
  auto load_s1 = [&]() { ld16c(s1v, (u32)((((4 * w + g) * 8 + (o >> 1)) * 256 + kd) * 32 + (o & 1) * 16), ws + OFF_S1); };
  auto load_wt = [&]() { ld16c(wt, (u32)((w * 64 + l) * 4608 + (kd + ((2 * w + (g >> 1)) & 7)) * 16), ws + OFF_WT); };
  auto load_rs = [&]() { ld4c(rs2, (u32)(epi * 4), ws + OFF_RS2); };
  v4f t2c, t2s, t3c, t3s;
  auto trig2 = [&]() {
    const float fw = (float)w, st2 = fw * (1.0f / 128.0f), a2 = fw * (float)(o + 64 * g) * (1.0f / 2048.0f);
#pragma unroll
    for (int e = 0; e < 4; ++e) { float b2 = fmaf((float)e, st2, a2); t2c[e] = cosr(b2); t2s[e] = sinr(b2); }
    PIN(t2c); PIN(t2s);
  };
  auto trig3 = [&]() {
    const float f7 = (float)(o & 7), st3 = f7 * (1.0f / 128.0f), a3 = f7 * (float)(2 * w + (o >> 3) + 64 * g) * (1.0f / 2048.0f);
#pragma unroll
    for (int e = 0; e < 4; ++e) { float b3 = fmaf((float)e, st3, a3); t3c[e] = cosr(b3); t3s[e] = sinr(b3); }
    PIN(t3c); PIN(t3s);
  };
  v4u f16; v4f t1c, t1s, re, im;
  const v4f z4 = {0.f, 0.f, 0.f, 0.f};
  auto imf = [](v4u f) { return __builtin_bit_cast(v8h, v4u{f[2] ^ 0x80008000u, f[3] ^ 0x80008000u, f[0], f[1]}); };
  auto segA0 = [&]() {
    f16 = *(LAS v4u*)(smem + L_TAB + l * 16);
    t1c = *(LAS v4f*)(smem + L_TAB + 2048 + l * 16); t1s = *(LAS v4f*)(smem + L_TAB + 3072 + l * 16);
  };
  auto segA1 = [&]() {
    PIN(s1v);
    const v8h a = __builtin_bit_cast(v8h, s1v);
    re = mfma16h(a, __builtin_bit_cast(v8h, f16), z4); im = mfma16h(a, imf(f16), z4);
  };
  auto segA2 = [&]() {
    twid(re, im, t1c, t1s);
    const v8h a = pack_ri(re, im);
    re = mfma16h(__builtin_bit_cast(v8h, f16), a, z4); im = mfma16h(imf(f16), a, z4);
  };
  auto segA3 = [&]() {
    twid(re, im, t2c, t2s);
    *(LAS v4h*)(smem + L_E1 + w * E1P + o * 32 + g * 8) = pack4h(re);
    *(LAS v4h*)(smem + L_E1 + (8 + w) * E1P + o * 32 + g * 8) = pack4h(im);
  };

  if (ffn) {
    v16f acc[2][2] = {};
    float mbr[2];
    const u32 wb0 = (u32)((8 * h + q) * 256 + ((fq ^ q) << 6) + blk * 32 + pp * 8);
    const u32 r1 = 16384 + sh * 16384, r2 = 32768 - sh * 16384;
    const int xrow = sh * 64 + (l & 31);
    const u32 xb0 = (u32)(xrow * 128 + ((h ^ ((xrow >> 1) & 7)) << 4));
    auto rd = [&](Frag1& f, LAS char* sb, int i) {
      LAS char* xa = sb + (xb0 ^ (i << 5));
      LAS char* wa_ = sb + wb0 + (i << 12);
      f.w1 = cat8(trread(wa_ + r1), trread(wa_ + r1 + 1024));
      f.x0 = *(LAS v8s*)xa; f.x1 = *(LAS v8s*)(xa + 4096);
      f.w2 = cat8(trread(wa_ + r2), trread(wa_ + r2 + 1024));
    };
    const u32 one = ((l & 15) == ((l >> 4) & 1)) ? 0x3f803f80u : 0u;
    const v8s sel = __builtin_bit_cast(v8s, v4u{one, one, one, one});
    v4f csa = {0.f, 0.f, 0.f, 0.f};
    Frag1 A, B;
    WAIT_VM(7);
    BARRIER();
    STAMPT(1); STAMPC(30);
    rd(A, smem, 0);
    dma2(aoff[0], aoff[1], (const char*)x + ((2 + rot) & 7) * 128, lbase + 2 * 49152 + w * 2048);
    u32 c1[4], c2[4];
    static_for<0, 8>([&](auto T) {
      constexpr int t = T, slot = t % 3, nslot = (t + 1) % 3, pslot = (t + 2) % 3;
      LAS char* sb = smem + slot * 49152;
      {
        const int kt = (t + 2 + rot) & 7;
        const char* s1 = t < 6 ? (const char*)Wg + (size_t)kt * (64 * F_ * 2) : bsrc + (size_t)((t - 6) * 3 + 1) * 131072;
        const char* s2 = t < 6 ? (const char*)Wu + (size_t)kt * (64 * F_ * 2) : bsrc + (size_t)((t - 6) * 3 + 2) * 131072;
        const u32 o2 = t < 6 ? boff : ro0, o3 = t < 6 ? boff1 : ro1;
        const u32 dst = lbase + pslot * 49152 + w * 2048;
        rd(B, sb, 1); SCHED();
        acc[0][0] = mfma32b(A.w1, A.x0, acc[0][0]); acc[0][1] = mfma32b(A.w1, A.x1, acc[0][1]);
        if constexpr (t == 0) csa = mfma16b(sel, __builtin_bit_cast(v4u, A.w1), csa);
        SCHED();
        dma1<0>(o2, s1, dst + 16384);
        if constexpr (t == 2) load_xe();
        if constexpr (t == 6) load_wt();
        acc[1][0] = mfma32b(A.w2, A.x0, acc[1][0]); acc[1][1] = mfma32b(A.w2, A.x1, acc[1][1]); SCHED();
        if constexpr (t == 0) { round1_out(); SCHED(); }
        if constexpr (t == 3) { trig2(); SCHED(); }
        if constexpr (t == 4) { trig3(); SCHED(); }
        rd(A, sb, 2); SCHED();
        acc[0][0] = mfma32b(B.w1, B.x0, acc[0][0]); SCHED();
        dma1<1024>(o3, s1, dst + 16384);
        acc[0][1] = mfma32b(B.w1, B.x1, acc[0][1]);
        if constexpr (t == 0) csa = mfma16b(sel, __builtin_bit_cast(v4u, B.w1), csa);
        acc[1][0] = mfma32b(B.w2, B.x0, acc[1][0]); acc[1][1] = mfma32b(B.w2, B.x1, acc[1][1]); SCHED();
        dma1<0>(o2, s2, dst + 32768);
        if constexpr (t == 6) {
          load_rs();
          if (kd == 0) {
#pragma unroll
            for (int r = 0; r < 4; ++r) ld16c(pq[r], (u32)(w * 4096 + r * 1024 + l * 16), ws + OFF_PQ);
          }
        }
        if constexpr (t == 7) { WAIT_VM(13); segA1(); SCHED(); }
        rd(B, sb, 3); SCHED();
        if constexpr (t == 7) {
#pragma unroll
          for (int i = 0; i < 4; ++i) { PIN(c1[i]); PIN(c2[i]); }
          PIN(mbr[0]); PIN(mbr[1]);
          const v8s cs1 = __builtin_bit_cast(v8s, v4u{c1[0], c1[1], c1[2], c1[3]}), cs2 = __builtin_bit_cast(v8s, v4u{c2[0], c2[1], c2[2], c2[3]});
          const u32 m0_ = pkbf(mbr[0], mbr[0]), m1_ = pkbf(mbr[1], mbr[1]);
          const v8s mb0 = __builtin_bit_cast(v8s, v4u{m0_, m0_, m0_, m0_}), mb1 = __builtin_bit_cast(v8s, v4u{m1_, m1_, m1_, m1_});
          acc[0][0] = mfma32b(cs1, mb0, acc[0][0]); acc[1][0] = mfma32b(cs2, mb0, acc[1][0]);
          acc[0][1] = mfma32b(cs1, mb1, acc[0][1]); acc[1][1] = mfma32b(cs2, mb1, acc[1][1]); SCHED();
          segA2(); SCHED();
        }
        acc[0][0] = mfma32b(A.w1, A.x0, acc[0][0]); acc[0][1] = mfma32b(A.w1, A.x1, acc[0][1]);
        if constexpr (t == 0) csa = mfma16b(sel, __builtin_bit_cast(v4u, A.w1), csa);
        SCHED();
        dma1<1024>(o3, s2, dst + 32768);
        acc[1][0] = mfma32b(A.w2, A.x0, acc[1][0]); acc[1][1] = mfma32b(A.w2, A.x1, acc[1][1]); SCHED();
      }
      if constexpr (t < 7) {
        const char* s0 = t < 5 ? (const char*)x + ((t + 3 + rot) & 7) * 128 : bsrc + (size_t)((t - 5) * 3) * 131072;
        const u32 o0 = t < 5 ? aoff[0] : ro0, o1 = t < 5 ? aoff[1] : ro1;
        const u32 dst = lbase + slot * 49152 + w * 2048;
        if constexpr (t == 0) csa = mfma16b(sel, __builtin_bit_cast(v4u, B.w1), csa);
        if constexpr (t >= 1 && t <= 4) pfw(t - 1, L_T + 512);
        WAIT_VM(t == 0 ? 8 : t == 1 ? 7 : t == 2 ? 9 : t <= 4 ? 8 : t == 5 ? 6 : 19);
        if constexpr (t == 5) { if (w == 0) check_a(*(LAS v4u*)(smem + L_FA + l * 16)); }
        BARRIER();
        if constexpr (t == 0 || t == 1 || t == 5 || t == 6) STAMPT(22 + t);
        acc[0][0] = mfma32b(B.w1, B.x0, acc[0][0]); SCHED();
        rd(A, smem + nslot * 49152, 0);
        if constexpr (t == 6) segA0();
        SCHED();
        if constexpr (t == 0) {
          if (l < 16) {
#pragma unroll
            for (int i = 0; i < 2; ++i) {
              const float hi = rbf(csa[i]);
              st4c((u32)(((j * 16 + p) * 256 + sh * 128 + fq * 32 + i * 16 + l) * 4), ws + OFF_CSP, pkbf(hi, csa[i] - hi));
            }
          }
        }
        if constexpr (t == 4) { if (w == 0) dma1s<0>((u32)(FL_A + l * 16), FL, lbase + L_FA); }
        if constexpr (t == 5) {
          const u32 co = (u32)(((j * 16 + (p & 1) + 8 * h) * 256 + fq * 32 + (l & 31)) * 4);
#pragma unroll
          for (int i = 0; i < 4; ++i) { ld4c(c1[i], co + sh * 512 + i * 2048, ws + OFF_CSP); ld4c(c2[i], co + (1 - sh) * 512 + i * 2048, ws + OFF_CSP); }
          ld4c(mbr[0], (u32)((p * 128 + xrow) * 4), ws + OFF_MB); ld4c(mbr[1], (u32)((p * 128 + xrow + 32) * 4), ws + OFF_MB);
          load_s1();
        }
        dma1<0>(o0, s0, dst);
        acc[0][1] = mfma32b(B.w1, B.x1, acc[0][1]);
        acc[1][0] = mfma32b(B.w2, B.x0, acc[1][0]);
        acc[1][1] = mfma32b(B.w2, B.x1, acc[1][1]); SCHED();
        dma1<1024>(o1, s0, dst);
        if constexpr (t == 1) { if (w == 0) { get_epoch(); signal_a(); } }
        SCHED();
      } else {
        acc[0][0] = mfma32b(B.w1, B.x0, acc[0][0]); acc[1][0] = mfma32b(B.w2, B.x0, acc[1][0]); SCHED();
        acc[0][1] = mfma32b(B.w1, B.x1, acc[0][1]); acc[1][1] = mfma32b(B.w2, B.x1, acc[1][1]); SCHED();
      }
    });
    STAMPT(2); STAMPC(31);
    auto fin = [&](const v16f& ag, const v16f& au, int rt) {
#pragma unroll
      for (int s2i = 0; s2i < 2; ++s2i) {
        v4u ov;
#pragma unroll
        for (int e = 0; e < 4; ++e) {
          const int r = s2i * 8 + 2 * e;
          v2f gg = v2f{relu(ag[r]), relu(ag[r + 1])} * v2f{au[r], au[r + 1]};
          ov[e] = pkbf(gg[0], gg[1]);
        }
        st16c((u32)(((p * 4 + sh * 2 + rt) * 112 + (j * 8 + fq * 2 + s2i)) * 1024 + l * 16), ws + OFF_H, ov);
      }
    };
    if (sh == 0) { fin(acc[0][0], acc[1][0], 0); fin(acc[0][1], acc[1][1], 1); }
    else { fin(acc[1][0], acc[0][0], 0); fin(acc[1][1], acc[0][1], 1); }
    STAMPT(6);
    segA3();
    WAIT_VM(4);
  } else {
    WAIT_VM(1);
    BARRIER();
    round1_out();
    do_tick();
    if (w == 7) {
#pragma unroll
      for (int i = 0; i < 8; ++i) PIN(sq[i]);
      float war = bflo(sq[0]) + bflo(sq[2]), wai = bflo(sq[1]) - bflo(sq[3]);
      float wbr = bflo(sq[4]) + bflo(sq[6]), wbi = bflo(sq[5]) - bflo(sq[7]);
      v4f pv = {0.25f * (war + wbr), 0.25f * (wai + wbi), 0.25f * (war - wbr), 0.25f * (wai - wbi)};
      st16c((u32)((((sp >> 2) * 4 + (l & 3)) * 64 + (sp & 3) * 16 + (l >> 2)) * 16), ws + OFF_PQ, pv);
    }
    WAIT_VM(0);
    BARRIER();
    STAMPT(1); STAMPC(30);
    if (w == 0) { if (!coord) get_epoch(); signal_a(); do_tick(); WAIT_VM(0); }
    BARRIER();
    STAMPT(24);
    dma1c((u32)(sp * 57344 + (w * 112 + l) * 64), (const char*)Wd, lbase + 49152);
    dma1c((u32)(sp * 57344 + (w * 112 + 48 + l) * 64), (const char*)Wd, lbase + 49152);
#pragma unroll
    for (int i = 0; i < 4; ++i) pfw(i, 49152);
    static_for<0, 6>([&](auto N) { issueW(N); });
    load_xe();
    trig2(); trig3(); segA0();
    STAMPT(25);
    if (w == 0) {
      if (!coord) __builtin_amdgcn_s_sleep(SLEEP_A);
      check_a(v4u{~E, 0u, 0u, 0u});
    }
    STAMPT(26);
    BARRIER();
    load_s1(); load_wt(); load_rs();
    WAIT_VM(0);
    segA1(); segA2(); segA3();
    STAMPT(2); STAMPC(31);
  }
  PIN(wt); PIN(xe); PIN(rs2);
  BARRIER();
  STAMPT(5);

  const u32 trG = (u32)(kq * 4096 + (4 * h + q) * 128 + ((dh ^ ((q >> 1) & 1)) << 6) + (blk ^ kh) * 32 + pp * 8);
  v8s Wf[14][2];
  auto readW = [&](auto N) {
    constexpr int n = N;
    LAS char* sb = smem + wslot(n) + trG;
    Wf[n][0] = cat8(trread(sb), trread(sb + 1024)); Wf[n][1] = cat8(trread(sb + 2048), trread(sb + 3072));
  };
  const char* hsrc = ws + OFF_H + ((size_t)(RG * 2 + (w >> 2)) * 112 + (w & 3) * 2) * 1024;
  const u32 so0 = (u32)((l >> 3) * 4096) + swz, so1 = so0 + 32768 - 1024;
  const char* ssrc = ws + OFF_S2 + (size_t)(w * 16) * 4096 + RG * 128;
  auto issueP = [&](int n, auto HALF) {
    constexpr int hf = HALF;
    if (n < 14) dma1s<hf * 1024>((u32)(l * 16), hsrc + n * 8192, lbase + aslot(n) + w * 2048);
    else dma1s<hf * 1024>(hf ? so1 : so0, ssrc + (size_t)(n - 14) * (128 * 4096), lbase + aslot(n) + w * 2048);
  };
  const int d = ct * 64 + dh * 32 + ((l & 31) ^ (kh << 4));
  const float gst = (float)d * (1.0f / 512.0f), ga0 = (float)(16 * kq + 2 * h) * gst;
  v8h G[8];
  auto gen = [&](auto FI) {
    constexpr int f = FI;
#pragma unroll
    for (int e2 = 0; e2 < 4; ++e2) {
      float a = fmaf((float)(64 * (f >> 1) + 8 * (f & 1) + 4 * (e2 >> 1) + (e2 & 1)), gst, ga0);
      float c = cosr(a), sn = sinr(a);
      if (f == 0 && e2 == 0) { if (kq == 0 && h == 0) { c = 0.5f; sn = (d & 1) ? 0.5f : -0.5f; } }
      G[f][2 * e2] = (_Float16)c; G[f][2 * e2 + 1] = (_Float16)sn;
    }
    PIN(G[f]);
  };

  float oscale;
  const u32 fhoff = (u32)(FL_H + (RG * 16 + (min(l, 55) >> 2)) * 64 + (l & 3) * 4);
  u32 fp;
  {
    v8h a;
    {
      LAS char* rp = smem + L_E1 + (4 * (g & 1) + (o >> 2)) * E1P + (2 * w + (g >> 1)) * 32 + (o & 3) * 8;
      a = __builtin_bit_cast(v8h, cat8(trread(rp), trread(rp + 8 * E1P)));
    }
    const v4u f8 = *(LAS v4u*)(smem + L_TAB + 1024 + l * 16);
    const v2u me = *(LAS v2u*)(smem + L_MX);
    issueW(6);
    static_for<0, 3>(readW);
    const int Ex = (int)me[0] < 16 ? 16 : ((int)me[0] > 240 ? 240 : (int)me[0]);
    const float wscale = __builtin_bit_cast(float, (u32)(246 - Ex) << 23);
    oscale = __builtin_bit_cast(float, (u32)(Ex - 7) << 23);
    re = mfma16h(__builtin_bit_cast(v8h, f8), a, z4); im = mfma16h(imf(f8), a, z4);
    static_for<3, 6>(readW);
    WAIT_VM(2);
    STAMPT(7);
    if (ffn && l == 0) st4c((u32)(FL_H + ((p * 2 + sh) * 16 + j) * 64 + fq * 4), FL, me[1]);
    issueW(7);
    if (kd != 0) {
#pragma unroll
      for (int r = 0; r < 4; ++r) {
        float fr = ((r & 1) ? bfhi(wt[r >> 1]) : bflo(wt[r >> 1])) * wscale, fi = ((r & 1) ? bfhi(wt[2 + (r >> 1)]) : bflo(wt[2 + (r >> 1)])) * wscale;
        float zr = re[r], zi = im[r];
        re[r] = zr * fr - zi * fi;
        im[r] = -(zr * fi + zi * fr);
      }
    } else {
      const int ka = 2 * w + (g >> 1), kr = o + 64 * (g & 1);
#pragma unroll
      for (int r = 0; r < 4; ++r) { v2f t = {re[r], im[r]}; *(LAS v2f*)(smem + L_ZX + ka * 1024 + (kr + 16 * r) * 8) = t; }
      BARRIER();
#pragma unroll
      for (int r = 0; r < 4; ++r) {
        PIN(pq[r]);
        const int km = (2048 - (ka + 16 * (kr + 16 * r))) & 2047;
        v2f zm = *(LAS v2f*)(smem + L_ZX + (km & 15) * 1024 + (km >> 4) * 8);
        float zr = re[r], zi = im[r];
        re[r] = (zr * pq[r][0] - zi * pq[r][1] + zm[0] * pq[r][2] + zm[1] * pq[r][3]) * wscale;
        im[r] = -(zr * pq[r][1] + zi * pq[r][0] + zm[0] * pq[r][3] - zm[1] * pq[r][2]) * wscale;
      }
    }
    LAS char* E2 = smem + (kd == 0 ? L_E1 : L_E2);
    a = pack_ri(re, im);
    re = mfma16h(a, __builtin_bit_cast(v8h, f8), z4); im = mfma16h(a, imf(f8), z4);
    twid(re, im, t3c, t3s);
    *(LAS v4h*)(E2 + (o & 7) * E2P + (2 * w + (o >> 3)) * 32 + g * 8) = pack4h(re);
    *(LAS v4h*)(E2 + (o & 7) * E2P + 512 + (2 * w + (o >> 3)) * 32 + g * 8) = pack4h(im);
    BARRIER();
    STAMPT(8);
    {
      LAS char* rp = E2 + w * E2P + o * 32 + g * 8;
      a = __builtin_bit_cast(v8h, cat8(*(LAS v4s*)rp, *(LAS v4s*)(rp + 512)));
    }
    issueW(8);
    re = mfma16h(a, __builtin_bit_cast(v8h, f16), z4); im = mfma16h(a, imf(f16), z4);
    issueW(9);
    twid(re, im, t1c, t1s);
    a = pack_ri(re, im);
    re = mfma16h(__builtin_bit_cast(v8h, f16), a, z4); im = mfma16h(imf(f16), a, z4);
    issueW(10);
    st8c((u32)(2 * kd * 4096 + w * 512 + l * 8), ws + OFF_S2, pack4h(re));
    st8c((u32)((2 * kd + 1) * 4096 + w * 512 + l * 8), ws + OFF_S2, pack4h(im));
  }
  STAMPT(9);
  gen(IC<0>{}); issueW(11);
  WAIT_VM(8);
  BARRIER();
  static_for<6, 9>(readW);
  gen(IC<1>{}); issueW(12); gen(IC<2>{}); issueW(13);
  if (!ffn) {
    WAIT_VM(0);
    BARRIER();
    if (w == 0) {
      if (l == 0) st4c((u32)(FL_S + b * 4), FL, E);
      __builtin_amdgcn_s_sleep(40);
    }
  }
  if (w == 0) ld4c(fp, fhoff, FL);
  gen(IC<3>{}); gen(IC<4>{});
  WAIT_VM(0);
  if (w == 0) {
    PIN(fp);
    u32 f = fp;
    int spins = 0;
    while (!__all(f == E) && ++spins < SPIN_CAP) {
      __builtin_amdgcn_s_sleep(4);
      f = ld4c_sync(fhoff, FL);
    }
  }
  BARRIER();
  STAMPT(10);
  if (ffn && tid == 0) st4c((u32)(FL_S + b * 4), FL, E);
  issueP(0, IC<0>{}); issueP(0, IC<1>{}); issueP(1, IC<0>{}); issueP(1, IC<1>{});
  static_for<9, 14>(readW);
  gen(IC<5>{});
  issueP(2, IC<0>{}); issueP(2, IC<1>{});
  gen(IC<6>{});
  issueP(3, IC<0>{}); issueP(3, IC<1>{});
  gen(IC<7>{});

  const u32 trS = (u32)(kq * 4096 + (4 * h + q) * 128 + ((ph ^ ((q >> 1) & 1)) << 6) + blk * 32 + pp * 8);
  const u32 hb = (u32)(ph * 8192 + kq * 2048 + l * 16);
  v16f accG[2] = {}, accS[2] = {};
  v8s fa[8], fb[8];
  v4u fs;
  float gv[8];
  const u32 xw = (u32)(w * 6144 + l * 16);
  auto xchg_out = [&](const v16f (&ac)[2], int base) {
    LAS char* X = smem + base + xw;
#pragma unroll
    for (int i = 0; i < 6; ++i) {
      const v16f& a = ac[(i + 2) >> 2]; const int r = ((i + 2) & 3) * 4;
      *(LAS v4f*)(X + i * 1024) = v4f{a[r], a[r + 1], a[r + 2], a[r + 3]};
    }
  };
  auto xchg_in = [&](const v16f (&ac)[2], int base, float (&o8)[8]) {
    v4f s0 = {ac[0][0], ac[0][1], ac[0][2], ac[0][3]}, s1 = {ac[0][4], ac[0][5], ac[0][6], ac[0][7]};
#pragma unroll
    for (int x_ = 1; x_ < 4; ++x_) {
      LAS char* X = smem + base + (w ^ (2 * x_)) * 6144 + (x_ - 1) * 2048 + l * 16;
      s0 += *(LAS v4f*)X; s1 += *(LAS v4f*)(X + 1024);
    }
#pragma unroll
    for (int e = 0; e < 4; ++e) { o8[e] = s0[e]; o8[4 + e] = s1[e]; }
  };
  v4f xg[6];
  float ov[8], ex[8];
  auto sadd = [](float a, float b) { float r; asm("v_add_f32 %0, %1, %2" : "=v"(r) : "v"(a), "v"(b)); return r; };
  auto smul = [](float a, float b) { float r; asm("v_mul_f32 %0, %1, %2" : "=v"(r) : "v"(a), "v"(b)); return r; };
  auto silu_step = [&](int i) {
    if (i < 3) {
#pragma unroll
      for (int e = 0; e < 8; ++e) { ov[e] = sadd(i == 0 ? accG[0][e] : ov[e], xg[2 * i + (e >> 2)][e & 3]); PIN(ov[e]); }
    } else if (i == 3) {
#pragma unroll
      for (int e = 0; e < 8; e += 2) { u32 t = pkbf_o(smul(ov[e], rs2), smul(ov[e + 1], rs2)); ov[e] = bflo(t); ov[e + 1] = bfhi(t); PIN(ov[e]); PIN(ov[e + 1]); }
    } else if (i == 4) {
#pragma unroll
      for (int e = 0; e < 8; ++e) { ex[e] = __builtin_amdgcn_exp2f(ov[e] * -1.44269504088896f); PIN(ex[e]); }
    } else if (i == 5) {
#pragma unroll
      for (int e = 0; e < 8; ++e) { ex[e] = __builtin_amdgcn_rcpf(1.0f + ex[e]); PIN(ex[e]); }
    } else if (i == 6) {
#pragma unroll
      for (int e = 0; e < 8; e += 2) { u32 t = pkbf_o(ov[e] * ex[e], ov[e + 1] * ex[e + 1]); gv[e] = bflo(t); gv[e + 1] = bfhi(t); PIN(gv[e]); PIN(gv[e + 1]); }
    }
  };
  static_for<0, 10>([&](auto V) {
    constexpr int v = V;
    v8s(&cur)[8] = (v & 1) ? fb : fa;
    v8s(&prv)[8] = (v & 1) ? fa : fb;
    auto mmp = [&](int s) {
      if constexpr (v >= 8) {
        accS[0] = mfma32h(G[(v - 8) * 4 + s], prv[2 * s], accS[0]); accS[1] = mfma32h(G[(v - 8) * 4 + s], prv[2 * s + 1], accS[1]);
        SCHED();
        if ((v - 8) * 4 + s >= 1) silu_step((v - 8) * 4 + s - 1);
      } else if constexpr (v >= 1) {
        accG[0] = mfma32b(Wf[2 * (v - 1) + (s >> 1)][s & 1], prv[2 * s], accG[0]);
        accG[1] = mfma32b(Wf[2 * (v - 1) + (s >> 1)][s & 1], prv[2 * s + 1], accG[1]);
      }
      SCHED();
    };
    if constexpr (v < 9) {
      WAIT_VM(v <= 7 ? 4 : 0);
      if constexpr (v == 5) { if (w == 0) { PIN(fs); poll4(fs, (u32)(FL_S + l * 16)); } }
      BARRIER();
      if constexpr (v == 0 || v == 8) STAMPT(11 + v);
      if constexpr (v == 4) { if (w == 0) ld16c(fs, (u32)(FL_S + l * 16), FL); }
      if constexpr (v == 8) {
#pragma unroll
        for (int x_ = 1; x_ < 4; ++x_) {
          LAS char* X = smem + 49152 + (w ^ (2 * x_)) * 6144 + (x_ - 1) * 2048 + l * 16;
          xg[2 * x_ - 2] = *(LAS v4f*)X; xg[2 * x_ - 1] = *(LAS v4f*)(X + 1024);
        }
        SCHED();
      }
      mmp(0);
#pragma unroll
      for (int s = 0; s < 4; ++s) {
        if constexpr (v >= 7) {
          LAS char* sb = smem + aslot(2 * v + (s >> 1)) + (s & 1) * 2048;
          cur[2 * s] = cat8(trread(sb + trS), trread(sb + trS + 1024));
          cur[2 * s + 1] = cat8(trread(sb + (trS ^ 64)), trread(sb + (trS ^ 64) + 1024));
        } else {
          LAS char* sb = smem + aslot(2 * v + (s >> 1)) + (s & 1) * 1024;
          cur[2 * s] = *(LAS v8s*)(sb + hb);
          cur[2 * s + 1] = *(LAS v8s*)(sb + (hb ^ 8192));
        }
      }
      SCHED();
      if constexpr (v <= 6) issueP(2 * v + 4, IC<0>{});
      mmp(1);
      if constexpr (v <= 6) issueP(2 * v + 4, IC<1>{});
      mmp(2);
      if constexpr (v <= 6) issueP(2 * v + 5, IC<0>{});
      mmp(3);
      if constexpr (v <= 6) issueP(2 * v + 5, IC<1>{});
    } else {
      mmp(0); mmp(1); mmp(2); mmp(3);
    }
    if constexpr (v == 7) xchg_out(accG, 49152);
    SCHED();
  });
  xchg_out(accS, 0);
  BARRIER();
  {
    float o8[8], pv[8];
    xchg_in(accS, 0, o8);
#pragma unroll
    for (int e = 0; e < 8; e += 2) { u32 t = pkbf_o(o8[e] * oscale, o8[e + 1] * oscale); pv[e] = bflo(t) * gv[e]; pv[e + 1] = bfhi(t) * gv[e + 1]; }
    u32 a0 = pkbf(pv[0], pv[1]), a1 = pkbf(pv[2], pv[3]), b0 = pkbf(pv[4], pv[5]), b1 = pkbf(pv[6], pv[7]);
    auto r0 = __builtin_amdgcn_permlane32_swap(a0, b0, false, false);
    auto r1 = __builtin_amdgcn_permlane32_swap(a1, b1, false, false);
    v4u pk = {r0[0], r1[0], r0[1], r1[1]};
    v4u ov4;
#pragma unroll
    for (int e = 0; e < 4; ++e) ov4[e] = pkbf(bflo(xe[e]) + bflo(pk[e]), bfhi(xe[e]) + bfhi(pk[e]));
    *(v4u*)((char*)out + eoff) = ov4;
  }
  asm volatile("km_pf_b:");
  STAMPT(20);
  STAMPT(21);
  STAMP_OUT(ws);
}

extern "C" void kernel_launch(void* const* d_in, const int* in_sizes, int n_in, void* d_out, int out_size, void* d_ws,
                              size_t ws_size, hipStream_t stream) {
  kmain<<<256, 512, 0, stream>>>((const u16*)d_in[0], (const u16*)d_in[1], (const u16*)d_in[2], (const u16*)d_in[4],
                                 (const u16*)d_in[5], (const u16*)d_in[6], (char*)d_ws + WS_BASE(ws_size), (u16*)d_out);
}
```

```cpp
#include <hip/hip_runtime.h>
#include <stdint.h>

typedef unsigned short u16;
typedef unsigned int u32;
typedef unsigned long long u64;
typedef short v4s __attribute__((ext_vector_type(4)));
typedef short v8s __attribute__((ext_vector_type(8)));
typedef _Float16 v4h __attribute__((ext_vector_type(4)));
typedef _Float16 v8h __attribute__((ext_vector_type(8)));
typedef __bf16 v2b __attribute__((ext_vector_type(2)));
typedef __bf16 v8b __attribute__((ext_vector_type(8)));
typedef float v2f __attribute__((ext_vector_type(2)));
typedef float v4f __attribute__((ext_vector_type(4)));
typedef float v16f __attribute__((ext_vector_type(16)));
typedef u32 v2u __attribute__((ext_vector_type(2)));
typedef u32 v4u __attribute__((ext_vector_type(4)));

#define LAS __attribute__((address_space(3)))
#define GAS __attribute__((address_space(1)))
#define DEVI __device__ __forceinline__

constexpr int D_ = 512, F_ = 1792, DF_ = 257;
constexpr size_t OFF_H = (size_t)24 << 20;
constexpr size_t OFF_S1 = (size_t)32 << 20;
constexpr size_t OFF_WT = (size_t)34 << 20;
constexpr size_t OFF_S2 = (size_t)37 << 20;
constexpr size_t OFF_M = (size_t)39 << 20;
constexpr size_t OFF_FL = OFF_M;
constexpr u32 FL_S = 0, FL_A = 1024, FL_H = 2048;
constexpr size_t OFF_BC = OFF_M + 49152;
constexpr size_t OFF_MB = OFF_M + 98304;
constexpr size_t OFF_RS2 = OFF_M + 106496;
constexpr size_t OFF_PQ = OFF_M + 65536;
constexpr size_t OFF_CSP = OFF_M + 131072;
constexpr int SPIN_CAP = 1 << 16;
#define SLEEP_A 48

#define WS_BASE(n) ((((n) - ((size_t)40 << 20))) & ~(size_t)0x1fffff)
#define STAMP 1
#define PROBE 0
#define PROBE_B 45
constexpr size_t OFF_STAMP = 0;
#define STAMP_DECL u32 stamps_[32] = {}
#define STAMPT(id) do { u64 t_; asm volatile("s_memrealtime %0\n\ts_waitcnt lgkmcnt(0)" : "=s"(t_) :: "memory"); stamps_[id] = (u32)t_; } while (0)
#define STAMPC(id) do { u64 t_; asm volatile("s_memtime %0\n\ts_waitcnt lgkmcnt(0)" : "=s"(t_) :: "memory"); stamps_[id] = (u32)t_; } while (0)
#define STAMP_OUT(wsp) do { if (blockIdx.x == PROBE_B && threadIdx.x < 64) { u32 dl_ = 500u + (PROBE == 99 ? (stamps_[31] - stamps_[30]) / 20u : PROBE == 98 ? (stamps_[2] - stamps_[1]) : (stamps_[PROBE % 32] - stamps_[0])); u32 t1_; do { u64 t_; asm volatile("s_memrealtime %0\n\ts_waitcnt lgkmcnt(0)" : "=s"(t_) :: "memory"); t1_ = (u32)t_; __builtin_amdgcn_s_sleep(1); } while (t1_ - stamps_[21] < dl_); } } while (0)

template <int I> struct IC { static constexpr int v = I; constexpr operator int() const { return I; } };
template <int B, int E, class Fn> DEVI void static_for(Fn&& f) {
  if constexpr (B < E) { f(IC<B>{}); static_for<B + 1, E>(f); }
}
#define WAIT_VM(n) asm volatile("s_waitcnt vmcnt(%0)" ::"n"(n) : "memory")
#define BARRIER() asm volatile("s_waitcnt lgkmcnt(0)\n\ts_barrier" ::: "memory")
#define SCHED() __builtin_amdgcn_sched_barrier(0)
#define PIN(x) asm volatile("" : "+v"(x))

DEVI int s_of_pi(int pi) {
  int RG = pi >> 6, i = pi & 63;
  return 128 * (4 * (RG & 3) + (i & 3)) + 8 * (i >> 2) + (RG >> 2);
}
DEVI void dma2(u32 voff0, u32 voff1, const char* sbase, u32 lds) {
  asm volatile("s_mov_b32 m0, %3\n\ts_nop 0\n\tglobal_load_lds_dwordx4 %0, %2\n\tglobal_load_lds_dwordx4 %1, %2 offset:1024" ::"v"(voff0), "v"(voff1), "s"(sbase), "s"(lds) : "memory");
}
DEVI void dma2c(u32 voff0, u32 voff1, const char* sbase, u32 lds) {
  asm volatile("s_mov_b32 m0, %3\n\ts_nop 0\n\tglobal_load_lds_dwordx4 %0, %2 sc1\n\tglobal_load_lds_dwordx4 %1, %2 offset:1024 sc1" ::"v"(voff0), "v"(voff1), "s"(sbase), "s"(lds) : "memory");
}
template <int OFF> DEVI void dma1(u32 voff, const char* sbase, u32 lds) {
  asm volatile("s_mov_b32 m0, %2\n\ts_nop 0\n\tglobal_load_lds_dwordx4 %0, %1 offset:%3" ::"v"(voff), "s"(sbase), "s"(lds), "n"(OFF) : "memory");
}
template <int OFF> DEVI void dma1s(u32 voff, const char* sbase, u32 lds) {
  asm volatile("s_mov_b32 m0, %2\n\ts_nop 0\n\tglobal_load_lds_dwordx4 %0, %1 offset:%3 sc1" ::"v"(voff), "s"(sbase), "s"(lds), "n"(OFF) : "memory");
}
DEVI void dma1c(u32 voff, const char* sbase, u32 lds) {
  asm volatile("s_mov_b32 m0, %2\n\ts_nop 0\n\tglobal_load_lds_dword %0, %1 sc1" ::"v"(voff), "s"(sbase), "s"(lds) : "memory");
}
DEVI v2u ld8c_sync(u32 voff, const char* sb) { v2u d; asm volatile("global_load_dwordx2 %0, %1, %2 sc1\n\ts_waitcnt vmcnt(0)" : "=&v"(d) : "v"(voff), "s"(sb) : "memory"); return d; }
DEVI u32 ld4c_sync(u32 voff, const char* sb) { u32 d; asm volatile("global_load_dword %0, %1, %2 sc1\n\ts_waitcnt vmcnt(0)" : "=&v"(d) : "v"(voff), "s"(sb) : "memory"); return d; }
DEVI v4u ld16c_sync(u32 voff, const char* sb) { v4u d; asm volatile("global_load_dwordx4 %0, %1, %2 sc1\n\ts_waitcnt vmcnt(0)" : "=&v"(d) : "v"(voff), "s"(sb) : "memory"); return d; }
template <class T> DEVI void ld16c(T& d, u32 voff, const char* sb) { asm volatile("global_load_dwordx4 %0, %1, %2 sc1" : "=&v"(d) : "v"(voff), "s"(sb) : "memory"); }
template <class T> DEVI void ld8c(T& d, u32 voff, const char* sb) { asm volatile("global_load_dwordx2 %0, %1, %2 sc1" : "=&v"(d) : "v"(voff), "s"(sb) : "memory"); }
template <class T> DEVI void ld4c(T& d, u32 voff, const char* sb) { asm volatile("global_load_dword %0, %1, %2 sc1" : "=&v"(d) : "v"(voff), "s"(sb) : "memory"); }
template <class T> DEVI void ld16(T& d, u32 voff, const char* sb) { asm volatile("global_load_dwordx4 %0, %1, %2" : "=&v"(d) : "v"(voff), "s"(sb) : "memory"); }
DEVI void ld2(u32& d, u32 voff, const char* sb) { asm volatile("global_load_ushort %0, %1, %2" : "=&v"(d) : "v"(voff), "s"(sb) : "memory"); }
template <class T> DEVI void st16c(u32 voff, const char* sb, T v) { asm volatile("global_store_dwordx4 %0, %1, %2 sc1\n\ts_nop 1" ::"v"(voff), "v"(v), "s"(sb) : "memory"); }
template <class T> DEVI void st8c(u32 voff, const char* sb, T v) { asm volatile("global_store_dwordx2 %0, %1, %2 sc1" ::"v"(voff), "v"(v), "s"(sb) : "memory"); }
template <class T> DEVI void st4c(u32 voff, const char* sb, T v) { asm volatile("global_store_dword %0, %1, %2 sc1" ::"v"(voff), "v"(v), "s"(sb) : "memory"); }

DEVI v4s trread(LAS char* p) { return __builtin_amdgcn_ds_read_tr16_b64_v4i16((LAS v4s*)p); }
DEVI v8s cat8(v4s a, v4s b) { return __builtin_shufflevector(a, b, 0, 1, 2, 3, 4, 5, 6, 7); }
DEVI float bflo(u32 v) { return __builtin_bit_cast(float, v << 16); }
DEVI float bfhi(u32 v) { return __builtin_bit_cast(float, v & 0xffff0000u); }
DEVI float relu(float v) { return __builtin_bit_cast(float, max(__builtin_bit_cast(int, v), 0)); }
DEVI float rbf(float v) { return (float)(__bf16)v; }
DEVI u32 pkbf(float a, float b) { v2b p = {(__bf16)a, (__bf16)b}; return __builtin_bit_cast(u32, p); }
DEVI u32 pkbf_o(float a, float b) { u32 r = pkbf(a, b); asm("" : "+v"(r)); return r; }
DEVI float cosr(float rev) { return __builtin_amdgcn_cosf(rev); }
DEVI float sinr(float rev) { return __builtin_amdgcn_sinf(rev); }
DEVI v4f mfma16h(v8h a, v8h b, v4f c) { return __builtin_amdgcn_mfma_f32_16x16x32_f16(a, b, c, 0, 0, 0); }
DEVI v4f mfma16b(v8s a, v4u b, v4f c) { return __builtin_amdgcn_mfma_f32_16x16x32_bf16(__builtin_bit_cast(v8b, a), __builtin_bit_cast(v8b, b), c, 0, 0, 0); }
DEVI v16f mfma32b(v8s a, v8s b, v16f c) { return __builtin_amdgcn_mfma_f32_32x32x16_bf16(__builtin_bit_cast(v8b, a), __builtin_bit_cast(v8b, b), c, 0, 0, 0); }
DEVI v16f mfma32h(v8h a, v8s b, v16f c) { return __builtin_amdgcn_mfma_f32_32x32x16_f16(a, __builtin_bit_cast(v8h, b), c, 0, 0, 0); }
DEVI v8h pack_ri(v4f re, v4f im) {
  v8h r;
  r[0] = (_Float16)re[0]; r[1] = (_Float16)re[1]; r[2] = (_Float16)re[2]; r[3] = (_Float16)re[3];
  r[4] = (_Float16)im[0]; r[5] = (_Float16)im[1]; r[6] = (_Float16)im[2]; r[7] = (_Float16)im[3];
  return r;
}
DEVI v4h pack4h(v4f a) { v4h r; r[0] = (_Float16)a[0]; r[1] = (_Float16)a[1]; r[2] = (_Float16)a[2]; r[3] = (_Float16)a[3]; return r; }
DEVI void twid(v4f& re, v4f& im, const v4f& c, const v4f& s) {
#pragma unroll
  for (int r = 0; r < 4; ++r) { float a = re[r], b = im[r]; re[r] = a * c[r] + b * s[r]; im[r] = b * c[r] - a * s[r]; }
}
template <int CTRL> DEVI float dppadd(float v) {
  return v + __builtin_bit_cast(float, __builtin_amdgcn_update_dpp(0, __builtin_bit_cast(int, v), CTRL, 0xf, 0xf, true));
}
DEVI float wave_sum(float v) {
  v = dppadd<0xB1>(v); v = dppadd<0x4E>(v); v = dppadd<0x141>(v); v = dppadd<0x140>(v);
  int bb = __builtin_bit_cast(int, v);
  float a0 = __builtin_bit_cast(float, __builtin_amdgcn_readlane(bb, 0)), a1 = __builtin_bit_cast(float, __builtin_amdgcn_readlane(bb, 16));
  float a2 = __builtin_bit_cast(float, __builtin_amdgcn_readlane(bb, 32)), a3 = __builtin_bit_cast(float, __builtin_amdgcn_readlane(bb, 48));
  return (a0 + a1) + (a2 + a3);
}
DEVI float dot2ones(u32 v, float acc) {
  const v2b ones = {(__bf16)1.0f, (__bf16)1.0f};
  return __builtin_amdgcn_fdot2_f32_bf16(__builtin_bit_cast(v2b, v), ones, acc, false);
}
template <int CTRL> DEVI u32 dppmax(u32 v) { return max(v, (u32)__builtin_amdgcn_update_dpp(0, (int)v, CTRL, 0xf, 0xf, true)); }
DEVI u32 wave_max(u32 v) {
  v = dppmax<0xB1>(v); v = dppmax<0x4E>(v); v = dppmax<0x141>(v); v = dppmax<0x140>(v);
  u32 a0 = __builtin_amdgcn_readlane(v, 0), a1 = __builtin_amdgcn_readlane(v, 16), a2 = __builtin_amdgcn_readlane(v, 32), a3 = __builtin_amdgcn_readlane(v, 48);
  return max(max(a0, a1), max(a2, a3));
}

struct Frag1 { v8s x0, x1, w1, w2; };

constexpr int L_SCR = 147456;
constexpr int L_CS = L_SCR, L_RED = L_SCR + 1024, L_MX = L_SCR + 1088, L_BC0 = L_SCR + 2048, L_BC1 = L_SCR + 2304;
constexpr int L_TAB = L_SCR + 3072;
constexpr int L_T = L_SCR + 7168, L_FA = L_SCR, L_E1 = L_T, L_E2 = 81920, L_ZX = 81920;
constexpr int L_C1 = 98304, L_IMG = L_C1 + 9216;
constexpr int L_FR = 131072;
constexpr int E1P = 544, E2P = 1024 + 72;
DEVI constexpr int wslot(int n) {
  constexpr int u[14] = {6, 7, 8, 0, 1, 2, 3, 4, 6, 7, 8, 0, 1, 2};
  return u[n] * 16384;
}
DEVI constexpr int aslot(int n) {
  constexpr int u[18] = {3, 4, 5, 9, 2, 6, 7, 8, 0, 1, 3, 4, 5, 9, 2, 6, 7, 8};
  return u[n] * 16384;
}

__global__ __launch_bounds__(512, 2) void kmain(const u16* __restrict__ x, const u16* __restrict__ wr, const u16* __restrict__ wi,
                                                const u16* __restrict__ Wg, const u16* __restrict__ Wu, const u16* __restrict__ Wd,
                                                char* __restrict__ ws, u16* __restrict__ out) {
  __shared__ __attribute__((aligned(16))) char smem_[163840];
  LAS char* smem = (LAS char*)smem_;
  const u32 lbase = (u32)(size_t)smem;
  const int tid = threadIdx.x, l = tid & 63;
  const int w = __builtin_amdgcn_readfirstlane(tid >> 6);
  const int b = blockIdx.x, xcd = b & 7, idx = b >> 3;
  const bool ffn = idx < 28;
  const int g = l >> 4, o = l & 15;
  const int h = l >> 5, blk = (l >> 4) & 1, q = (l & 15) >> 2, pp = l & 3;
  const char* FL = ws + OFF_FL;
  const char* BC = ws + OFF_BC;
  STAMP_DECL; STAMPT(0);

  const int pi = b * 8 + w;
  v4u xr;
  {
    u64 pc; u32 len;
    asm volatile("s_getpc_b64 %0\nkm_pf_a:\n\ts_mov_b32 %1, km_pf_b-km_pf_a" : "=s"(pc), "=s"(len));
    dma1<0>(min((u32)(((idx & 3) * 8 + w) * 1024 + l * 16), len) & ~15u, (const char*)(pc & ~15ull), lbase + L_IMG + w * 1024);
  }
  ld16(xr, (u32)(s_of_pi(pi) * 1024 + l * 16), (const char*)x);
  if (w == 0) dma1c((u32)((l & 1) * 4), BC, lbase + L_BC0);
  const int fkb = (b & 7) * 2 + (w >> 2), fcq = w & 3;
  {
    const u32 ks = (u32)((idx >> 1) + 16 * fkb + 256 * (4 * (idx & 1) + ((l >> 3) & 3)));
    const u32 base = ((ks * (DF_ * 2)) & ~15u) + fcq * 128;
    const u32 dlt = (u32)((const char*)wi - (const char*)wr);
    (void)dlt;
    const u32 c0 = ((l & 7) ^ (((l >> 4) & 1) << 2)) << 4;
    if (l < 32) dma1<0>(base + c0, (const char*)wr, lbase + L_FR + w * 2048);
    else dma1<0>(base + c0, (const char*)wi, lbase + L_FR + w * 2048);
    if (fcq == 3) {
      const u32 t0 = min(base + 128 + (c0 & 16), (u32)(2048 * DF_ * 2 - 16));
      if (l < 32) dma1<0>(t0, (const char*)wr, lbase + L_FR + w * 2048 + 1024);
      else dma1<0>(t0, (const char*)wi, lbase + L_FR + w * 2048 + 1024);
    }
  }
  const int p = (xcd >> 1) * 4 + idx / 7, j = (xcd & 1) * 7 + idx % 7;
  const int sh = w >> 2, fq = w & 3;
  u32 aoff[2];
#pragma unroll
  for (int i = 0; i < 2; ++i) {
    int row = (2 * w + i) * 8 + (l >> 3);
    aoff[i] = s_of_pi((p & 15) * 128 + row) * 1024 + (((l & 7) ^ ((row >> 1) & 7)) << 4) - i * 1024;
  }
  const u32 boff = (u32)(2 * w * 4 + (l >> 4)) * (F_ * 2) + j * 256 + (((l & 15) ^ ((l >> 4) << 2)) << 4);
  const u32 boff1 = boff + 4 * F_ * 2 - 1024;
  const int rot = p >> 1;
  auto issue = [&](int t, int slot) {
    const int kt = (t + rot) & 7;
    u32 dst = lbase + slot * 49152 + w * 2048;
    dma2(aoff[0], aoff[1], (const char*)x + kt * 128, dst);
    dma2(boff, boff1, (const char*)Wg + (size_t)kt * (64 * F_ * 2), dst + 16384);
    dma2(boff, boff1, (const char*)Wu + (size_t)kt * (64 * F_ * 2), dst + 32768);
  };
  const int sp = xcd * 4 + (idx - 28);
  if (ffn) issue(0, 0);

  u32 E = 0, tick = 0;
  const bool coord = (b == 224) && (w == 0);
  auto do_tick = [&]() { if (coord) { ++tick; if (l == 0) st8c(0u, BC, v2u{E, tick}); } };
  auto get_epoch = [&]() {
    const v2u b0 = *(LAS v2u*)(smem + L_BC0);
    v2u b1 = *(LAS v2u*)(smem + L_BC1);
    int spins = 0;
    while (__builtin_amdgcn_readfirstlane((b0[0] ^ b1[0]) | (b0[1] ^ b1[1])) == 0 && ++spins < SPIN_CAP) {
      __builtin_amdgcn_s_sleep(4);
      b1 = ld8c_sync(0u, BC);
    }
    E = __builtin_amdgcn_readfirstlane(b1[0]);
  };
  auto signal_a = [&]() {
    v4u r0 = *(LAS v4u*)(smem + L_RED), r1 = *(LAS v4u*)(smem + L_RED + 16);
    u32 m = max(max(max(r0[0], r0[1]), max(r0[2], r0[3])), max(max(r1[0], r1[1]), max(r1[2], r1[3])));
    if (l == 0) { st4c((u32)(FL_A + b * 4), FL, E | (m >> 7)); *(LAS u32*)(smem + L_MX + 4) = E; }
  };
  auto poll4 = [&](v4u f, u32 voff) -> v4u {
    int spins = 0;
    while (!__all((((f[0] ^ E) | (f[1] ^ E) | (f[2] ^ E) | (f[3] ^ E)) >> 8) == 0) && ++spins < SPIN_CAP) {
      do_tick();
      __builtin_amdgcn_s_sleep(12);
      f = ld16c_sync(voff, FL);
    }
    return f;
  };
  auto check_a = [&](v4u f) {
    f = poll4(f, (u32)(FL_A + l * 16));
    u32 m = wave_max(max(max(f[0], f[1]), max(f[2], f[3]))) & 0xffu;
    if (l == 0) *(LAS u32*)(smem + L_MX) = m;
  };
  if (coord) {
#pragma unroll
    for (int i = 0; i < 5; ++i) dma2c(i ? (u32)(FL_H + ((i - 1) * 128 + l) * 64) : (u32)(l * 16), i ? (u32)(FL_H + ((i - 1) * 128 + 64 + l) * 64 - 1024) : (u32)(l * 16), FL, lbase + i * 2048);
  }
  u32 sq[8];
  if (!ffn && w == 7) {
    const int kss = (sp >> 1) + 16 * (l >> 2) + 256 * (4 * (sp & 1) + (l & 3)), km = (2048 - kss) & 2047;
    const u32 o1 = (u32)(kss * DF_ * 2), o2 = (u32)(km * DF_ * 2);
    ld2(sq[0], o1, (const char*)wr); ld2(sq[1], o1, (const char*)wi); ld2(sq[2], o2, (const char*)wr); ld2(sq[3], o2, (const char*)wi);
    ld2(sq[4], o1 + 512, (const char*)wr); ld2(sq[5], o1 + 512, (const char*)wi); ld2(sq[6], o2 + 512, (const char*)wr); ld2(sq[7], o2 + 512, (const char*)wi);
  }

  {
    const float fo = (float)o, fg = (float)(4 * g);
    if (w < 2) {
      const float st = fo * (1.0f / 32.0f), a0 = fmaf(fg, st, w ? 0.25f : 0.f);
      v4u hi, lo;
#pragma unroll
      for (int e2 = 0; e2 < 4; ++e2) {
        float c0 = cosr(fmaf((float)(16 * (e2 >> 1) + 2 * (e2 & 1)), st, a0)), c1 = cosr(fmaf((float)(16 * (e2 >> 1) + 2 * (e2 & 1) + 1), st, a0));
        u32 ch = pkbf_o(c0, c1);
        hi[e2] = ch; lo[e2] = pkbf(c0 - bflo(ch), c1 - bfhi(ch));
      }
      *(LAS v4u*)(smem + L_C1 + w * 2048 + l * 16) = hi; *(LAS v4u*)(smem + L_C1 + w * 2048 + 1024 + l * 16) = lo;
    } else if (w < 5) {
      const float st = w == 2 ? (float)(o & 7) * (1.0f / 16.0f) : fo * (w == 3 ? 1.0f / 16.0f : 1.0f / 8.0f);
      const float a0 = (w == 2 && o >= 8) ? 0.25f : 0.f;
      const float sc = w == 2 ? 0.0625f : ((w == 4 && (g >> 1) != (o >> 3)) ? 0.f : 1.0f);
      v8h m;
#pragma unroll
      for (int e = 0; e < 4; ++e) { float a = fmaf(fg + (float)e, st, a0); m[e] = (_Float16)(sc * cosr(a)); m[4 + e] = (_Float16)(sc * sinr(a)); }
      *(LAS v8h*)(smem + (w == 2 ? L_C1 + 4096 : L_TAB + (w - 3) * 1024) + l * 16) = m;
    } else {
      const float st = w == 7 ? fo * (1.0f / 256.0f) : (fo + (w == 6 ? 16.f : 0.f)) * (1.0f / 512.0f);
      v4f c, sn;
#pragma unroll
      for (int e = 0; e < 4; ++e) { float a = (fg + (float)e) * st; c[e] = cosr(a); sn[e] = sinr(a); }
      LAS char* dst = smem + (w == 7 ? L_TAB + 2048 : L_C1 + 5120 + (w - 5) * 2048) + l * 16;
      *(LAS v4f*)dst = c; *(LAS v4f*)(dst + 1024) = sn;
    }
    BARRIER();
    LAS char* C1 = smem + L_C1 + l * 16;
    const v4u Fhr = *(LAS v4u*)C1, Flr = *(LAS v4u*)(C1 + 1024), Fhi = *(LAS v4u*)(C1 + 2048), Fli = *(LAS v4u*)(C1 + 3072);
    const v8h M2 = *(LAS v8h*)(C1 + 4096);
    v4f twc[2], tws[2];
#pragma unroll
    for (int u = 0; u < 2; ++u) { twc[u] = *(LAS v4f*)(C1 + 5120 + u * 2048); tws[u] = *(LAS v4f*)(C1 + 6144 + u * 2048); }
    if (ffn) WAIT_VM(8); else if (w == 7) WAIT_VM(10); else WAIT_VM(2);
    if (coord) {
      WAIT_VM(0);
      v4u f[10];
#pragma unroll
      for (int i = 0; i < 10; ++i) f[i] = *(LAS v4u*)(smem + i * 1024 + l * 16);
      u32 c = __builtin_amdgcn_readfirstlane(*(LAS u32*)(smem + L_BC0)) >> 8;
      bool hit;
      do {
        c = (c + 1) & 0xffffffu; hit = false;
#pragma unroll
        for (int i = 0; i < 10; ++i) hit |= ((f[i][0] >> 8) == c) | ((f[i][1] >> 8) == c) | ((f[i][2] >> 8) == c) | ((f[i][3] >> 8) == c);
      } while (__any(hit));
      E = c << 8;
      do_tick();
    }
    PIN(xr);
    STAMPT(3);
    const float c0 = bflo(__builtin_amdgcn_readfirstlane(xr[0]));
    v2f xv[4], s2 = {0.f, 0.f}, q2 = {0.f, 0.f};
#pragma unroll
    for (int e = 0; e < 4; ++e) {
      xv[e] = v2f{bflo(xr[e]), bfhi(xr[e])};
      v2f t = xv[e] - v2f{c0, c0};
      s2 += t; q2 = __builtin_elementwise_fma(t, t, q2);
    }
    const float dmean = wave_sum(s2[0] + s2[1]) * (1.0f / 512.0f), msq = wave_sum(q2[0] + q2[1]) * (1.0f / 512.0f);
    do_tick();
    const float mb = rbf(c0 + dmean);
    const float var = rbf(fmaxf(msq - dmean * dmean, 0.f));
    const float sd = rbf(__builtin_amdgcn_sqrtf(rbf(var + 1e-5f)));
    const float rs = __builtin_amdgcn_rcpf(sd);
#pragma unroll
    for (int e = 0; e < 4; ++e) {
      v2f t = xv[e] - v2f{mb, mb};
      u32 tb = pkbf_o(t[0], t[1]);
      t = v2f{bflo(tb), bfhi(tb)} * v2f{rs, rs};
      xr[e] = pkbf(t[0], t[1]);
    }
    *(LAS v4u*)(smem + L_IMG + w * 1024 + l * 16) = xr;
    if (l < 2) st4c((u32)(pi * 4 + l * 8192), ws + OFF_MB, l ? rs * rs : -mb);
    const v8s xf = cat8(trread(smem + L_IMG + w * 1024 + l * 8), trread(smem + L_IMG + w * 1024 + 512 + l * 8));
    const int kt1 = (1 + rot) & 7;
    const u32 dst1 = lbase + 49152 + w * 2048;
    if (ffn) dma2(aoff[0], aoff[1], (const char*)x + kt1 * 128, dst1);
    const v8s xg_ = __builtin_bit_cast(v8s, __builtin_bit_cast(v4u, xf) ^ v4u{0x80000000u, 0x80000000u, 0x80000000u, 0x80000000u});
    const v4f z = {0.f, 0.f, 0.f, 0.f};
    v4f dre[2], dim[2];
#pragma unroll
    for (int u = 0; u < 2; ++u) { dre[u] = mfma16b(u ? xg_ : xf, Flr, z); dim[u] = mfma16b(u ? xg_ : xf, Fli, z); }
#pragma unroll
    for (int u = 0; u < 2; ++u) { dre[u] = mfma16b(u ? xg_ : xf, Fhr, dre[u]); dim[u] = mfma16b(u ? xg_ : xf, Fhi, dim[u]); }
#pragma unroll
    for (int u = 0; u < 2; ++u) {
      twid(dre[u], dim[u], twc[u], tws[u]);
      const v8h bb = pack_ri(dre[u], dim[u]);
      v4f d2 = mfma16h(bb, M2, z);
      if (u == 0) {
        const u32 nv = o == 8 ? 0xac002c00u : 0u;
        v4f n = mfma16h(bb, __builtin_bit_cast(v8h, v4u{nv, nv, 0u, 0u}), z);
        if (l == 8) d2[0] = n[0];
      }
      *(LAS v4h*)(smem + L_T + ((o >> 3) * 8 + w) * 544 + (o & 7) * 64 + u * 32 + g * 8) = pack4h(d2);
    }
    if (ffn) {
      dma2(boff, boff1, (const char*)Wg + (size_t)kt1 * (64 * F_ * 2), dst1 + 16384);
      dma2(boff, boff1, (const char*)Wu + (size_t)kt1 * (64 * F_ * 2), dst1 + 32768);
    }
    STAMPT(4);
  }
  auto round1_out = [&]() {
    LAS char* rp = smem + L_T + (4 * (g & 1) + (o >> 2)) * 544 + (w * 32 + (g >> 1) * 16) * 2 + (o & 3) * 8;
    const v8s su = cat8(trread(rp), trread(rp + 8 * 544));
    LAS char* fp_ = smem + L_FR + w * 2048 + q * 128 + (((2 * g + (pp >> 1)) ^ ((q >> 1) << 2)) << 4) + (pp & 1) * 8;
    const v4u fu = __builtin_bit_cast(v4u, cat8(trread(fp_), trread(fp_ + 512)));
    st16c((u32)(b * 8192 + (w * 32 + (g >> 1) * 16 + o) * 32 + (g & 1) * 16), ws + OFF_S1, su);
    st16c((u32)((idx * 16 + fkb) * 4608 + (fcq * 64 + l) * 16), ws + OFF_WT, fu);
    typedef unsigned short v2us __attribute__((ext_vector_type(2)));
    const int dl = (idx >> 1) & 7;
    u32 msk = (fcq == 0 && l < dl) ? 0u : 0x7fff7fffu;
    v2us m2 = {0, 0};
#pragma unroll
    for (int e = 0; e < 4; ++e) { u32 t = fu[e] & msk; m2 = __builtin_elementwise_max(m2, __builtin_bit_cast(v2us, t)); }
    if (fcq == 3) {
      LAS char* tp = smem + L_FR + w * 2048 + 1024 + q * 128 + (pp >> 1) * 16 + (pp & 1) * 8;
      const v4u tu = __builtin_bit_cast(v4u, cat8(trread(tp), trread(tp + 512)));
      if (l < 16) st16c((u32)((idx * 16 + fkb) * 4608 + (256 + l) * 16), ws + OFF_WT, tu);
      msk = (o <= dl) ? 0x7fff7fffu : 0u;
#pragma unroll
      for (int e = 0; e < 4; ++e) { u32 t = tu[e] & msk; m2 = __builtin_elementwise_max(m2, __builtin_bit_cast(v2us, t)); }
    }
    const u32 mx = wave_max(max((u32)m2[0], (u32)m2[1]));
    if (l == 0) *(LAS u32*)(smem + L_RED + w * 4) = mx;
    if (w == 0 && !coord) dma1c((u32)((l & 1) * 4), BC, lbase + L_BC1);
  };

  const int RG = xcd * 4 + (idx >> 3), ct = idx & 7;
  const int kd = xcd * 32 + idx;
  const u32 swz = ((l & 7) ^ (((l >> 4) & 1) << 2)) << 4;
  const u32 ro0 = (u32)((l >> 3) * 1024) + swz, ro1 = ro0 + 8192 - 1024;
  const char* bsrc = (const char*)Wd + (size_t)(w * 16) * 1024 + ct * 128;
  auto issueW = [&](int n) { dma2(ro0, ro1, bsrc + (size_t)n * 131072, lbase + wslot(n) + w * 2048); };
  auto pfw = [&](int i, u32 zone) { dma1<0>((u32)(l * 16), (const char*)Wd + 786432 + idx * 32768 + (i * 8 + w) * 1024, lbase + zone + w * 1024); };
  const int dh = w & 1, kq = w >> 1, ph = kq >> 1, kh = kq & 1;
  const int epi = RG * 64 + ph * 32 + (l & 31);
  const u32 eoff = (u32)(s_of_pi(epi) * D_ + ct * 64 + dh * 32 + kh * 16 + h * 8) * 2;
  v4u wt, s1v, xe; v4f pq[4]; float rs2;
  auto load_xe = [&]() { ld16(xe, eoff, (const char*)x); };
  auto load_s1 = [&]() { ld16c(s1v, (u32)((((4 * w + g) * 8 + (o >> 1)) * 256 + kd) * 32 + (o & 1) * 16), ws + OFF_S1); };
  auto load_wt = [&]() { ld16c(wt, (u32)((w * 64 + l) * 4608 + (kd + ((2 * w + (g >> 1)) & 7)) * 16), ws + OFF_WT); };
  auto load_rs = [&]() { ld4c(rs2, (u32)(epi * 4), ws + OFF_RS2); };
  v4f t2c, t2s, t3c, t3s;
  auto trig2 = [&]() {
    const float fw = (float)w, st2 = fw * (1.0f / 128.0f), a2 = fw * (float)(o + 64 * g) * (1.0f / 2048.0f);
#pragma unroll
    for (int e = 0; e < 4; ++e) { float b2 = fmaf((float)e, st2, a2); t2c[e] = cosr(b2); t2s[e] = sinr(b2); }
    PIN(t2c); PIN(t2s);
  };
  auto trig3 = [&]() {
    const float f7 = (float)(o & 7), st3 = f7 * (1.0f / 128.0f), a3 = f7 * (float)(2 * w + (o >> 3) + 64 * g) * (1.0f / 2048.0f);
#pragma unroll
    for (int e = 0; e < 4; ++e) { float b3 = fmaf((float)e, st3, a3); t3c[e] = cosr(b3); t3s[e] = sinr(b3); }
    PIN(t3c); PIN(t3s);
  };
  v4u f16; v4f t1c, t1s, re, im;
  const v4f z4 = {0.f, 0.f, 0.f, 0.f};
  auto imf = [](v4u f) { return __builtin_bit_cast(v8h, v4u{f[2] ^ 0x80008000u, f[3] ^ 0x80008000u, f[0], f[1]}); };
  auto segA0 = [&]() {
    f16 = *(LAS v4u*)(smem + L_TAB + l * 16);
    t1c = *(LAS v4f*)(smem + L_TAB + 2048 + l * 16); t1s = *(LAS v4f*)(smem + L_TAB + 3072 + l * 16);
  };
  auto segA1 = [&]() {
    PIN(s1v);
    const v8h a = __builtin_bit_cast(v8h, s1v);
    re = mfma16h(a, __builtin_bit_cast(v8h, f16), z4); im = mfma16h(a, imf(f16), z4);
  };
  auto segA2 = [&]() {
    twid(re, im, t1c, t1s);
    const v8h a = pack_ri(re, im);
    re = mfma16h(__builtin_bit_cast(v8h, f16), a, z4); im = mfma16h(imf(f16), a, z4);
  };
  auto segA3 = [&]() {
    twid(re, im, t2c, t2s);
    *(LAS v4h*)(smem + L_E1 + w * E1P + o * 32 + g * 8) = pack4h(re);
    *(LAS v4h*)(smem + L_E1 + (8 + w) * E1P + o * 32 + g * 8) = pack4h(im);
  };

  if (ffn) {
    v16f acc[2][2] = {};
    float mbr[2];
    const u32 wb0 = (u32)((8 * h + q) * 256 + ((fq ^ q) << 6) + blk * 32 + pp * 8);
    const u32 r1 = 16384 + sh * 16384, r2 = 32768 - sh * 16384;
    const int xrow = sh * 64 + (l & 31);
    const u32 xb0 = (u32)(xrow * 128 + ((h ^ ((xrow >> 1) & 7)) << 4));
    auto rd = [&](Frag1& f, LAS char* sb, int i) {
      LAS char* xa = sb + (xb0 ^ (i << 5));
      LAS char* wa_ = sb + wb0 + (i << 12);
      f.w1 = cat8(trread(wa_ + r1), trread(wa_ + r1 + 1024));
      f.x0 = *(LAS v8s*)xa; f.x1 = *(LAS v8s*)(xa + 4096);
      f.w2 = cat8(trread(wa_ + r2), trread(wa_ + r2 + 1024));
    };
    const u32 one = ((l & 15) == ((l >> 4) & 1)) ? 0x3f803f80u : 0u;
    const v8s sel = __builtin_bit_cast(v8s, v4u{one, one, one, one});
    v4f csa = {0.f, 0.f, 0.f, 0.f};
    Frag1 A, B;
    WAIT_VM(7);
    BARRIER();
    STAMPT(1); STAMPC(30);
    rd(A, smem, 0);
    dma2(aoff[0], aoff[1], (const char*)x + ((2 + rot) & 7) * 128, lbase + 2 * 49152 + w * 2048);
    u32 c1[4], c2[4];
    static_for<0, 8>([&](auto T) {
      constexpr int t = T, slot = t % 3, nslot = (t + 1) % 3, pslot = (t + 2) % 3;
      LAS char* sb = smem + slot * 49152;
      {
        const int kt = (t + 2 + rot) & 7;
        const char* s1 = t < 6 ? (const char*)Wg + (size_t)kt * (64 * F_ * 2) : bsrc + (size_t)((t - 6) * 3 + 1) * 131072;
        const char* s2 = t < 6 ? (const char*)Wu + (size_t)kt * (64 * F_ * 2) : bsrc + (size_t)((t - 6) * 3 + 2) * 131072;
        const u32 o2 = t < 6 ? boff : ro0, o3 = t < 6 ? boff1 : ro1;
        const u32 dst = lbase + pslot * 49152 + w * 2048;
        rd(B, sb, 1); SCHED();
        acc[0][0] = mfma32b(A.w1, A.x0, acc[0][0]); acc[0][1] = mfma32b(A.w1, A.x1, acc[0][1]);
        if constexpr (t == 0) csa = mfma16b(sel, __builtin_bit_cast(v4u, A.w1), csa);
        SCHED();
        dma1<0>(o2, s1, dst + 16384);
        if constexpr (t == 2) load_xe();
        if constexpr (t == 6) load_wt();
        acc[1][0] = mfma32b(A.w2, A.x0, acc[1][0]); acc[1][1] = mfma32b(A.w2, A.x1, acc[1][1]); SCHED();
        if constexpr (t == 0) { round1_out(); SCHED(); }
        if constexpr (t == 3) { trig2(); SCHED(); }
        if constexpr (t == 4) { trig3(); SCHED(); }
        rd(A, sb, 2); SCHED();
        acc[0][0] = mfma32b(B.w1, B.x0, acc[0][0]); SCHED();
        dma1<1024>(o3, s1, dst + 16384);
        acc[0][1] = mfma32b(B.w1, B.x1, acc[0][1]);
        if constexpr (t == 0) csa = mfma16b(sel, __builtin_bit_cast(v4u, B.w1), csa);
        acc[1][0] = mfma32b(B.w2, B.x0, acc[1][0]); acc[1][1] = mfma32b(B.w2, B.x1, acc[1][1]); SCHED();
        dma1<0>(o2, s2, dst + 32768);
        if constexpr (t == 6) {
          load_rs();
          if (kd == 0) {
#pragma unroll
            for (int r = 0; r < 4; ++r) ld16c(pq[r], (u32)(w * 4096 + r * 1024 + l * 16), ws + OFF_PQ);
          }
        }
        if constexpr (t == 7) { WAIT_VM(13); segA1(); SCHED(); }
        rd(B, sb, 3); SCHED();
        if constexpr (t == 7) {
#pragma unroll
          for (int i = 0; i < 4; ++i) { PIN(c1[i]); PIN(c2[i]); }
          PIN(mbr[0]); PIN(mbr[1]);
          const v8s cs1 = __builtin_bit_cast(v8s, v4u{c1[0], c1[1], c1[2], c1[3]}), cs2 = __builtin_bit_cast(v8s, v4u{c2[0], c2[1], c2[2], c2[3]});
          const u32 m0_ = pkbf(mbr[0], mbr[0]), m1_ = pkbf(mbr[1], mbr[1]);
          const v8s mb0 = __builtin_bit_cast(v8s, v4u{m0_, m0_, m0_, m0_}), mb1 = __builtin_bit_cast(v8s, v4u{m1_, m1_, m1_, m1_});
          acc[0][0] = mfma32b(cs1, mb0, acc[0][0]); acc[1][0] = mfma32b(cs2, mb0, acc[1][0]);
          acc[0][1] = mfma32b(cs1, mb1, acc[0][1]); acc[1][1] = mfma32b(cs2, mb1, acc[1][1]); SCHED();
          segA2(); SCHED();
        }
        acc[0][0] = mfma32b(A.w1, A.x0, acc[0][0]); acc[0][1] = mfma32b(A.w1, A.x1, acc[0][1]);
        if constexpr (t == 0) csa = mfma16b(sel, __builtin_bit_cast(v4u, A.w1), csa);
        SCHED();
        dma1<1024>(o3, s2, dst + 32768);
        acc[1][0] = mfma32b(A.w2, A.x0, acc[1][0]); acc[1][1] = mfma32b(A.w2, A.x1, acc[1][1]); SCHED();
      }
      if constexpr (t < 7) {
        const char* s0 = t < 5 ? (const char*)x + ((t + 3 + rot) & 7) * 128 : bsrc + (size_t)((t - 5) * 3) * 131072;
        const u32 o0 = t < 5 ? aoff[0] : ro0, o1 = t < 5 ? aoff[1] : ro1;
        const u32 dst = lbase + slot * 49152 + w * 2048;
        if constexpr (t == 0) csa = mfma16b(sel, __builtin_bit_cast(v4u, B.w1), csa);
        if constexpr (t >= 1 && t <= 4) pfw(t - 1, L_T + 512);
        WAIT_VM(t == 0 ? 8 : t == 1 ? 7 : t == 2 ? 9 : t <= 4 ? 8 : t == 5 ? 6 : 19);
        if constexpr (t == 5) { if (w == 0) check_a(*(LAS v4u*)(smem + L_FA + l * 16)); }
        BARRIER();
        STAMPT(22 + t);
        acc[0][0] = mfma32b(B.w1, B.x0, acc[0][0]); SCHED();
        rd(A, smem + nslot * 49152, 0);
        if constexpr (t == 6) segA0();
        SCHED();
        if constexpr (t == 0) {
          if (l < 16) {
#pragma unroll
            for (int i = 0; i < 2; ++i) {
              const float hi = rbf(csa[i]);
              st4c((u32)(((j * 16 + p) * 256 + sh * 128 + fq * 32 + i * 16 + l) * 4), ws + OFF_CSP, pkbf(hi, csa[i] - hi));
            }
          }
        }
        if constexpr (t == 4) { if (w == 0) dma1s<0>((u32)(FL_A + l * 16), FL, lbase + L_FA); }
        if constexpr (t == 5) {
          const u32 co = (u32)(((j * 16 + (p & 1) + 8 * h) * 256 + fq * 32 + (l & 31)) * 4);
#pragma unroll
          for (int i = 0; i < 4; ++i) { ld4c(c1[i], co + sh * 512 + i * 2048, ws + OFF_CSP); ld4c(c2[i], co + (1 - sh) * 512 + i * 2048, ws + OFF_CSP); }
          ld4c(mbr[0], (u32)((p * 128 + xrow) * 4), ws + OFF_MB); ld4c(mbr[1], (u32)((p * 128 + xrow + 32) * 4), ws + OFF_MB);
          load_s1();
        }
        dma1<0>(o0, s0, dst);
        acc[0][1] = mfma32b(B.w1, B.x1, acc[0][1]);
        acc[1][0] = mfma32b(B.w2, B.x0, acc[1][0]);
        acc[1][1] = mfma32b(B.w2, B.x1, acc[1][1]); SCHED();
        dma1<1024>(o1, s0, dst);
        if constexpr (t == 1) { if (w == 0) { get_epoch(); signal_a(); } }
        SCHED();
      } else {
        acc[0][0] = mfma32b(B.w1, B.x0, acc[0][0]); acc[1][0] = mfma32b(B.w2, B.x0, acc[1][0]); SCHED();
        acc[0][1] = mfma32b(B.w1, B.x1, acc[0][1]); acc[1][1] = mfma32b(B.w2, B.x1, acc[1][1]); SCHED();
      }
    });
    STAMPT(2); STAMPC(31);
    auto fin = [&](const v16f& ag, const v16f& au, int rt) {
#pragma unroll
      for (int s2i = 0; s2i < 2; ++s2i) {
        v4u ov;
#pragma unroll
        for (int e = 0; e < 4; ++e) {
          const int r = s2i * 8 + 2 * e;
          v2f gg = v2f{relu(ag[r]), relu(ag[r + 1])} * v2f{au[r], au[r + 1]};
          ov[e] = pkbf(gg[0], gg[1]);
        }
        st16c((u32)(((p * 4 + sh * 2 + rt) * 112 + (j * 8 + fq * 2 + s2i)) * 1024 + l * 16), ws + OFF_H, ov);
      }
    };
    if (sh == 0) { fin(acc[0][0], acc[1][0], 0); fin(acc[0][1], acc[1][1], 1); }
    else { fin(acc[1][0], acc[0][0], 0); fin(acc[1][1], acc[0][1], 1); }
    STAMPT(6);
    segA3();
    WAIT_VM(4);
  } else {
    WAIT_VM(1);
    BARRIER();
    round1_out();
    do_tick();
    if (w == 7) {
#pragma unroll
      for (int i = 0; i < 8; ++i) PIN(sq[i]);
      float war = bflo(sq[0]) + bflo(sq[2]), wai = bflo(sq[1]) - bflo(sq[3]);
      float wbr = bflo(sq[4]) + bflo(sq[6]), wbi = bflo(sq[5]) - bflo(sq[7]);
      v4f pv = {0.25f * (war + wbr), 0.25f * (wai + wbi), 0.25f * (war - wbr), 0.25f * (wai - wbi)};
      st16c((u32)((((sp >> 2) * 4 + (l & 3)) * 64 + (sp & 3) * 16 + (l >> 2)) * 16), ws + OFF_PQ, pv);
    }
    WAIT_VM(0);
    BARRIER();
    STAMPT(1); STAMPC(30);
    if (w == 0) { if (!coord) get_epoch(); signal_a(); do_tick(); WAIT_VM(0); }
    BARRIER();
    STAMPT(24);
    dma1c((u32)(sp * 57344 + (w * 112 + l) * 64), (const char*)Wd, lbase + 49152);
    dma1c((u32)(sp * 57344 + (w * 112 + 48 + l) * 64), (const char*)Wd, lbase + 49152);
#pragma unroll
    for (int i = 0; i < 4; ++i) pfw(i, 49152);
    static_for<0, 6>([&](auto N) { issueW(N); });
    load_xe();
    trig2(); trig3(); segA0();
    STAMPT(25);
    if (w == 0) {
      if (!coord) __builtin_amdgcn_s_sleep(SLEEP_A);
      check_a(v4u{~E, 0u, 0u, 0u});
    }
    STAMPT(26);
    BARRIER();
    load_s1(); load_wt(); load_rs();
    WAIT_VM(0);
    segA1(); segA2(); segA3();
    STAMPT(2); STAMPC(31);
  }
  PIN(wt); PIN(xe); PIN(rs2);
  BARRIER();
  STAMPT(5);

  const u32 trG = (u32)(kq * 4096 + (4 * h + q) * 128 + ((dh ^ ((q >> 1) & 1)) << 6) + (blk ^ kh) * 32 + pp * 8);
  v8s Wf[14][2];
  auto readW = [&](auto N) {
    constexpr int n = N;
    LAS char* sb = smem + wslot(n) + trG;
    Wf[n][0] = cat8(trread(sb), trread(sb + 1024)); Wf[n][1] = cat8(trread(sb + 2048), trread(sb + 3072));
  };
  const char* hsrc = ws + OFF_H + ((size_t)(RG * 2 + (w >> 2)) * 112 + (w & 3) * 2) * 1024;
  const u32 so0 = (u32)((l >> 3) * 4096) + swz, so1 = so0 + 32768 - 1024;
  const char* ssrc = ws + OFF_S2 + (size_t)(w * 16) * 4096 + RG * 128;
  auto issueP = [&](int n, auto HALF) {
    constexpr int hf = HALF;
    if (n < 14) dma1s<hf * 1024>((u32)(l * 16), hsrc + n * 8192, lbase + aslot(n) + w * 2048);
    else dma1s<hf * 1024>(hf ? so1 : so0, ssrc + (size_t)(n - 14) * (128 * 4096), lbase + aslot(n) + w * 2048);
  };
  const int d = ct * 64 + dh * 32 + ((l & 31) ^ (kh << 4));
  const float gst = (float)d * (1.0f / 512.0f), ga0 = (float)(16 * kq + 2 * h) * gst;
  v8h G[8];
  auto gen = [&](auto FI) {
    constexpr int f = FI;
#pragma unroll
    for (int e2 = 0; e2 < 4; ++e2) {
      float a = fmaf((float)(64 * (f >> 1) + 8 * (f & 1) + 4 * (e2 >> 1) + (e2 & 1)), gst, ga0);
      float c = cosr(a), sn = sinr(a);
      if (f == 0 && e2 == 0) { if (kq == 0 && h == 0) { c = 0.5f; sn = (d & 1) ? 0.5f : -0.5f; } }
      G[f][2 * e2] = (_Float16)c; G[f][2 * e2 + 1] = (_Float16)sn;
    }
    PIN(G[f]);
  };

  float oscale;
  const u32 fhoff = (u32)(FL_H + (RG * 16 + (min(l, 55) >> 2)) * 64 + (l & 3) * 4);
  u32 fp;
  {
    v8h a;
    {
      LAS char* rp = smem + L_E1 + (4 * (g & 1) + (o >> 2)) * E1P + (2 * w + (g >> 1)) * 32 + (o & 3) * 8;
      a = __builtin_bit_cast(v8h, cat8(trread(rp), trread(rp + 8 * E1P)));
    }
    const v4u f8 = *(LAS v4u*)(smem + L_TAB + 1024 + l * 16);
    const v2u me = *(LAS v2u*)(smem + L_MX);
    issueW(6);
    static_for<0, 3>(readW);
    const int Ex = (int)me[0] < 16 ? 16 : ((int)me[0] > 240 ? 240 : (int)me[0]);
    const float wscale = __builtin_bit_cast(float, (u32)(246 - Ex) << 23);
    oscale = __builtin_bit_cast(float, (u32)(Ex - 7) << 23);
    re = mfma16h(__builtin_bit_cast(v8h, f8), a, z4); im = mfma16h(imf(f8), a, z4);
    static_for<3, 6>(readW);
    WAIT_VM(2);
    STAMPT(7);
    if (ffn && l == 0) st4c((u32)(FL_H + ((p * 2 + sh) * 16 + j) * 64 + fq * 4), FL, me[1]);
    issueW(7);
    if (kd != 0) {
#pragma unroll
      for (int r = 0; r < 4; ++r) {
        float fr = ((r & 1) ? bfhi(wt[r >> 1]) : bflo(wt[r >> 1])) * wscale, fi = ((r & 1) ? bfhi(wt[2 + (r >> 1)]) : bflo(wt[2 + (r >> 1)])) * wscale;
        float zr = re[r], zi = im[r];
        re[r] = zr * fr - zi * fi;
        im[r] = -(zr * fi + zi * fr);
      }
    } else {
      const int ka = 2 * w + (g >> 1), kr = o + 64 * (g & 1);
#pragma unroll
      for (int r = 0; r < 4; ++r) { v2f t = {re[r], im[r]}; *(LAS v2f*)(smem + L_ZX + ka * 1024 + (kr + 16 * r) * 8) = t; }
      BARRIER();
#pragma unroll
      for (int r = 0; r < 4; ++r) {
        PIN(pq[r]);
        const int km = (2048 - (ka + 16 * (kr + 16 * r))) & 2047;
        v2f zm = *(LAS v2f*)(smem + L_ZX + (km & 15) * 1024 + (km >> 4) * 8);
        float zr = re[r], zi = im[r];
        re[r] = (zr * pq[r][0] - zi * pq[r][1] + zm[0] * pq[r][2] + zm[1] * pq[r][3]) * wscale;
        im[r] = -(zr * pq[r][1] + zi * pq[r][0] + zm[0] * pq[r][3] - zm[1] * pq[r][2]) * wscale;
      }
    }
    LAS char* E2 = smem + (kd == 0 ? L_E1 : L_E2);
    a = pack_ri(re, im);
    re = mfma16h(a, __builtin_bit_cast(v8h, f8), z4); im = mfma16h(a, imf(f8), z4);
    twid(re, im, t3c, t3s);
    *(LAS v4h*)(E2 + (o & 7) * E2P + (2 * w + (o >> 3)) * 32 + g * 8) = pack4h(re);
    *(LAS v4h*)(E2 + (o & 7) * E2P + 512 + (2 * w + (o >> 3)) * 32 + g * 8) = pack4h(im);
    BARRIER();
    STAMPT(8);
    {
      LAS char* rp = E2 + w * E2P + o * 32 + g * 8;
      a = __builtin_bit_cast(v8h, cat8(*(LAS v4s*)rp, *(LAS v4s*)(rp + 512)));
    }
    issueW(8);
    re = mfma16h(a, __builtin_bit_cast(v8h, f16), z4); im = mfma16h(a, imf(f16), z4);
    issueW(9);
    twid(re, im, t1c, t1s);
    a = pack_ri(re, im);
    re = mfma16h(__builtin_bit_cast(v8h, f16), a, z4); im = mfma16h(imf(f16), a, z4);
    issueW(10);
    st8c((u32)(2 * kd * 4096 + w * 512 + l * 8), ws + OFF_S2, pack4h(re));
    st8c((u32)((2 * kd + 1) * 4096 + w * 512 + l * 8), ws + OFF_S2, pack4h(im));
  }
  STAMPT(9);
  gen(IC<0>{}); issueW(11);
  WAIT_VM(8);
  BARRIER();
  static_for<6, 9>(readW);
  gen(IC<1>{}); issueW(12); gen(IC<2>{}); issueW(13);
  if (!ffn) {
    WAIT_VM(0);
    BARRIER();
    if (w == 0) {
      if (l == 0) st4c((u32)(FL_S + b * 4), FL, E);
      __builtin_amdgcn_s_sleep(40);
    }
  }
  if (w == 0) ld4c(fp, fhoff, FL);
  gen(IC<3>{}); gen(IC<4>{});
  WAIT_VM(0);
  if (w == 0) {
    PIN(fp);
    u32 f = fp;
    int spins = 0;
    while (!__all(f == E) && ++spins < SPIN_CAP) {
      __builtin_amdgcn_s_sleep(4);
      f = ld4c_sync(fhoff, FL);
    }
  }
  BARRIER();
  STAMPT(10);
  if (ffn && tid == 0) st4c((u32)(FL_S + b * 4), FL, E);
  issueP(0, IC<0>{}); issueP(0, IC<1>{}); issueP(1, IC<0>{}); issueP(1, IC<1>{});
  static_for<9, 14>(readW);
  gen(IC<5>{});
  issueP(2, IC<0>{}); issueP(2, IC<1>{});
  gen(IC<6>{});
  issueP(3, IC<0>{}); issueP(3, IC<1>{});
  gen(IC<7>{});

  const u32 trS = (u32)(kq * 4096 + (4 * h + q) * 128 + ((ph ^ ((q >> 1) & 1)) << 6) + blk * 32 + pp * 8);
  const u32 hb = (u32)(ph * 8192 + kq * 2048 + l * 16);
  v16f accG[2] = {}, accS[2] = {};
  v8s fa[8], fb[8];
  v4u fs;
  float gv[8];
  const u32 xw = (u32)(w * 6144 + l * 16);
  auto xchg_out = [&](const v16f (&ac)[2], int base) {
    LAS char* X = smem + base + xw;
#pragma unroll
    for (int i = 0; i < 6; ++i) {
      const v16f& a = ac[(i + 2) >> 2]; const int r = ((i + 2) & 3) * 4;
      *(LAS v4f*)(X + i * 1024) = v4f{a[r], a[r + 1], a[r + 2], a[r + 3]};
    }
  };
  auto xchg_in = [&](const v16f (&ac)[2], int base, float (&o8)[8]) {
    v4f s0 = {ac[0][0], ac[0][1], ac[0][2], ac[0][3]}, s1 = {ac[0][4], ac[0][5], ac[0][6], ac[0][7]};
#pragma unroll
    for (int x_ = 1; x_ < 4; ++x_) {
      LAS char* X = smem + base + (w ^ (2 * x_)) * 6144 + (x_ - 1) * 2048 + l * 16;
      s0 += *(LAS v4f*)X; s1 += *(LAS v4f*)(X + 1024);
    }
#pragma unroll
    for (int e = 0; e < 4; ++e) { o8[e] = s0[e]; o8[4 + e] = s1[e]; }
  };
  v4f xg[6];
  float ov[8], ex[8];
  auto sadd = [](float a, float b) { float r; asm("v_add_f32 %0, %1, %2" : "=v"(r) : "v"(a), "v"(b)); return r; };
  auto smul = [](float a, float b) { float r; asm("v_mul_f32 %0, %1, %2" : "=v"(r) : "v"(a), "v"(b)); return r; };
  auto silu_step = [&](int i) {
    if (i < 3) {
#pragma unroll
      for (int e = 0; e < 8; ++e) { ov[e] = sadd(i == 0 ? accG[0][e] : ov[e], xg[2 * i + (e >> 2)][e & 3]); PIN(ov[e]); }
    } else if (i == 3) {
#pragma unroll
      for (int e = 0; e < 8; e += 2) { u32 t = pkbf_o(smul(ov[e], rs2), smul(ov[e + 1], rs2)); ov[e] = bflo(t); ov[e + 1] = bfhi(t); PIN(ov[e]); PIN(ov[e + 1]); }
    } else if (i == 4) {
#pragma unroll
      for (int e = 0; e < 8; ++e) { ex[e] = __builtin_amdgcn_exp2f(ov[e] * -1.44269504088896f); PIN(ex[e]); }
    } else if (i == 5) {
#pragma unroll
      for (int e = 0; e < 8; ++e) { ex[e] = __builtin_amdgcn_rcpf(1.0f + ex[e]); PIN(ex[e]); }
    } else if (i == 6) {
#pragma unroll
      for (int e = 0; e < 8; e += 2) { u32 t = pkbf_o(ov[e] * ex[e], ov[e + 1] * ex[e + 1]); gv[e] = bflo(t); gv[e + 1] = bfhi(t); PIN(gv[e]); PIN(gv[e + 1]); }
    }
  };
  static_for<0, 10>([&](auto V) {
    constexpr int v = V;
    v8s(&cur)[8] = (v & 1) ? fb : fa;
    v8s(&prv)[8] = (v & 1) ? fa : fb;
    auto mmp = [&](int s) {
      if constexpr (v >= 8) {
        accS[0] = mfma32h(G[(v - 8) * 4 + s], prv[2 * s], accS[0]); accS[1] = mfma32h(G[(v - 8) * 4 + s], prv[2 * s + 1], accS[1]);
        SCHED();
        if ((v - 8) * 4 + s >= 1) silu_step((v - 8) * 4 + s - 1);
      } else if constexpr (v >= 1) {
        accG[0] = mfma32b(Wf[2 * (v - 1) + (s >> 1)][s & 1], prv[2 * s], accG[0]);
        accG[1] = mfma32b(Wf[2 * (v - 1) + (s >> 1)][s & 1], prv[2 * s + 1], accG[1]);
      }
      SCHED();
    };
    if constexpr (v < 9) {
      WAIT_VM(v <= 7 ? 4 : 0);
      if constexpr (v == 5) { if (w == 0) { PIN(fs); poll4(fs, (u32)(FL_S + l * 16)); } }
      BARRIER();
      if constexpr (v == 0 || v == 8) STAMPT(11 + v);
      if constexpr (v == 4) { if (w == 0) ld16c(fs, (u32)(FL_S + l * 16), FL); }
      if constexpr (v == 8) {
#pragma unroll
        for (int x_ = 1; x_ < 4; ++x_) {
          LAS char* X = smem + 49152 + (w ^ (2 * x_)) * 6144 + (x_ - 1) * 2048 + l * 16;
          xg[2 * x_ - 2] = *(LAS v4f*)X; xg[2 * x_ - 1] = *(LAS v4f*)(X + 1024);
        }
        SCHED();
      }
      mmp(0);
#pragma unroll
      for (int s = 0; s < 4; ++s) {
        if constexpr (v >= 7) {
          LAS char* sb = smem + aslot(2 * v + (s >> 1)) + (s & 1) * 2048;
          cur[2 * s] = cat8(trread(sb + trS), trread(sb + trS + 1024));
          cur[2 * s + 1] = cat8(trread(sb + (trS ^ 64)), trread(sb + (trS ^ 64) + 1024));
        } else {
          LAS char* sb = smem + aslot(2 * v + (s >> 1)) + (s & 1) * 1024;
          cur[2 * s] = *(LAS v8s*)(sb + hb);
          cur[2 * s + 1] = *(LAS v8s*)(sb + (hb ^ 8192));
        }
      }
      SCHED();
      if constexpr (v <= 6) issueP(2 * v + 4, IC<0>{});
      mmp(1);
      if constexpr (v <= 6) issueP(2 * v + 4, IC<1>{});
      mmp(2);
      if constexpr (v <= 6) issueP(2 * v + 5, IC<0>{});
      mmp(3);
      if constexpr (v <= 6) issueP(2 * v + 5, IC<1>{});
    } else {
      mmp(0); mmp(1); mmp(2); mmp(3);
    }
    if constexpr (v == 7) xchg_out(accG, 49152);
    SCHED();
  });
  xchg_out(accS, 0);
  BARRIER();
  {
    float o8[8], pv[8];
    xchg_in(accS, 0, o8);
#pragma unroll
    for (int e = 0; e < 8; e += 2) { u32 t = pkbf_o(o8[e] * oscale, o8[e + 1] * oscale); pv[e] = bflo(t) * gv[e]; pv[e + 1] = bfhi(t) * gv[e + 1]; }
    u32 a0 = pkbf(pv[0], pv[1]), a1 = pkbf(pv[2], pv[3]), b0 = pkbf(pv[4], pv[5]), b1 = pkbf(pv[6], pv[7]);
    auto r0 = __builtin_amdgcn_permlane32_swap(a0, b0, false, false);
    auto r1 = __builtin_amdgcn_permlane32_swap(a1, b1, false, false);
    v4u pk = {r0[0], r1[0], r0[1], r1[1]};
    v4u ov4;
#pragma unroll
    for (int e = 0; e < 4; ++e) ov4[e] = pkbf(bflo(xe[e]) + bflo(pk[e]), bfhi(xe[e]) + bfhi(pk[e]));
    *(v4u*)((char*)out + eoff) = ov4;
  }
  asm volatile("km_pf_b:");
  STAMPT(20);
  WAIT_VM(0);
  STAMPT(21);
  STAMP_OUT(ws);
}

extern "C" void kernel_launch(void* const* d_in, const int* in_sizes, int n_in, void* d_out, int out_size, void* d_ws,
                              size_t ws_size, hipStream_t stream) {
  kmain<<<256, 512, 0, stream>>>((const u16*)d_in[0], (const u16*)d_in[1], (const u16*)d_in[2], (const u16*)d_in[4],
                                 (const u16*)d_in[5], (const u16*)d_in[6], (char*)d_ws + WS_BASE(ws_size), (u16*)d_out);
}
```

```cpp
#include <hip/hip_runtime.h>
#include <stdint.h>

typedef unsigned short u16;
typedef unsigned int u32;
typedef unsigned long long u64;
typedef short v4s __attribute__((ext_vector_type(4)));
typedef short v8s __attribute__((ext_vector_type(8)));
typedef _Float16 v4h __attribute__((ext_vector_type(4)));
typedef _Float16 v8h __attribute__((ext_vector_type(8)));
typedef __bf16 v2b __attribute__((ext_vector_type(2)));
typedef __bf16 v8b __attribute__((ext_vector_type(8)));
typedef float v2f __attribute__((ext_vector_type(2)));
typedef float v4f __attribute__((ext_vector_type(4)));
typedef float v16f __attribute__((ext_vector_type(16)));
typedef u32 v2u __attribute__((ext_vector_type(2)));
typedef u32 v4u __attribute__((ext_vector_type(4)));

#define LAS __attribute__((address_space(3)))
#define GAS __attribute__((address_space(1)))
#define DEVI __device__ __forceinline__

constexpr int D_ = 512, F_ = 1792, DF_ = 257;
constexpr size_t OFF_H = (size_t)24 << 20;
constexpr size_t OFF_S1 = (size_t)32 << 20;
constexpr size_t OFF_WT = (size_t)34 << 20;
constexpr size_t OFF_S2 = (size_t)37 << 20;
constexpr size_t OFF_M = (size_t)39 << 20;
constexpr size_t OFF_FL = OFF_M;
constexpr u32 FL_S = 0, FL_A = 1024, FL_H = 2048;
constexpr size_t OFF_BC = OFF_M + 49152;
constexpr size_t OFF_MB = OFF_M + 98304;
constexpr size_t OFF_RS2 = OFF_M + 106496;
constexpr size_t OFF_PQ = OFF_M + 65536;
constexpr size_t OFF_CSP = OFF_M + 131072;
constexpr int SPIN_CAP = 1 << 16;
#define SLEEP_A 48

#define WS_BASE(n) ((((n) - ((size_t)40 << 20))) & ~(size_t)0x1fffff)
#define STAMP_DECL
#define STAMPT(id)
#define STAMPC(id)
#define STAMP_OUT(wsp)

template <int I> struct IC { static constexpr int v = I; constexpr operator int() const { return I; } };
template <int B, int E, class Fn> DEVI void static_for(Fn&& f) {
  if constexpr (B < E) { f(IC<B>{}); static_for<B + 1, E>(f); }
}
#define WAIT_VM(n) asm volatile("s_waitcnt vmcnt(%0)" ::"n"(n) : "memory")
#define BARRIER() asm volatile("s_waitcnt lgkmcnt(0)\n\ts_barrier" ::: "memory")
#define SCHED() __builtin_amdgcn_sched_barrier(0)
#define PIN(x) asm volatile("" : "+v"(x))

DEVI int s_of_pi(int pi) {
  int RG = pi >> 6, i = pi & 63;
  return 128 * (4 * (RG & 3) + (i & 3)) + 8 * (i >> 2) + (RG >> 2);
}
DEVI void dma2(u32 voff0, u32 voff1, const char* sbase, u32 lds) {
  asm volatile("s_mov_b32 m0, %3\n\ts_nop 0\n\tglobal_load_lds_dwordx4 %0, %2\n\tglobal_load_lds_dwordx4 %1, %2 offset:1024" ::"v"(voff0), "v"(voff1), "s"(sbase), "s"(lds) : "memory");
}
DEVI void dma2c(u32 voff0, u32 voff1, const char* sbase, u32 lds) {
  asm volatile("s_mov_b32 m0, %3\n\ts_nop 0\n\tglobal_load_lds_dwordx4 %0, %2 sc1\n\tglobal_load_lds_dwordx4 %1, %2 offset:1024 sc1" ::"v"(voff0), "v"(voff1), "s"(sbase), "s"(lds) : "memory");
}
template <int OFF> DEVI void dma1(u32 voff, const char* sbase, u32 lds) {
  asm volatile("s_mov_b32 m0, %2\n\ts_nop 0\n\tglobal_load_lds_dwordx4 %0, %1 offset:%3" ::"v"(voff), "s"(sbase), "s"(lds), "n"(OFF) : "memory");
}
template <int OFF> DEVI void dma1s(u32 voff, const char* sbase, u32 lds) {
  asm volatile("s_mov_b32 m0, %2\n\ts_nop 0\n\tglobal_load_lds_dwordx4 %0, %1 offset:%3 sc1" ::"v"(voff), "s"(sbase), "s"(lds), "n"(OFF) : "memory");
}
DEVI void dma1c(u32 voff, const char* sbase, u32 lds) {
  asm volatile("s_mov_b32 m0, %2\n\ts_nop 0\n\tglobal_load_lds_dword %0, %1 sc1" ::"v"(voff), "s"(sbase), "s"(lds) : "memory");
}
DEVI v2u ld8c_sync(u32 voff, const char* sb) { v2u d; asm volatile("global_load_dwordx2 %0, %1, %2 sc1\n\ts_waitcnt vmcnt(0)" : "=&v"(d) : "v"(voff), "s"(sb) : "memory"); return d; }
DEVI u32 ld4c_sync(u32 voff, const char* sb) { u32 d; asm volatile("global_load_dword %0, %1, %2 sc1\n\ts_waitcnt vmcnt(0)" : "=&v"(d) : "v"(voff), "s"(sb) : "memory"); return d; }
DEVI v4u ld16c_sync(u32 voff, const char* sb) { v4u d; asm volatile("global_load_dwordx4 %0, %1, %2 sc1\n\ts_waitcnt vmcnt(0)" : "=&v"(d) : "v"(voff), "s"(sb) : "memory"); return d; }
template <class T> DEVI void ld16c(T& d, u32 voff, const char* sb) { asm volatile("global_load_dwordx4 %0, %1, %2 sc1" : "=&v"(d) : "v"(voff), "s"(sb) : "memory"); }
template <class T> DEVI void ld8c(T& d, u32 voff, const char* sb) { asm volatile("global_load_dwordx2 %0, %1, %2 sc1" : "=&v"(d) : "v"(voff), "s"(sb) : "memory"); }
template <class T> DEVI void ld4c(T& d, u32 voff, const char* sb) { asm volatile("global_load_dword %0, %1, %2 sc1" : "=&v"(d) : "v"(voff), "s"(sb) : "memory"); }
template <class T> DEVI void ld16(T& d, u32 voff, const char* sb) { asm volatile("global_load_dwordx4 %0, %1, %2" : "=&v"(d) : "v"(voff), "s"(sb) : "memory"); }
DEVI void ld2(u32& d, u32 voff, const char* sb) { asm volatile("global_load_ushort %0, %1, %2" : "=&v"(d) : "v"(voff), "s"(sb) : "memory"); }
template <class T> DEVI void st16c(u32 voff, const char* sb, T v) { asm volatile("global_store_dwordx4 %0, %1, %2 sc1\n\ts_nop 1" ::"v"(voff), "v"(v), "s"(sb) : "memory"); }
template <class T> DEVI void st8c(u32 voff, const char* sb, T v) { asm volatile("global_store_dwordx2 %0, %1, %2 sc1" ::"v"(voff), "v"(v), "s"(sb) : "memory"); }
template <class T> DEVI void st4c(u32 voff, const char* sb, T v) { asm volatile("global_store_dword %0, %1, %2 sc1" ::"v"(voff), "v"(v), "s"(sb) : "memory"); }

DEVI v4s trread(LAS char* p) { return __builtin_amdgcn_ds_read_tr16_b64_v4i16((LAS v4s*)p); }
DEVI v8s cat8(v4s a, v4s b) { return __builtin_shufflevector(a, b, 0, 1, 2, 3, 4, 5, 6, 7); }
DEVI float bflo(u32 v) { return __builtin_bit_cast(float, v << 16); }
DEVI float bfhi(u32 v) { return __builtin_bit_cast(float, v & 0xffff0000u); }
DEVI float relu(float v) { return __builtin_bit_cast(float, max(__builtin_bit_cast(int, v), 0)); }
DEVI float rbf(float v) { return (float)(__bf16)v; }
DEVI u32 pkbf(float a, float b) { v2b p = {(__bf16)a, (__bf16)b}; return __builtin_bit_cast(u32, p); }
DEVI u32 pkbf_o(float a, float b) { u32 r = pkbf(a, b); asm("" : "+v"(r)); return r; }
DEVI float cosr(float rev) { return __builtin_amdgcn_cosf(rev); }
DEVI float sinr(float rev) { return __builtin_amdgcn_sinf(rev); }
DEVI v4f mfma16h(v8h a, v8h b, v4f c) { return __builtin_amdgcn_mfma_f32_16x16x32_f16(a, b, c, 0, 0, 0); }
DEVI v4f mfma16b(v8s a, v4u b, v4f c) { return __builtin_amdgcn_mfma_f32_16x16x32_bf16(__builtin_bit_cast(v8b, a), __builtin_bit_cast(v8b, b), c, 0, 0, 0); }
DEVI v16f mfma32b(v8s a, v8s b, v16f c) { return __builtin_amdgcn_mfma_f32_32x32x16_bf16(__builtin_bit_cast(v8b, a), __builtin_bit_cast(v8b, b), c, 0, 0, 0); }
DEVI v16f mfma32h(v8h a, v8s b, v16f c) { return __builtin_amdgcn_mfma_f32_32x32x16_f16(a, __builtin_bit_cast(v8h, b), c, 0, 0, 0); }
DEVI v8h pack_ri(v4f re, v4f im) {
  v8h r;
  r[0] = (_Float16)re[0]; r[1] = (_Float16)re[1]; r[2] = (_Float16)re[2]; r[3] = (_Float16)re[3];
  r[4] = (_Float16)im[0]; r[5] = (_Float16)im[1]; r[6] = (_Float16)im[2]; r[7] = (_Float16)im[3];
  return r;
}
DEVI v4h pack4h(v4f a) { v4h r; r[0] = (_Float16)a[0]; r[1] = (_Float16)a[1]; r[2] = (_Float16)a[2]; r[3] = (_Float16)a[3]; return r; }
DEVI void twid(v4f& re, v4f& im, const v4f& c, const v4f& s) {
#pragma unroll
  for (int r = 0; r < 4; ++r) { float a = re[r], b = im[r]; re[r] = a * c[r] + b * s[r]; im[r] = b * c[r] - a * s[r]; }
}
template <int CTRL> DEVI float dppadd(float v) {
  return v + __builtin_bit_cast(float, __builtin_amdgcn_update_dpp(0, __builtin_bit_cast(int, v), CTRL, 0xf, 0xf, true));
}
DEVI float wave_sum(float v) {
  v = dppadd<0xB1>(v); v = dppadd<0x4E>(v); v = dppadd<0x141>(v); v = dppadd<0x140>(v);
  int bb = __builtin_bit_cast(int, v);
  float a0 = __builtin_bit_cast(float, __builtin_amdgcn_readlane(bb, 0)), a1 = __builtin_bit_cast(float, __builtin_amdgcn_readlane(bb, 16));
  float a2 = __builtin_bit_cast(float, __builtin_amdgcn_readlane(bb, 32)), a3 = __builtin_bit_cast(float, __builtin_amdgcn_readlane(bb, 48));
  return (a0 + a1) + (a2 + a3);
}
DEVI float dot2ones(u32 v, float acc) {
  const v2b ones = {(__bf16)1.0f, (__bf16)1.0f};
  return __builtin_amdgcn_fdot2_f32_bf16(__builtin_bit_cast(v2b, v), ones, acc, false);
}
template <int CTRL> DEVI u32 dppmax(u32 v) { return max(v, (u32)__builtin_amdgcn_update_dpp(0, (int)v, CTRL, 0xf, 0xf, true)); }
DEVI u32 wave_max(u32 v) {
  v = dppmax<0xB1>(v); v = dppmax<0x4E>(v); v = dppmax<0x141>(v); v = dppmax<0x140>(v);
  u32 a0 = __builtin_amdgcn_readlane(v, 0), a1 = __builtin_amdgcn_readlane(v, 16), a2 = __builtin_amdgcn_readlane(v, 32), a3 = __builtin_amdgcn_readlane(v, 48);
  return max(max(a0, a1), max(a2, a3));
}

struct Frag1 { v8s x0, x1, w1, w2; };

constexpr int L_SCR = 147456;
constexpr int L_CS = L_SCR, L_RED = L_SCR + 1024, L_MX = L_SCR + 1088, L_BC0 = L_SCR + 2048, L_BC1 = L_SCR + 2304;
constexpr int L_TAB = L_SCR + 3072;
constexpr int L_T = L_SCR + 7168, L_FA = L_SCR, L_E1 = L_T, L_E2 = 81920, L_ZX = 81920;
constexpr int L_C1 = 98304, L_IMG = L_C1 + 9216;
constexpr int L_FR = 131072;
constexpr int E1P = 544, E2P = 1024 + 72;
DEVI constexpr int wslot(int n) {
  constexpr int u[14] = {6, 7, 8, 0, 1, 2, 3, 4, 6, 7, 8, 0, 1, 2};
  return u[n] * 16384;
}
DEVI constexpr int aslot(int n) {
  constexpr int u[18] = {3, 4, 5, 9, 2, 6, 7, 8, 0, 1, 3, 4, 5, 9, 2, 6, 7, 8};
  return u[n] * 16384;
}

__global__ __launch_bounds__(512, 2) void kmain(const u16* __restrict__ x, const u16* __restrict__ wr, const u16* __restrict__ wi,
                                                const u16* __restrict__ Wg, const u16* __restrict__ Wu, const u16* __restrict__ Wd,
                                                char* __restrict__ ws, u16* __restrict__ out) {
  __shared__ __attribute__((aligned(16))) char smem_[163840];
  LAS char* smem = (LAS char*)smem_;
  const u32 lbase = (u32)(size_t)smem;
  const int tid = threadIdx.x, l = tid & 63;
  const int w = __builtin_amdgcn_readfirstlane(tid >> 6);
  const int b = blockIdx.x, xcd = b & 7, idx = b >> 3;
  const bool ffn = idx < 28;
  const int g = l >> 4, o = l & 15;
  const int h = l >> 5, blk = (l >> 4) & 1, q = (l & 15) >> 2, pp = l & 3;
  const char* FL = ws + OFF_FL;
  const char* BC = ws + OFF_BC;
  STAMP_DECL; STAMPT(0);

  const int pi = b * 8 + w;
  v4u xr;
  {
    u64 pc; u32 len;
    asm volatile("s_getpc_b64 %0\nkm_pf_a:\n\ts_mov_b32 %1, km_pf_b-km_pf_a" : "=s"(pc), "=s"(len));
    dma1<0>(min((u32)(((idx & 3) * 8 + w) * 1024 + l * 16), len) & ~15u, (const char*)(pc & ~15ull), lbase + L_IMG + w * 1024);
  }
  ld16(xr, (u32)(s_of_pi(pi) * 1024 + l * 16), (const char*)x);
  if (w == 0) dma1c((u32)((l & 1) * 4), BC, lbase + L_BC0);
  const int fkb = (b & 7) * 2 + (w >> 2), fcq = w & 3;
  {
    const u32 ks = (u32)((idx >> 1) + 16 * fkb + 256 * (4 * (idx & 1) + ((l >> 3) & 3)));
    const u32 base = ((ks * (DF_ * 2)) & ~15u) + fcq * 128;
    const u32 dlt = (u32)((const char*)wi - (const char*)wr);
    (void)dlt;
    const u32 c0 = ((l & 7) ^ (((l >> 4) & 1) << 2)) << 4;
    if (l < 32) dma1<0>(base + c0, (const char*)wr, lbase + L_FR + w * 2048);
    else dma1<0>(base + c0, (const char*)wi, lbase + L_FR + w * 2048);
    if (fcq == 3) {
      const u32 t0 = min(base + 128 + (c0 & 16), (u32)(2048 * DF_ * 2 - 16));
      if (l < 32) dma1<0>(t0, (const char*)wr, lbase + L_FR + w * 2048 + 1024);
      else dma1<0>(t0, (const char*)wi, lbase + L_FR + w * 2048 + 1024);
    }
  }
  const int p = (xcd >> 1) * 4 + idx / 7, j = (xcd & 1) * 7 + idx % 7;
  const int sh = w >> 2, fq = w & 3;
  u32 aoff[2];
#pragma unroll
  for (int i = 0; i < 2; ++i) {
    int row = (2 * w + i) * 8 + (l >> 3);
    aoff[i] = s_of_pi((p & 15) * 128 + row) * 1024 + (((l & 7) ^ ((row >> 1) & 7)) << 4) - i * 1024;
  }
  const u32 boff = (u32)(2 * w * 4 + (l >> 4)) * (F_ * 2) + j * 256 + (((l & 15) ^ ((l >> 4) << 2)) << 4);
  const u32 boff1 = boff + 4 * F_ * 2 - 1024;
  const int rot = p >> 1;
  auto issue = [&](int t, int slot) {
    const int kt = (t + rot) & 7;
    u32 dst = lbase + slot * 49152 + w * 2048;
    dma2(aoff[0], aoff[1], (const char*)x + kt * 128, dst);
    dma2(boff, boff1, (const char*)Wg + (size_t)kt * (64 * F_ * 2), dst + 16384);
    dma2(boff, boff1, (const char*)Wu + (size_t)kt * (64 * F_ * 2), dst + 32768);
  };
  const int sp = xcd * 4 + (idx - 28);
  if (ffn) issue(0, 0);

  u32 E = 0, tick = 0;
  const bool coord = (b == 224) && (w == 0);
  auto do_tick = [&]() { if (coord) { ++tick; if (l == 0) st8c(0u, BC, v2u{E, tick}); } };
  auto get_epoch = [&]() {
    const v2u b0 = *(LAS v2u*)(smem + L_BC0);
    v2u b1 = *(LAS v2u*)(smem + L_BC1);
    int spins = 0;
    while (__builtin_amdgcn_readfirstlane((b0[0] ^ b1[0]) | (b0[1] ^ b1[1])) == 0 && ++spins < SPIN_CAP) {
      __builtin_amdgcn_s_sleep(4);
      b1 = ld8c_sync(0u, BC);
    }
    E = __builtin_amdgcn_readfirstlane(b1[0]);
  };
  auto signal_a = [&]() {
    v4u r0 = *(LAS v4u*)(smem + L_RED), r1 = *(LAS v4u*)(smem + L_RED + 16);
    u32 m = max(max(max(r0[0], r0[1]), max(r0[2], r0[3])), max(max(r1[0], r1[1]), max(r1[2], r1[3])));
    if (l == 0) { st4c((u32)(FL_A + b * 4), FL, E | (m >> 7)); *(LAS u32*)(smem + L_MX + 4) = E; }
  };
  auto poll4 = [&](v4u f, u32 voff) -> v4u {
    int spins = 0;
    while (!__all((((f[0] ^ E) | (f[1] ^ E) | (f[2] ^ E) | (f[3] ^ E)) >> 8) == 0) && ++spins < SPIN_CAP) {
      do_tick();
      __builtin_amdgcn_s_sleep(12);
      f = ld16c_sync(voff, FL);
    }
    return f;
  };
  auto check_a = [&](v4u f) {
    f = poll4(f, (u32)(FL_A + l * 16));
    u32 m = wave_max(max(max(f[0], f[1]), max(f[2], f[3]))) & 0xffu;
    if (l == 0) *(LAS u32*)(smem + L_MX) = m;
  };
  if (coord) {
#pragma unroll
    for (int i = 0; i < 5; ++i) dma2c(i ? (u32)(FL_H + ((i - 1) * 128 + l) * 64) : (u32)(l * 16), i ? (u32)(FL_H + ((i - 1) * 128 + 64 + l) * 64 - 1024) : (u32)(l * 16), FL, lbase + i * 2048);
  }
  u32 sq[8];
  if (!ffn && w == 7) {
    const int kss = (sp >> 1) + 16 * (l >> 2) + 256 * (4 * (sp & 1) + (l & 3)), km = (2048 - kss) & 2047;
    const u32 o1 = (u32)(kss * DF_ * 2), o2 = (u32)(km * DF_ * 2);
    ld2(sq[0], o1, (const char*)wr); ld2(sq[1], o1, (const char*)wi); ld2(sq[2], o2, (const char*)wr); ld2(sq[3], o2, (const char*)wi);
    ld2(sq[4], o1 + 512, (const char*)wr); ld2(sq[5], o1 + 512, (const char*)wi); ld2(sq[6], o2 + 512, (const char*)wr); ld2(sq[7], o2 + 512, (const char*)wi);
  }

  {
    const float fo = (float)o, fg = (float)(4 * g);
    if (w < 2) {
      const float st = fo * (1.0f / 32.0f), a0 = fmaf(fg, st, w ? 0.25f : 0.f);
      v4u hi, lo;
#pragma unroll
      for (int e2 = 0; e2 < 4; ++e2) {
        float c0 = cosr(fmaf((float)(16 * (e2 >> 1) + 2 * (e2 & 1)), st, a0)), c1 = cosr(fmaf((float)(16 * (e2 >> 1) + 2 * (e2 & 1) + 1), st, a0));
        u32 ch = pkbf_o(c0, c1);
        hi[e2] = ch; lo[e2] = pkbf(c0 - bflo(ch), c1 - bfhi(ch));
      }
      *(LAS v4u*)(smem + L_C1 + w * 2048 + l * 16) = hi; *(LAS v4u*)(smem + L_C1 + w * 2048 + 1024 + l * 16) = lo;
    } else if (w < 5) {
      const float st = w == 2 ? (float)(o & 7) * (1.0f / 16.0f) : fo * (w == 3 ? 1.0f / 16.0f : 1.0f / 8.0f);
      const float a0 = (w == 2 && o >= 8) ? 0.25f : 0.f;
      const float sc = w == 2 ? 0.0625f : ((w == 4 && (g >> 1) != (o >> 3)) ? 0.f : 1.0f);
      v8h m;
#pragma unroll
      for (int e = 0; e < 4; ++e) { float a = fmaf(fg + (float)e, st, a0); m[e] = (_Float16)(sc * cosr(a)); m[4 + e] = (_Float16)(sc * sinr(a)); }
      *(LAS v8h*)(smem + (w == 2 ? L_C1 + 4096 : L_TAB + (w - 3) * 1024) + l * 16) = m;
    } else {
      const float st = w == 7 ? fo * (1.0f / 256.0f) : (fo + (w == 6 ? 16.f : 0.f)) * (1.0f / 512.0f);
      v4f c, sn;
#pragma unroll
      for (int e = 0; e < 4; ++e) { float a = (fg + (float)e) * st; c[e] = cosr(a); sn[e] = sinr(a); }
      LAS char* dst = smem + (w == 7 ? L_TAB + 2048 : L_C1 + 5120 + (w - 5) * 2048) + l * 16;
      *(LAS v4f*)dst = c; *(LAS v4f*)(dst + 1024) = sn;
    }
    BARRIER();
    LAS char* C1 = smem + L_C1 + l * 16;
    const v4u Fhr = *(LAS v4u*)C1, Flr = *(LAS v4u*)(C1 + 1024), Fhi = *(LAS v4u*)(C1 + 2048), Fli = *(LAS v4u*)(C1 + 3072);
    const v8h M2 = *(LAS v8h*)(C1 + 4096);
    v4f twc[2], tws[2];
#pragma unroll
    for (int u = 0; u < 2; ++u) { twc[u] = *(LAS v4f*)(C1 + 5120 + u * 2048); tws[u] = *(LAS v4f*)(C1 + 6144 + u * 2048); }
    if (ffn) WAIT_VM(8); else if (w == 7) WAIT_VM(10); else WAIT_VM(2);
    if (coord) {
      WAIT_VM(0);
      v4u f[10];
#pragma unroll
      for (int i = 0; i < 10; ++i) f[i] = *(LAS v4u*)(smem + i * 1024 + l * 16);
      u32 c = __builtin_amdgcn_readfirstlane(*(LAS u32*)(smem + L_BC0)) >> 8;
      bool hit;
      do {
        c = (c + 1) & 0xffffffu; hit = false;
#pragma unroll
        for (int i = 0; i < 10; ++i) hit |= ((f[i][0] >> 8) == c) | ((f[i][1] >> 8) == c) | ((f[i][2] >> 8) == c) | ((f[i][3] >> 8) == c);
      } while (__any(hit));
      E = c << 8;
      do_tick();
    }
    PIN(xr);
    STAMPT(3);
    const float c0 = bflo(__builtin_amdgcn_readfirstlane(xr[0]));
    v2f xv[4], s2 = {0.f, 0.f}, q2 = {0.f, 0.f};
#pragma unroll
    for (int e = 0; e < 4; ++e) {
      xv[e] = v2f{bflo(xr[e]), bfhi(xr[e])};
      v2f t = xv[e] - v2f{c0, c0};
      s2 += t; q2 = __builtin_elementwise_fma(t, t, q2);
    }
    const float dmean = wave_sum(s2[0] + s2[1]) * (1.0f / 512.0f), msq = wave_sum(q2[0] + q2[1]) * (1.0f / 512.0f);
    do_tick();
    const float mb = rbf(c0 + dmean);
    const float var = rbf(fmaxf(msq - dmean * dmean, 0.f));
    const float sd = rbf(__builtin_amdgcn_sqrtf(rbf(var + 1e-5f)));
    const float rs = __builtin_amdgcn_rcpf(sd);
#pragma unroll
    for (int e = 0; e < 4; ++e) {
      v2f t = xv[e] - v2f{mb, mb};
      u32 tb = pkbf_o(t[0], t[1]);
      t = v2f{bflo(tb), bfhi(tb)} * v2f{rs, rs};
      xr[e] = pkbf(t[0], t[1]);
    }
    *(LAS v4u*)(smem + L_IMG + w * 1024 + l * 16) = xr;
    if (l < 2) st4c((u32)(pi * 4 + l * 8192), ws + OFF_MB, l ? rs * rs : -mb);
    const v8s xf = cat8(trread(smem + L_IMG + w * 1024 + l * 8), trread(smem + L_IMG + w * 1024 + 512 + l * 8));
    const int kt1 = (1 + rot) & 7;
    const u32 dst1 = lbase + 49152 + w * 2048;
    if (ffn) dma2(aoff[0], aoff[1], (const char*)x + kt1 * 128, dst1);
    const v8s xg_ = __builtin_bit_cast(v8s, __builtin_bit_cast(v4u, xf) ^ v4u{0x80000000u, 0x80000000u, 0x80000000u, 0x80000000u});
    const v4f z = {0.f, 0.f, 0.f, 0.f};
    v4f dre[2], dim[2];
#pragma unroll
    for (int u = 0; u < 2; ++u) { dre[u] = mfma16b(u ? xg_ : xf, Flr, z); dim[u] = mfma16b(u ? xg_ : xf, Fli, z); }
#pragma unroll
    for (int u = 0; u < 2; ++u) { dre[u] = mfma16b(u ? xg_ : xf, Fhr, dre[u]); dim[u] = mfma16b(u ? xg_ : xf, Fhi, dim[u]); }
#pragma unroll
    for (int u = 0; u < 2; ++u) {
      twid(dre[u], dim[u], twc[u], tws[u]);
      const v8h bb = pack_ri(dre[u], dim[u]);
      v4f d2 = mfma16h(bb, M2, z);
      if (u == 0) {
        const u32 nv = o == 8 ? 0xac002c00u : 0u;
        v4f n = mfma16h(bb, __builtin_bit_cast(v8h, v4u{nv, nv, 0u, 0u}), z);
        if (l == 8) d2[0] = n[0];
      }
      *(LAS v4h*)(smem + L_T + ((o >> 3) * 8 + w) * 544 + (o & 7) * 64 + u * 32 + g * 8) = pack4h(d2);
    }
    if (ffn) {
      dma2(boff, boff1, (const char*)Wg + (size_t)kt1 * (64 * F_ * 2), dst1 + 16384);
      dma2(boff, boff1, (const char*)Wu + (size_t)kt1 * (64 * F_ * 2), dst1 + 32768);
    }
    STAMPT(4);
  }
  v8s su; v4u fu, tu;
  auto round1_read = [&]() {
    LAS char* rp = smem + L_T + (4 * (g & 1) + (o >> 2)) * 544 + (w * 32 + (g >> 1) * 16) * 2 + (o & 3) * 8;
    su = cat8(trread(rp), trread(rp + 8 * 544));
    LAS char* fp_ = smem + L_FR + w * 2048 + q * 128 + (((2 * g + (pp >> 1)) ^ ((q >> 1) << 2)) << 4) + (pp & 1) * 8;
    fu = __builtin_bit_cast(v4u, cat8(trread(fp_), trread(fp_ + 512)));
    LAS char* tp = smem + L_FR + w * 2048 + 1024 + q * 128 + (pp >> 1) * 16 + (pp & 1) * 8;
    tu = __builtin_bit_cast(v4u, cat8(trread(tp), trread(tp + 512)));
  };
  auto round1_max = [&]() {
    typedef unsigned short v2us __attribute__((ext_vector_type(2)));
    const int dl = (idx >> 1) & 7;
    const u32 msk = (fcq == 0 && l < dl) ? 0u : 0x7fff7fffu, msk2 = (fcq == 3 && o <= dl) ? 0x7fff7fffu : 0u;
    v2us m2 = {0, 0};
#pragma unroll
    for (int e = 0; e < 4; ++e) {
      u32 t = fu[e] & msk, t2 = tu[e] & msk2;
      m2 = __builtin_elementwise_max(m2, __builtin_elementwise_max(__builtin_bit_cast(v2us, t), __builtin_bit_cast(v2us, t2)));
    }
    const u32 mx = wave_max(max((u32)m2[0], (u32)m2[1]));
    if (l == 0) *(LAS u32*)(smem + L_RED + w * 4) = mx;
  };
  auto round1_store = [&]() {
    st16c((u32)(b * 8192 + (w * 32 + (g >> 1) * 16 + o) * 32 + (g & 1) * 16), ws + OFF_S1, su);
    st16c((u32)((idx * 16 + fkb) * 4608 + (fcq * 64 + l) * 16), ws + OFF_WT, fu);
    if (fcq == 3 && l < 16) st16c((u32)((idx * 16 + fkb) * 4608 + (256 + l) * 16), ws + OFF_WT, tu);
    if (w == 0 && !coord) dma1c((u32)((l & 1) * 4), BC, lbase + L_BC1);
  };

  const int RG = xcd * 4 + (idx >> 3), ct = idx & 7;
  const int kd = xcd * 32 + idx;
  const u32 swz = ((l & 7) ^ (((l >> 4) & 1) << 2)) << 4;
  const u32 ro0 = (u32)((l >> 3) * 1024) + swz, ro1 = ro0 + 8192 - 1024;
  const char* bsrc = (const char*)Wd + (size_t)(w * 16) * 1024 + ct * 128;
  auto issueW = [&](int n) { dma2(ro0, ro1, bsrc + (size_t)n * 131072, lbase + wslot(n) + w * 2048); };
  auto pfw = [&](int i, u32 zone) { dma1<0>((u32)(l * 16), (const char*)Wd + 786432 + idx * 32768 + (i * 8 + w) * 1024, lbase + zone + w * 1024); };
  const int dh = w & 1, kq = w >> 1, ph = kq >> 1, kh = kq & 1;
  const int epi = RG * 64 + ph * 32 + (l & 31);
  const u32 eoff = (u32)(s_of_pi(epi) * D_ + ct * 64 + dh * 32 + kh * 16 + h * 8) * 2;
  v4u wt, s1v, xe; v4f pq[4]; float rs2;
  auto load_xe = [&]() { ld16(xe, eoff, (const char*)x); };
  auto load_s1 = [&]() { ld16c(s1v, (u32)((((4 * w + g) * 8 + (o >> 1)) * 256 + kd) * 32 + (o & 1) * 16), ws + OFF_S1); };
  auto load_wt = [&]() { ld16c(wt, (u32)((w * 64 + l) * 4608 + (kd + ((2 * w + (g >> 1)) & 7)) * 16), ws + OFF_WT); };
  auto load_rs = [&]() { ld4c(rs2, (u32)(epi * 4), ws + OFF_RS2); };
  v4f t2c, t2s, t3c, t3s;
  auto trig2 = [&]() {
    const float fw = (float)w, st2 = fw * (1.0f / 128.0f), a2 = fw * (float)(o + 64 * g) * (1.0f / 2048.0f);
#pragma unroll
    for (int e = 0; e < 4; ++e) { float b2 = fmaf((float)e, st2, a2); t2c[e] = cosr(b2); t2s[e] = sinr(b2); }
    PIN(t2c); PIN(t2s);
  };
  auto trig3 = [&]() {
    const float f7 = (float)(o & 7), st3 = f7 * (1.0f / 128.0f), a3 = f7 * (float)(2 * w + (o >> 3) + 64 * g) * (1.0f / 2048.0f);
#pragma unroll
    for (int e = 0; e < 4; ++e) { float b3 = fmaf((float)e, st3, a3); t3c[e] = cosr(b3); t3s[e] = sinr(b3); }
    PIN(t3c); PIN(t3s);
  };
  v4u f16; v4f t1c, t1s, re, im;
  const v4f z4 = {0.f, 0.f, 0.f, 0.f};
  auto imf = [](v4u f) { return __builtin_bit_cast(v8h, v4u{f[2] ^ 0x80008000u, f[3] ^ 0x80008000u, f[0], f[1]}); };
  auto segA0 = [&]() {
    f16 = *(LAS v4u*)(smem + L_TAB + l * 16);
    t1c = *(LAS v4f*)(smem + L_TAB + 2048 + l * 16); t1s = *(LAS v4f*)(smem + L_TAB + 3072 + l * 16);
  };
  auto segA1 = [&]() {
    PIN(s1v);
    const v8h a = __builtin_bit_cast(v8h, s1v);
    re = mfma16h(a, __builtin_bit_cast(v8h, f16), z4); im = mfma16h(a, imf(f16), z4);
  };
  auto segA2 = [&]() {
    twid(re, im, t1c, t1s);
    const v8h a = pack_ri(re, im);
    re = mfma16h(__builtin_bit_cast(v8h, f16), a, z4); im = mfma16h(imf(f16), a, z4);
  };
  auto segA3 = [&]() {
    twid(re, im, t2c, t2s);
    *(LAS v4h*)(smem + L_E1 + w * E1P + o * 32 + g * 8) = pack4h(re);
    *(LAS v4h*)(smem + L_E1 + (8 + w) * E1P + o * 32 + g * 8) = pack4h(im);
  };

  if (ffn) {
    v16f acc[2][2] = {};
    float mbr[2];
    const u32 wb0 = (u32)((8 * h + q) * 256 + ((fq ^ q) << 6) + blk * 32 + pp * 8);
    const u32 r1 = 16384 + sh * 16384, r2 = 32768 - sh * 16384;
    const int xrow = sh * 64 + (l & 31);
    const u32 xb0 = (u32)(xrow * 128 + ((h ^ ((xrow >> 1) & 7)) << 4));
    auto rd = [&](Frag1& f, LAS char* sb, int i) {
      LAS char* xa = sb + (xb0 ^ (i << 5));
      LAS char* wa_ = sb + wb0 + (i << 12);
      f.w1 = cat8(trread(wa_ + r1), trread(wa_ + r1 + 1024));
      f.x0 = *(LAS v8s*)xa; f.x1 = *(LAS v8s*)(xa + 4096);
      f.w2 = cat8(trread(wa_ + r2), trread(wa_ + r2 + 1024));
    };
    const u32 one = ((l & 15) == ((l >> 4) & 1)) ? 0x3f803f80u : 0u;
    const v8s sel = __builtin_bit_cast(v8s, v4u{one, one, one, one});
    v4f csa = {0.f, 0.f, 0.f, 0.f};
    Frag1 A, B;
    WAIT_VM(7);
    BARRIER();
    STAMPT(1); STAMPC(30);
    rd(A, smem, 0);
    round1_read();
    dma2(aoff[0], aoff[1], (const char*)x + ((2 + rot) & 7) * 128, lbase + 2 * 49152 + w * 2048);
    u32 c1[4], c2[4];
    static_for<0, 8>([&](auto T) {
      constexpr int t = T, slot = t % 3, nslot = (t + 1) % 3, pslot = (t + 2) % 3;
      LAS char* sb = smem + slot * 49152;
      {
        const int kt = (t + 2 + rot) & 7;
        const char* s1 = t < 6 ? (const char*)Wg + (size_t)kt * (64 * F_ * 2) : bsrc + (size_t)((t - 6) * 3 + 1) * 131072;
        const char* s2 = t < 6 ? (const char*)Wu + (size_t)kt * (64 * F_ * 2) : bsrc + (size_t)((t - 6) * 3 + 2) * 131072;
        const u32 o2 = t < 6 ? boff : ro0, o3 = t < 6 ? boff1 : ro1;
        const u32 dst = lbase + pslot * 49152 + w * 2048;
        rd(B, sb, 1); SCHED();
        acc[0][0] = mfma32b(A.w1, A.x0, acc[0][0]); acc[0][1] = mfma32b(A.w1, A.x1, acc[0][1]);
        if constexpr (t == 0) csa = mfma16b(sel, __builtin_bit_cast(v4u, A.w1), csa);
        SCHED();
        dma1<0>(o2, s1, dst + 16384);
        if constexpr (t == 2) load_xe();
        if constexpr (t == 6) load_wt();
        acc[1][0] = mfma32b(A.w2, A.x0, acc[1][0]); acc[1][1] = mfma32b(A.w2, A.x1, acc[1][1]); SCHED();
        if constexpr (t == 0) { round1_store(); round1_max(); SCHED(); }
        if constexpr (t == 3) { trig2(); SCHED(); }
        if constexpr (t == 4) { trig3(); SCHED(); }
        rd(A, sb, 2); SCHED();
        acc[0][0] = mfma32b(B.w1, B.x0, acc[0][0]); SCHED();
        dma1<1024>(o3, s1, dst + 16384);
        acc[0][1] = mfma32b(B.w1, B.x1, acc[0][1]);
        if constexpr (t == 0) csa = mfma16b(sel, __builtin_bit_cast(v4u, B.w1), csa);
        acc[1][0] = mfma32b(B.w2, B.x0, acc[1][0]); acc[1][1] = mfma32b(B.w2, B.x1, acc[1][1]); SCHED();
        dma1<0>(o2, s2, dst + 32768);
        if constexpr (t == 6) {
          load_rs();
          if (kd == 0) {
#pragma unroll
            for (int r = 0; r < 4; ++r) ld16c(pq[r], (u32)(w * 4096 + r * 1024 + l * 16), ws + OFF_PQ);
          }
        }
        if constexpr (t == 7) { WAIT_VM(13); segA1(); SCHED(); }
        rd(B, sb, 3); SCHED();
        if constexpr (t == 7) {
#pragma unroll
          for (int i = 0; i < 4; ++i) { PIN(c1[i]); PIN(c2[i]); }
          PIN(mbr[0]); PIN(mbr[1]);
          const v8s cs1 = __builtin_bit_cast(v8s, v4u{c1[0], c1[1], c1[2], c1[3]}), cs2 = __builtin_bit_cast(v8s, v4u{c2[0], c2[1], c2[2], c2[3]});
          const u32 m0_ = pkbf(mbr[0], mbr[0]), m1_ = pkbf(mbr[1], mbr[1]);
          const v8s mb0 = __builtin_bit_cast(v8s, v4u{m0_, m0_, m0_, m0_}), mb1 = __builtin_bit_cast(v8s, v4u{m1_, m1_, m1_, m1_});
          acc[0][0] = mfma32b(cs1, mb0, acc[0][0]); acc[1][0] = mfma32b(cs2, mb0, acc[1][0]);
          acc[0][1] = mfma32b(cs1, mb1, acc[0][1]); acc[1][1] = mfma32b(cs2, mb1, acc[1][1]); SCHED();
          segA2(); SCHED();
        }
        acc[0][0] = mfma32b(A.w1, A.x0, acc[0][0]); acc[0][1] = mfma32b(A.w1, A.x1, acc[0][1]);
        if constexpr (t == 0) csa = mfma16b(sel, __builtin_bit_cast(v4u, A.w1), csa);
        SCHED();
        dma1<1024>(o3, s2, dst + 32768);
        acc[1][0] = mfma32b(A.w2, A.x0, acc[1][0]); acc[1][1] = mfma32b(A.w2, A.x1, acc[1][1]); SCHED();
      }
      if constexpr (t < 7) {
        const char* s0 = t < 5 ? (const char*)x + ((t + 3 + rot) & 7) * 128 : bsrc + (size_t)((t - 5) * 3) * 131072;
        const u32 o0 = t < 5 ? aoff[0] : ro0, o1 = t < 5 ? aoff[1] : ro1;
        const u32 dst = lbase + slot * 49152 + w * 2048;
        if constexpr (t == 0) csa = mfma16b(sel, __builtin_bit_cast(v4u, B.w1), csa);
        if constexpr (t >= 1 && t <= 4) pfw(t - 1, L_T + 512);
        WAIT_VM(t == 0 ? 8 : t == 1 ? 7 : t == 2 ? 9 : t <= 4 ? 8 : t == 5 ? 6 : 19);
        if constexpr (t == 5) { if (w == 0) check_a(*(LAS v4u*)(smem + L_FA + l * 16)); }
        BARRIER();
        if constexpr (t == 0 || t == 1 || t == 5 || t == 6) STAMPT(22 + t);
        acc[0][0] = mfma32b(B.w1, B.x0, acc[0][0]); SCHED();
        rd(A, smem + nslot * 49152, 0);
        if constexpr (t == 6) segA0();
        SCHED();
        if constexpr (t == 0) {
          if (l < 16) {
#pragma unroll
            for (int i = 0; i < 2; ++i) {
              const float hi = rbf(csa[i]);
              st4c((u32)(((j * 16 + p) * 256 + sh * 128 + fq * 32 + i * 16 + l) * 4), ws + OFF_CSP, pkbf(hi, csa[i] - hi));
            }
          }
        }
        if constexpr (t == 4) { if (w == 0) dma1s<0>((u32)(FL_A + l * 16), FL, lbase + L_FA); }
        if constexpr (t == 5) {
          const u32 co = (u32)(((j * 16 + (p & 1) + 8 * h) * 256 + fq * 32 + (l & 31)) * 4);
#pragma unroll
          for (int i = 0; i < 4; ++i) { ld4c(c1[i], co + sh * 512 + i * 2048, ws + OFF_CSP); ld4c(c2[i], co + (1 - sh) * 512 + i * 2048, ws + OFF_CSP); }
          ld4c(mbr[0], (u32)((p * 128 + xrow) * 4), ws + OFF_MB); ld4c(mbr[1], (u32)((p * 128 + xrow + 32) * 4), ws + OFF_MB);
          load_s1();
        }
        dma1<0>(o0, s0, dst);
        acc[0][1] = mfma32b(B.w1, B.x1, acc[0][1]);
        acc[1][0] = mfma32b(B.w2, B.x0, acc[1][0]);
        acc[1][1] = mfma32b(B.w2, B.x1, acc[1][1]); SCHED();
        dma1<1024>(o1, s0, dst);
        if constexpr (t == 1) { if (w == 0) { get_epoch(); signal_a(); } }
        SCHED();
      } else {
        acc[0][0] = mfma32b(B.w1, B.x0, acc[0][0]); acc[1][0] = mfma32b(B.w2, B.x0, acc[1][0]); SCHED();
        acc[0][1] = mfma32b(B.w1, B.x1, acc[0][1]); acc[1][1] = mfma32b(B.w2, B.x1, acc[1][1]); SCHED();
      }
    });
    STAMPT(2); STAMPC(31);
    auto fin = [&](const v16f& ag, const v16f& au, int rt) {
#pragma unroll
      for (int s2i = 0; s2i < 2; ++s2i) {
        v4u ov;
#pragma unroll
        for (int e = 0; e < 4; ++e) {
          const int r = s2i * 8 + 2 * e;
          v2f gg = v2f{relu(ag[r]), relu(ag[r + 1])} * v2f{au[r], au[r + 1]};
          ov[e] = pkbf(gg[0], gg[1]);
        }
        st16c((u32)(((p * 4 + sh * 2 + rt) * 112 + (j * 8 + fq * 2 + s2i)) * 1024 + l * 16), ws + OFF_H, ov);
      }
    };
    if (sh == 0) { fin(acc[0][0], acc[1][0], 0); fin(acc[0][1], acc[1][1], 1); }
    else { fin(acc[1][0], acc[0][0], 0); fin(acc[1][1], acc[0][1], 1); }
    STAMPT(6);
    segA3();
    WAIT_VM(4);
  } else {
    WAIT_VM(1);
    BARRIER();
    round1_read(); round1_store(); round1_max();
    do_tick();
    if (w == 7) {
#pragma unroll
      for (int i = 0; i < 8; ++i) PIN(sq[i]);
      float war = bflo(sq[0]) + bflo(sq[2]), wai = bflo(sq[1]) - bflo(sq[3]);
      float wbr = bflo(sq[4]) + bflo(sq[6]), wbi = bflo(sq[5]) - bflo(sq[7]);
      v4f pv = {0.25f * (war + wbr), 0.25f * (wai + wbi), 0.25f * (war - wbr), 0.25f * (wai - wbi)};
      st16c((u32)((((sp >> 2) * 4 + (l & 3)) * 64 + (sp & 3) * 16 + (l >> 2)) * 16), ws + OFF_PQ, pv);
    }
    WAIT_VM(0);
    BARRIER();
    STAMPT(1); STAMPC(30);
    if (w == 0) { if (!coord) get_epoch(); signal_a(); do_tick(); WAIT_VM(0); }
    BARRIER();
    STAMPT(24);
    dma1c((u32)(sp * 57344 + (w * 112 + l) * 64), (const char*)Wd, lbase + 49152);
    dma1c((u32)(sp * 57344 + (w * 112 + 48 + l) * 64), (const char*)Wd, lbase + 49152);
#pragma unroll
    for (int i = 0; i < 4; ++i) pfw(i, 49152);
    static_for<0, 6>([&](auto N) { issueW(N); });
    load_xe();
    trig2(); trig3(); segA0();
    STAMPT(25);
    if (w == 0) {
      if (!coord) __builtin_amdgcn_s_sleep(SLEEP_A);
      check_a(v4u{~E, 0u, 0u, 0u});
    }
    STAMPT(26);
    BARRIER();
    load_s1(); load_wt(); load_rs();
    WAIT_VM(0);
    segA1(); segA2(); segA3();
    STAMPT(2); STAMPC(31);
  }
  PIN(wt); PIN(xe); PIN(rs2);
  BARRIER();
  STAMPT(5);

  const u32 trG = (u32)(kq * 4096 + (4 * h + q) * 128 + ((dh ^ ((q >> 1) & 1)) << 6) + (blk ^ kh) * 32 + pp * 8);
  v8s Wf[14][2];
  auto readW = [&](auto N) {
    constexpr int n = N;
    LAS char* sb = smem + wslot(n) + trG;
    Wf[n][0] = cat8(trread(sb), trread(sb + 1024)); Wf[n][1] = cat8(trread(sb + 2048), trread(sb + 3072));
  };
  const char* hsrc = ws + OFF_H + ((size_t)(RG * 2 + (w >> 2)) * 112 + (w & 3) * 2) * 1024;
  const u32 so0 = (u32)((l >> 3) * 4096) + swz, so1 = so0 + 32768 - 1024;
  const char* ssrc = ws + OFF_S2 + (size_t)(w * 16) * 4096 + RG * 128;
  auto issueP = [&](int n, auto HALF) {
    constexpr int hf = HALF;
    if (n < 14) dma1s<hf * 1024>((u32)(l * 16), hsrc + n * 8192, lbase + aslot(n) + w * 2048);
    else dma1s<hf * 1024>(hf ? so1 : so0, ssrc + (size_t)(n - 14) * (128 * 4096), lbase + aslot(n) + w * 2048);
  };
  const int d = ct * 64 + dh * 32 + ((l & 31) ^ (kh << 4));
  const float gst = (float)d * (1.0f / 512.0f), ga0 = (float)(16 * kq + 2 * h) * gst;
  v8h G[8];
  auto gen = [&](auto FI) {
    constexpr int f = FI;
#pragma unroll
    for (int e2 = 0; e2 < 4; ++e2) {
      float a = fmaf((float)(64 * (f >> 1) + 8 * (f & 1) + 4 * (e2 >> 1) + (e2 & 1)), gst, ga0);
      float c = cosr(a), sn = sinr(a);
      if (f == 0 && e2 == 0) { if (kq == 0 && h == 0) { c = 0.5f; sn = (d & 1) ? 0.5f : -0.5f; } }
      G[f][2 * e2] = (_Float16)c; G[f][2 * e2 + 1] = (_Float16)sn;
    }
    PIN(G[f]);
  };

  float oscale;
  const u32 fhoff = (u32)(FL_H + (RG * 16 + (min(l, 55) >> 2)) * 64 + (l & 3) * 4);
  u32 fp;
  {
    v8h a;
    {
      LAS char* rp = smem + L_E1 + (4 * (g & 1) + (o >> 2)) * E1P + (2 * w + (g >> 1)) * 32 + (o & 3) * 8;
      a = __builtin_bit_cast(v8h, cat8(trread(rp), trread(rp + 8 * E1P)));
    }
    const v4u f8 = *(LAS v4u*)(smem + L_TAB + 1024 + l * 16);
    const v2u me = *(LAS v2u*)(smem + L_MX);
    issueW(6);
    static_for<0, 3>(readW);
    const int Ex = (int)me[0] < 16 ? 16 : ((int)me[0] > 240 ? 240 : (int)me[0]);
    const float wscale = __builtin_bit_cast(float, (u32)(246 - Ex) << 23);
    oscale = __builtin_bit_cast(float, (u32)(Ex - 7) << 23);
    re = mfma16h(__builtin_bit_cast(v8h, f8), a, z4); im = mfma16h(imf(f8), a, z4);
    static_for<3, 6>(readW);
    WAIT_VM(2);
    STAMPT(7);
    if (ffn && l == 0) st4c((u32)(FL_H + ((p * 2 + sh) * 16 + j) * 64 + fq * 4), FL, me[1]);
    issueW(7);
    if (kd != 0) {
#pragma unroll
      for (int r = 0; r < 4; ++r) {
        float fr = ((r & 1) ? bfhi(wt[r >> 1]) : bflo(wt[r >> 1])) * wscale, fi = ((r & 1) ? bfhi(wt[2 + (r >> 1)]) : bflo(wt[2 + (r >> 1)])) * wscale;
        float zr = re[r], zi = im[r];
        re[r] = zr * fr - zi * fi;
        im[r] = -(zr * fi + zi * fr);
      }
    } else {
      const int ka = 2 * w + (g >> 1), kr = o + 64 * (g & 1);
#pragma unroll
      for (int r = 0; r < 4; ++r) { v2f t = {re[r], im[r]}; *(LAS v2f*)(smem + L_ZX + ka * 1024 + (kr + 16 * r) * 8) = t; }
      BARRIER();
#pragma unroll
      for (int r = 0; r < 4; ++r) {
        PIN(pq[r]);
        const int km = (2048 - (ka + 16 * (kr + 16 * r))) & 2047;
        v2f zm = *(LAS v2f*)(smem + L_ZX + (km & 15) * 1024 + (km >> 4) * 8);
        float zr = re[r], zi = im[r];
        re[r] = (zr * pq[r][0] - zi * pq[r][1] + zm[0] * pq[r][2] + zm[1] * pq[r][3]) * wscale;
        im[r] = -(zr * pq[r][1] + zi * pq[r][0] + zm[0] * pq[r][3] - zm[1] * pq[r][2]) * wscale;
      }
    }
    LAS char* E2 = smem + (kd == 0 ? L_E1 : L_E2);
    a = pack_ri(re, im);
    re = mfma16h(a, __builtin_bit_cast(v8h, f8), z4); im = mfma16h(a, imf(f8), z4);
    twid(re, im, t3c, t3s);
    *(LAS v4h*)(E2 + (o & 7) * E2P + (2 * w + (o >> 3)) * 32 + g * 8) = pack4h(re);
    *(LAS v4h*)(E2 + (o & 7) * E2P + 512 + (2 * w + (o >> 3)) * 32 + g * 8) = pack4h(im);
    BARRIER();
    STAMPT(8);
    {
      LAS char* rp = E2 + w * E2P + o * 32 + g * 8;
      a = __builtin_bit_cast(v8h, cat8(*(LAS v4s*)rp, *(LAS v4s*)(rp + 512)));
    }
    issueW(8);
    re = mfma16h(a, __builtin_bit_cast(v8h, f16), z4); im = mfma16h(a, imf(f16), z4);
    issueW(9);
    twid(re, im, t1c, t1s);
    a = pack_ri(re, im);
    re = mfma16h(__builtin_bit_cast(v8h, f16), a, z4); im = mfma16h(imf(f16), a, z4);
    issueW(10);
    st8c((u32)(2 * kd * 4096 + w * 512 + l * 8), ws + OFF_S2, pack4h(re));
    st8c((u32)((2 * kd + 1) * 4096 + w * 512 + l * 8), ws + OFF_S2, pack4h(im));
  }
  STAMPT(9);
  gen(IC<0>{}); issueW(11);
  WAIT_VM(8);
  BARRIER();
  static_for<6, 9>(readW);
  gen(IC<1>{}); issueW(12); gen(IC<2>{}); issueW(13);
  if (!ffn) {
    WAIT_VM(0);
    BARRIER();
    if (w == 0) {
      if (l == 0) st4c((u32)(FL_S + b * 4), FL, E);
      __builtin_amdgcn_s_sleep(40);
    }
  }
  if (w == 0) ld4c(fp, fhoff, FL);
  gen(IC<3>{}); gen(IC<4>{});
  WAIT_VM(0);
  if (w == 0) {
    PIN(fp);
    u32 f = fp;
    int spins = 0;
    while (!__all(f == E) && ++spins < SPIN_CAP) {
      __builtin_amdgcn_s_sleep(4);
      f = ld4c_sync(fhoff, FL);
    }
  }
  BARRIER();
  STAMPT(10);
  if (ffn && tid == 0) st4c((u32)(FL_S + b * 4), FL, E);
  issueP(0, IC<0>{}); issueP(0, IC<1>{}); issueP(1, IC<0>{}); issueP(1, IC<1>{});
  static_for<9, 14>(readW);
  gen(IC<5>{});
  issueP(2, IC<0>{}); issueP(2, IC<1>{});
  gen(IC<6>{});
  issueP(3, IC<0>{}); issueP(3, IC<1>{});
  gen(IC<7>{});

  const u32 trS = (u32)(kq * 4096 + (4 * h + q) * 128 + ((ph ^ ((q >> 1) & 1)) << 6) + blk * 32 + pp * 8);
  const u32 hb = (u32)(ph * 8192 + kq * 2048 + l * 16);
  v16f accG[2] = {}, accS[2] = {};
  v8s fa[8], fb[8];
  v4u fs;
  float gv[8];
  const u32 xw = (u32)(w * 6144 + l * 16);
  auto xchg_out = [&](const v16f (&ac)[2], int base) {
    LAS char* X = smem + base + xw;
#pragma unroll
    for (int i = 0; i < 6; ++i) {
      const v16f& a = ac[(i + 2) >> 2]; const int r = ((i + 2) & 3) * 4;
      *(LAS v4f*)(X + i * 1024) = v4f{a[r], a[r + 1], a[r + 2], a[r + 3]};
    }
  };
  auto xchg_in = [&](const v16f (&ac)[2], int base, float (&o8)[8]) {
    v4f s0 = {ac[0][0], ac[0][1], ac[0][2], ac[0][3]}, s1 = {ac[0][4], ac[0][5], ac[0][6], ac[0][7]};
#pragma unroll
    for (int x_ = 1; x_ < 4; ++x_) {
      LAS char* X = smem + base + (w ^ (2 * x_)) * 6144 + (x_ - 1) * 2048 + l * 16;
      s0 += *(LAS v4f*)X; s1 += *(LAS v4f*)(X + 1024);
    }
#pragma unroll
    for (int e = 0; e < 4; ++e) { o8[e] = s0[e]; o8[4 + e] = s1[e]; }
  };
  v4f xg[6];
  float ov[8], ex[8];
  auto sadd = [](float a, float b) { float r; asm("v_add_f32 %0, %1, %2" : "=v"(r) : "v"(a), "v"(b)); return r; };
  auto smul = [](float a, float b) { float r; asm("v_mul_f32 %0, %1, %2" : "=v"(r) : "v"(a), "v"(b)); return r; };
  auto silu_step = [&](int i) {
    if (i < 3) {
#pragma unroll
      for (int e = 0; e < 8; ++e) { ov[e] = sadd(i == 0 ? accG[0][e] : ov[e], xg[2 * i + (e >> 2)][e & 3]); PIN(ov[e]); }
    } else if (i == 3) {
#pragma unroll
      for (int e = 0; e < 8; e += 2) { u32 t = pkbf_o(smul(ov[e], rs2), smul(ov[e + 1], rs2)); ov[e] = bflo(t); ov[e + 1] = bfhi(t); PIN(ov[e]); PIN(ov[e + 1]); }
    } else if (i == 4) {
#pragma unroll
      for (int e = 0; e < 8; ++e) { ex[e] = __builtin_amdgcn_exp2f(ov[e] * -1.44269504088896f); PIN(ex[e]); }
    } else if (i == 5) {
#pragma unroll
      for (int e = 0; e < 8; ++e) { ex[e] = __builtin_amdgcn_rcpf(1.0f + ex[e]); PIN(ex[e]); }
    } else if (i == 6) {
#pragma unroll
      for (int e = 0; e < 8; e += 2) { u32 t = pkbf_o(ov[e] * ex[e], ov[e + 1] * ex[e + 1]); gv[e] = bflo(t); gv[e + 1] = bfhi(t); PIN(gv[e]); PIN(gv[e + 1]); }
    }
  };
  static_for<0, 10>([&](auto V) {
    constexpr int v = V;
    v8s(&cur)[8] = (v & 1) ? fb : fa;
    v8s(&prv)[8] = (v & 1) ? fa : fb;
    auto mmp = [&](int s) {
      if constexpr (v >= 8) {
        accS[0] = mfma32h(G[(v - 8) * 4 + s], prv[2 * s], accS[0]); accS[1] = mfma32h(G[(v - 8) * 4 + s], prv[2 * s + 1], accS[1]);
        SCHED();
        if ((v - 8) * 4 + s >= 1) silu_step((v - 8) * 4 + s - 1);
      } else if constexpr (v >= 1) {
        accG[0] = mfma32b(Wf[2 * (v - 1) + (s >> 1)][s & 1], prv[2 * s], accG[0]);
        accG[1] = mfma32b(Wf[2 * (v - 1) + (s >> 1)][s & 1], prv[2 * s + 1], accG[1]);
      }
      SCHED();
    };
    if constexpr (v < 9) {
      WAIT_VM(v <= 7 ? 4 : 0);
      if constexpr (v == 5) { if (w == 0) { PIN(fs); poll4(fs, (u32)(FL_S + l * 16)); } }
      BARRIER();
      if constexpr (v == 0 || v == 8) STAMPT(11 + v);
      if constexpr (v == 4) { if (w == 0) ld16c(fs, (u32)(FL_S + l * 16), FL); }
      if constexpr (v == 8) {
#pragma unroll
        for (int x_ = 1; x_ < 4; ++x_) {
          LAS char* X = smem + 49152 + (w ^ (2 * x_)) * 6144 + (x_ - 1) * 2048 + l * 16;
          xg[2 * x_ - 2] = *(LAS v4f*)X; xg[2 * x_ - 1] = *(LAS v4f*)(X + 1024);
        }
        SCHED();
      }
      mmp(0);
#pragma unroll
      for (int s = 0; s < 4; ++s) {
        if constexpr (v >= 7) {
          LAS char* sb = smem + aslot(2 * v + (s >> 1)) + (s & 1) * 2048;
          cur[2 * s] = cat8(trread(sb + trS), trread(sb + trS + 1024));
          cur[2 * s + 1] = cat8(trread(sb + (trS ^ 64)), trread(sb + (trS ^ 64) + 1024));
        } else {
          LAS char* sb = smem + aslot(2 * v + (s >> 1)) + (s & 1) * 1024;
          cur[2 * s] = *(LAS v8s*)(sb + hb);
          cur[2 * s + 1] = *(LAS v8s*)(sb + (hb ^ 8192));
        }
      }
      SCHED();
      if constexpr (v <= 6) issueP(2 * v + 4, IC<0>{});
      mmp(1);
      if constexpr (v <= 6) issueP(2 * v + 4, IC<1>{});
      mmp(2);
      if constexpr (v <= 6) issueP(2 * v + 5, IC<0>{});
      mmp(3);
      if constexpr (v <= 6) issueP(2 * v + 5, IC<1>{});
    } else {
      mmp(0); mmp(1); mmp(2); mmp(3);
    }
    if constexpr (v == 7) xchg_out(accG, 49152);
    SCHED();
  });
  xchg_out(accS, 0);
  BARRIER();
  {
    float o8[8], pv[8];
    xchg_in(accS, 0, o8);
#pragma unroll
    for (int e = 0; e < 8; e += 2) { u32 t = pkbf_o(o8[e] * oscale, o8[e + 1] * oscale); pv[e] = bflo(t) * gv[e]; pv[e + 1] = bfhi(t) * gv[e + 1]; }
    u32 a0 = pkbf(pv[0], pv[1]), a1 = pkbf(pv[2], pv[3]), b0 = pkbf(pv[4], pv[5]), b1 = pkbf(pv[6], pv[7]);
    auto r0 = __builtin_amdgcn_permlane32_swap(a0, b0, false, false);
    auto r1 = __builtin_amdgcn_permlane32_swap(a1, b1, false, false);
    v4u pk = {r0[0], r1[0], r0[1], r1[1]};
    v4u ov4;
#pragma unroll
    for (int e = 0; e < 4; ++e) ov4[e] = pkbf(bflo(xe[e]) + bflo(pk[e]), bfhi(xe[e]) + bfhi(pk[e]));
    *(v4u*)((char*)out + eoff) = ov4;
  }
  asm volatile("km_pf_b:");
  STAMPT(20);
  STAMPT(21);
  STAMP_OUT(ws);
}

extern "C" void kernel_launch(void* const* d_in, const int* in_sizes, int n_in, void* d_out, int out_size, void* d_ws,
                              size_t ws_size, hipStream_t stream) {
  kmain<<<256, 512, 0, stream>>>((const u16*)d_in[0], (const u16*)d_in[1], (const u16*)d_in[2], (const u16*)d_in[4],
                                 (const u16*)d_in[5], (const u16*)d_in[6], (char*)d_ws + WS_BASE(ws_size), (u16*)d_out);
}
```

```cpp
#include <hip/hip_runtime.h>
#include <stdint.h>

typedef unsigned short u16;
typedef unsigned int u32;
typedef unsigned long long u64;
typedef short v4s __attribute__((ext_vector_type(4)));
typedef short v8s __attribute__((ext_vector_type(8)));
typedef _Float16 v4h __attribute__((ext_vector_type(4)));
typedef _Float16 v8h __attribute__((ext_vector_type(8)));
typedef __bf16 v2b __attribute__((ext_vector_type(2)));
typedef __bf16 v8b __attribute__((ext_vector_type(8)));
typedef float v2f __attribute__((ext_vector_type(2)));
typedef float v4f __attribute__((ext_vector_type(4)));
typedef float v16f __attribute__((ext_vector_type(16)));
typedef u32 v2u __attribute__((ext_vector_type(2)));
typedef u32 v4u __attribute__((ext_vector_type(4)));

#define LAS __attribute__((address_space(3)))
#define GAS __attribute__((address_space(1)))
#define DEVI __device__ __forceinline__

constexpr int D_ = 512, F_ = 1792, DF_ = 257;
constexpr size_t OFF_H = (size_t)24 << 20;
constexpr size_t OFF_S1 = (size_t)32 << 20;
constexpr size_t OFF_WT = (size_t)34 << 20;
constexpr size_t OFF_S2 = (size_t)37 << 20;
constexpr size_t OFF_M = (size_t)39 << 20;
constexpr size_t OFF_FL = OFF_M;
constexpr u32 FL_S = 0, FL_A = 1024, FL_H = 2048;
constexpr size_t OFF_BC = OFF_M + 49152;
constexpr size_t OFF_MB = OFF_M + 98304;
constexpr size_t OFF_RS2 = OFF_M + 106496;
constexpr size_t OFF_PQ = OFF_M + 65536;
constexpr size_t OFF_CSP = OFF_M + 131072;
constexpr int SPIN_CAP = 1 << 16;
#define SLEEP_A 48

#define WS_BASE(n) ((((n) - ((size_t)40 << 20))) & ~(size_t)0x1fffff)
#define STAMP_DECL
#define STAMPT(id)
#define STAMPC(id)
#define STAMP_OUT(wsp)

template <int I> struct IC { static constexpr int v = I; constexpr operator int() const { return I; } };
template <int B, int E, class Fn> DEVI void static_for(Fn&& f) {
  if constexpr (B < E) { f(IC<B>{}); static_for<B + 1, E>(f); }
}
#define WAIT_VM(n) asm volatile("s_waitcnt vmcnt(%0)" ::"n"(n) : "memory")
#define BARRIER() asm volatile("s_waitcnt lgkmcnt(0)\n\ts_barrier" ::: "memory")
#define SCHED() __builtin_amdgcn_sched_barrier(0)
#define PIN(x) asm volatile("" : "+v"(x))

DEVI int s_of_pi(int pi) {
  int RG = pi >> 6, i = pi & 63;
  return 128 * (4 * (RG & 3) + (i & 3)) + 8 * (i >> 2) + (RG >> 2);
}
DEVI void dma2(u32 voff0, u32 voff1, const char* sbase, u32 lds) {
  asm volatile("s_mov_b32 m0, %3\n\ts_nop 0\n\tglobal_load_lds_dwordx4 %0, %2\n\tglobal_load_lds_dwordx4 %1, %2 offset:1024" ::"v"(voff0), "v"(voff1), "s"(sbase), "s"(lds) : "memory");
}
DEVI void dma2c(u32 voff0, u32 voff1, const char* sbase, u32 lds) {
  asm volatile("s_mov_b32 m0, %3\n\ts_nop 0\n\tglobal_load_lds_dwordx4 %0, %2 sc1\n\tglobal_load_lds_dwordx4 %1, %2 offset:1024 sc1" ::"v"(voff0), "v"(voff1), "s"(sbase), "s"(lds) : "memory");
}
template <int OFF> DEVI void dma1(u32 voff, const char* sbase, u32 lds) {
  asm volatile("s_mov_b32 m0, %2\n\ts_nop 0\n\tglobal_load_lds_dwordx4 %0, %1 offset:%3" ::"v"(voff), "s"(sbase), "s"(lds), "n"(OFF) : "memory");
}
template <int OFF> DEVI void dma1s(u32 voff, const char* sbase, u32 lds) {
  asm volatile("s_mov_b32 m0, %2\n\ts_nop 0\n\tglobal_load_lds_dwordx4 %0, %1 offset:%3 sc1" ::"v"(voff), "s"(sbase), "s"(lds), "n"(OFF) : "memory");
}
DEVI void dma1c(u32 voff, const char* sbase, u32 lds) {
  asm volatile("s_mov_b32 m0, %2\n\ts_nop 0\n\tglobal_load_lds_dword %0, %1 sc1" ::"v"(voff), "s"(sbase), "s"(lds) : "memory");
}
DEVI v2u ld8c_sync(u32 voff, const char* sb) { v2u d; asm volatile("global_load_dwordx2 %0, %1, %2 sc1\n\ts_waitcnt vmcnt(0)" : "=&v"(d) : "v"(voff), "s"(sb) : "memory"); return d; }
DEVI u32 ld4c_sync(u32 voff, const char* sb) { u32 d; asm volatile("global_load_dword %0, %1, %2 sc1\n\ts_waitcnt vmcnt(0)" : "=&v"(d) : "v"(voff), "s"(sb) : "memory"); return d; }
DEVI v4u ld16c_sync(u32 voff, const char* sb) { v4u d; asm volatile("global_load_dwordx4 %0, %1, %2 sc1\n\ts_waitcnt vmcnt(0)" : "=&v"(d) : "v"(voff), "s"(sb) : "memory"); return d; }
template <class T> DEVI void ld16c(T& d, u32 voff, const char* sb) { asm volatile("global_load_dwordx4 %0, %1, %2 sc1" : "=&v"(d) : "v"(voff), "s"(sb) : "memory"); }
template <class T> DEVI void ld8c(T& d, u32 voff, const char* sb) { asm volatile("global_load_dwordx2 %0, %1, %2 sc1" : "=&v"(d) : "v"(voff), "s"(sb) : "memory"); }
template <class T> DEVI void ld4c(T& d, u32 voff, const char* sb) { asm volatile("global_load_dword %0, %1, %2 sc1" : "=&v"(d) : "v"(voff), "s"(sb) : "memory"); }
template <class T> DEVI void ld16(T& d, u32 voff, const char* sb) { asm volatile("global_load_dwordx4 %0, %1, %2" : "=&v"(d) : "v"(voff), "s"(sb) : "memory"); }
DEVI void ld2(u32& d, u32 voff, const char* sb) { asm volatile("global_load_ushort %0, %1, %2" : "=&v"(d) : "v"(voff), "s"(sb) : "memory"); }
template <class T> DEVI void st16c(u32 voff, const char* sb, T v) { asm volatile("global_store_dwordx4 %0, %1, %2 sc1\n\ts_nop 1" ::"v"(voff), "v"(v), "s"(sb) : "memory"); }
template <class T> DEVI void st8c(u32 voff, const char* sb, T v) { asm volatile("global_store_dwordx2 %0, %1, %2 sc1" ::"v"(voff), "v"(v), "s"(sb) : "memory"); }
template <class T> DEVI void st4c(u32 voff, const char* sb, T v) { asm volatile("global_store_dword %0, %1, %2 sc1" ::"v"(voff), "v"(v), "s"(sb) : "memory"); }

DEVI v4s trread(LAS char* p) { return __builtin_amdgcn_ds_read_tr16_b64_v4i16((LAS v4s*)p); }
DEVI v8s cat8(v4s a, v4s b) { return __builtin_shufflevector(a, b, 0, 1, 2, 3, 4, 5, 6, 7); }
DEVI float bflo(u32 v) { return __builtin_bit_cast(float, v << 16); }
DEVI float bfhi(u32 v) { return __builtin_bit_cast(float, v & 0xffff0000u); }
DEVI float relu(float v) { return __builtin_bit_cast(float, max(__builtin_bit_cast(int, v), 0)); }
DEVI float rbf(float v) { return (float)(__bf16)v; }
DEVI u32 pkbf(float a, float b) { v2b p = {(__bf16)a, (__bf16)b}; return __builtin_bit_cast(u32, p); }
DEVI u32 pkbf_o(float a, float b) { u32 r = pkbf(a, b); asm("" : "+v"(r)); return r; }
DEVI float cosr(float rev) { return __builtin_amdgcn_cosf(rev); }
DEVI float sinr(float rev) { return __builtin_amdgcn_sinf(rev); }
DEVI v4f mfma16h(v8h a, v8h b, v4f c) { return __builtin_amdgcn_mfma_f32_16x16x32_f16(a, b, c, 0, 0, 0); }
DEVI v4f mfma16b(v8s a, v4u b, v4f c) { return __builtin_amdgcn_mfma_f32_16x16x32_bf16(__builtin_bit_cast(v8b, a), __builtin_bit_cast(v8b, b), c, 0, 0, 0); }
DEVI v16f mfma32b(v8s a, v8s b, v16f c) { return __builtin_amdgcn_mfma_f32_32x32x16_bf16(__builtin_bit_cast(v8b, a), __builtin_bit_cast(v8b, b), c, 0, 0, 0); }
DEVI v16f mfma32h(v8h a, v8s b, v16f c) { return __builtin_amdgcn_mfma_f32_32x32x16_f16(a, __builtin_bit_cast(v8h, b), c, 0, 0, 0); }
DEVI v8h pack_ri(v4f re, v4f im) {
  v8h r;
  r[0] = (_Float16)re[0]; r[1] = (_Float16)re[1]; r[2] = (_Float16)re[2]; r[3] = (_Float16)re[3];
  r[4] = (_Float16)im[0]; r[5] = (_Float16)im[1]; r[6] = (_Float16)im[2]; r[7] = (_Float16)im[3];
  return r;
}
DEVI v4h pack4h(v4f a) { v4h r; r[0] = (_Float16)a[0]; r[1] = (_Float16)a[1]; r[2] = (_Float16)a[2]; r[3] = (_Float16)a[3]; return r; }
DEVI void twid(v4f& re, v4f& im, const v4f& c, const v4f& s) {
#pragma unroll
  for (int r = 0; r < 4; ++r) { float a = re[r], b = im[r]; re[r] = a * c[r] + b * s[r]; im[r] = b * c[r] - a * s[r]; }
}
template <int CTRL> DEVI float dppadd(float v) {
  return v + __builtin_bit_cast(float, __builtin_amdgcn_update_dpp(0, __builtin_bit_cast(int, v), CTRL, 0xf, 0xf, true));
}
DEVI float wave_sum(float v) {
  v = dppadd<0xB1>(v); v = dppadd<0x4E>(v); v = dppadd<0x141>(v); v = dppadd<0x140>(v);
  int bb = __builtin_bit_cast(int, v);
  float a0 = __builtin_bit_cast(float, __builtin_amdgcn_readlane(bb, 0)), a1 = __builtin_bit_cast(float, __builtin_amdgcn_readlane(bb, 16));
  float a2 = __builtin_bit_cast(float, __builtin_amdgcn_readlane(bb, 32)), a3 = __builtin_bit_cast(float, __builtin_amdgcn_readlane(bb, 48));
  return (a0 + a1) + (a2 + a3);
}
DEVI float dot2ones(u32 v, float acc) {
  const v2b ones = {(__bf16)1.0f, (__bf16)1.0f};
  return __builtin_amdgcn_fdot2_f32_bf16(__builtin_bit_cast(v2b, v), ones, acc, false);
}
template <int CTRL> DEVI u32 dppmax(u32 v) { return max(v, (u32)__builtin_amdgcn_update_dpp(0, (int)v, CTRL, 0xf, 0xf, true)); }
DEVI u32 wave_max(u32 v) {
  v = dppmax<0xB1>(v); v = dppmax<0x4E>(v); v = dppmax<0x141>(v); v = dppmax<0x140>(v);
  u32 a0 = __builtin_amdgcn_readlane(v, 0), a1 = __builtin_amdgcn_readlane(v, 16), a2 = __builtin_amdgcn_readlane(v, 32), a3 = __builtin_amdgcn_readlane(v, 48);
  return max(max(a0, a1), max(a2, a3));
}

struct Frag1 { v8s x0, x1, w1, w2; };

constexpr int L_SCR = 147456;
constexpr int L_CS = L_SCR, L_RED = L_SCR + 1024, L_MX = L_SCR + 1088, L_BC0 = L_SCR + 2048, L_BC1 = L_SCR + 2304;
constexpr int L_TAB = L_SCR + 3072;
constexpr int L_T = L_SCR + 7168, L_FA = L_SCR, L_E1 = L_T, L_E2 = 81920, L_ZX = 81920;
constexpr int L_C1 = 98304, L_IMG = L_C1 + 9216;
constexpr int L_FR = 131072;
constexpr int E1P = 544, E2P = 1024 + 72;
DEVI constexpr int wslot(int n) {
  constexpr int u[14] = {6, 7, 8, 0, 1, 2, 3, 4, 6, 7, 8, 0, 1, 2};
  return u[n] * 16384;
}
DEVI constexpr int aslot(int n) {
  constexpr int u[18] = {3, 4, 5, 9, 2, 6, 7, 8, 0, 1, 3, 4, 5, 9, 2, 6, 7, 8};
  return u[n] * 16384;
}

__global__ __launch_bounds__(512, 2) void kmain(const u16* __restrict__ x, const u16* __restrict__ wr, const u16* __restrict__ wi,
                                                const u16* __restrict__ Wg, const u16* __restrict__ Wu, const u16* __restrict__ Wd,
                                                char* __restrict__ ws, u16* __restrict__ out) {
  __shared__ __attribute__((aligned(16))) char smem_[163840];
  LAS char* smem = (LAS char*)smem_;
  const u32 lbase = (u32)(size_t)smem;
  const int tid = threadIdx.x, l = tid & 63;
  const int w = __builtin_amdgcn_readfirstlane(tid >> 6);
  const int b = blockIdx.x, xcd = b & 7, idx = b >> 3;
  const bool ffn = idx < 28;
  const int g = l >> 4, o = l & 15;
  const int h = l >> 5, blk = (l >> 4) & 1, q = (l & 15) >> 2, pp = l & 3;
  const char* FL = ws + OFF_FL;
  const char* BC = ws + OFF_BC;
  STAMP_DECL; STAMPT(0);

  const int pi = b * 8 + w;
  v4u xr;
  {
    u64 pc; u32 len;
    asm volatile("s_getpc_b64 %0\nkm_pf_a:\n\ts_mov_b32 %1, km_pf_b-km_pf_a" : "=s"(pc), "=s"(len));
    dma1<0>(min((u32)(((idx & 3) * 8 + w) * 1024 + l * 16), len) & ~15u, (const char*)(pc & ~15ull), lbase + L_IMG + w * 1024);
  }
  ld16(xr, (u32)(s_of_pi(pi) * 1024 + l * 16), (const char*)x);
  if (w == 0) dma1c((u32)((l & 1) * 4), BC, lbase + L_BC0);
  const int fkb = (b & 7) * 2 + (w >> 2), fcq = w & 3;
  {
    const u32 ks = (u32)((idx >> 1) + 16 * fkb + 256 * (4 * (idx & 1) + ((l >> 3) & 3)));
    const u32 base = ((ks * (DF_ * 2)) & ~15u) + fcq * 128;
    const u32 dlt = (u32)((const char*)wi - (const char*)wr);
    (void)dlt;
    const u32 c0 = ((l & 7) ^ (((l >> 4) & 1) << 2)) << 4;
    if (l < 32) dma1<0>(base + c0, (const char*)wr, lbase + L_FR + w * 2048);
    else dma1<0>(base + c0, (const char*)wi, lbase + L_FR + w * 2048);
    if (fcq == 3) {
      const u32 t0 = min(base + 128 + (c0 & 16), (u32)(2048 * DF_ * 2 - 16));
      if (l < 32) dma1<0>(t0, (const char*)wr, lbase + L_FR + w * 2048 + 1024);
      else dma1<0>(t0, (const char*)wi, lbase + L_FR + w * 2048 + 1024);
    }
  }
  const int p = (xcd >> 1) * 4 + idx / 7, j = (xcd & 1) * 7 + idx % 7;
  const int sh = w >> 2, fq = w & 3;
  u32 aoff[2];
#pragma unroll
  for (int i = 0; i < 2; ++i) {
    int row = (2 * w + i) * 8 + (l >> 3);
    aoff[i] = s_of_pi((p & 15) * 128 + row) * 1024 + (((l & 7) ^ ((row >> 1) & 7)) << 4) - i * 1024;
  }
  const u32 boff = (u32)(2 * w * 4 + (l >> 4)) * (F_ * 2) + j * 256 + (((l & 15) ^ ((l >> 4) << 2)) << 4);
  const u32 boff1 = boff + 4 * F_ * 2 - 1024;
  const int rot = p >> 1;
  auto issue = [&](int t, int slot) {
    const int kt = (t + rot) & 7;
    u32 dst = lbase + slot * 49152 + w * 2048;
    dma2(aoff[0], aoff[1], (const char*)x + kt * 128, dst);
    dma2(boff, boff1, (const char*)Wg + (size_t)kt * (64 * F_ * 2), dst + 16384);
    dma2(boff, boff1, (const char*)Wu + (size_t)kt * (64 * F_ * 2), dst + 32768);
  };
  const int sp = xcd * 4 + (idx - 28);
  if (ffn) issue(0, 0);

  u32 E = 0, tick = 0;
  const bool coord = (b == 224) && (w == 0);
  auto do_tick = [&]() { if (coord) { ++tick; if (l == 0) st8c(0u, BC, v2u{E, tick}); } };
  auto get_epoch = [&]() {
    const v2u b0 = *(LAS v2u*)(smem + L_BC0);
    v2u b1 = *(LAS v2u*)(smem + L_BC1);
    int spins = 0;
    while (__builtin_amdgcn_readfirstlane((b0[0] ^ b1[0]) | (b0[1] ^ b1[1])) == 0 && ++spins < SPIN_CAP) {
      __builtin_amdgcn_s_sleep(4);
      b1 = ld8c_sync(0u, BC);
    }
    E = __builtin_amdgcn_readfirstlane(b1[0]);
  };
  auto signal_a = [&]() {
    v4u r0 = *(LAS v4u*)(smem + L_RED), r1 = *(LAS v4u*)(smem + L_RED + 16);
    u32 m = max(max(max(r0[0], r0[1]), max(r0[2], r0[3])), max(max(r1[0], r1[1]), max(r1[2], r1[3])));
    if (l == 0) { st4c((u32)(FL_A + b * 4), FL, E | (m >> 7)); *(LAS u32*)(smem + L_MX + 4) = E; }
  };
  auto poll4 = [&](v4u f, u32 voff) -> v4u {
    int spins = 0;
    while (!__all((((f[0] ^ E) | (f[1] ^ E) | (f[2] ^ E) | (f[3] ^ E)) >> 8) == 0) && ++spins < SPIN_CAP) {
      do_tick();
      __builtin_amdgcn_s_sleep(12);
      f = ld16c_sync(voff, FL);
    }
    return f;
  };
  auto check_a = [&](v4u f) {
    f = poll4(f, (u32)(FL_A + l * 16));
    u32 m = wave_max(max(max(f[0], f[1]), max(f[2], f[3]))) & 0xffu;
    if (l == 0) *(LAS u32*)(smem + L_MX) = m;
  };
  if (coord) {
#pragma unroll
    for (int i = 0; i < 5; ++i) dma2c(i ? (u32)(FL_H + ((i - 1) * 128 + l) * 64) : (u32)(l * 16), i ? (u32)(FL_H + ((i - 1) * 128 + 64 + l) * 64 - 1024) : (u32)(l * 16), FL, lbase + i * 2048);
  }
  u32 sq[8];
  if (!ffn && w == 7) {
    const int kss = (sp >> 1) + 16 * (l >> 2) + 256 * (4 * (sp & 1) + (l & 3)), km = (2048 - kss) & 2047;
    const u32 o1 = (u32)(kss * DF_ * 2), o2 = (u32)(km * DF_ * 2);
    ld2(sq[0], o1, (const char*)wr); ld2(sq[1], o1, (const char*)wi); ld2(sq[2], o2, (const char*)wr); ld2(sq[3], o2, (const char*)wi);
    ld2(sq[4], o1 + 512, (const char*)wr); ld2(sq[5], o1 + 512, (const char*)wi); ld2(sq[6], o2 + 512, (const char*)wr); ld2(sq[7], o2 + 512, (const char*)wi);
  }

  {
    const float fo = (float)o, fg = (float)(4 * g);
    if (w < 2) {
      const float st = fo * (1.0f / 32.0f), a0 = fmaf(fg, st, w ? 0.25f : 0.f);
      v4u hi, lo;
#pragma unroll
      for (int e2 = 0; e2 < 4; ++e2) {
        float c0 = cosr(fmaf((float)(16 * (e2 >> 1) + 2 * (e2 & 1)), st, a0)), c1 = cosr(fmaf((float)(16 * (e2 >> 1) + 2 * (e2 & 1) + 1), st, a0));
        u32 ch = pkbf_o(c0, c1);
        hi[e2] = ch; lo[e2] = pkbf(c0 - bflo(ch), c1 - bfhi(ch));
      }
      *(LAS v4u*)(smem + L_C1 + w * 2048 + l * 16) = hi; *(LAS v4u*)(smem + L_C1 + w * 2048 + 1024 + l * 16) = lo;
    } else if (w < 5) {
      const float st = w == 2 ? (float)(o & 7) * (1.0f / 16.0f) : fo * (w == 3 ? 1.0f / 16.0f : 1.0f / 8.0f);
      const float a0 = (w == 2 && o >= 8) ? 0.25f : 0.f;
      const float sc = w == 2 ? 0.0625f : ((w == 4 && (g >> 1) != (o >> 3)) ? 0.f : 1.0f);
      v8h m;
#pragma unroll
      for (int e = 0; e < 4; ++e) { float a = fmaf(fg + (float)e, st, a0); m[e] = (_Float16)(sc * cosr(a)); m[4 + e] = (_Float16)(sc * sinr(a)); }
      *(LAS v8h*)(smem + (w == 2 ? L_C1 + 4096 : L_TAB + (w - 3) * 1024) + l * 16) = m;
    } else {
      const float st = w == 7 ? fo * (1.0f / 256.0f) : (fo + (w == 6 ? 16.f : 0.f)) * (1.0f / 512.0f);
      v4f c, sn;
#pragma unroll
      for (int e = 0; e < 4; ++e) { float a = (fg + (float)e) * st; c[e] = cosr(a); sn[e] = sinr(a); }
      LAS char* dst = smem + (w == 7 ? L_TAB + 2048 : L_C1 + 5120 + (w - 5) * 2048) + l * 16;
      *(LAS v4f*)dst = c; *(LAS v4f*)(dst + 1024) = sn;
    }
    BARRIER();
    LAS char* C1 = smem + L_C1 + l * 16;
    const v4u Fhr = *(LAS v4u*)C1, Flr = *(LAS v4u*)(C1 + 1024), Fhi = *(LAS v4u*)(C1 + 2048), Fli = *(LAS v4u*)(C1 + 3072);
    const v8h M2 = *(LAS v8h*)(C1 + 4096);
    v4f twc[2], tws[2];
#pragma unroll
    for (int u = 0; u < 2; ++u) { twc[u] = *(LAS v4f*)(C1 + 5120 + u * 2048); tws[u] = *(LAS v4f*)(C1 + 6144 + u * 2048); }
    if (ffn) WAIT_VM(8); else if (w == 7) WAIT_VM(10); else WAIT_VM(2);
    if (coord) {
      WAIT_VM(0);
      v4u f[10];
#pragma unroll
      for (int i = 0; i < 10; ++i) f[i] = *(LAS v4u*)(smem + i * 1024 + l * 16);
      u32 c = __builtin_amdgcn_readfirstlane(*(LAS u32*)(smem + L_BC0)) >> 8;
      bool hit;
      do {
        c = (c + 1) & 0xffffffu; hit = false;
#pragma unroll
        for (int i = 0; i < 10; ++i) hit |= ((f[i][0] >> 8) == c) | ((f[i][1] >> 8) == c) | ((f[i][2] >> 8) == c) | ((f[i][3] >> 8) == c);
      } while (__any(hit));
      E = c << 8;
      do_tick();
    }
    PIN(xr);
    STAMPT(3);
    const float c0 = bflo(__builtin_amdgcn_readfirstlane(xr[0]));
    v2f xv[4], s2 = {0.f, 0.f}, q2 = {0.f, 0.f};
#pragma unroll
    for (int e = 0; e < 4; ++e) {
      xv[e] = v2f{bflo(xr[e]), bfhi(xr[e])};
      v2f t = xv[e] - v2f{c0, c0};
      s2 += t; q2 = __builtin_elementwise_fma(t, t, q2);
    }
    const float dmean = wave_sum(s2[0] + s2[1]) * (1.0f / 512.0f), msq = wave_sum(q2[0] + q2[1]) * (1.0f / 512.0f);
    do_tick();
    const float mb = rbf(c0 + dmean);
    const float var = rbf(fmaxf(msq - dmean * dmean, 0.f));
    const float sd = rbf(__builtin_amdgcn_sqrtf(rbf(var + 1e-5f)));
    const float rs = __builtin_amdgcn_rcpf(sd);
#pragma unroll
    for (int e = 0; e < 4; ++e) {
      v2f t = xv[e] - v2f{mb, mb};
      u32 tb = pkbf_o(t[0], t[1]);
      t = v2f{bflo(tb), bfhi(tb)} * v2f{rs, rs};
      xr[e] = pkbf(t[0], t[1]);
    }
    *(LAS v4u*)(smem + L_IMG + w * 1024 + l * 16) = xr;
    if (l < 2) st4c((u32)(pi * 4 + l * 8192), ws + OFF_MB, l ? rs * rs : -mb);
    const v8s xf = cat8(trread(smem + L_IMG + w * 1024 + l * 8), trread(smem + L_IMG + w * 1024 + 512 + l * 8));
    const int kt1 = (1 + rot) & 7;
    const u32 dst1 = lbase + 49152 + w * 2048;
    if (ffn) dma2(aoff[0], aoff[1], (const char*)x + kt1 * 128, dst1);
    const v8s xg_ = __builtin_bit_cast(v8s, __builtin_bit_cast(v4u, xf) ^ v4u{0x80000000u, 0x80000000u, 0x80000000u, 0x80000000u});
    const v4f z = {0.f, 0.f, 0.f, 0.f};
    v4f dre[2], dim[2];
#pragma unroll
    for (int u = 0; u < 2; ++u) { dre[u] = mfma16b(u ? xg_ : xf, Flr, z); dim[u] = mfma16b(u ? xg_ : xf, Fli, z); }
#pragma unroll
    for (int u = 0; u < 2; ++u) { dre[u] = mfma16b(u ? xg_ : xf, Fhr, dre[u]); dim[u] = mfma16b(u ? xg_ : xf, Fhi, dim[u]); }
#pragma unroll
    for (int u = 0; u < 2; ++u) {
      twid(dre[u], dim[u], twc[u], tws[u]);
      const v8h bb = pack_ri(dre[u], dim[u]);
      v4f d2 = mfma16h(bb, M2, z);
      if (u == 0) {
        const u32 nv = o == 8 ? 0xac002c00u : 0u;
        v4f n = mfma16h(bb, __builtin_bit_cast(v8h, v4u{nv, nv, 0u, 0u}), z);
        if (l == 8) d2[0] = n[0];
      }
      *(LAS v4h*)(smem + L_T + ((o >> 3) * 8 + w) * 544 + (o & 7) * 64 + u * 32 + g * 8) = pack4h(d2);
    }
    if (ffn) {
      dma2(boff, boff1, (const char*)Wg + (size_t)kt1 * (64 * F_ * 2), dst1 + 16384);
      dma2(boff, boff1, (const char*)Wu + (size_t)kt1 * (64 * F_ * 2), dst1 + 32768);
    }
    STAMPT(4);
  }
  v8s su; v4u fu, tu;
  auto round1_read = [&]() {
    LAS char* rp = smem + L_T + (4 * (g & 1) + (o >> 2)) * 544 + (w * 32 + (g >> 1) * 16) * 2 + (o & 3) * 8;
    su = cat8(trread(rp), trread(rp + 8 * 544));
    LAS char* fp_ = smem + L_FR + w * 2048 + q * 128 + (((2 * g + (pp >> 1)) ^ ((q >> 1) << 2)) << 4) + (pp & 1) * 8;
    fu = __builtin_bit_cast(v4u, cat8(trread(fp_), trread(fp_ + 512)));
    LAS char* tp = smem + L_FR + w * 2048 + 1024 + q * 128 + (pp >> 1) * 16 + (pp & 1) * 8;
    tu = __builtin_bit_cast(v4u, cat8(trread(tp), trread(tp + 512)));
  };
  auto round1_max = [&]() {
    typedef unsigned short v2us __attribute__((ext_vector_type(2)));
    const int dl = (idx >> 1) & 7;
    const u32 msk = (fcq == 0 && l < dl) ? 0u : 0x7fff7fffu, msk2 = (fcq == 3 && o <= dl) ? 0x7fff7fffu : 0u;
    v2us m2 = {0, 0};
#pragma unroll
    for (int e = 0; e < 4; ++e) {
      u32 t = fu[e] & msk, t2 = tu[e] & msk2;
      m2 = __builtin_elementwise_max(m2, __builtin_elementwise_max(__builtin_bit_cast(v2us, t), __builtin_bit_cast(v2us, t2)));
    }
    const u32 mx = wave_max(max((u32)m2[0], (u32)m2[1]));
    if (l == 0) *(LAS u32*)(smem + L_RED + w * 4) = mx;
  };
  auto round1_store = [&]() {
    st16c((u32)(b * 8192 + (w * 32 + (g >> 1) * 16 + o) * 32 + (g & 1) * 16), ws + OFF_S1, su);
    st16c((u32)((idx * 16 + fkb) * 4608 + (fcq * 64 + l) * 16), ws + OFF_WT, fu);
    if (fcq == 3 && l < 16) st16c((u32)((idx * 16 + fkb) * 4608 + (256 + l) * 16), ws + OFF_WT, tu);
    if (w == 0 && !coord) dma1c((u32)((l & 1) * 4), BC, lbase + L_BC1);
  };

  const int RG = xcd * 4 + (idx >> 3), ct = idx & 7;
  const int kd = xcd * 32 + idx;
  const u32 swz = ((l & 7) ^ (((l >> 4) & 1) << 2)) << 4;
  const u32 ro0 = (u32)((l >> 3) * 1024) + swz, ro1 = ro0 + 8192 - 1024;
  const char* bsrc = (const char*)Wd + (size_t)(w * 16) * 1024 + ct * 128;
  auto issueW = [&](int n) { dma2(ro0, ro1, bsrc + (size_t)n * 131072, lbase + wslot(n) + w * 2048); };
  const int dh = w & 1, kq = w >> 1, ph = kq >> 1, kh = kq & 1;
  const int epi = RG * 64 + ph * 32 + (l & 31);
  const u32 eoff = (u32)(s_of_pi(epi) * D_ + ct * 64 + dh * 32 + kh * 16 + h * 8) * 2;
  v4u wt, s1v, xe; v4f pq[4]; float rs2;
  auto load_xe = [&]() { ld16(xe, eoff, (const char*)x); };
  auto load_s1 = [&]() { ld16c(s1v, (u32)((((4 * w + g) * 8 + (o >> 1)) * 256 + kd) * 32 + (o & 1) * 16), ws + OFF_S1); };
  auto load_wt = [&]() { ld16c(wt, (u32)((w * 64 + l) * 4608 + (kd + ((2 * w + (g >> 1)) & 7)) * 16), ws + OFF_WT); };
  auto load_rs = [&]() { ld4c(rs2, (u32)(epi * 4), ws + OFF_RS2); };
  v4f t2c, t2s, t3c, t3s;
  auto trig2 = [&]() {
    const float fw = (float)w, st2 = fw * (1.0f / 128.0f), a2 = fw * (float)(o + 64 * g) * (1.0f / 2048.0f);
#pragma unroll
    for (int e = 0; e < 4; ++e) { float b2 = fmaf((float)e, st2, a2); t2c[e] = cosr(b2); t2s[e] = sinr(b2); }
    PIN(t2c); PIN(t2s);
  };
  auto trig3 = [&]() {
    const float f7 = (float)(o & 7), st3 = f7 * (1.0f / 128.0f), a3 = f7 * (float)(2 * w + (o >> 3) + 64 * g) * (1.0f / 2048.0f);
#pragma unroll
    for (int e = 0; e < 4; ++e) { float b3 = fmaf((float)e, st3, a3); t3c[e] = cosr(b3); t3s[e] = sinr(b3); }
    PIN(t3c); PIN(t3s);
  };
  v4u f16; v4f t1c, t1s, re, im;
  const v4f z4 = {0.f, 0.f, 0.f, 0.f};
  auto imf = [](v4u f) { return __builtin_bit_cast(v8h, v4u{f[2] ^ 0x80008000u, f[3] ^ 0x80008000u, f[0], f[1]}); };
  auto segA0 = [&]() {
    f16 = *(LAS v4u*)(smem + L_TAB + l * 16);
    t1c = *(LAS v4f*)(smem + L_TAB + 2048 + l * 16); t1s = *(LAS v4f*)(smem + L_TAB + 3072 + l * 16);
  };
  auto segA1 = [&]() {
    PIN(s1v);
    const v8h a = __builtin_bit_cast(v8h, s1v);
    re = mfma16h(a, __builtin_bit_cast(v8h, f16), z4); im = mfma16h(a, imf(f16), z4);
  };
  auto segA2 = [&]() {
    twid(re, im, t1c, t1s);
    const v8h a = pack_ri(re, im);
    re = mfma16h(__builtin_bit_cast(v8h, f16), a, z4); im = mfma16h(imf(f16), a, z4);
  };
  auto segA3 = [&]() {
    twid(re, im, t2c, t2s);
    *(LAS v4h*)(smem + L_E1 + w * E1P + o * 32 + g * 8) = pack4h(re);
    *(LAS v4h*)(smem + L_E1 + (8 + w) * E1P + o * 32 + g * 8) = pack4h(im);
  };

  if (ffn) {
    v16f acc[2][2] = {};
    float mbr[2];
    const u32 wb0 = (u32)((8 * h + q) * 256 + ((fq ^ q) << 6) + blk * 32 + pp * 8);
    const u32 r1 = 16384 + sh * 16384, r2 = 32768 - sh * 16384;
    const int xrow = sh * 64 + (l & 31);
    const u32 xb0 = (u32)(xrow * 128 + ((h ^ ((xrow >> 1) & 7)) << 4));
    auto rd = [&](Frag1& f, LAS char* sb, int i) {
      LAS char* xa = sb + (xb0 ^ (i << 5));
      LAS char* wa_ = sb + wb0 + (i << 12);
      f.w1 = cat8(trread(wa_ + r1), trread(wa_ + r1 + 1024));
      f.x0 = *(LAS v8s*)xa; f.x1 = *(LAS v8s*)(xa + 4096);
      f.w2 = cat8(trread(wa_ + r2), trread(wa_ + r2 + 1024));
    };
    const u32 one = ((l & 15) == ((l >> 4) & 1)) ? 0x3f803f80u : 0u;
    const v8s sel = __builtin_bit_cast(v8s, v4u{one, one, one, one});
    v4f csa = {0.f, 0.f, 0.f, 0.f};
    Frag1 A, B;
    WAIT_VM(7);
    BARRIER();
    STAMPT(1); STAMPC(30);
    rd(A, smem, 0);
    round1_read();
    dma2(aoff[0], aoff[1], (const char*)x + ((2 + rot) & 7) * 128, lbase + 2 * 49152 + w * 2048);
    u32 c1[4], c2[4];
    static_for<0, 8>([&](auto T) {
      constexpr int t = T, slot = t % 3, nslot = (t + 1) % 3, pslot = (t + 2) % 3;
      LAS char* sb = smem + slot * 49152;
      {
        const int kt = (t + 2 + rot) & 7;
        const char* s1 = t < 6 ? (const char*)Wg + (size_t)kt * (64 * F_ * 2) : bsrc + (size_t)((t - 6) * 3 + 1) * 131072;
        const char* s2 = t < 6 ? (const char*)Wu + (size_t)kt * (64 * F_ * 2) : bsrc + (size_t)((t - 6) * 3 + 2) * 131072;
        const u32 o2 = t < 6 ? boff : ro0, o3 = t < 6 ? boff1 : ro1;
        const u32 dst = lbase + pslot * 49152 + w * 2048;
        rd(B, sb, 1); SCHED();
        acc[0][0] = mfma32b(A.w1, A.x0, acc[0][0]); acc[0][1] = mfma32b(A.w1, A.x1, acc[0][1]);
        if constexpr (t == 0) csa = mfma16b(sel, __builtin_bit_cast(v4u, A.w1), csa);
        SCHED();
        dma1<0>(o2, s1, dst + 16384);
        if constexpr (t == 2) load_xe();
        if constexpr (t == 6) load_wt();
        acc[1][0] = mfma32b(A.w2, A.x0, acc[1][0]); acc[1][1] = mfma32b(A.w2, A.x1, acc[1][1]); SCHED();
        if constexpr (t == 0) { round1_store(); round1_max(); SCHED(); }
        if constexpr (t == 3) { trig2(); SCHED(); }
        if constexpr (t == 4) { trig3(); SCHED(); }
        rd(A, sb, 2); SCHED();
        acc[0][0] = mfma32b(B.w1, B.x0, acc[0][0]); SCHED();
        dma1<1024>(o3, s1, dst + 16384);
        acc[0][1] = mfma32b(B.w1, B.x1, acc[0][1]);
        if constexpr (t == 0) csa = mfma16b(sel, __builtin_bit_cast(v4u, B.w1), csa);
        acc[1][0] = mfma32b(B.w2, B.x0, acc[1][0]); acc[1][1] = mfma32b(B.w2, B.x1, acc[1][1]); SCHED();
        dma1<0>(o2, s2, dst + 32768);
        if constexpr (t == 6) {
          load_rs();
          if (kd == 0) {
#pragma unroll
            for (int r = 0; r < 4; ++r) ld16c(pq[r], (u32)(w * 4096 + r * 1024 + l * 16), ws + OFF_PQ);
          }
        }
        if constexpr (t == 7) { WAIT_VM(13); segA1(); SCHED(); }
        rd(B, sb, 3); SCHED();
        if constexpr (t == 7) {
#pragma unroll
          for (int i = 0; i < 4; ++i) { PIN(c1[i]); PIN(c2[i]); }
          PIN(mbr[0]); PIN(mbr[1]);
          const v8s cs1 = __builtin_bit_cast(v8s, v4u{c1[0], c1[1], c1[2], c1[3]}), cs2 = __builtin_bit_cast(v8s, v4u{c2[0], c2[1], c2[2], c2[3]});
          const u32 m0_ = pkbf(mbr[0], mbr[0]), m1_ = pkbf(mbr[1], mbr[1]);
          const v8s mb0 = __builtin_bit_cast(v8s, v4u{m0_, m0_, m0_, m0_}), mb1 = __builtin_bit_cast(v8s, v4u{m1_, m1_, m1_, m1_});
          acc[0][0] = mfma32b(cs1, mb0, acc[0][0]); acc[1][0] = mfma32b(cs2, mb0, acc[1][0]);
          acc[0][1] = mfma32b(cs1, mb1, acc[0][1]); acc[1][1] = mfma32b(cs2, mb1, acc[1][1]); SCHED();
          segA2(); SCHED();
        }
        acc[0][0] = mfma32b(A.w1, A.x0, acc[0][0]); acc[0][1] = mfma32b(A.w1, A.x1, acc[0][1]);
        if constexpr (t == 0) csa = mfma16b(sel, __builtin_bit_cast(v4u, A.w1), csa);
        SCHED();
        dma1<1024>(o3, s2, dst + 32768);
        acc[1][0] = mfma32b(A.w2, A.x0, acc[1][0]); acc[1][1] = mfma32b(A.w2, A.x1, acc[1][1]); SCHED();
      }
      if constexpr (t < 7) {
        const char* s0 = t < 5 ? (const char*)x + ((t + 3 + rot) & 7) * 128 : bsrc + (size_t)((t - 5) * 3) * 131072;
        const u32 o0 = t < 5 ? aoff[0] : ro0, o1 = t < 5 ? aoff[1] : ro1;
        const u32 dst = lbase + slot * 49152 + w * 2048;
        if constexpr (t == 0) csa = mfma16b(sel, __builtin_bit_cast(v4u, B.w1), csa);
        WAIT_VM(t == 0 ? 8 : t == 2 ? 7 : t == 6 ? 19 : 6);
        if constexpr (t == 5) { if (w == 0) check_a(*(LAS v4u*)(smem + L_FA + l * 16)); }
        BARRIER();
        if constexpr (t == 0 || t == 1 || t == 5 || t == 6) STAMPT(22 + t);
        acc[0][0] = mfma32b(B.w1, B.x0, acc[0][0]); SCHED();
        rd(A, smem + nslot * 49152, 0);
        if constexpr (t == 6) segA0();
        SCHED();
        if constexpr (t == 0) {
          if (l < 16) {
#pragma unroll
            for (int i = 0; i < 2; ++i) {
              const float hi = rbf(csa[i]);
              st4c((u32)(((j * 16 + p) * 256 + sh * 128 + fq * 32 + i * 16 + l) * 4), ws + OFF_CSP, pkbf(hi, csa[i] - hi));
            }
          }
        }
        if constexpr (t == 4) { if (w == 0) dma1s<0>((u32)(FL_A + l * 16), FL, lbase + L_FA); }
        if constexpr (t == 5) {
          const u32 co = (u32)(((j * 16 + (p & 1) + 8 * h) * 256 + fq * 32 + (l & 31)) * 4);
#pragma unroll
          for (int i = 0; i < 4; ++i) { ld4c(c1[i], co + sh * 512 + i * 2048, ws + OFF_CSP); ld4c(c2[i], co + (1 - sh) * 512 + i * 2048, ws + OFF_CSP); }
          ld4c(mbr[0], (u32)((p * 128 + xrow) * 4), ws + OFF_MB); ld4c(mbr[1], (u32)((p * 128 + xrow + 32) * 4), ws + OFF_MB);
          load_s1();
        }
        dma1<0>(o0, s0, dst);
        acc[0][1] = mfma32b(B.w1, B.x1, acc[0][1]);
        acc[1][0] = mfma32b(B.w2, B.x0, acc[1][0]);
        acc[1][1] = mfma32b(B.w2, B.x1, acc[1][1]); SCHED();
        dma1<1024>(o1, s0, dst);
        if constexpr (t == 1) { if (w == 0) { get_epoch(); signal_a(); } }
        SCHED();
      } else {
        acc[0][0] = mfma32b(B.w1, B.x0, acc[0][0]); acc[1][0] = mfma32b(B.w2, B.x0, acc[1][0]); SCHED();
        acc[0][1] = mfma32b(B.w1, B.x1, acc[0][1]); acc[1][1] = mfma32b(B.w2, B.x1, acc[1][1]); SCHED();
      }
    });
    STAMPT(2); STAMPC(31);
    auto fin = [&](const v16f& ag, const v16f& au, int rt) {
#pragma unroll
      for (int s2i = 0; s2i < 2; ++s2i) {
        v4u ov;
#pragma unroll
        for (int e = 0; e < 4; ++e) {
          const int r = s2i * 8 + 2 * e;
          v2f gg = v2f{relu(ag[r]), relu(ag[r + 1])} * v2f{au[r], au[r + 1]};
          ov[e] = pkbf(gg[0], gg[1]);
        }
        st16c((u32)(((p * 4 + sh * 2 + rt) * 112 + (j * 8 + fq * 2 + s2i)) * 1024 + l * 16), ws + OFF_H, ov);
      }
    };
    if (sh == 0) { fin(acc[0][0], acc[1][0], 0); fin(acc[0][1], acc[1][1], 1); }
    else { fin(acc[1][0], acc[0][0], 0); fin(acc[1][1], acc[0][1], 1); }
    STAMPT(6);
    segA3();
    WAIT_VM(4);
  } else {
    WAIT_VM(1);
    BARRIER();
    round1_read(); round1_store(); round1_max();
    do_tick();
    if (w == 7) {
#pragma unroll
      for (int i = 0; i < 8; ++i) PIN(sq[i]);
      float war = bflo(sq[0]) + bflo(sq[2]), wai = bflo(sq[1]) - bflo(sq[3]);
      float wbr = bflo(sq[4]) + bflo(sq[6]), wbi = bflo(sq[5]) - bflo(sq[7]);
      v4f pv = {0.25f * (war + wbr), 0.25f * (wai + wbi), 0.25f * (war - wbr), 0.25f * (wai - wbi)};
      st16c((u32)((((sp >> 2) * 4 + (l & 3)) * 64 + (sp & 3) * 16 + (l >> 2)) * 16), ws + OFF_PQ, pv);
    }
    WAIT_VM(0);
    BARRIER();
    STAMPT(1); STAMPC(30);
    if (w == 0) { if (!coord) get_epoch(); signal_a(); do_tick(); WAIT_VM(0); }
    BARRIER();
    STAMPT(24);
    dma1c((u32)(sp * 57344 + (w * 112 + l) * 64), (const char*)Wd, lbase + 49152);
    dma1c((u32)(sp * 57344 + (w * 112 + 48 + l) * 64), (const char*)Wd, lbase + 49152);
    static_for<0, 6>([&](auto N) { issueW(N); });
    load_xe();
    trig2(); trig3(); segA0();
    STAMPT(25);
    if (w == 0) {
      if (!coord) __builtin_amdgcn_s_sleep(SLEEP_A);
      check_a(v4u{~E, 0u, 0u, 0u});
    }
    STAMPT(26);
    BARRIER();
    load_s1(); load_wt(); load_rs();
    WAIT_VM(0);
    segA1(); segA2(); segA3();
    STAMPT(2); STAMPC(31);
  }
  PIN(wt); PIN(xe); PIN(rs2);
  BARRIER();
  STAMPT(5);

  const u32 trG = (u32)(kq * 4096 + (4 * h + q) * 128 + ((dh ^ ((q >> 1) & 1)) << 6) + (blk ^ kh) * 32 + pp * 8);
  v8s Wf[14][2];
  auto readW = [&](auto N) {
    constexpr int n = N;
    LAS char* sb = smem + wslot(n) + trG;
    Wf[n][0] = cat8(trread(sb), trread(sb + 1024)); Wf[n][1] = cat8(trread(sb + 2048), trread(sb + 3072));
  };
  const char* hsrc = ws + OFF_H + ((size_t)(RG * 2 + (w >> 2)) * 112 + (w & 3) * 2) * 1024;
  const u32 so0 = (u32)((l >> 3) * 4096) + swz, so1 = so0 + 32768 - 1024;
  const char* ssrc = ws + OFF_S2 + (size_t)(w * 16) * 4096 + RG * 128;
  auto issueP = [&](int n, auto HALF) {
    constexpr int hf = HALF;
    if (n < 14) dma1s<hf * 1024>((u32)(l * 16), hsrc + n * 8192, lbase + aslot(n) + w * 2048);
    else dma1s<hf * 1024>(hf ? so1 : so0, ssrc + (size_t)(n - 14) * (128 * 4096), lbase + aslot(n) + w * 2048);
  };
  const int d = ct * 64 + dh * 32 + ((l & 31) ^ (kh << 4));
  const float gst = (float)d * (1.0f / 512.0f), ga0 = (float)(16 * kq + 2 * h) * gst;
  v8h G[8];
  auto gen = [&](auto FI) {
    constexpr int f = FI;
#pragma unroll
    for (int e2 = 0; e2 < 4; ++e2) {
      float a = fmaf((float)(64 * (f >> 1) + 8 * (f & 1) + 4 * (e2 >> 1) + (e2 & 1)), gst, ga0);
      float c = cosr(a), sn = sinr(a);
      if (f == 0 && e2 == 0) { if (kq == 0 && h == 0) { c = 0.5f; sn = (d & 1) ? 0.5f : -0.5f; } }
      G[f][2 * e2] = (_Float16)c; G[f][2 * e2 + 1] = (_Float16)sn;
    }
    PIN(G[f]);
  };

  float oscale;
  const u32 fhoff = (u32)(FL_H + (RG * 16 + (min(l, 55) >> 2)) * 64 + (l & 3) * 4);
  u32 fp;
  {
    v8h a;
    {
      LAS char* rp = smem + L_E1 + (4 * (g & 1) + (o >> 2)) * E1P + (2 * w + (g >> 1)) * 32 + (o & 3) * 8;
      a = __builtin_bit_cast(v8h, cat8(trread(rp), trread(rp + 8 * E1P)));
    }
    const v4u f8 = *(LAS v4u*)(smem + L_TAB + 1024 + l * 16);
    const v2u me = *(LAS v2u*)(smem + L_MX);
    issueW(6);
    static_for<0, 3>(readW);
    const int Ex = (int)me[0] < 16 ? 16 : ((int)me[0] > 240 ? 240 : (int)me[0]);
    const float wscale = __builtin_bit_cast(float, (u32)(246 - Ex) << 23);
    oscale = __builtin_bit_cast(float, (u32)(Ex - 7) << 23);
    re = mfma16h(__builtin_bit_cast(v8h, f8), a, z4); im = mfma16h(imf(f8), a, z4);
    static_for<3, 6>(readW);
    WAIT_VM(2);
    STAMPT(7);
    if (ffn && l == 0) st4c((u32)(FL_H + ((p * 2 + sh) * 16 + j) * 64 + fq * 4), FL, me[1]);
    issueW(7);
    if (kd != 0) {
#pragma unroll
      for (int r = 0; r < 4; ++r) {
        float fr = ((r & 1) ? bfhi(wt[r >> 1]) : bflo(wt[r >> 1])) * wscale, fi = ((r & 1) ? bfhi(wt[2 + (r >> 1)]) : bflo(wt[2 + (r >> 1)])) * wscale;
        float zr = re[r], zi = im[r];
        re[r] = zr * fr - zi * fi;
        im[r] = -(zr * fi + zi * fr);
      }
    } else {
      const int ka = 2 * w + (g >> 1), kr = o + 64 * (g & 1);
#pragma unroll
      for (int r = 0; r < 4; ++r) { v2f t = {re[r], im[r]}; *(LAS v2f*)(smem + L_ZX + ka * 1024 + (kr + 16 * r) * 8) = t; }
      BARRIER();
#pragma unroll
      for (int r = 0; r < 4; ++r) {
        PIN(pq[r]);
        const int km = (2048 - (ka + 16 * (kr + 16 * r))) & 2047;
        v2f zm = *(LAS v2f*)(smem + L_ZX + (km & 15) * 1024 + (km >> 4) * 8);
        float zr = re[r], zi = im[r];
        re[r] = (zr * pq[r][0] - zi * pq[r][1] + zm[0] * pq[r][2] + zm[1] * pq[r][3]) * wscale;
        im[r] = -(zr * pq[r][1] + zi * pq[r][0] + zm[0] * pq[r][3] - zm[1] * pq[r][2]) * wscale;
      }
    }
    LAS char* E2 = smem + (kd == 0 ? L_E1 : L_E2);
    a = pack_ri(re, im);
    re = mfma16h(a, __builtin_bit_cast(v8h, f8), z4); im = mfma16h(a, imf(f8), z4);
    twid(re, im, t3c, t3s);
    *(LAS v4h*)(E2 + (o & 7) * E2P + (2 * w + (o >> 3)) * 32 + g * 8) = pack4h(re);
    *(LAS v4h*)(E2 + (o & 7) * E2P + 512 + (2 * w + (o >> 3)) * 32 + g * 8) = pack4h(im);
    BARRIER();
    STAMPT(8);
    {
      LAS char* rp = E2 + w * E2P + o * 32 + g * 8;
      a = __builtin_bit_cast(v8h, cat8(*(LAS v4s*)rp, *(LAS v4s*)(rp + 512)));
    }
    issueW(8);
    re = mfma16h(a, __builtin_bit_cast(v8h, f16), z4); im = mfma16h(a, imf(f16), z4);
    issueW(9);
    twid(re, im, t1c, t1s);
    a = pack_ri(re, im);
    re = mfma16h(__builtin_bit_cast(v8h, f16), a, z4); im = mfma16h(imf(f16), a, z4);
    issueW(10);
    st8c((u32)(2 * kd * 4096 + w * 512 + l * 8), ws + OFF_S2, pack4h(re));
    st8c((u32)((2 * kd + 1) * 4096 + w * 512 + l * 8), ws + OFF_S2, pack4h(im));
  }
  STAMPT(9);
  gen(IC<0>{}); issueW(11);
  WAIT_VM(8);
  BARRIER();
  static_for<6, 9>(readW);
  gen(IC<1>{}); issueW(12); gen(IC<2>{}); issueW(13);
  if (!ffn) {
    WAIT_VM(0);
    BARRIER();
    if (w == 0) {
      if (l == 0) st4c((u32)(FL_S + b * 4), FL, E);
      __builtin_amdgcn_s_sleep(40);
    }
  }
  if (w == 0) ld4c(fp, fhoff, FL);
  gen(IC<3>{}); gen(IC<4>{});
  WAIT_VM(0);
  if (w == 0) {
    PIN(fp);
    u32 f = fp;
    int spins = 0;
    while (!__all(f == E) && ++spins < SPIN_CAP) {
      __builtin_amdgcn_s_sleep(4);
      f = ld4c_sync(fhoff, FL);
    }
  }
  BARRIER();
  STAMPT(10);
  if (ffn && tid == 0) st4c((u32)(FL_S + b * 4), FL, E);
  issueP(0, IC<0>{}); issueP(0, IC<1>{}); issueP(1, IC<0>{}); issueP(1, IC<1>{});
  static_for<9, 14>(readW);
  gen(IC<5>{});
  issueP(2, IC<0>{}); issueP(2, IC<1>{});
  gen(IC<6>{});
  issueP(3, IC<0>{}); issueP(3, IC<1>{});
  gen(IC<7>{});

  const u32 trS = (u32)(kq * 4096 + (4 * h + q) * 128 + ((ph ^ ((q >> 1) & 1)) << 6) + blk * 32 + pp * 8);
  const u32 hb = (u32)(ph * 8192 + kq * 2048 + l * 16);
  v16f accG[2] = {}, accS[2] = {};
  v8s fa[8], fb[8];
  v4u fs;
  float gv[8];
  const u32 xw = (u32)(w * 6144 + l * 16);
  auto xchg_out = [&](const v16f (&ac)[2], int base) {
    LAS char* X = smem + base + xw;
#pragma unroll
    for (int i = 0; i < 6; ++i) {
      const v16f& a = ac[(i + 2) >> 2]; const int r = ((i + 2) & 3) * 4;
      *(LAS v4f*)(X + i * 1024) = v4f{a[r], a[r + 1], a[r + 2], a[r + 3]};
    }
  };
  auto xchg_in = [&](const v16f (&ac)[2], int base, float (&o8)[8]) {
    v4f s0 = {ac[0][0], ac[0][1], ac[0][2], ac[0][3]}, s1 = {ac[0][4], ac[0][5], ac[0][6], ac[0][7]};
#pragma unroll
    for (int x_ = 1; x_ < 4; ++x_) {
      LAS char* X = smem + base + (w ^ (2 * x_)) * 6144 + (x_ - 1) * 2048 + l * 16;
      s0 += *(LAS v4f*)X; s1 += *(LAS v4f*)(X + 1024);
    }
#pragma unroll
    for (int e = 0; e < 4; ++e) { o8[e] = s0[e]; o8[4 + e] = s1[e]; }
  };
  v4f xg[6];
  float ov[8], ex[8];
  auto sadd = [](float a, float b) { float r; asm("v_add_f32 %0, %1, %2" : "=v"(r) : "v"(a), "v"(b)); return r; };
  auto smul = [](float a, float b) { float r; asm("v_mul_f32 %0, %1, %2" : "=v"(r) : "v"(a), "v"(b)); return r; };
  auto silu_step = [&](int i) {
    if (i < 3) {
#pragma unroll
      for (int e = 0; e < 8; ++e) { ov[e] = sadd(i == 0 ? accG[0][e] : ov[e], xg[2 * i + (e >> 2)][e & 3]); PIN(ov[e]); }
    } else if (i == 3) {
#pragma unroll
      for (int e = 0; e < 8; e += 2) { u32 t = pkbf_o(smul(ov[e], rs2), smul(ov[e + 1], rs2)); ov[e] = bflo(t); ov[e + 1] = bfhi(t); PIN(ov[e]); PIN(ov[e + 1]); }
    } else if (i == 4) {
#pragma unroll
      for (int e = 0; e < 8; ++e) { ex[e] = __builtin_amdgcn_exp2f(ov[e] * -1.44269504088896f); PIN(ex[e]); }
    } else if (i == 5) {
#pragma unroll
      for (int e = 0; e < 8; ++e) { ex[e] = __builtin_amdgcn_rcpf(1.0f + ex[e]); PIN(ex[e]); }
    } else if (i == 6) {
#pragma unroll
      for (int e = 0; e < 8; e += 2) { u32 t = pkbf_o(ov[e] * ex[e], ov[e + 1] * ex[e + 1]); gv[e] = bflo(t); gv[e + 1] = bfhi(t); PIN(gv[e]); PIN(gv[e + 1]); }
    }
  };
  static_for<0, 10>([&](auto V) {
    constexpr int v = V;
    v8s(&cur)[8] = (v & 1) ? fb : fa;
    v8s(&prv)[8] = (v & 1) ? fa : fb;
    auto mmp = [&](int s) {
      if constexpr (v >= 8) {
        accS[0] = mfma32h(G[(v - 8) * 4 + s], prv[2 * s], accS[0]); accS[1] = mfma32h(G[(v - 8) * 4 + s], prv[2 * s + 1], accS[1]);
        SCHED();
        if ((v - 8) * 4 + s >= 1) silu_step((v - 8) * 4 + s - 1);
      } else if constexpr (v >= 1) {
        accG[0] = mfma32b(Wf[2 * (v - 1) + (s >> 1)][s & 1], prv[2 * s], accG[0]);
        accG[1] = mfma32b(Wf[2 * (v - 1) + (s >> 1)][s & 1], prv[2 * s + 1], accG[1]);
      }
      SCHED();
    };
    if constexpr (v < 9) {
      WAIT_VM(v <= 7 ? 4 : 0);
      if constexpr (v == 5) { if (w == 0) { PIN(fs); poll4(fs, (u32)(FL_S + l * 16)); } }
      BARRIER();
      if constexpr (v == 0 || v == 8) STAMPT(11 + v);
      if constexpr (v == 4) { if (w == 0) ld16c(fs, (u32)(FL_S + l * 16), FL); }
      if constexpr (v == 8) {
#pragma unroll
        for (int x_ = 1; x_ < 4; ++x_) {
          LAS char* X = smem + 49152 + (w ^ (2 * x_)) * 6144 + (x_ - 1) * 2048 + l * 16;
          xg[2 * x_ - 2] = *(LAS v4f*)X; xg[2 * x_ - 1] = *(LAS v4f*)(X + 1024);
        }
        SCHED();
      }
      mmp(0);
#pragma unroll
      for (int s = 0; s < 4; ++s) {
        if constexpr (v >= 7) {
          LAS char* sb = smem + aslot(2 * v + (s >> 1)) + (s & 1) * 2048;
          cur[2 * s] = cat8(trread(sb + trS), trread(sb + trS + 1024));
          cur[2 * s + 1] = cat8(trread(sb + (trS ^ 64)), trread(sb + (trS ^ 64) + 1024));
        } else {
          LAS char* sb = smem + aslot(2 * v + (s >> 1)) + (s & 1) * 1024;
          cur[2 * s] = *(LAS v8s*)(sb + hb);
          cur[2 * s + 1] = *(LAS v8s*)(sb + (hb ^ 8192));
        }
      }
      SCHED();
      if constexpr (v <= 6) issueP(2 * v + 4, IC<0>{});
      mmp(1);
      if constexpr (v <= 6) issueP(2 * v + 4, IC<1>{});
      mmp(2);
      if constexpr (v <= 6) issueP(2 * v + 5, IC<0>{});
      mmp(3);
      if constexpr (v <= 6) issueP(2 * v + 5, IC<1>{});
    } else {
      mmp(0); mmp(1); mmp(2); mmp(3);
    }
    if constexpr (v == 7) xchg_out(accG, 49152);
    SCHED();
  });
  xchg_out(accS, 0);
  BARRIER();
  {
    float o8[8], pv[8];
    xchg_in(accS, 0, o8);
#pragma unroll
    for (int e = 0; e < 8; e += 2) { u32 t = pkbf_o(o8[e] * oscale, o8[e + 1] * oscale); pv[e] = bflo(t) * gv[e]; pv[e + 1] = bfhi(t) * gv[e + 1]; }
    u32 a0 = pkbf(pv[0], pv[1]), a1 = pkbf(pv[2], pv[3]), b0 = pkbf(pv[4], pv[5]), b1 = pkbf(pv[6], pv[7]);
    auto r0 = __builtin_amdgcn_permlane32_swap(a0, b0, false, false);
    auto r1 = __builtin_amdgcn_permlane32_swap(a1, b1, false, false);
    v4u pk = {r0[0], r1[0], r0[1], r1[1]};
    v4u ov4;
#pragma unroll
    for (int e = 0; e < 4; ++e) ov4[e] = pkbf(bflo(xe[e]) + bflo(pk[e]), bfhi(xe[e]) + bfhi(pk[e]));
    *(v4u*)((char*)out + eoff) = ov4;
  }
  asm volatile("km_pf_b:");
  STAMPT(20);
  STAMPT(21);
  STAMP_OUT(ws);
}

extern "C" void kernel_launch(void* const* d_in, const int* in_sizes, int n_in, void* d_out, int out_size, void* d_ws,
                              size_t ws_size, hipStream_t stream) {
  kmain<<<256, 512, 0, stream>>>((const u16*)d_in[0], (const u16*)d_in[1], (const u16*)d_in[2], (const u16*)d_in[4],
                                 (const u16*)d_in[5], (const u16*)d_in[6], (char*)d_ws + WS_BASE(ws_size), (u16*)d_out);
}
```

```cpp
#include <hip/hip_runtime.h>
#include <stdint.h>

typedef unsigned short u16;
typedef unsigned int u32;
typedef unsigned long long u64;
typedef short v4s __attribute__((ext_vector_type(4)));
typedef short v8s __attribute__((ext_vector_type(8)));
typedef _Float16 v4h __attribute__((ext_vector_type(4)));
typedef _Float16 v8h __attribute__((ext_vector_type(8)));
typedef __bf16 v2b __attribute__((ext_vector_type(2)));
typedef __bf16 v8b __attribute__((ext_vector_type(8)));
typedef float v2f __attribute__((ext_vector_type(2)));
typedef float v4f __attribute__((ext_vector_type(4)));
typedef float v16f __attribute__((ext_vector_type(16)));
typedef u32 v2u __attribute__((ext_vector_type(2)));
typedef u32 v4u __attribute__((ext_vector_type(4)));

#define LAS __attribute__((address_space(3)))
#define GAS __attribute__((address_space(1)))
#define DEVI __device__ __forceinline__

constexpr int D_ = 512, F_ = 1792, DF_ = 257;
constexpr size_t OFF_H = (size_t)24 << 20;
constexpr size_t OFF_S1 = (size_t)32 << 20;
constexpr size_t OFF_WT = (size_t)34 << 20;
constexpr size_t OFF_S2 = (size_t)37 << 20;
constexpr size_t OFF_M = (size_t)39 << 20;
constexpr size_t OFF_FL = OFF_M;
constexpr u32 FL_S = 0, FL_A = 1024, FL_B = 2048, FL_H = 4096;
constexpr size_t OFF_BC = OFF_M + 49152;
constexpr size_t OFF_MB = OFF_M + 98304;
constexpr size_t OFF_RS2 = OFF_M + 106496;
constexpr size_t OFF_PQ = OFF_M + 65536;
constexpr size_t OFF_CSP = OFF_M + 131072;
constexpr int SPIN_CAP = 1 << 16;
#define SLEEP_A 48

#define WS_BASE(n) ((((n) - ((size_t)40 << 20))) & ~(size_t)0x1fffff)
#define STAMP 1
#define PROBE 0
#define PROBE_B 45
constexpr size_t OFF_STAMP = 0;
#define STAMP_DECL u32 stamps_[32] = {}
#define STAMPT(id) do { u64 t_; asm volatile("s_memrealtime %0\n\ts_waitcnt lgkmcnt(0)" : "=s"(t_) :: "memory"); stamps_[id] = (u32)t_; } while (0)
#define STAMPC(id) do { u64 t_; asm volatile("s_memtime %0\n\ts_waitcnt lgkmcnt(0)" : "=s"(t_) :: "memory"); stamps_[id] = (u32)t_; } while (0)
#define STAMP_OUT(wsp) do { if (blockIdx.x == PROBE_B && threadIdx.x < 64) { u32 dl_ = 500u + (PROBE == 99 ? (stamps_[31] - stamps_[30]) / 20u : PROBE == 98 ? (stamps_[2] - stamps_[1]) : (stamps_[PROBE % 32] - stamps_[0])); u32 t1_; do { u64 t_; asm volatile("s_memrealtime %0\n\ts_waitcnt lgkmcnt(0)" : "=s"(t_) :: "memory"); t1_ = (u32)t_; __builtin_amdgcn_s_sleep(1); } while (t1_ - stamps_[21] < dl_); } } while (0)

template <int I> struct IC { static constexpr int v = I; constexpr operator int() const { return I; } };
template <int B, int E, class Fn> DEVI void static_for(Fn&& f) {
  if constexpr (B < E) { f(IC<B>{}); static_for<B + 1, E>(f); }
}
#define WAIT_VM(n) asm volatile("s_waitcnt vmcnt(%0)" ::"n"(n) : "memory")
#define BARRIER() asm volatile("s_waitcnt lgkmcnt(0)\n\ts_barrier" ::: "memory")
#define SCHED() __builtin_amdgcn_sched_barrier(0)
#define PIN(x) asm volatile("" : "+v"(x))

DEVI int s_of_pi(int pi) {
  int RG = pi >> 6, i = pi & 63;
  return 128 * (4 * (RG & 3) + (i & 3)) + 8 * (i >> 2) + (RG >> 2);
}
DEVI void dma2(u32 voff0, u32 voff1, const char* sbase, u32 lds) {
  asm volatile("s_mov_b32 m0, %3\n\ts_nop 0\n\tglobal_load_lds_dwordx4 %0, %2\n\tglobal_load_lds_dwordx4 %1, %2 offset:1024" ::"v"(voff0), "v"(voff1), "s"(sbase), "s"(lds) : "memory");
}
DEVI void dma2c(u32 voff0, u32 voff1, const char* sbase, u32 lds) {
  asm volatile("s_mov_b32 m0, %3\n\ts_nop 0\n\tglobal_load_lds_dwordx4 %0, %2 sc1\n\tglobal_load_lds_dwordx4 %1, %2 offset:1024 sc1" ::"v"(voff0), "v"(voff1), "s"(sbase), "s"(lds) : "memory");
}
template <int OFF> DEVI void dma1(u32 voff, const char* sbase, u32 lds) {
  asm volatile("s_mov_b32 m0, %2\n\ts_nop 0\n\tglobal_load_lds_dwordx4 %0, %1 offset:%3" ::"v"(voff), "s"(sbase), "s"(lds), "n"(OFF) : "memory");
}
template <int OFF> DEVI void dma1s(u32 voff, const char* sbase, u32 lds) {
  asm volatile("s_mov_b32 m0, %2\n\ts_nop 0\n\tglobal_load_lds_dwordx4 %0, %1 offset:%3 sc1" ::"v"(voff), "s"(sbase), "s"(lds), "n"(OFF) : "memory");
}
DEVI void dma1c(u32 voff, const char* sbase, u32 lds) {
  asm volatile("s_mov_b32 m0, %2\n\ts_nop 0\n\tglobal_load_lds_dword %0, %1 sc1" ::"v"(voff), "s"(sbase), "s"(lds) : "memory");
}
DEVI v2u ld8c_sync(u32 voff, const char* sb) { v2u d; asm volatile("global_load_dwordx2 %0, %1, %2 sc1\n\ts_waitcnt vmcnt(0)" : "=&v"(d) : "v"(voff), "s"(sb) : "memory"); return d; }
DEVI u32 ld4c_sync(u32 voff, const char* sb) { u32 d; asm volatile("global_load_dword %0, %1, %2 sc1\n\ts_waitcnt vmcnt(0)" : "=&v"(d) : "v"(voff), "s"(sb) : "memory"); return d; }
DEVI v4u ld16c_sync(u32 voff, const char* sb) { v4u d; asm volatile("global_load_dwordx4 %0, %1, %2 sc1\n\ts_waitcnt vmcnt(0)" : "=&v"(d) : "v"(voff), "s"(sb) : "memory"); return d; }
template <class T> DEVI void ld16c(T& d, u32 voff, const char* sb) { asm volatile("global_load_dwordx4 %0, %1, %2 sc1" : "=&v"(d) : "v"(voff), "s"(sb) : "memory"); }
template <class T> DEVI void ld8c(T& d, u32 voff, const char* sb) { asm volatile("global_load_dwordx2 %0, %1, %2 sc1" : "=&v"(d) : "v"(voff), "s"(sb) : "memory"); }
template <class T> DEVI void ld4c(T& d, u32 voff, const char* sb) { asm volatile("global_load_dword %0, %1, %2 sc1" : "=&v"(d) : "v"(voff), "s"(sb) : "memory"); }
template <class T> DEVI void ld16(T& d, u32 voff, const char* sb) { asm volatile("global_load_dwordx4 %0, %1, %2" : "=&v"(d) : "v"(voff), "s"(sb) : "memory"); }
DEVI void ld2(u32& d, u32 voff, const char* sb) { asm volatile("global_load_ushort %0, %1, %2" : "=&v"(d) : "v"(voff), "s"(sb) : "memory"); }
template <class T> DEVI void st16c(u32 voff, const char* sb, T v) { asm volatile("global_store_dwordx4 %0, %1, %2 sc1\n\ts_nop 1" ::"v"(voff), "v"(v), "s"(sb) : "memory"); }
template <class T> DEVI void st8c(u32 voff, const char* sb, T v) { asm volatile("global_store_dwordx2 %0, %1, %2 sc1" ::"v"(voff), "v"(v), "s"(sb) : "memory"); }
template <class T> DEVI void st4c(u32 voff, const char* sb, T v) { asm volatile("global_store_dword %0, %1, %2 sc1" ::"v"(voff), "v"(v), "s"(sb) : "memory"); }

DEVI v4s trread(LAS char* p) { return __builtin_amdgcn_ds_read_tr16_b64_v4i16((LAS v4s*)p); }
DEVI v8s cat8(v4s a, v4s b) { return __builtin_shufflevector(a, b, 0, 1, 2, 3, 4, 5, 6, 7); }
DEVI float bflo(u32 v) { return __builtin_bit_cast(float, v << 16); }
DEVI float bfhi(u32 v) { return __builtin_bit_cast(float, v & 0xffff0000u); }
DEVI float relu(float v) { return __builtin_bit_cast(float, max(__builtin_bit_cast(int, v), 0)); }
DEVI float rbf(float v) { return (float)(__bf16)v; }
DEVI u32 pkbf(float a, float b) { v2b p = {(__bf16)a, (__bf16)b}; return __builtin_bit_cast(u32, p); }
DEVI u32 pkbf_o(float a, float b) { u32 r = pkbf(a, b); asm("" : "+v"(r)); return r; }
DEVI float cosr(float rev) { return __builtin_amdgcn_cosf(rev); }
DEVI float sinr(float rev) { return __builtin_amdgcn_sinf(rev); }
DEVI v4f mfma16h(v8h a, v8h b, v4f c) { return __builtin_amdgcn_mfma_f32_16x16x32_f16(a, b, c, 0, 0, 0); }
DEVI v4f mfma16b(v8s a, v4u b, v4f c) { return __builtin_amdgcn_mfma_f32_16x16x32_bf16(__builtin_bit_cast(v8b, a), __builtin_bit_cast(v8b, b), c, 0, 0, 0); }
DEVI v16f mfma32b(v8s a, v8s b, v16f c) { return __builtin_amdgcn_mfma_f32_32x32x16_bf16(__builtin_bit_cast(v8b, a), __builtin_bit_cast(v8b, b), c, 0, 0, 0); }
DEVI v16f mfma32h(v8h a, v8s b, v16f c) { return __builtin_amdgcn_mfma_f32_32x32x16_f16(a, __builtin_bit_cast(v8h, b), c, 0, 0, 0); }
DEVI v8h pack_ri(v4f re, v4f im) {
  v8h r;
  r[0] = (_Float16)re[0]; r[1] = (_Float16)re[1]; r[2] = (_Float16)re[2]; r[3] = (_Float16)re[3];
  r[4] = (_Float16)im[0]; r[5] = (_Float16)im[1]; r[6] = (_Float16)im[2]; r[7] = (_Float16)im[3];
  return r;
}
DEVI v4h pack4h(v4f a) { v4h r; r[0] = (_Float16)a[0]; r[1] = (_Float16)a[1]; r[2] = (_Float16)a[2]; r[3] = (_Float16)a[3]; return r; }
DEVI void twid(v4f& re, v4f& im, const v4f& c, const v4f& s) {
#pragma unroll
  for (int r = 0; r < 4; ++r) { float a = re[r], b = im[r]; re[r] = a * c[r] + b * s[r]; im[r] = b * c[r] - a * s[r]; }
}
template <int CTRL> DEVI float dppadd(float v) {
  return v + __builtin_bit_cast(float, __builtin_amdgcn_update_dpp(0, __builtin_bit_cast(int, v), CTRL, 0xf, 0xf, true));
}
DEVI float wave_sum(float v) {
  v = dppadd<0xB1>(v); v = dppadd<0x4E>(v); v = dppadd<0x141>(v); v = dppadd<0x140>(v);
  int bb = __builtin_bit_cast(int, v);
  float a0 = __builtin_bit_cast(float, __builtin_amdgcn_readlane(bb, 0)), a1 = __builtin_bit_cast(float, __builtin_amdgcn_readlane(bb, 16));
  float a2 = __builtin_bit_cast(float, __builtin_amdgcn_readlane(bb, 32)), a3 = __builtin_bit_cast(float, __builtin_amdgcn_readlane(bb, 48));
  return (a0 + a1) + (a2 + a3);
}
DEVI float dot2ones(u32 v, float acc) {
  const v2b ones = {(__bf16)1.0f, (__bf16)1.0f};
  return __builtin_amdgcn_fdot2_f32_bf16(__builtin_bit_cast(v2b, v), ones, acc, false);
}
template <int CTRL> DEVI u32 dppmax(u32 v) { return max(v, (u32)__builtin_amdgcn_update_dpp(0, (int)v, CTRL, 0xf, 0xf, true)); }
DEVI u32 wave_max(u32 v) {
  v = dppmax<0xB1>(v); v = dppmax<0x4E>(v); v = dppmax<0x141>(v); v = dppmax<0x140>(v);
  u32 a0 = __builtin_amdgcn_readlane(v, 0), a1 = __builtin_amdgcn_readlane(v, 16), a2 = __builtin_amdgcn_readlane(v, 32), a3 = __builtin_amdgcn_readlane(v, 48);
  return max(max(a0, a1), max(a2, a3));
}

struct Frag1 { v8s x0, x1, w1, w2; };

constexpr int L_SCR = 147456;
constexpr int L_CS = L_SCR, L_RED = L_SCR + 1024, L_MX = L_SCR + 1088, L_BC0 = L_SCR + 2048, L_BC1 = L_SCR + 2304;
constexpr int L_TAB = L_SCR + 3072;
constexpr int L_T = L_SCR + 7168, L_FA = L_SCR, L_E1 = L_T, L_E2 = 81920, L_ZX = 81920;
constexpr int L_C1 = 98304, L_IMG = L_C1 + 9216;
constexpr int L_FR = 131072;
constexpr int E1P = 544, E2P = 1024 + 72;
DEVI constexpr int wslot(int n) {
  constexpr int u[14] = {6, 7, 8, 0, 1, 2, 3, 4, 6, 7, 8, 0, 1, 2};
  return u[n] * 16384;
}
DEVI constexpr int aslot(int n) {
  constexpr int u[18] = {3, 4, 5, 9, 2, 6, 7, 8, 0, 1, 3, 4, 5, 9, 2, 6, 7, 8};
  return u[n] * 16384;
}

__global__ __launch_bounds__(512, 2) void kmain(const u16* __restrict__ x, const u16* __restrict__ wr, const u16* __restrict__ wi,
                                                const u16* __restrict__ Wg, const u16* __restrict__ Wu, const u16* __restrict__ Wd,
                                                char* __restrict__ ws, u16* __restrict__ out) {
  __shared__ __attribute__((aligned(16))) char smem_[163840];
  LAS char* smem = (LAS char*)smem_;
  const u32 lbase = (u32)(size_t)smem;
  const int tid = threadIdx.x, l = tid & 63;
  const int w = __builtin_amdgcn_readfirstlane(tid >> 6);
  const int b = blockIdx.x, xcd = b & 7, idx = b >> 3;
  const bool ffn = idx < 28;
  const int g = l >> 4, o = l & 15;
  const int h = l >> 5, blk = (l >> 4) & 1, q = (l & 15) >> 2, pp = l & 3;
  const char* FL = ws + OFF_FL;
  const char* BC = ws + OFF_BC;
  STAMP_DECL; STAMPT(0);

  const int pi = b * 8 + w;
  v4u xr;
  {
    u64 pc; u32 len;
    asm volatile("s_getpc_b64 %0\nkm_pf_a:\n\ts_mov_b32 %1, km_pf_b-km_pf_a" : "=s"(pc), "=s"(len));
    dma1<0>(min((u32)(((idx & 3) * 8 + w) * 1024 + l * 16), len) & ~15u, (const char*)(pc & ~15ull), lbase + L_IMG + w * 1024);
  }
  ld16(xr, (u32)(s_of_pi(pi) * 1024 + l * 16), (const char*)x);
  if (w == 0) dma1c((u32)((l & 1) * 4), BC, lbase + L_BC0);
  const int fkb = (b & 7) * 2 + (w >> 2), fcq = w & 3;
  {
    const u32 ks = (u32)((idx >> 1) + 16 * fkb + 256 * (4 * (idx & 1) + ((l >> 3) & 3)));
    const u32 base = ((ks * (DF_ * 2)) & ~15u) + fcq * 128;
    const u32 dlt = (u32)((const char*)wi - (const char*)wr);
    (void)dlt;
    const u32 c0 = ((l & 7) ^ (((l >> 4) & 1) << 2)) << 4;
    if (l < 32) dma1<0>(base + c0, (const char*)wr, lbase + L_FR + w * 2048);
    else dma1<0>(base + c0, (const char*)wi, lbase + L_FR + w * 2048);
    if (fcq == 3) {
      const u32 t0 = min(base + 128 + (c0 & 16), (u32)(2048 * DF_ * 2 - 16));
      if (l < 32) dma1<0>(t0, (const char*)wr, lbase + L_FR + w * 2048 + 1024);
      else dma1<0>(t0, (const char*)wi, lbase + L_FR + w * 2048 + 1024);
    }
  }
  const int p = (xcd >> 1) * 4 + idx / 7, j = (xcd & 1) * 7 + idx % 7;
  const int sh = w >> 2, fq = w & 3;
  u32 aoff[2];
#pragma unroll
  for (int i = 0; i < 2; ++i) {
    int row = (2 * w + i) * 8 + (l >> 3);
    aoff[i] = s_of_pi((p & 15) * 128 + row) * 1024 + (((l & 7) ^ ((row >> 1) & 7)) << 4) - i * 1024;
  }
  const u32 boff = (u32)(2 * w * 4 + (l >> 4)) * (F_ * 2) + j * 256 + (((l & 15) ^ ((l >> 4) << 2)) << 4);
  const u32 boff1 = boff + 4 * F_ * 2 - 1024;
  const int rot = p >> 1;
  auto issue = [&](int t, int slot) {
    const int kt = (t + rot) & 7;
    u32 dst = lbase + slot * 49152 + w * 2048;
    dma2(aoff[0], aoff[1], (const char*)x + kt * 128, dst);
    dma2(boff, boff1, (const char*)Wg + (size_t)kt * (64 * F_ * 2), dst + 16384);
    dma2(boff, boff1, (const char*)Wu + (size_t)kt * (64 * F_ * 2), dst + 32768);
  };
  const int sp = xcd * 4 + (idx - 28);
  if (ffn) issue(0, 0);

  u32 E = 0, tick = 0;
  const bool coord = (b == 224) && (w == 0);
  auto do_tick = [&]() { if (coord) { ++tick; if (l == 0) st8c(0u, BC, v2u{E, tick}); } };
  auto get_epoch = [&]() {
    const v2u b0 = *(LAS v2u*)(smem + L_BC0);
    v2u b1 = *(LAS v2u*)(smem + L_BC1);
    int spins = 0;
    while (__builtin_amdgcn_readfirstlane((b0[0] ^ b1[0]) | (b0[1] ^ b1[1])) == 0 && ++spins < SPIN_CAP) {
      __builtin_amdgcn_s_sleep(4);
      b1 = ld8c_sync(0u, BC);
    }
    E = __builtin_amdgcn_readfirstlane(b1[0]);
  };
  auto signal_a = [&]() { if (l == 0) { st4c((u32)(FL_A + b * 4), FL, E); *(LAS u32*)(smem + L_MX + 4) = E; } };
  auto signal_b = [&]() {
    v4u r0 = *(LAS v4u*)(smem + L_RED), r1 = *(LAS v4u*)(smem + L_RED + 16);
    u32 m = max(max(max(r0[0], r0[1]), max(r0[2], r0[3])), max(max(r1[0], r1[1]), max(r1[2], r1[3])));
    if (l == 0) st4c((u32)(FL_B + b * 4), FL, E | (m >> 7));
  };
  auto poll4 = [&](v4u f, u32 voff) -> v4u {
    int spins = 0;
    while (!__all((((f[0] ^ E) | (f[1] ^ E) | (f[2] ^ E) | (f[3] ^ E)) >> 8) == 0) && ++spins < SPIN_CAP) {
      do_tick();
      __builtin_amdgcn_s_sleep(12);
      f = ld16c_sync(voff, FL);
    }
    return f;
  };
  auto check_b = [&](v4u f) {
    f = poll4(f, (u32)(FL_B + l * 16));
    u32 m = wave_max(max(max(f[0], f[1]), max(f[2], f[3]))) & 0xffu;
    if (l == 0) *(LAS u32*)(smem + L_MX) = m;
  };
  if (coord) {
#pragma unroll
    for (int i = 0; i < 6; ++i) dma2c(i > 1 ? (u32)(FL_H + ((i - 2) * 128 + l) * 64) : (u32)(i * 2048 + l * 16), i > 1 ? (u32)(FL_H + ((i - 2) * 128 + 64 + l) * 64 - 1024) : (u32)(i * 2048 + l * 16), FL, lbase + i * 2048);
  }
  u32 sq[8];
  if (!ffn && w == 7) {
    const int kss = (sp >> 1) + 16 * (l >> 2) + 256 * (4 * (sp & 1) + (l & 3)), km = (2048 - kss) & 2047;
    const u32 o1 = (u32)(kss * DF_ * 2), o2 = (u32)(km * DF_ * 2);
    ld2(sq[0], o1, (const char*)wr); ld2(sq[1], o1, (const char*)wi); ld2(sq[2], o2, (const char*)wr); ld2(sq[3], o2, (const char*)wi);
    ld2(sq[4], o1 + 512, (const char*)wr); ld2(sq[5], o1 + 512, (const char*)wi); ld2(sq[6], o2 + 512, (const char*)wr); ld2(sq[7], o2 + 512, (const char*)wi);
  }

  {
    const float fo = (float)o, fg = (float)(4 * g);
    if (w < 2) {
      const float st = fo * (1.0f / 32.0f), a0 = fmaf(fg, st, w ? 0.25f : 0.f);
      v4u hi, lo;
#pragma unroll
      for (int e2 = 0; e2 < 4; ++e2) {
        float c0 = cosr(fmaf((float)(16 * (e2 >> 1) + 2 * (e2 & 1)), st, a0)), c1 = cosr(fmaf((float)(16 * (e2 >> 1) + 2 * (e2 & 1) + 1), st, a0));
        u32 ch = pkbf_o(c0, c1);
        hi[e2] = ch; lo[e2] = pkbf(c0 - bflo(ch), c1 - bfhi(ch));
      }
      *(LAS v4u*)(smem + L_C1 + w * 2048 + l * 16) = hi; *(LAS v4u*)(smem + L_C1 + w * 2048 + 1024 + l * 16) = lo;
    } else if (w < 5) {
      const float st = w == 2 ? (float)(o & 7) * (1.0f / 16.0f) : fo * (w == 3 ? 1.0f / 16.0f : 1.0f / 8.0f);
      const float a0 = (w == 2 && o >= 8) ? 0.25f : 0.f;
      const float sc = w == 2 ? 0.0625f : ((w == 4 && (g >> 1) != (o >> 3)) ? 0.f : 1.0f);
      v8h m;
#pragma unroll
      for (int e = 0; e < 4; ++e) { float a = fmaf(fg + (float)e, st, a0); m[e] = (_Float16)(sc * cosr(a)); m[4 + e] = (_Float16)(sc * sinr(a)); }
      *(LAS v8h*)(smem + (w == 2 ? L_C1 + 4096 : L_TAB + (w - 3) * 1024) + l * 16) = m;
    } else {
      const float st = w == 7 ? fo * (1.0f / 256.0f) : (fo + (w == 6 ? 16.f : 0.f)) * (1.0f / 512.0f);
      v4f c, sn;
#pragma unroll
      for (int e = 0; e < 4; ++e) { float a = (fg + (float)e) * st; c[e] = cosr(a); sn[e] = sinr(a); }
      LAS char* dst = smem + (w == 7 ? L_TAB + 2048 : L_C1 + 5120 + (w - 5) * 2048) + l * 16;
      *(LAS v4f*)dst = c; *(LAS v4f*)(dst + 1024) = sn;
    }
    BARRIER();
    LAS char* C1 = smem + L_C1 + l * 16;
    const v4u Fhr = *(LAS v4u*)C1, Flr = *(LAS v4u*)(C1 + 1024), Fhi = *(LAS v4u*)(C1 + 2048), Fli = *(LAS v4u*)(C1 + 3072);
    const v8h M2 = *(LAS v8h*)(C1 + 4096);
    v4f twc[2], tws[2];
#pragma unroll
    for (int u = 0; u < 2; ++u) { twc[u] = *(LAS v4f*)(C1 + 5120 + u * 2048); tws[u] = *(LAS v4f*)(C1 + 6144 + u * 2048); }
    if (ffn) WAIT_VM(8); else if (w == 7) WAIT_VM(10); else WAIT_VM(2);
    if (coord) {
      WAIT_VM(0);
      v4u f[12];
#pragma unroll
      for (int i = 0; i < 12; ++i) f[i] = *(LAS v4u*)(smem + i * 1024 + l * 16);
      u32 c = __builtin_amdgcn_readfirstlane(*(LAS u32*)(smem + L_BC0)) >> 8;
      bool hit;
      do {
        c = (c + 1) & 0xffffffu; hit = false;
#pragma unroll
        for (int i = 0; i < 12; ++i) hit |= ((f[i][0] >> 8) == c) | ((f[i][1] >> 8) == c) | ((f[i][2] >> 8) == c) | ((f[i][3] >> 8) == c);
      } while (__any(hit));
      E = c << 8;
      do_tick();
    }
    PIN(xr);
    STAMPT(3);
    const float c0 = bflo(__builtin_amdgcn_readfirstlane(xr[0]));
    v2f xv[4], s2 = {0.f, 0.f}, q2 = {0.f, 0.f};
#pragma unroll
    for (int e = 0; e < 4; ++e) {
      xv[e] = v2f{bflo(xr[e]), bfhi(xr[e])};
      v2f t = xv[e] - v2f{c0, c0};
      s2 += t; q2 = __builtin_elementwise_fma(t, t, q2);
    }
    const float dmean = wave_sum(s2[0] + s2[1]) * (1.0f / 512.0f), msq = wave_sum(q2[0] + q2[1]) * (1.0f / 512.0f);
    do_tick();
    const float mb = rbf(c0 + dmean);
    const float var = rbf(fmaxf(msq - dmean * dmean, 0.f));
    const float sd = rbf(__builtin_amdgcn_sqrtf(rbf(var + 1e-5f)));
    const float rs = __builtin_amdgcn_rcpf(sd);
#pragma unroll
    for (int e = 0; e < 4; ++e) {
      v2f t = xv[e] - v2f{mb, mb};
      u32 tb = pkbf_o(t[0], t[1]);
      t = v2f{bflo(tb), bfhi(tb)} * v2f{rs, rs};
      xr[e] = pkbf(t[0], t[1]);
    }
    *(LAS v4u*)(smem + L_IMG + w * 1024 + l * 16) = xr;
    if (l < 2) st4c((u32)(pi * 4 + l * 8192), ws + OFF_MB, l ? rs * rs : -mb);
    const v8s xf = cat8(trread(smem + L_IMG + w * 1024 + l * 8), trread(smem + L_IMG + w * 1024 + 512 + l * 8));
    const int kt1 = (1 + rot) & 7;
    const u32 dst1 = lbase + 49152 + w * 2048;
    if (ffn) dma2(aoff[0], aoff[1], (const char*)x + kt1 * 128, dst1);
    const v8s xg_ = __builtin_bit_cast(v8s, __builtin_bit_cast(v4u, xf) ^ v4u{0x80000000u, 0x80000000u, 0x80000000u, 0x80000000u});
    const v4f z = {0.f, 0.f, 0.f, 0.f};
    v4f dre[2], dim[2];
#pragma unroll
    for (int u = 0; u < 2; ++u) { dre[u] = mfma16b(u ? xg_ : xf, Flr, z); dim[u] = mfma16b(u ? xg_ : xf, Fli, z); }
#pragma unroll
    for (int u = 0; u < 2; ++u) { dre[u] = mfma16b(u ? xg_ : xf, Fhr, dre[u]); dim[u] = mfma16b(u ? xg_ : xf, Fhi, dim[u]); }
#pragma unroll
    for (int u = 0; u < 2; ++u) {
      twid(dre[u], dim[u], twc[u], tws[u]);
      const v8h bb = pack_ri(dre[u], dim[u]);
      v4f d2 = mfma16h(bb, M2, z);
      if (u == 0) {
        const u32 nv = o == 8 ? 0xac002c00u : 0u;
        v4f n = mfma16h(bb, __builtin_bit_cast(v8h, v4u{nv, nv, 0u, 0u}), z);
        if (l == 8) d2[0] = n[0];
      }
      *(LAS v4h*)(smem + L_T + ((o >> 3) * 8 + w) * 544 + (o & 7) * 64 + u * 32 + g * 8) = pack4h(d2);
    }
    if (ffn) {
      dma2(boff, boff1, (const char*)Wg + (size_t)kt1 * (64 * F_ * 2), dst1 + 16384);
      dma2(boff, boff1, (const char*)Wu + (size_t)kt1 * (64 * F_ * 2), dst1 + 32768);
    }
    STAMPT(4);
  }
  v8s su; v4u fu, tu;
  auto round1_read = [&]() {
    LAS char* rp = smem + L_T + (4 * (g & 1) + (o >> 2)) * 544 + (w * 32 + (g >> 1) * 16) * 2 + (o & 3) * 8;
    su = cat8(trread(rp), trread(rp + 8 * 544));
    LAS char* fp_ = smem + L_FR + w * 2048 + q * 128 + (((2 * g + (pp >> 1)) ^ ((q >> 1) << 2)) << 4) + (pp & 1) * 8;
    fu = __builtin_bit_cast(v4u, cat8(trread(fp_), trread(fp_ + 512)));
    LAS char* tp = smem + L_FR + w * 2048 + 1024 + q * 128 + (pp >> 1) * 16 + (pp & 1) * 8;
    tu = __builtin_bit_cast(v4u, cat8(trread(tp), trread(tp + 512)));
  };
  auto round1_max = [&]() {
    typedef unsigned short v2us __attribute__((ext_vector_type(2)));
    const int dl = (idx >> 1) & 7;
    const u32 msk = (fcq == 0 && l < dl) ? 0u : 0x7fff7fffu, msk2 = (fcq == 3 && o <= dl) ? 0x7fff7fffu : 0u;
    v2us m2 = {0, 0};
#pragma unroll
    for (int e = 0; e < 4; ++e) {
      u32 t = fu[e] & msk, t2 = tu[e] & msk2;
      m2 = __builtin_elementwise_max(m2, __builtin_elementwise_max(__builtin_bit_cast(v2us, t), __builtin_bit_cast(v2us, t2)));
    }
    const u32 mx = wave_max(max((u32)m2[0], (u32)m2[1]));
    if (l == 0) *(LAS u32*)(smem + L_RED + w * 4) = mx;
  };
  auto round1_store = [&]() {
    st16c((u32)(b * 8192 + (w * 32 + (g >> 1) * 16 + o) * 32 + (g & 1) * 16), ws + OFF_S1, su);
    st16c((u32)((idx * 16 + fkb) * 4608 + (fcq * 64 + l) * 16), ws + OFF_WT, fu);
    if (fcq == 3 && l < 16) st16c((u32)((idx * 16 + fkb) * 4608 + (256 + l) * 16), ws + OFF_WT, tu);
  };

  const int RG = xcd * 4 + (idx >> 3), ct = idx & 7;
  const int kd = xcd * 32 + idx;
  const u32 swz = ((l & 7) ^ (((l >> 4) & 1) << 2)) << 4;
  const u32 ro0 = (u32)((l >> 3) * 1024) + swz, ro1 = ro0 + 8192 - 1024;
  const char* bsrc = (const char*)Wd + (size_t)(w * 16) * 1024 + ct * 128;
  auto issueW = [&](int n) { dma2(ro0, ro1, bsrc + (size_t)n * 131072, lbase + wslot(n) + w * 2048); };
  auto pfw = [&](int i, u32 zone) { dma1<0>((u32)(l * 16), (const char*)Wd + 786432 + idx * 32768 + (i * 8 + w) * 1024, lbase + zone + w * 1024); };
  const int dh = w & 1, kq = w >> 1, ph = kq >> 1, kh = kq & 1;
  const int epi = RG * 64 + ph * 32 + (l & 31);
  const u32 eoff = (u32)(s_of_pi(epi) * D_ + ct * 64 + dh * 32 + kh * 16 + h * 8) * 2;
  v4u wt, s1v, xe; v4f pq[4]; float rs2;
  auto load_xe = [&]() { ld16(xe, eoff, (const char*)x); };
  auto load_s1 = [&]() { ld16c(s1v, (u32)((((4 * w + g) * 8 + (o >> 1)) * 256 + kd) * 32 + (o & 1) * 16), ws + OFF_S1); };
  auto load_wt = [&]() { ld16c(wt, (u32)((w * 64 + l) * 4608 + (kd + ((2 * w + (g >> 1)) & 7)) * 16), ws + OFF_WT); };
  auto load_rs = [&]() { ld4c(rs2, (u32)(epi * 4), ws + OFF_RS2); };
  v4f t2c, t2s, t3c, t3s;
  auto trig2 = [&]() {
    const float fw = (float)w, st2 = fw * (1.0f / 128.0f), a2 = fw * (float)(o + 64 * g) * (1.0f / 2048.0f);
#pragma unroll
    for (int e = 0; e < 4; ++e) { float b2 = fmaf((float)e, st2, a2); t2c[e] = cosr(b2); t2s[e] = sinr(b2); }
    PIN(t2c); PIN(t2s);
  };
  auto trig3 = [&]() {
    const float f7 = (float)(o & 7), st3 = f7 * (1.0f / 128.0f), a3 = f7 * (float)(2 * w + (o >> 3) + 64 * g) * (1.0f / 2048.0f);
#pragma unroll
    for (int e = 0; e < 4; ++e) { float b3 = fmaf((float)e, st3, a3); t3c[e] = cosr(b3); t3s[e] = sinr(b3); }
    PIN(t3c); PIN(t3s);
  };
  v4u f16; v4f t1c, t1s, re, im;
  const v4f z4 = {0.f, 0.f, 0.f, 0.f};
  auto imf = [](v4u f) { return __builtin_bit_cast(v8h, v4u{f[2] ^ 0x80008000u, f[3] ^ 0x80008000u, f[0], f[1]}); };
  auto segA0 = [&]() {
    f16 = *(LAS v4u*)(smem + L_TAB + l * 16);
    t1c = *(LAS v4f*)(smem + L_TAB + 2048 + l * 16); t1s = *(LAS v4f*)(smem + L_TAB + 3072 + l * 16);
  };
  auto segA1 = [&]() {
    PIN(s1v);
    const v8h a = __builtin_bit_cast(v8h, s1v);
    re = mfma16h(a, __builtin_bit_cast(v8h, f16), z4); im = mfma16h(a, imf(f16), z4);
  };
  auto segA2 = [&]() {
    twid(re, im, t1c, t1s);
    const v8h a = pack_ri(re, im);
    re = mfma16h(__builtin_bit_cast(v8h, f16), a, z4); im = mfma16h(imf(f16), a, z4);
  };
  auto segA3 = [&]() {
    twid(re, im, t2c, t2s);
    *(LAS v4h*)(smem + L_E1 + w * E1P + o * 32 + g * 8) = pack4h(re);
    *(LAS v4h*)(smem + L_E1 + (8 + w) * E1P + o * 32 + g * 8) = pack4h(im);
  };

  if (ffn) {
    v16f acc[2][2] = {};
    float mbr[2];
    const u32 wb0 = (u32)((8 * h + q) * 256 + ((fq ^ q) << 6) + blk * 32 + pp * 8);
    const u32 r1 = 16384 + sh * 16384, r2 = 32768 - sh * 16384;
    const int xrow = sh * 64 + (l & 31);
    const u32 xb0 = (u32)(xrow * 128 + ((h ^ ((xrow >> 1) & 7)) << 4));
    auto rd = [&](Frag1& f, LAS char* sb, int i) {
      LAS char* xa = sb + (xb0 ^ (i << 5));
      LAS char* wa_ = sb + wb0 + (i << 12);
      f.w1 = cat8(trread(wa_ + r1), trread(wa_ + r1 + 1024));
      f.x0 = *(LAS v8s*)xa; f.x1 = *(LAS v8s*)(xa + 4096);
      f.w2 = cat8(trread(wa_ + r2), trread(wa_ + r2 + 1024));
    };
    const u32 one = ((l & 15) == ((l >> 4) & 1)) ? 0x3f803f80u : 0u;
    const v8s sel = __builtin_bit_cast(v8s, v4u{one, one, one, one});
    v4f csa = {0.f, 0.f, 0.f, 0.f};
    Frag1 A, B;
    WAIT_VM(7);
    BARRIER();
    STAMPT(1); STAMPC(30);
    rd(A, smem, 0);
#pragma unroll
    for (int i = 0; i < 4; ++i) csa = mfma16b(sel, __builtin_bit_cast(v4u, cat8(trread(smem + wb0 + r1 + i * 4096), trread(smem + wb0 + r1 + i * 4096 + 1024))), csa);
    round1_read();
    dma2(aoff[0], aoff[1], (const char*)x + ((2 + rot) & 7) * 128, lbase + 2 * 49152 + w * 2048);
    u32 c1[4], c2[4];
    static_for<0, 8>([&](auto T) {
      constexpr int t = T, slot = t % 3, nslot = (t + 1) % 3, pslot = (t + 2) % 3;
      LAS char* sb = smem + slot * 49152;
      {
        const int kt = (t + 2 + rot) & 7;
        const char* s1 = t < 6 ? (const char*)Wg + (size_t)kt * (64 * F_ * 2) : bsrc + (size_t)((t - 6) * 3 + 1) * 131072;
        const char* s2 = t < 6 ? (const char*)Wu + (size_t)kt * (64 * F_ * 2) : bsrc + (size_t)((t - 6) * 3 + 2) * 131072;
        const u32 o2 = t < 6 ? boff : ro0, o3 = t < 6 ? boff1 : ro1;
        const u32 dst = lbase + pslot * 49152 + w * 2048;
        rd(B, sb, 1); SCHED();
        acc[0][0] = mfma32b(A.w1, A.x0, acc[0][0]); acc[0][1] = mfma32b(A.w1, A.x1, acc[0][1]);
        SCHED();
        dma1<0>(o2, s1, dst + 16384);
        if constexpr (t == 2) load_xe();
        if constexpr (t == 6) { if (w == 0) dma1s<0>((u32)(FL_B + l * 16), FL, lbase + L_FA); }
        if constexpr (t == 7) load_wt();
        acc[1][0] = mfma32b(A.w2, A.x0, acc[1][0]); acc[1][1] = mfma32b(A.w2, A.x1, acc[1][1]); SCHED();
        if constexpr (t == 0) {
          if (l < 16) {
#pragma unroll
            for (int i = 0; i < 2; ++i) {
              const float hi = rbf(csa[i]);
              st4c((u32)(((j * 16 + p) * 256 + sh * 128 + fq * 32 + i * 16 + l) * 4), ws + OFF_CSP, pkbf(hi, csa[i] - hi));
            }
          }
        }
        if constexpr (t == 0) { if (w == 0) dma1c((u32)((l & 1) * 4), BC, lbase + L_BC1); round1_max(); SCHED(); }
        if constexpr (t == 3) { trig2(); SCHED(); }
        if constexpr (t == 4) { trig3(); SCHED(); }
        rd(A, sb, 2); SCHED();
        acc[0][0] = mfma32b(B.w1, B.x0, acc[0][0]); SCHED();
        dma1<1024>(o3, s1, dst + 16384);
        acc[0][1] = mfma32b(B.w1, B.x1, acc[0][1]);
        acc[1][0] = mfma32b(B.w2, B.x0, acc[1][0]); acc[1][1] = mfma32b(B.w2, B.x1, acc[1][1]); SCHED();
        dma1<0>(o2, s2, dst + 32768);
        if constexpr (t == 7) {
          if (kd == 0) {
#pragma unroll
            for (int r = 0; r < 4; ++r) ld16c(pq[r], (u32)(w * 4096 + r * 1024 + l * 16), ws + OFF_PQ);
          }
        }
        if constexpr (t == 7) WAIT_VM(13);
        rd(B, sb, 3); SCHED();
        if constexpr (t == 7) {
#pragma unroll
          for (int i = 0; i < 4; ++i) { PIN(c1[i]); PIN(c2[i]); }
          PIN(mbr[0]); PIN(mbr[1]);
          const v8s cs1 = __builtin_bit_cast(v8s, v4u{c1[0], c1[1], c1[2], c1[3]}), cs2 = __builtin_bit_cast(v8s, v4u{c2[0], c2[1], c2[2], c2[3]});
          const u32 m0_ = pkbf(mbr[0], mbr[0]), m1_ = pkbf(mbr[1], mbr[1]);
          const v8s mb0 = __builtin_bit_cast(v8s, v4u{m0_, m0_, m0_, m0_}), mb1 = __builtin_bit_cast(v8s, v4u{m1_, m1_, m1_, m1_});
          acc[0][0] = mfma32b(cs1, mb0, acc[0][0]); acc[1][0] = mfma32b(cs2, mb0, acc[1][0]);
          acc[0][1] = mfma32b(cs1, mb1, acc[0][1]); acc[1][1] = mfma32b(cs2, mb1, acc[1][1]); SCHED();
        }
        acc[0][0] = mfma32b(A.w1, A.x0, acc[0][0]); acc[0][1] = mfma32b(A.w1, A.x1, acc[0][1]);
        SCHED();
        dma1<1024>(o3, s2, dst + 32768);
        acc[1][0] = mfma32b(A.w2, A.x0, acc[1][0]); acc[1][1] = mfma32b(A.w2, A.x1, acc[1][1]); SCHED();
      }
      if constexpr (t < 7) {
        const char* s0 = t < 5 ? (const char*)x + ((t + 3 + rot) & 7) * 128 : bsrc + (size_t)((t - 5) * 3) * 131072;
        const u32 o0 = t < 5 ? aoff[0] : ro0, o1 = t < 5 ? aoff[1] : ro1;
        const u32 dst = lbase + slot * 49152 + w * 2048;
        if constexpr (t == 0) round1_store();
        if constexpr (t >= 1 && t <= 4) pfw(t - 1, L_T + 512);
        WAIT_VM(t == 0 ? 10 : t <= 2 ? 9 : t <= 4 ? 8 : t == 5 ? 6 : 17);
        if constexpr (t == 5) { if (w == 0) poll4(*(LAS v4u*)(smem + L_FA + l * 16), (u32)(FL_A + l * 16)); }
        if constexpr (t == 6) { if (w == 0) { WAIT_VM(3); check_b(*(LAS v4u*)(smem + L_FA + l * 16)); } }
        BARRIER();
        if constexpr (t == 0 || t == 1 || t == 5 || t == 6) STAMPT(22 + t);
        acc[0][0] = mfma32b(B.w1, B.x0, acc[0][0]); SCHED();
        rd(A, smem + nslot * 49152, 0);
        if constexpr (t == 6) segA0();
        SCHED();
        if constexpr (t == 4) { if (w == 0) dma1s<0>((u32)(FL_A + l * 16), FL, lbase + L_FA); }
        if constexpr (t == 5) {
          const u32 co = (u32)(((j * 16 + (p & 1) + 8 * h) * 256 + fq * 32 + (l & 31)) * 4);
#pragma unroll
          for (int i = 0; i < 4; ++i) { ld4c(c1[i], co + sh * 512 + i * 2048, ws + OFF_CSP); ld4c(c2[i], co + (1 - sh) * 512 + i * 2048, ws + OFF_CSP); }
          ld4c(mbr[0], (u32)((p * 128 + xrow) * 4), ws + OFF_MB); ld4c(mbr[1], (u32)((p * 128 + xrow + 32) * 4), ws + OFF_MB);
          load_rs();
        }
        if constexpr (t == 6) load_s1();
        dma1<0>(o0, s0, dst);
        acc[0][1] = mfma32b(B.w1, B.x1, acc[0][1]);
        acc[1][0] = mfma32b(B.w2, B.x0, acc[1][0]);
        acc[1][1] = mfma32b(B.w2, B.x1, acc[1][1]); SCHED();
        dma1<1024>(o1, s0, dst);
        if constexpr (t == 1) { if (w == 0) { get_epoch(); signal_a(); } }
        if constexpr (t == 2) { if (w == 0) signal_b(); }
        SCHED();
      } else {
        acc[0][0] = mfma32b(B.w1, B.x0, acc[0][0]); acc[1][0] = mfma32b(B.w2, B.x0, acc[1][0]); SCHED();
        acc[0][1] = mfma32b(B.w1, B.x1, acc[0][1]); acc[1][1] = mfma32b(B.w2, B.x1, acc[1][1]); SCHED();
      }
    });
    STAMPT(2); STAMPC(31);
    auto fin = [&](const v16f& ag, const v16f& au, int rt) {
#pragma unroll
      for (int s2i = 0; s2i < 2; ++s2i) {
        v4u ov;
#pragma unroll
        for (int e = 0; e < 4; ++e) {
          const int r = s2i * 8 + 2 * e;
          v2f gg = v2f{relu(ag[r]), relu(ag[r + 1])} * v2f{au[r], au[r + 1]};
          ov[e] = pkbf(gg[0], gg[1]);
        }
        st16c((u32)(((p * 4 + sh * 2 + rt) * 112 + (j * 8 + fq * 2 + s2i)) * 1024 + l * 16), ws + OFF_H, ov);
      }
    };
    if (sh == 0) { fin(acc[0][0], acc[1][0], 0); fin(acc[0][1], acc[1][1], 1); }
    else { fin(acc[1][0], acc[0][0], 0); fin(acc[1][1], acc[0][1], 1); }
    STAMPT(6);
    WAIT_VM(4);
    segA1(); segA2(); segA3();
  } else {
    WAIT_VM(1);
    BARRIER();
    round1_read(); round1_store(); round1_max();
    if (w == 0 && !coord) dma1c((u32)((l & 1) * 4), BC, lbase + L_BC1);
    do_tick();
    if (w == 7) {
#pragma unroll
      for (int i = 0; i < 8; ++i) PIN(sq[i]);
      float war = bflo(sq[0]) + bflo(sq[2]), wai = bflo(sq[1]) - bflo(sq[3]);
      float wbr = bflo(sq[4]) + bflo(sq[6]), wbi = bflo(sq[5]) - bflo(sq[7]);
      v4f pv = {0.25f * (war + wbr), 0.25f * (wai + wbi), 0.25f * (war - wbr), 0.25f * (wai - wbi)};
      st16c((u32)((((sp >> 2) * 4 + (l & 3)) * 64 + (sp & 3) * 16 + (l >> 2)) * 16), ws + OFF_PQ, pv);
    }
    WAIT_VM(0);
    BARRIER();
    STAMPT(1); STAMPC(30);
    if (w == 0) { if (!coord) get_epoch(); signal_a(); signal_b(); do_tick(); WAIT_VM(0); }
    BARRIER();
    STAMPT(24);
    dma1c((u32)(sp * 57344 + (w * 112 + l) * 64), (const char*)Wd, lbase + 49152);
    dma1c((u32)(sp * 57344 + (w * 112 + 48 + l) * 64), (const char*)Wd, lbase + 49152);
#pragma unroll
    for (int i = 0; i < 4; ++i) pfw(i, 49152);
    static_for<0, 6>([&](auto N) { issueW(N); });
    load_xe();
    trig2(); trig3(); segA0();
    STAMPT(25);
    if (w == 0) {
      if (!coord) __builtin_amdgcn_s_sleep(SLEEP_A);
      check_b(v4u{~E, 0u, 0u, 0u});
    }
    STAMPT(26);
    BARRIER();
    load_s1(); load_wt(); load_rs();
    WAIT_VM(0);
    segA1(); segA2(); segA3();
    STAMPT(2); STAMPC(31);
  }
  PIN(wt); PIN(xe); PIN(rs2);
  BARRIER();
  STAMPT(5);

  const u32 trG = (u32)(kq * 4096 + (4 * h + q) * 128 + ((dh ^ ((q >> 1) & 1)) << 6) + (blk ^ kh) * 32 + pp * 8);
  v8s Wf[14][2];
  auto readW = [&](auto N) {
    constexpr int n = N;
    LAS char* sb = smem + wslot(n) + trG;
    Wf[n][0] = cat8(trread(sb), trread(sb + 1024)); Wf[n][1] = cat8(trread(sb + 2048), trread(sb + 3072));
  };
  const char* hsrc = ws + OFF_H + ((size_t)(RG * 2 + (w >> 2)) * 112 + (w & 3) * 2) * 1024;
  const u32 so0 = (u32)((l >> 3) * 4096) + swz, so1 = so0 + 32768 - 1024;
  const char* ssrc = ws + OFF_S2 + (size_t)(w * 16) * 4096 + RG * 128;
  auto issueP = [&](int n, auto HALF) {
    constexpr int hf = HALF;
    if (n < 14) dma1s<hf * 1024>((u32)(l * 16), hsrc + n * 8192, lbase + aslot(n) + w * 2048);
    else dma1s<hf * 1024>(hf ? so1 : so0, ssrc + (size_t)(n - 14) * (128 * 4096), lbase + aslot(n) + w * 2048);
  };
  const int d = ct * 64 + dh * 32 + ((l & 31) ^ (kh << 4));
  const float gst = (float)d * (1.0f / 512.0f), ga0 = (float)(16 * kq + 2 * h) * gst;
  v8h G[8];
  auto gen = [&](auto FI) {
    constexpr int f = FI;
#pragma unroll
    for (int e2 = 0; e2 < 4; ++e2) {
      float a = fmaf((float)(64 * (f >> 1) + 8 * (f & 1) + 4 * (e2 >> 1) + (e2 & 1)), gst, ga0);
      float c = cosr(a), sn = sinr(a);
      if (f == 0 && e2 == 0) { if (kq == 0 && h == 0) { c = 0.5f; sn = (d & 1) ? 0.5f : -0.5f; } }
      G[f][2 * e2] = (_Float16)c; G[f][2 * e2 + 1] = (_Float16)sn;
    }
    PIN(G[f]);
  };

  float oscale;
  const u32 fhoff = (u32)(FL_H + (RG * 16 + (min(l, 55) >> 2)) * 64 + (l & 3) * 4);
  u32 fp;
  {
    v8h a;
    {
      LAS char* rp = smem + L_E1 + (4 * (g & 1) + (o >> 2)) * E1P + (2 * w + (g >> 1)) * 32 + (o & 3) * 8;
      a = __builtin_bit_cast(v8h, cat8(trread(rp), trread(rp + 8 * E1P)));
    }
    const v4u f8 = *(LAS v4u*)(smem + L_TAB + 1024 + l * 16);
    const v2u me = *(LAS v2u*)(smem + L_MX);
    issueW(6);
    static_for<0, 3>(readW);
    const int Ex = (int)me[0] < 16 ? 16 : ((int)me[0] > 240 ? 240 : (int)me[0]);
    const float wscale = __builtin_bit_cast(float, (u32)(246 - Ex) << 23);
    oscale = __builtin_bit_cast(float, (u32)(Ex - 7) << 23);
    re = mfma16h(__builtin_bit_cast(v8h, f8), a, z4); im = mfma16h(imf(f8), a, z4);
    static_for<3, 6>(readW);
    WAIT_VM(2);
    STAMPT(7);
    if (ffn && l == 0) st4c((u32)(FL_H + ((p * 2 + sh) * 16 + j) * 64 + fq * 4), FL, me[1]);
    issueW(7);
    if (kd != 0) {
#pragma unroll
      for (int r = 0; r < 4; ++r) {
        float fr = ((r & 1) ? bfhi(wt[r >> 1]) : bflo(wt[r >> 1])) * wscale, fi = ((r & 1) ? bfhi(wt[2 + (r >> 1)]) : bflo(wt[2 + (r >> 1)])) * wscale;
        float zr = re[r], zi = im[r];
        re[r] = zr * fr - zi * fi;
        im[r] = -(zr * fi + zi * fr);
      }
    } else {
      const int ka = 2 * w + (g >> 1), kr = o + 64 * (g & 1);
#pragma unroll
      for (int r = 0; r < 4; ++r) { v2f t = {re[r], im[r]}; *(LAS v2f*)(smem + L_ZX + ka * 1024 + (kr + 16 * r) * 8) = t; }
      BARRIER();
#pragma unroll
      for (int r = 0; r < 4; ++r) {
        PIN(pq[r]);
        const int km = (2048 - (ka + 16 * (kr + 16 * r))) & 2047;
        v2f zm = *(LAS v2f*)(smem + L_ZX + (km & 15) * 1024 + (km >> 4) * 8);
        float zr = re[r], zi = im[r];
        re[r] = (zr * pq[r][0] - zi * pq[r][1] + zm[0] * pq[r][2] + zm[1] * pq[r][3]) * wscale;
        im[r] = -(zr * pq[r][1] + zi * pq[r][0] + zm[0] * pq[r][3] - zm[1] * pq[r][2]) * wscale;
      }
    }
    LAS char* E2 = smem + (kd == 0 ? L_E1 : L_E2);
    a = pack_ri(re, im);
    re = mfma16h(a, __builtin_bit_cast(v8h, f8), z4); im = mfma16h(a, imf(f8), z4);
    twid(re, im, t3c, t3s);
    *(LAS v4h*)(E2 + (o & 7) * E2P + (2 * w + (o >> 3)) * 32 + g * 8) = pack4h(re);
    *(LAS v4h*)(E2 + (o & 7) * E2P + 512 + (2 * w + (o >> 3)) * 32 + g * 8) = pack4h(im);
    BARRIER();
    STAMPT(8);
    {
      LAS char* rp = E2 + w * E2P + o * 32 + g * 8;
      a = __builtin_bit_cast(v8h, cat8(*(LAS v4s*)rp, *(LAS v4s*)(rp + 512)));
    }
    issueW(8);
    re = mfma16h(a, __builtin_bit_cast(v8h, f16), z4); im = mfma16h(a, imf(f16), z4);
    issueW(9);
    twid(re, im, t1c, t1s);
    a = pack_ri(re, im);
    re = mfma16h(__builtin_bit_cast(v8h, f16), a, z4); im = mfma16h(imf(f16), a, z4);
    issueW(10);
    st8c((u32)(2 * kd * 4096 + w * 512 + l * 8), ws + OFF_S2, pack4h(re));
    st8c((u32)((2 * kd + 1) * 4096 + w * 512 + l * 8), ws + OFF_S2, pack4h(im));
  }
  STAMPT(9);
  gen(IC<0>{}); issueW(11);
  WAIT_VM(8);
  BARRIER();
  static_for<6, 9>(readW);
  gen(IC<1>{}); issueW(12); gen(IC<2>{}); issueW(13);
  if (!ffn) {
    WAIT_VM(0);
    BARRIER();
    if (w == 0) {
      if (l == 0) st4c((u32)(FL_S + b * 4), FL, E);
      __builtin_amdgcn_s_sleep(40);
    }
  }
  if (w == 0) ld4c(fp, fhoff, FL);
  gen(IC<3>{}); gen(IC<4>{});
  WAIT_VM(0);
  if (w == 0) {
    PIN(fp);
    u32 f = fp;
    int spins = 0;
    while (!__all(f == E) && ++spins < SPIN_CAP) {
      __builtin_amdgcn_s_sleep(4);
      f = ld4c_sync(fhoff, FL);
    }
  }
  BARRIER();
  STAMPT(10);
  if (ffn && tid == 0) st4c((u32)(FL_S + b * 4), FL, E);
  issueP(0, IC<0>{}); issueP(0, IC<1>{}); issueP(1, IC<0>{}); issueP(1, IC<1>{});
  static_for<9, 14>(readW);
  gen(IC<5>{});
  issueP(2, IC<0>{}); issueP(2, IC<1>{});
  gen(IC<6>{});
  issueP(3, IC<0>{}); issueP(3, IC<1>{});
  gen(IC<7>{});

  const u32 trS = (u32)(kq * 4096 + (4 * h + q) * 128 + ((ph ^ ((q >> 1) & 1)) << 6) + blk * 32 + pp * 8);
  const u32 hb = (u32)(ph * 8192 + kq * 2048 + l * 16);
  v16f accG[2] = {}, accS[2] = {};
  v8s fa[8], fb[8];
  v4u fs;
  float gv[8];
  const u32 xw = (u32)(w * 6144 + l * 16);
  auto xchg_out = [&](const v16f (&ac)[2], int base) {
    LAS char* X = smem + base + xw;
#pragma unroll
    for (int i = 0; i < 6; ++i) {
      const v16f& a = ac[(i + 2) >> 2]; const int r = ((i + 2) & 3) * 4;
      *(LAS v4f*)(X + i * 1024) = v4f{a[r], a[r + 1], a[r + 2], a[r + 3]};
    }
  };
  auto xchg_in = [&](const v16f (&ac)[2], int base, float (&o8)[8]) {
    v4f s0 = {ac[0][0], ac[0][1], ac[0][2], ac[0][3]}, s1 = {ac[0][4], ac[0][5], ac[0][6], ac[0][7]};
#pragma unroll
    for (int x_ = 1; x_ < 4; ++x_) {
      LAS char* X = smem + base + (w ^ (2 * x_)) * 6144 + (x_ - 1) * 2048 + l * 16;
      s0 += *(LAS v4f*)X; s1 += *(LAS v4f*)(X + 1024);
    }
#pragma unroll
    for (int e = 0; e < 4; ++e) { o8[e] = s0[e]; o8[4 + e] = s1[e]; }
  };
  v4f xg[6];
  float ov[8], ex[8];
  auto sadd = [](float a, float b) { float r; asm("v_add_f32 %0, %1, %2" : "=v"(r) : "v"(a), "v"(b)); return r; };
  auto smul = [](float a, float b) { float r; asm("v_mul_f32 %0, %1, %2" : "=v"(r) : "v"(a), "v"(b)); return r; };
  auto silu_step = [&](int i) {
    if (i < 3) {
#pragma unroll
      for (int e = 0; e < 8; ++e) { ov[e] = sadd(i == 0 ? accG[0][e] : ov[e], xg[2 * i + (e >> 2)][e & 3]); PIN(ov[e]); }
    } else if (i == 3) {
#pragma unroll
      for (int e = 0; e < 8; e += 2) { u32 t = pkbf_o(smul(ov[e], rs2), smul(ov[e + 1], rs2)); ov[e] = bflo(t); ov[e + 1] = bfhi(t); PIN(ov[e]); PIN(ov[e + 1]); }
    } else if (i == 4) {
#pragma unroll
      for (int e = 0; e < 8; ++e) { ex[e] = __builtin_amdgcn_exp2f(ov[e] * -1.44269504088896f); PIN(ex[e]); }
    } else if (i == 5) {
#pragma unroll
      for (int e = 0; e < 8; ++e) { ex[e] = __builtin_amdgcn_rcpf(1.0f + ex[e]); PIN(ex[e]); }
    } else if (i == 6) {
#pragma unroll
      for (int e = 0; e < 8; e += 2) { u32 t = pkbf_o(ov[e] * ex[e], ov[e + 1] * ex[e + 1]); gv[e] = bflo(t); gv[e + 1] = bfhi(t); PIN(gv[e]); PIN(gv[e + 1]); }
    }
  };
  static_for<0, 10>([&](auto V) {
    constexpr int v = V;
    v8s(&cur)[8] = (v & 1) ? fb : fa;
    v8s(&prv)[8] = (v & 1) ? fa : fb;
    auto mmp = [&](int s) {
      if constexpr (v >= 8) {
        accS[0] = mfma32h(G[(v - 8) * 4 + s], prv[2 * s], accS[0]); accS[1] = mfma32h(G[(v - 8) * 4 + s], prv[2 * s + 1], accS[1]);
        SCHED();
        if ((v - 8) * 4 + s >= 1) silu_step((v - 8) * 4 + s - 1);
      } else if constexpr (v >= 1) {
        accG[0] = mfma32b(Wf[2 * (v - 1) + (s >> 1)][s & 1], prv[2 * s], accG[0]);
        accG[1] = mfma32b(Wf[2 * (v - 1) + (s >> 1)][s & 1], prv[2 * s + 1], accG[1]);
      }
      SCHED();
    };
    if constexpr (v < 9) {
      WAIT_VM(v <= 7 ? 4 : 0);
      if constexpr (v == 5) { if (w == 0) { PIN(fs); poll4(fs, (u32)(FL_S + l * 16)); } }
      BARRIER();
      if constexpr (v == 0 || v == 8) STAMPT(11 + v);
      if constexpr (v == 4) { if (w == 0) ld16c(fs, (u32)(FL_S + l * 16), FL); }
      if constexpr (v == 8) {
#pragma unroll
        for (int x_ = 1; x_ < 4; ++x_) {
          LAS char* X = smem + 49152 + (w ^ (2 * x_)) * 6144 + (x_ - 1) * 2048 + l * 16;
          xg[2 * x_ - 2] = *(LAS v4f*)X; xg[2 * x_ - 1] = *(LAS v4f*)(X + 1024);
        }
        SCHED();
      }
      mmp(0);
#pragma unroll
      for (int s = 0; s < 4; ++s) {
        if constexpr (v >= 7) {
          LAS char* sb = smem + aslot(2 * v + (s >> 1)) + (s & 1) * 2048;
          cur[2 * s] = cat8(trread(sb + trS), trread(sb + trS + 1024));
          cur[2 * s + 1] = cat8(trread(sb + (trS ^ 64)), trread(sb + (trS ^ 64) + 1024));
        } else {
          LAS char* sb = smem + aslot(2 * v + (s >> 1)) + (s & 1) * 1024;
          cur[2 * s] = *(LAS v8s*)(sb + hb);
          cur[2 * s + 1] = *(LAS v8s*)(sb + (hb ^ 8192));
        }
      }
      SCHED();
      if constexpr (v <= 6) issueP(2 * v + 4, IC<0>{});
      mmp(1);
      if constexpr (v <= 6) issueP(2 * v + 4, IC<1>{});
      mmp(2);
      if constexpr (v <= 6) issueP(2 * v + 5, IC<0>{});
      mmp(3);
      if constexpr (v <= 6) issueP(2 * v + 5, IC<1>{});
    } else {
      mmp(0); mmp(1); mmp(2); mmp(3);
    }
    if constexpr (v == 7) xchg_out(accG, 49152);
    SCHED();
  });
  xchg_out(accS, 0);
  BARRIER();
  {
    float o8[8], pv[8];
    xchg_in(accS, 0, o8);
#pragma unroll
    for (int e = 0; e < 8; e += 2) { u32 t = pkbf_o(o8[e] * oscale, o8[e + 1] * oscale); pv[e] = bflo(t) * gv[e]; pv[e + 1] = bfhi(t) * gv[e + 1]; }
    u32 a0 = pkbf(pv[0], pv[1]), a1 = pkbf(pv[2], pv[3]), b0 = pkbf(pv[4], pv[5]), b1 = pkbf(pv[6], pv[7]);
    auto r0 = __builtin_amdgcn_permlane32_swap(a0, b0, false, false);
    auto r1 = __builtin_amdgcn_permlane32_swap(a1, b1, false, false);
    v4u pk = {r0[0], r1[0], r0[1], r1[1]};
    v4u ov4;
#pragma unroll
    for (int e = 0; e < 4; ++e) ov4[e] = pkbf(bflo(xe[e]) + bflo(pk[e]), bfhi(xe[e]) + bfhi(pk[e]));
    *(v4u*)((char*)out + eoff) = ov4;
  }
  asm volatile("km_pf_b:");
  STAMPT(20);
  WAIT_VM(0);
  STAMPT(21);
  STAMP_OUT(ws);
}

extern "C" void kernel_launch(void* const* d_in, const int* in_sizes, int n_in, void* d_out, int out_size, void* d_ws,
                              size_t ws_size, hipStream_t stream) {
  kmain<<<256, 512, 0, stream>>>((const u16*)d_in[0], (const u16*)d_in[1], (const u16*)d_in[2], (const u16*)d_in[4],
                                 (const u16*)d_in[5], (const u16*)d_in[6], (char*)d_ws + WS_BASE(ws_size), (u16*)d_out);
}
```

```cpp
#include <hip/hip_runtime.h>
#include <stdint.h>

typedef unsigned short u16;
typedef unsigned int u32;
typedef unsigned long long u64;
typedef short v4s __attribute__((ext_vector_type(4)));
typedef short v8s __attribute__((ext_vector_type(8)));
typedef _Float16 v4h __attribute__((ext_vector_type(4)));
typedef _Float16 v8h __attribute__((ext_vector_type(8)));
typedef __bf16 v2b __attribute__((ext_vector_type(2)));
typedef __bf16 v8b __attribute__((ext_vector_type(8)));
typedef float v2f __attribute__((ext_vector_type(2)));
typedef float v4f __attribute__((ext_vector_type(4)));
typedef float v16f __attribute__((ext_vector_type(16)));
typedef u32 v2u __attribute__((ext_vector_type(2)));
typedef u32 v4u __attribute__((ext_vector_type(4)));

#define LAS __attribute__((address_space(3)))
#define GAS __attribute__((address_space(1)))
#define DEVI __device__ __forceinline__

constexpr int D_ = 512, F_ = 1792, DF_ = 257;
constexpr size_t OFF_H = (size_t)24 << 20;
constexpr size_t OFF_S1 = (size_t)32 << 20;
constexpr size_t OFF_WT = (size_t)34 << 20;
constexpr size_t OFF_S2 = (size_t)37 << 20;
constexpr size_t OFF_M = (size_t)39 << 20;
constexpr size_t OFF_FL = OFF_M;
constexpr u32 FL_S = 0, FL_A = 1024, FL_H = 2048;
constexpr size_t OFF_BC = OFF_M + 49152;
constexpr size_t OFF_MB = OFF_M + 98304;
constexpr size_t OFF_RS2 = OFF_M + 106496;
constexpr size_t OFF_PQ = OFF_M + 65536;
constexpr size_t OFF_CSP = OFF_M + 131072;
constexpr int SPIN_CAP = 1 << 16;
#define SLEEP_A 16

#define WS_BASE(n) ((((n) - ((size_t)40 << 20))) & ~(size_t)0x1fffff)
#define STAMP_DECL
#define STAMPT(id)
#define STAMPC(id)
#define STAMP_OUT(wsp)

template <int I> struct IC { static constexpr int v = I; constexpr operator int() const { return I; } };
template <int B, int E, class Fn> DEVI void static_for(Fn&& f) {
  if constexpr (B < E) { f(IC<B>{}); static_for<B + 1, E>(f); }
}
#define WAIT_VM(n) asm volatile("s_waitcnt vmcnt(%0)" ::"n"(n) : "memory")
#define BARRIER() asm volatile("s_waitcnt lgkmcnt(0)\n\ts_barrier" ::: "memory")
#define SCHED() __builtin_amdgcn_sched_barrier(0)
#define PIN(x) asm volatile("" : "+v"(x))

DEVI int s_of_pi(int pi) {
  int RG = pi >> 6, i = pi & 63;
  return 128 * (4 * (RG & 3) + (i & 3)) + 8 * (i >> 2) + (RG >> 2);
}
DEVI void dma2(u32 voff0, u32 voff1, const char* sbase, u32 lds) {
  asm volatile("s_mov_b32 m0, %3\n\ts_nop 0\n\tglobal_load_lds_dwordx4 %0, %2\n\tglobal_load_lds_dwordx4 %1, %2 offset:1024" ::"v"(voff0), "v"(voff1), "s"(sbase), "s"(lds) : "memory");
}
DEVI void dma2c(u32 voff0, u32 voff1, const char* sbase, u32 lds) {
  asm volatile("s_mov_b32 m0, %3\n\ts_nop 0\n\tglobal_load_lds_dwordx4 %0, %2 sc1\n\tglobal_load_lds_dwordx4 %1, %2 offset:1024 sc1" ::"v"(voff0), "v"(voff1), "s"(sbase), "s"(lds) : "memory");
}
template <int OFF> DEVI void dma1(u32 voff, const char* sbase, u32 lds) {
  asm volatile("s_mov_b32 m0, %2\n\ts_nop 0\n\tglobal_load_lds_dwordx4 %0, %1 offset:%3" ::"v"(voff), "s"(sbase), "s"(lds), "n"(OFF) : "memory");
}
template <int OFF> DEVI void dma1s(u32 voff, const char* sbase, u32 lds) {
  asm volatile("s_mov_b32 m0, %2\n\ts_nop 0\n\tglobal_load_lds_dwordx4 %0, %1 offset:%3 sc1" ::"v"(voff), "s"(sbase), "s"(lds), "n"(OFF) : "memory");
}
DEVI void dma1c(u32 voff, const char* sbase, u32 lds) {
  asm volatile("s_mov_b32 m0, %2\n\ts_nop 0\n\tglobal_load_lds_dword %0, %1 sc1" ::"v"(voff), "s"(sbase), "s"(lds) : "memory");
}
DEVI v2u ld8c_sync(u32 voff, const char* sb) { v2u d; asm volatile("global_load_dwordx2 %0, %1, %2 sc1\n\ts_waitcnt vmcnt(0)" : "=&v"(d) : "v"(voff), "s"(sb) : "memory"); return d; }
DEVI u32 ld4c_sync(u32 voff, const char* sb) { u32 d; asm volatile("global_load_dword %0, %1, %2 sc1\n\ts_waitcnt vmcnt(0)" : "=&v"(d) : "v"(voff), "s"(sb) : "memory"); return d; }
DEVI v4u ld16c_sync(u32 voff, const char* sb) { v4u d; asm volatile("global_load_dwordx4 %0, %1, %2 sc1\n\ts_waitcnt vmcnt(0)" : "=&v"(d) : "v"(voff), "s"(sb) : "memory"); return d; }
template <class T> DEVI void ld16c(T& d, u32 voff, const char* sb) { asm volatile("global_load_dwordx4 %0, %1, %2 sc1" : "=&v"(d) : "v"(voff), "s"(sb) : "memory"); }
template <class T> DEVI void ld8c(T& d, u32 voff, const char* sb) { asm volatile("global_load_dwordx2 %0, %1, %2 sc1" : "=&v"(d) : "v"(voff), "s"(sb) : "memory"); }
template <class T> DEVI void ld4c(T& d, u32 voff, const char* sb) { asm volatile("global_load_dword %0, %1, %2 sc1" : "=&v"(d) : "v"(voff), "s"(sb) : "memory"); }
template <class T> DEVI void ld16(T& d, u32 voff, const char* sb) { asm volatile("global_load_dwordx4 %0, %1, %2" : "=&v"(d) : "v"(voff), "s"(sb) : "memory"); }
DEVI void ld2(u32& d, u32 voff, const char* sb) { asm volatile("global_load_ushort %0, %1, %2" : "=&v"(d) : "v"(voff), "s"(sb) : "memory"); }
template <class T> DEVI void st16c(u32 voff, const char* sb, T v) { asm volatile("global_store_dwordx4 %0, %1, %2 sc1\n\ts_nop 1" ::"v"(voff), "v"(v), "s"(sb) : "memory"); }
template <class T> DEVI void st8c(u32 voff, const char* sb, T v) { asm volatile("global_store_dwordx2 %0, %1, %2 sc1" ::"v"(voff), "v"(v), "s"(sb) : "memory"); }
template <class T> DEVI void st4c(u32 voff, const char* sb, T v) { asm volatile("global_store_dword %0, %1, %2 sc1" ::"v"(voff), "v"(v), "s"(sb) : "memory"); }

DEVI v4s trread(LAS char* p) { return __builtin_amdgcn_ds_read_tr16_b64_v4i16((LAS v4s*)p); }
DEVI v8s cat8(v4s a, v4s b) { return __builtin_shufflevector(a, b, 0, 1, 2, 3, 4, 5, 6, 7); }
DEVI float bflo(u32 v) { return __builtin_bit_cast(float, v << 16); }
DEVI float bfhi(u32 v) { return __builtin_bit_cast(float, v & 0xffff0000u); }
DEVI float relu(float v) { return __builtin_bit_cast(float, max(__builtin_bit_cast(int, v), 0)); }
DEVI float rbf(float v) { return (float)(__bf16)v; }
DEVI u32 pkbf(float a, float b) { v2b p = {(__bf16)a, (__bf16)b}; return __builtin_bit_cast(u32, p); }
DEVI u32 pkbf_o(float a, float b) { u32 r = pkbf(a, b); asm("" : "+v"(r)); return r; }
DEVI float cosr(float rev) { return __builtin_amdgcn_cosf(rev); }
DEVI float sinr(float rev) { return __builtin_amdgcn_sinf(rev); }
DEVI v4f mfma16h(v8h a, v8h b, v4f c) { return __builtin_amdgcn_mfma_f32_16x16x32_f16(a, b, c, 0, 0, 0); }
DEVI v4f mfma16b(v8s a, v4u b, v4f c) { return __builtin_amdgcn_mfma_f32_16x16x32_bf16(__builtin_bit_cast(v8b, a), __builtin_bit_cast(v8b, b), c, 0, 0, 0); }
DEVI v16f mfma32b(v8s a, v8s b, v16f c) { return __builtin_amdgcn_mfma_f32_32x32x16_bf16(__builtin_bit_cast(v8b, a), __builtin_bit_cast(v8b, b), c, 0, 0, 0); }
DEVI v16f mfma32h(v8h a, v8s b, v16f c) { return __builtin_amdgcn_mfma_f32_32x32x16_f16(a, __builtin_bit_cast(v8h, b), c, 0, 0, 0); }
DEVI v8h pack_ri(v4f re, v4f im) {
  v8h r;
  r[0] = (_Float16)re[0]; r[1] = (_Float16)re[1]; r[2] = (_Float16)re[2]; r[3] = (_Float16)re[3];
  r[4] = (_Float16)im[0]; r[5] = (_Float16)im[1]; r[6] = (_Float16)im[2]; r[7] = (_Float16)im[3];
  return r;
}
DEVI v4h pack4h(v4f a) { v4h r; r[0] = (_Float16)a[0]; r[1] = (_Float16)a[1]; r[2] = (_Float16)a[2]; r[3] = (_Float16)a[3]; return r; }
DEVI void twid(v4f& re, v4f& im, const v4f& c, const v4f& s) {
#pragma unroll
  for (int r = 0; r < 4; ++r) { float a = re[r], b = im[r]; re[r] = a * c[r] + b * s[r]; im[r] = b * c[r] - a * s[r]; }
}
template <int CTRL> DEVI float dppadd(float v) {
  return v + __builtin_bit_cast(float, __builtin_amdgcn_update_dpp(0, __builtin_bit_cast(int, v), CTRL, 0xf, 0xf, true));
}
DEVI float wave_sum(float v) {
  v = dppadd<0xB1>(v); v = dppadd<0x4E>(v); v = dppadd<0x141>(v); v = dppadd<0x140>(v);
  int bb = __builtin_bit_cast(int, v);
  float a0 = __builtin_bit_cast(float, __builtin_amdgcn_readlane(bb, 0)), a1 = __builtin_bit_cast(float, __builtin_amdgcn_readlane(bb, 16));
  float a2 = __builtin_bit_cast(float, __builtin_amdgcn_readlane(bb, 32)), a3 = __builtin_bit_cast(float, __builtin_amdgcn_readlane(bb, 48));
  return (a0 + a1) + (a2 + a3);
}
DEVI float dot2ones(u32 v, float acc) {
  const v2b ones = {(__bf16)1.0f, (__bf16)1.0f};
  return __builtin_amdgcn_fdot2_f32_bf16(__builtin_bit_cast(v2b, v), ones, acc, false);
}
template <int CTRL> DEVI u32 dppmax(u32 v) { return max(v, (u32)__builtin_amdgcn_update_dpp(0, (int)v, CTRL, 0xf, 0xf, true)); }
DEVI u32 wave_max(u32 v) {
  v = dppmax<0xB1>(v); v = dppmax<0x4E>(v); v = dppmax<0x141>(v); v = dppmax<0x140>(v);
  u32 a0 = __builtin_amdgcn_readlane(v, 0), a1 = __builtin_amdgcn_readlane(v, 16), a2 = __builtin_amdgcn_readlane(v, 32), a3 = __builtin_amdgcn_readlane(v, 48);
  return max(max(a0, a1), max(a2, a3));
}

struct Frag1 { v8s x0, x1, w1, w2; };

constexpr int L_SCR = 147456;
constexpr int L_CS = L_SCR, L_RED = L_SCR + 1024, L_MX = L_SCR + 1088, L_BC0 = L_SCR + 2048, L_BC1 = L_SCR + 2304;
constexpr int L_TAB = L_SCR + 3072;
constexpr int L_T = L_SCR + 7168, L_FA = L_SCR, L_E1 = L_T, L_E2 = 81920, L_ZX = 81920;
constexpr int L_C1 = 98304, L_IMG = L_C1 + 9216;
constexpr int L_FR = 131072;
constexpr int E1P = 544, E2P = 1024 + 72;
DEVI constexpr int wslot(int n) {
  constexpr int u[14] = {6, 7, 8, 0, 1, 2, 3, 4, 6, 7, 8, 0, 1, 2};
  return u[n] * 16384;
}
DEVI constexpr int aslot(int n) {
  constexpr int u[18] = {3, 4, 5, 9, 2, 6, 7, 8, 0, 1, 3, 4, 5, 9, 2, 6, 7, 8};
  return u[n] * 16384;
}

__global__ __launch_bounds__(512, 2) void kmain(const u16* __restrict__ x, const u16* __restrict__ wr, const u16* __restrict__ wi,
                                                const u16* __restrict__ Wg, const u16* __restrict__ Wu, const u16* __restrict__ Wd,
                                                char* __restrict__ ws, u16* __restrict__ out) {
  __shared__ __attribute__((aligned(16))) char smem_[163840];
  LAS char* smem = (LAS char*)smem_;
  const u32 lbase = (u32)(size_t)smem;
  const int tid = threadIdx.x, l = tid & 63;
  const int w = __builtin_amdgcn_readfirstlane(tid >> 6);
  const int b = blockIdx.x, xcd = b & 7, idx = b >> 3;
  const bool ffn = idx < 28;
  const int g = l >> 4, o = l & 15;
  const int h = l >> 5, blk = (l >> 4) & 1, q = (l & 15) >> 2, pp = l & 3;
  const char* FL = ws + OFF_FL;
  const char* BC = ws + OFF_BC;
  STAMP_DECL; STAMPT(0);
  asm volatile("" ::"s"(x), "s"(wr), "s"(wi), "s"(Wg), "s"(Wu), "s"(Wd), "s"(ws), "s"(out));

  const int pi = b * 8 + w;
  v4u xr;
  ld16(xr, (u32)(s_of_pi(pi) * 1024 + l * 16), (const char*)x);
  if (!ffn) {
    u64 pc; u32 len;
    asm volatile("s_getpc_b64 %0\nkm_pf_a:\n\ts_mov_b32 %1, km_pf_b-km_pf_a" : "=s"(pc), "=s"(len));
    dma1<0>(min((u32)(((idx & 3) * 8 + w) * 1024 + l * 16), len) & ~15u, (const char*)(pc & ~15ull), lbase + L_FA);
  }
  if (w == 0) dma1c((u32)((l & 1) * 4), BC, lbase + L_BC0);
  const int fkb = (b & 7) * 2 + (w >> 2), fcq = w & 3;
  {
    const u32 ks = (u32)((idx >> 1) + 16 * fkb + 256 * (4 * (idx & 1) + ((l >> 3) & 3)));
    const u32 base = ((ks * (DF_ * 2)) & ~15u) + fcq * 128;
    const u32 dlt = (u32)((const char*)wi - (const char*)wr);
    (void)dlt;
    const u32 c0 = ((l & 7) ^ (((l >> 4) & 1) << 2)) << 4;
    if (l < 32) dma1<0>(base + c0, (const char*)wr, lbase + L_FR + w * 2048);
    else dma1<0>(base + c0, (const char*)wi, lbase + L_FR + w * 2048);
    if (fcq == 3) {
      const u32 t0 = min(base + 128 + (c0 & 16), (u32)(2048 * DF_ * 2 - 16));
      if (l < 32) dma1<0>(t0, (const char*)wr, lbase + L_FR + w * 2048 + 1024);
      else dma1<0>(t0, (const char*)wi, lbase + L_FR + w * 2048 + 1024);
    }
  }
  const int p = (xcd >> 1) * 4 + idx / 7, j = (xcd & 1) * 7 + idx % 7;
  const int sh = w >> 2, fq = w & 3;
  u32 aoff[2];
#pragma unroll
  for (int i = 0; i < 2; ++i) {
    int row = (2 * w + i) * 8 + (l >> 3);
    aoff[i] = s_of_pi((p & 15) * 128 + row) * 1024 + (((l & 7) ^ ((row >> 1) & 7)) << 4) - i * 1024;
  }
  const u32 boff = (u32)(2 * w * 4 + (l >> 4)) * (F_ * 2) + j * 256 + (((l & 15) ^ ((l >> 4) << 2)) << 4);
  const u32 boff1 = boff + 4 * F_ * 2 - 1024;
  const int rot = p >> 1;
  auto issue = [&](int t, int slot) {
    const int kt = (t + rot) & 7;
    u32 dst = lbase + slot * 49152 + w * 2048;
    dma2(aoff[0], aoff[1], (const char*)x + kt * 128, dst);
    dma2(boff, boff1, (const char*)Wg + (size_t)kt * (64 * F_ * 2), dst + 16384);
    dma2(boff, boff1, (const char*)Wu + (size_t)kt * (64 * F_ * 2), dst + 32768);
  };
  const int sp = xcd * 4 + (idx - 28);
  if (ffn) issue(0, 0);

  u32 E = 0, tick = 0;
  const bool coord = (b == 224) && (w == 0);
  auto do_tick = [&]() { if (coord) { ++tick; if (l == 0) st8c(0u, BC, v2u{E, tick}); } };
  auto get_epoch = [&]() {
    const v2u b0 = *(LAS v2u*)(smem + L_BC0);
    v2u b1 = *(LAS v2u*)(smem + L_BC1);
    int spins = 0;
    while (__builtin_amdgcn_readfirstlane((b0[0] ^ b1[0]) | (b0[1] ^ b1[1])) == 0 && ++spins < SPIN_CAP) {
      __builtin_amdgcn_s_sleep(4);
      b1 = ld8c_sync(0u, BC);
    }
    E = __builtin_amdgcn_readfirstlane(b1[0]);
  };
  auto signal_a = [&]() {
    v4u r0 = *(LAS v4u*)(smem + L_RED), r1 = *(LAS v4u*)(smem + L_RED + 16);
    u32 m = max(max(max(r0[0], r0[1]), max(r0[2], r0[3])), max(max(r1[0], r1[1]), max(r1[2], r1[3])));
    if (l == 0) { st4c((u32)(FL_A + b * 4), FL, E | (m >> 7)); *(LAS u32*)(smem + L_MX + 4) = E; }
  };
  auto poll4 = [&](v4u f, u32 voff) -> v4u {
    int spins = 0;
    while (!__all((((f[0] ^ E) | (f[1] ^ E) | (f[2] ^ E) | (f[3] ^ E)) >> 8) == 0) && ++spins < SPIN_CAP) {
      do_tick();
      __builtin_amdgcn_s_sleep(12);
      f = ld16c_sync(voff, FL);
    }
    return f;
  };
  auto check_a = [&](v4u f) {
    f = poll4(f, (u32)(FL_A + l * 16));
    u32 m = wave_max(max(max(f[0], f[1]), max(f[2], f[3]))) & 0xffu;
    if (l == 0) *(LAS u32*)(smem + L_MX) = m;
  };
  if (coord) {
#pragma unroll
    for (int i = 0; i < 5; ++i) dma2c(i ? (u32)(FL_H + ((i - 1) * 128 + l) * 64) : (u32)(l * 16), i ? (u32)(FL_H + ((i - 1) * 128 + 64 + l) * 64 - 1024) : (u32)(l * 16), FL, lbase + i * 2048);
  }
  u32 sq[8];
  if (!ffn && w == 7) {
    const int kss = (sp >> 1) + 16 * (l >> 2) + 256 * (4 * (sp & 1) + (l & 3)), km = (2048 - kss) & 2047;
    const u32 o1 = (u32)(kss * DF_ * 2), o2 = (u32)(km * DF_ * 2);
    ld2(sq[0], o1, (const char*)wr); ld2(sq[1], o1, (const char*)wi); ld2(sq[2], o2, (const char*)wr); ld2(sq[3], o2, (const char*)wi);
    ld2(sq[4], o1 + 512, (const char*)wr); ld2(sq[5], o1 + 512, (const char*)wi); ld2(sq[6], o2 + 512, (const char*)wr); ld2(sq[7], o2 + 512, (const char*)wi);
  }

  {
    const float fo = (float)o, fg = (float)(4 * g);
    if (w < 2) {
      const float st = fo * (1.0f / 32.0f), a0 = fmaf(fg, st, w ? 0.25f : 0.f);
      v4u hi, lo;
#pragma unroll
      for (int e2 = 0; e2 < 4; ++e2) {
        float c0 = cosr(fmaf((float)(16 * (e2 >> 1) + 2 * (e2 & 1)), st, a0)), c1 = cosr(fmaf((float)(16 * (e2 >> 1) + 2 * (e2 & 1) + 1), st, a0));
        u32 ch = pkbf_o(c0, c1);
        hi[e2] = ch; lo[e2] = pkbf(c0 - bflo(ch), c1 - bfhi(ch));
      }
      *(LAS v4u*)(smem + L_C1 + w * 2048 + l * 16) = hi; *(LAS v4u*)(smem + L_C1 + w * 2048 + 1024 + l * 16) = lo;
    } else if (w < 5) {
      const float st = w == 2 ? (float)(o & 7) * (1.0f / 16.0f) : fo * (w == 3 ? 1.0f / 16.0f : 1.0f / 8.0f);
      const float a0 = (w == 2 && o >= 8) ? 0.25f : 0.f;
      const float sc = w == 2 ? 0.0625f : ((w == 4 && (g >> 1) != (o >> 3)) ? 0.f : 1.0f);
      v8h m;
#pragma unroll
      for (int e = 0; e < 4; ++e) { float a = fmaf(fg + (float)e, st, a0); m[e] = (_Float16)(sc * cosr(a)); m[4 + e] = (_Float16)(sc * sinr(a)); }
      *(LAS v8h*)(smem + (w == 2 ? L_C1 + 4096 : L_TAB + (w - 3) * 1024) + l * 16) = m;
    } else {
      const float st = w == 7 ? fo * (1.0f / 256.0f) : (fo + (w == 6 ? 16.f : 0.f)) * (1.0f / 512.0f);
      v4f c, sn;
#pragma unroll
      for (int e = 0; e < 4; ++e) { float a = (fg + (float)e) * st; c[e] = cosr(a); sn[e] = sinr(a); }
      LAS char* dst = smem + (w == 7 ? L_TAB + 2048 : L_C1 + 5120 + (w - 5) * 2048) + l * 16;
      *(LAS v4f*)dst = c; *(LAS v4f*)(dst + 1024) = sn;
    }
    BARRIER();
    LAS char* C1 = smem + L_C1 + l * 16;
    const v4u Fhr = *(LAS v4u*)C1, Flr = *(LAS v4u*)(C1 + 1024), Fhi = *(LAS v4u*)(C1 + 2048), Fli = *(LAS v4u*)(C1 + 3072);
    const v8h M2 = *(LAS v8h*)(C1 + 4096);
    v4f twc[2], tws[2];
#pragma unroll
    for (int u = 0; u < 2; ++u) { twc[u] = *(LAS v4f*)(C1 + 5120 + u * 2048); tws[u] = *(LAS v4f*)(C1 + 6144 + u * 2048); }
    if (ffn) WAIT_VM(8); else if (w == 7) WAIT_VM(11); else WAIT_VM(3);
    if (coord) {
      WAIT_VM(0);
      v4u f[10];
#pragma unroll
      for (int i = 0; i < 10; ++i) f[i] = *(LAS v4u*)(smem + i * 1024 + l * 16);
      u32 c = __builtin_amdgcn_readfirstlane(*(LAS u32*)(smem + L_BC0)) >> 8;
      bool hit;
      do {
        c = (c + 1) & 0xffffffu; hit = false;
#pragma unroll
        for (int i = 0; i < 10; ++i) hit |= ((f[i][0] >> 8) == c) | ((f[i][1] >> 8) == c) | ((f[i][2] >> 8) == c) | ((f[i][3] >> 8) == c);
      } while (__any(hit));
      E = c << 8;
      do_tick();
    }
    PIN(xr);
    STAMPT(3);
    const float c0 = bflo(__builtin_amdgcn_readfirstlane(xr[0]));
    v2f xv[4], s2 = {0.f, 0.f}, q2 = {0.f, 0.f};
#pragma unroll
    for (int e = 0; e < 4; ++e) {
      xv[e] = v2f{bflo(xr[e]), bfhi(xr[e])};
      v2f t = xv[e] - v2f{c0, c0};
      s2 += t; q2 = __builtin_elementwise_fma(t, t, q2);
    }
    const float dmean = wave_sum(s2[0] + s2[1]) * (1.0f / 512.0f), msq = wave_sum(q2[0] + q2[1]) * (1.0f / 512.0f);
    do_tick();
    const float mb = rbf(c0 + dmean);
    const float var = rbf(fmaxf(msq - dmean * dmean, 0.f));
    const float sd = rbf(__builtin_amdgcn_sqrtf(rbf(var + 1e-5f)));
    const float rs = __builtin_amdgcn_rcpf(sd);
#pragma unroll
    for (int e = 0; e < 4; ++e) {
      v2f t = xv[e] - v2f{mb, mb};
      u32 tb = pkbf_o(t[0], t[1]);
      t = v2f{bflo(tb), bfhi(tb)} * v2f{rs, rs};
      xr[e] = pkbf(t[0], t[1]);
    }
    *(LAS v4u*)(smem + L_IMG + w * 1024 + l * 16) = xr;
    if (l < 2) st4c((u32)(pi * 4 + l * 8192), ws + OFF_MB, l ? rs * rs : -mb);
    const v8s xf = cat8(trread(smem + L_IMG + w * 1024 + l * 8), trread(smem + L_IMG + w * 1024 + 512 + l * 8));
    const int kt1 = (1 + rot) & 7;
    const u32 dst1 = lbase + 49152 + w * 2048;
    if (ffn) dma2(aoff[0], aoff[1], (const char*)x + kt1 * 128, dst1);
    const v8s xg_ = __builtin_bit_cast(v8s, __builtin_bit_cast(v4u, xf) ^ v4u{0x80000000u, 0x80000000u, 0x80000000u, 0x80000000u});
    const v4f z = {0.f, 0.f, 0.f, 0.f};
    v4f dre[2], dim[2];
#pragma unroll
    for (int u = 0; u < 2; ++u) { dre[u] = mfma16b(u ? xg_ : xf, Flr, z); dim[u] = mfma16b(u ? xg_ : xf, Fli, z); }
#pragma unroll
    for (int u = 0; u < 2; ++u) { dre[u] = mfma16b(u ? xg_ : xf, Fhr, dre[u]); dim[u] = mfma16b(u ? xg_ : xf, Fhi, dim[u]); }
#pragma unroll
    for (int u = 0; u < 2; ++u) {
      twid(dre[u], dim[u], twc[u], tws[u]);
      const v8h bb = pack_ri(dre[u], dim[u]);
      v4f d2 = mfma16h(bb, M2, z);
      if (u == 0) {
        const u32 nv = o == 8 ? 0xac002c00u : 0u;
        v4f n = mfma16h(bb, __builtin_bit_cast(v8h, v4u{nv, nv, 0u, 0u}), z);
        if (l == 8) d2[0] = n[0];
      }
      *(LAS v4h*)(smem + L_T + ((o >> 3) * 8 + w) * 544 + (o & 7) * 64 + u * 32 + g * 8) = pack4h(d2);
    }
    if (ffn) {
      dma2(boff, boff1, (const char*)Wg + (size_t)kt1 * (64 * F_ * 2), dst1 + 16384);
      dma2(boff, boff1, (const char*)Wu + (size_t)kt1 * (64 * F_ * 2), dst1 + 32768);
    }
    STAMPT(4);
  }
  v8s su; v4u fu, tu;
  auto round1_read = [&]() {
    LAS char* rp = smem + L_T + (4 * (g & 1) + (o >> 2)) * 544 + (w * 32 + (g >> 1) * 16) * 2 + (o & 3) * 8;
    su = cat8(trread(rp), trread(rp + 8 * 544));
    LAS char* fp_ = smem + L_FR + w * 2048 + q * 128 + (((2 * g + (pp >> 1)) ^ ((q >> 1) << 2)) << 4) + (pp & 1) * 8;
    fu = __builtin_bit_cast(v4u, cat8(trread(fp_), trread(fp_ + 512)));
    LAS char* tp = smem + L_FR + w * 2048 + 1024 + q * 128 + (pp >> 1) * 16 + (pp & 1) * 8;
    tu = __builtin_bit_cast(v4u, cat8(trread(tp), trread(tp + 512)));
  };
  auto round1_max = [&]() {
    typedef unsigned short v2us __attribute__((ext_vector_type(2)));
    const int dl = (idx >> 1) & 7;
    const u32 msk = (fcq == 0 && l < dl) ? 0u : 0x7fff7fffu, msk2 = (fcq == 3 && o <= dl) ? 0x7fff7fffu : 0u;
    v2us m2 = {0, 0};
#pragma unroll
    for (int e = 0; e < 4; ++e) {
      u32 t = fu[e] & msk, t2 = tu[e] & msk2;
      m2 = __builtin_elementwise_max(m2, __builtin_elementwise_max(__builtin_bit_cast(v2us, t), __builtin_bit_cast(v2us, t2)));
    }
    const u32 mx = wave_max(max((u32)m2[0], (u32)m2[1]));
    if (l == 0) *(LAS u32*)(smem + L_RED + w * 4) = mx;
  };
  auto round1_store = [&]() {
    st16c((u32)(b * 8192 + (w * 32 + (g >> 1) * 16 + o) * 32 + (g & 1) * 16), ws + OFF_S1, su);
    st16c((u32)((idx * 16 + fkb) * 4608 + (fcq * 64 + l) * 16), ws + OFF_WT, fu);
    if (fcq == 3 && l < 16) st16c((u32)((idx * 16 + fkb) * 4608 + (256 + l) * 16), ws + OFF_WT, tu);
  };

  const int RG = xcd * 4 + (idx >> 3), ct = idx & 7;
  const int kd = xcd * 32 + ((idx + 3) & 31);
  const u32 swz = ((l & 7) ^ (((l >> 4) & 1) << 2)) << 4;
  const u32 ro0 = (u32)((l >> 3) * 1024) + swz, ro1 = ro0 + 8192 - 1024;
  const char* bsrc = (const char*)Wd + (size_t)(w * 16) * 1024 + ct * 128;
  auto issueW = [&](int n) { dma2(ro0, ro1, bsrc + (size_t)n * 131072, lbase + wslot(n) + w * 2048); };
  const int dh = w & 1, kq = w >> 1, ph = kq >> 1, kh = kq & 1;
  const int epi = RG * 64 + ph * 32 + (l & 31);
  const u32 eoff = (u32)(s_of_pi(epi) * D_ + ct * 64 + dh * 32 + kh * 16 + h * 8) * 2;
  v4u wt, s1v, xe; v4f pq[4]; float rs2;
  auto load_xe = [&]() { ld16(xe, eoff, (const char*)x); };
  auto load_s1 = [&]() { ld16c(s1v, (u32)((((4 * w + g) * 8 + (o >> 1)) * 256 + kd) * 32 + (o & 1) * 16), ws + OFF_S1); };
  auto load_wt = [&]() { ld16c(wt, (u32)((w * 64 + l) * 4608 + (kd + ((2 * w + (g >> 1)) & 7)) * 16), ws + OFF_WT); };
  auto load_rs = [&]() { ld4c(rs2, (u32)(epi * 4), ws + OFF_RS2); };
  v4f t2c, t2s, t3c, t3s;
  auto trig2 = [&]() {
    const float fw = (float)w, st2 = fw * (1.0f / 128.0f), a2 = fw * (float)(o + 64 * g) * (1.0f / 2048.0f);
#pragma unroll
    for (int e = 0; e < 4; ++e) { float b2 = fmaf((float)e, st2, a2); t2c[e] = cosr(b2); t2s[e] = sinr(b2); }
    PIN(t2c); PIN(t2s);
  };
  auto trig3 = [&]() {
    const float f7 = (float)(o & 7), st3 = f7 * (1.0f / 128.0f), a3 = f7 * (float)(2 * w + (o >> 3) + 64 * g) * (1.0f / 2048.0f);
#pragma unroll
    for (int e = 0; e < 4; ++e) { float b3 = fmaf((float)e, st3, a3); t3c[e] = cosr(b3); t3s[e] = sinr(b3); }
    PIN(t3c); PIN(t3s);
  };
  v4u f16; v4f t1c, t1s, re, im;
  const v4f z4 = {0.f, 0.f, 0.f, 0.f};
  auto imf = [](v4u f) { return __builtin_bit_cast(v8h, v4u{f[2] ^ 0x80008000u, f[3] ^ 0x80008000u, f[0], f[1]}); };
  auto segA0 = [&]() {
    f16 = *(LAS v4u*)(smem + L_TAB + l * 16);
    t1c = *(LAS v4f*)(smem + L_TAB + 2048 + l * 16); t1s = *(LAS v4f*)(smem + L_TAB + 3072 + l * 16);
  };
  auto segA1 = [&]() {
    PIN(s1v);
    const v8h a = __builtin_bit_cast(v8h, s1v);
    re = mfma16h(a, __builtin_bit_cast(v8h, f16), z4); im = mfma16h(a, imf(f16), z4);
  };
  auto segA2 = [&]() {
    twid(re, im, t1c, t1s);
    const v8h a = pack_ri(re, im);
    re = mfma16h(__builtin_bit_cast(v8h, f16), a, z4); im = mfma16h(imf(f16), a, z4);
  };
  auto segA3 = [&]() {
    twid(re, im, t2c, t2s);
    *(LAS v4h*)(smem + L_E1 + w * E1P + o * 32 + g * 8) = pack4h(re);
    *(LAS v4h*)(smem + L_E1 + (8 + w) * E1P + o * 32 + g * 8) = pack4h(im);
  };

  if (ffn) {
    v16f acc[2][2] = {};
    float mbr[2];
    const u32 wb0 = (u32)((8 * h + q) * 256 + ((fq ^ q) << 6) + blk * 32 + pp * 8);
    const u32 r1 = 16384 + sh * 16384, r2 = 32768 - sh * 16384;
    const int xrow = sh * 64 + (l & 31);
    const u32 xb0 = (u32)(xrow * 128 + ((h ^ ((xrow >> 1) & 7)) << 4));
    auto rd = [&](Frag1& f, LAS char* sb, int i) {
      LAS char* xa = sb + (xb0 ^ (i << 5));
      LAS char* wa_ = sb + wb0 + (i << 12);
      f.w1 = cat8(trread(wa_ + r1), trread(wa_ + r1 + 1024));
      f.x0 = *(LAS v8s*)xa; f.x1 = *(LAS v8s*)(xa + 4096);
      f.w2 = cat8(trread(wa_ + r2), trread(wa_ + r2 + 1024));
    };
    const u32 one = ((l & 15) == ((l >> 4) & 1)) ? 0x3f803f80u : 0u;
    const v8s sel = __builtin_bit_cast(v8s, v4u{one, one, one, one});
    v4f csa = {0.f, 0.f, 0.f, 0.f};
    Frag1 A, B;
    WAIT_VM(7);
    BARRIER();
    STAMPT(1); STAMPC(30);
    rd(A, smem, 0);
    round1_read();
    dma2(aoff[0], aoff[1], (const char*)x + ((2 + rot) & 7) * 128, lbase + 2 * 49152 + w * 2048);
    u32 c1[4], c2[4];
    static_for<0, 8>([&](auto T) {
      constexpr int t = T, slot = t % 3, nslot = (t + 1) % 3, pslot = (t + 2) % 3;
      LAS char* sb = smem + slot * 49152;
      {
        const int kt = (t + 2 + rot) & 7;
        const char* s1 = t < 6 ? (const char*)Wg + (size_t)kt * (64 * F_ * 2) : bsrc + (size_t)((t - 6) * 3 + 1) * 131072;
        const char* s2 = t < 6 ? (const char*)Wu + (size_t)kt * (64 * F_ * 2) : bsrc + (size_t)((t - 6) * 3 + 2) * 131072;
        const u32 o2 = t < 6 ? boff : ro0, o3 = t < 6 ? boff1 : ro1;
        const u32 dst = lbase + pslot * 49152 + w * 2048;
        rd(B, sb, 1); SCHED();
        acc[0][0] = mfma32b(A.w1, A.x0, acc[0][0]); acc[0][1] = mfma32b(A.w1, A.x1, acc[0][1]);
        if constexpr (t == 0) csa = mfma16b(sel, __builtin_bit_cast(v4u, A.w1), csa);
        SCHED();
        dma1<0>(o2, s1, dst + 16384);
        if constexpr (t == 2) load_xe();
        if constexpr (t == 6) load_wt();
        acc[1][0] = mfma32b(A.w2, A.x0, acc[1][0]); acc[1][1] = mfma32b(A.w2, A.x1, acc[1][1]); SCHED();
        if constexpr (t == 0) { round1_store(); round1_max(); SCHED(); }
        if constexpr (t == 3) { trig2(); SCHED(); }
        if constexpr (t == 4) { trig3(); SCHED(); }
        rd(A, sb, 2); SCHED();
        acc[0][0] = mfma32b(B.w1, B.x0, acc[0][0]); SCHED();
        dma1<1024>(o3, s1, dst + 16384);
        acc[0][1] = mfma32b(B.w1, B.x1, acc[0][1]);
        if constexpr (t == 0) csa = mfma16b(sel, __builtin_bit_cast(v4u, B.w1), csa);
        acc[1][0] = mfma32b(B.w2, B.x0, acc[1][0]); acc[1][1] = mfma32b(B.w2, B.x1, acc[1][1]); SCHED();
        dma1<0>(o2, s2, dst + 32768);
        if constexpr (t == 6) {
          load_rs();
        }
        if constexpr (t == 7) { WAIT_VM(13); segA1(); SCHED(); }
        rd(B, sb, 3); SCHED();
        if constexpr (t == 7) {
#pragma unroll
          for (int i = 0; i < 4; ++i) { PIN(c1[i]); PIN(c2[i]); }
          PIN(mbr[0]); PIN(mbr[1]);
          const v8s cs1 = __builtin_bit_cast(v8s, v4u{c1[0], c1[1], c1[2], c1[3]}), cs2 = __builtin_bit_cast(v8s, v4u{c2[0], c2[1], c2[2], c2[3]});
          const u32 m0_ = pkbf(mbr[0], mbr[0]), m1_ = pkbf(mbr[1], mbr[1]);
          const v8s mb0 = __builtin_bit_cast(v8s, v4u{m0_, m0_, m0_, m0_}), mb1 = __builtin_bit_cast(v8s, v4u{m1_, m1_, m1_, m1_});
          acc[0][0] = mfma32b(cs1, mb0, acc[0][0]); acc[1][0] = mfma32b(cs2, mb0, acc[1][0]);
          acc[0][1] = mfma32b(cs1, mb1, acc[0][1]); acc[1][1] = mfma32b(cs2, mb1, acc[1][1]); SCHED();
          segA2(); SCHED();
        }
        acc[0][0] = mfma32b(A.w1, A.x0, acc[0][0]); acc[0][1] = mfma32b(A.w1, A.x1, acc[0][1]);
        if constexpr (t == 0) csa = mfma16b(sel, __builtin_bit_cast(v4u, A.w1), csa);
        SCHED();
        dma1<1024>(o3, s2, dst + 32768);
        acc[1][0] = mfma32b(A.w2, A.x0, acc[1][0]); acc[1][1] = mfma32b(A.w2, A.x1, acc[1][1]); SCHED();
      }
      if constexpr (t < 7) {
        const char* s0 = t < 5 ? (const char*)x + ((t + 3 + rot) & 7) * 128 : bsrc + (size_t)((t - 5) * 3) * 131072;
        const u32 o0 = t < 5 ? aoff[0] : ro0, o1 = t < 5 ? aoff[1] : ro1;
        const u32 dst = lbase + slot * 49152 + w * 2048;
        if constexpr (t == 0) csa = mfma16b(sel, __builtin_bit_cast(v4u, B.w1), csa);
        WAIT_VM(t == 0 ? 8 : t == 2 ? 7 : t == 6 ? 19 : 6);
        if constexpr (t == 5) { if (w == 0) check_a(*(LAS v4u*)(smem + L_FA + l * 16)); }
        BARRIER();
        STAMPT(22 + t);
        acc[0][0] = mfma32b(B.w1, B.x0, acc[0][0]); SCHED();
        rd(A, smem + nslot * 49152, 0);
        if constexpr (t == 6) segA0();
        SCHED();
        if constexpr (t == 0) {
          if (l < 16) {
#pragma unroll
            for (int i = 0; i < 2; ++i) {
              const float hi = rbf(csa[i]);
              st4c((u32)(((j * 16 + p) * 256 + sh * 128 + fq * 32 + i * 16 + l) * 4), ws + OFF_CSP, pkbf(hi, csa[i] - hi));
            }
          }
        }
        if constexpr (t == 0) { if (w == 0) dma1c((u32)((l & 1) * 4), BC, lbase + L_BC1); }
        if constexpr (t == 4) { if (w == 0) dma1s<0>((u32)(FL_A + l * 16), FL, lbase + L_FA); }
        if constexpr (t == 5) {
          const u32 co = (u32)(((j * 16 + (p & 1) + 8 * h) * 256 + fq * 32 + (l & 31)) * 4);
#pragma unroll
          for (int i = 0; i < 4; ++i) { ld4c(c1[i], co + sh * 512 + i * 2048, ws + OFF_CSP); ld4c(c2[i], co + (1 - sh) * 512 + i * 2048, ws + OFF_CSP); }
          ld4c(mbr[0], (u32)((p * 128 + xrow) * 4), ws + OFF_MB); ld4c(mbr[1], (u32)((p * 128 + xrow + 32) * 4), ws + OFF_MB);
          load_s1();
        }
        dma1<0>(o0, s0, dst);
        acc[0][1] = mfma32b(B.w1, B.x1, acc[0][1]);
        acc[1][0] = mfma32b(B.w2, B.x0, acc[1][0]);
        acc[1][1] = mfma32b(B.w2, B.x1, acc[1][1]); SCHED();
        dma1<1024>(o1, s0, dst);
        if constexpr (t == 1) { if (w == 0) { get_epoch(); signal_a(); } }
        SCHED();
      } else {
        acc[0][0] = mfma32b(B.w1, B.x0, acc[0][0]); acc[1][0] = mfma32b(B.w2, B.x0, acc[1][0]); SCHED();
        acc[0][1] = mfma32b(B.w1, B.x1, acc[0][1]); acc[1][1] = mfma32b(B.w2, B.x1, acc[1][1]); SCHED();
      }
    });
    STAMPT(2); STAMPC(31);
    auto fin = [&](const v16f& ag, const v16f& au, int rt) {
#pragma unroll
      for (int s2i = 0; s2i < 2; ++s2i) {
        v4u ov;
#pragma unroll
        for (int e = 0; e < 4; ++e) {
          const int r = s2i * 8 + 2 * e;
          v2f gg = v2f{relu(ag[r]), relu(ag[r + 1])} * v2f{au[r], au[r + 1]};
          ov[e] = pkbf(gg[0], gg[1]);
        }
        st16c((u32)(((p * 4 + sh * 2 + rt) * 112 + (j * 8 + fq * 2 + s2i)) * 1024 + l * 16), ws + OFF_H, ov);
      }
    };
    if (sh == 0) { fin(acc[0][0], acc[1][0], 0); fin(acc[0][1], acc[1][1], 1); }
    else { fin(acc[1][0], acc[0][0], 0); fin(acc[1][1], acc[0][1], 1); }
    STAMPT(6);
    segA3();
    WAIT_VM(4);
  } else {
    WAIT_VM(1);
    BARRIER();
    round1_read(); round1_store(); round1_max();
    if (w == 0 && !coord) dma1c((u32)((l & 1) * 4), BC, lbase + L_BC1);
    do_tick();
    if (w == 7) {
#pragma unroll
      for (int i = 0; i < 8; ++i) PIN(sq[i]);
      float war = bflo(sq[0]) + bflo(sq[2]), wai = bflo(sq[1]) - bflo(sq[3]);
      float wbr = bflo(sq[4]) + bflo(sq[6]), wbi = bflo(sq[5]) - bflo(sq[7]);
      v4f pv = {0.25f * (war + wbr), 0.25f * (wai + wbi), 0.25f * (war - wbr), 0.25f * (wai - wbi)};
      st16c((u32)((((sp >> 2) * 4 + (l & 3)) * 64 + (sp & 3) * 16 + (l >> 2)) * 16), ws + OFF_PQ, pv);
    }
    WAIT_VM(0);
    BARRIER();
    STAMPT(1); STAMPC(30);
    if (w == 0) { if (!coord) get_epoch(); signal_a(); do_tick(); WAIT_VM(0); }
    BARRIER();
    STAMPT(24);
    dma1c((u32)(sp * 57344 + (w * 112 + l) * 64), (const char*)Wd, lbase + 49152);
    dma1c((u32)(sp * 57344 + (w * 112 + 48 + l) * 64), (const char*)Wd, lbase + 49152);
    static_for<0, 6>([&](auto N) { issueW(N); });
    static_for<6, 14>([&](auto N) { dma2(ro0, ro1, bsrc - 512 + (size_t)N * 131072, lbase + 49152 + w * 2048); });
    load_xe();
    trig2(); trig3(); segA0();
    STAMPT(25);
    if (w == 0) {
      if (!coord) __builtin_amdgcn_s_sleep(SLEEP_A);
      check_a(v4u{~E, 0u, 0u, 0u});
    }
    STAMPT(26);
    BARRIER();
    load_s1(); load_wt(); load_rs();
    if (kd == 0) {
#pragma unroll
      for (int r = 0; r < 4; ++r) ld16c(pq[r], (u32)(w * 4096 + r * 1024 + l * 16), ws + OFF_PQ);
    }
    WAIT_VM(0);
    segA1(); segA2(); segA3();
    STAMPT(2); STAMPC(31);
  }
  PIN(wt); PIN(xe); PIN(rs2);
  BARRIER();
  STAMPT(5);

  const u32 trG = (u32)(kq * 4096 + (4 * h + q) * 128 + ((dh ^ ((q >> 1) & 1)) << 6) + (blk ^ kh) * 32 + pp * 8);
  v8s Wf[14][2];
  auto readW = [&](auto N) {
    constexpr int n = N;
    LAS char* sb = smem + wslot(n) + trG;
    Wf[n][0] = cat8(trread(sb), trread(sb + 1024)); Wf[n][1] = cat8(trread(sb + 2048), trread(sb + 3072));
  };
  const char* hsrc = ws + OFF_H + ((size_t)(RG * 2 + (w >> 2)) * 112 + (w & 3) * 2) * 1024;
  const u32 so0 = (u32)((l >> 3) * 4096) + swz, so1 = so0 + 32768 - 1024;
  const char* ssrc = ws + OFF_S2 + (size_t)(w * 16) * 4096 + RG * 128;
  auto issueP = [&](int n, auto HALF) {
    constexpr int hf = HALF;
    if (n < 14) dma1s<hf * 1024>((u32)(l * 16), hsrc + n * 8192, lbase + aslot(n) + w * 2048);
    else dma1s<hf * 1024>(hf ? so1 : so0, ssrc + (size_t)(n - 14) * (128 * 4096), lbase + aslot(n) + w * 2048);
  };
  const int d = ct * 64 + dh * 32 + ((l & 31) ^ (kh << 4));
  const float gst = (float)d * (1.0f / 512.0f), ga0 = (float)(16 * kq + 2 * h) * gst;
  v8h G[8];
  auto gen = [&](auto FI) {
    constexpr int f = FI;
#pragma unroll
    for (int e2 = 0; e2 < 4; ++e2) {
      float a = fmaf((float)(64 * (f >> 1) + 8 * (f & 1) + 4 * (e2 >> 1) + (e2 & 1)), gst, ga0);
      float c = cosr(a), sn = sinr(a);
      if (f == 0 && e2 == 0) { if (kq == 0 && h == 0) { c = 0.5f; sn = (d & 1) ? 0.5f : -0.5f; } }
      G[f][2 * e2] = (_Float16)c; G[f][2 * e2 + 1] = (_Float16)sn;
    }
    PIN(G[f]);
  };

  float oscale;
  const u32 fhoff = (u32)(FL_H + (RG * 16 + (min(l, 55) >> 2)) * 64 + (l & 3) * 4);
  u32 fp;
  {
    v8h a;
    {
      LAS char* rp = smem + L_E1 + (4 * (g & 1) + (o >> 2)) * E1P + (2 * w + (g >> 1)) * 32 + (o & 3) * 8;
      a = __builtin_bit_cast(v8h, cat8(trread(rp), trread(rp + 8 * E1P)));
    }
    const v4u f8 = *(LAS v4u*)(smem + L_TAB + 1024 + l * 16);
    const v2u me = *(LAS v2u*)(smem + L_MX);
    issueW(6);
    static_for<0, 3>(readW);
    const int Ex = (int)me[0] < 16 ? 16 : ((int)me[0] > 240 ? 240 : (int)me[0]);
    const float wscale = __builtin_bit_cast(float, (u32)(246 - Ex) << 23);
    oscale = __builtin_bit_cast(float, (u32)(Ex - 7) << 23);
    re = mfma16h(__builtin_bit_cast(v8h, f8), a, z4); im = mfma16h(imf(f8), a, z4);
    static_for<3, 6>(readW);
    WAIT_VM(2);
    STAMPT(7);
    if (ffn && l == 0) st4c((u32)(FL_H + ((p * 2 + sh) * 16 + j) * 64 + fq * 4), FL, me[1]);
    issueW(7);
    if (kd != 0) {
#pragma unroll
      for (int r = 0; r < 4; ++r) {
        float fr = ((r & 1) ? bfhi(wt[r >> 1]) : bflo(wt[r >> 1])) * wscale, fi = ((r & 1) ? bfhi(wt[2 + (r >> 1)]) : bflo(wt[2 + (r >> 1)])) * wscale;
        float zr = re[r], zi = im[r];
        re[r] = zr * fr - zi * fi;
        im[r] = -(zr * fi + zi * fr);
      }
    } else {
      const int ka = 2 * w + (g >> 1), kr = o + 64 * (g & 1);
#pragma unroll
      for (int r = 0; r < 4; ++r) { v2f t = {re[r], im[r]}; *(LAS v2f*)(smem + L_ZX + ka * 1024 + (kr + 16 * r) * 8) = t; }
      BARRIER();
#pragma unroll
      for (int r = 0; r < 4; ++r) {
        PIN(pq[r]);
        const int km = (2048 - (ka + 16 * (kr + 16 * r))) & 2047;
        v2f zm = *(LAS v2f*)(smem + L_ZX + (km & 15) * 1024 + (km >> 4) * 8);
        float zr = re[r], zi = im[r];
        re[r] = (zr * pq[r][0] - zi * pq[r][1] + zm[0] * pq[r][2] + zm[1] * pq[r][3]) * wscale;
        im[r] = -(zr * pq[r][1] + zi * pq[r][0] + zm[0] * pq[r][3] - zm[1] * pq[r][2]) * wscale;
      }
    }
    LAS char* E2 = smem + (kd == 0 ? L_E1 : L_E2);
    a = pack_ri(re, im);
    re = mfma16h(a, __builtin_bit_cast(v8h, f8), z4); im = mfma16h(a, imf(f8), z4);
    twid(re, im, t3c, t3s);
    *(LAS v4h*)(E2 + (o & 7) * E2P + (2 * w + (o >> 3)) * 32 + g * 8) = pack4h(re);
    *(LAS v4h*)(E2 + (o & 7) * E2P + 512 + (2 * w + (o >> 3)) * 32 + g * 8) = pack4h(im);
    BARRIER();
    STAMPT(8);
    {
      LAS char* rp = E2 + w * E2P + o * 32 + g * 8;
      a = __builtin_bit_cast(v8h, cat8(*(LAS v4s*)rp, *(LAS v4s*)(rp + 512)));
    }
    issueW(8);
    re = mfma16h(a, __builtin_bit_cast(v8h, f16), z4); im = mfma16h(a, imf(f16), z4);
    issueW(9);
    twid(re, im, t1c, t1s);
    a = pack_ri(re, im);
    re = mfma16h(__builtin_bit_cast(v8h, f16), a, z4); im = mfma16h(imf(f16), a, z4);
    issueW(10);
    st8c((u32)(2 * kd * 4096 + w * 512 + l * 8), ws + OFF_S2, pack4h(re));
    st8c((u32)((2 * kd + 1) * 4096 + w * 512 + l * 8), ws + OFF_S2, pack4h(im));
  }
  STAMPT(9);
  gen(IC<0>{}); issueW(11);
  WAIT_VM(8);
  BARRIER();
  static_for<6, 9>(readW);
  gen(IC<1>{}); issueW(12); gen(IC<2>{}); issueW(13);
  if (!ffn) {
    WAIT_VM(0);
    BARRIER();
    if (w == 0) {
      if (l == 0) st4c((u32)(FL_S + b * 4), FL, E);
      __builtin_amdgcn_s_sleep(16);
    }
  }
  if (w == 0) ld4c(fp, fhoff, FL);
  gen(IC<3>{}); gen(IC<4>{});
  WAIT_VM(0);
  if (w == 0) {
    PIN(fp);
    u32 f = fp;
    int spins = 0;
    while (!__all(f == E) && ++spins < SPIN_CAP) {
      __builtin_amdgcn_s_sleep(4);
      f = ld4c_sync(fhoff, FL);
    }
  }
  BARRIER();
  STAMPT(10);
  if (ffn && tid == 0) st4c((u32)(FL_S + b * 4), FL, E);
  issueP(0, IC<0>{}); issueP(0, IC<1>{}); issueP(1, IC<0>{}); issueP(1, IC<1>{});
  static_for<9, 14>(readW);
  gen(IC<5>{});
  issueP(2, IC<0>{}); issueP(2, IC<1>{});
  gen(IC<6>{});
  issueP(3, IC<0>{}); issueP(3, IC<1>{});
  gen(IC<7>{});

  const u32 trS = (u32)(kq * 4096 + (4 * h + q) * 128 + ((ph ^ ((q >> 1) & 1)) << 6) + blk * 32 + pp * 8);
  const u32 hb = (u32)(ph * 8192 + kq * 2048 + l * 16);
  v16f accG[2] = {}, accS[2] = {};
  v8s fa[8], fb[8];
  v4u fs;
  float gv[8];
  const u32 xw = (u32)(w * 6144 + l * 16);
  auto xchg_out = [&](const v16f (&ac)[2], int base) {
    LAS char* X = smem + base + xw;
#pragma unroll
    for (int i = 0; i < 6; ++i) {
      const v16f& a = ac[(i + 2) >> 2]; const int r = ((i + 2) & 3) * 4;
      *(LAS v4f*)(X + i * 1024) = v4f{a[r], a[r + 1], a[r + 2], a[r + 3]};
    }
  };
  auto xchg_in = [&](const v16f (&ac)[2], int base, float (&o8)[8]) {
    v4f s0 = {ac[0][0], ac[0][1], ac[0][2], ac[0][3]}, s1 = {ac[0][4], ac[0][5], ac[0][6], ac[0][7]};
#pragma unroll
    for (int x_ = 1; x_ < 4; ++x_) {
      LAS char* X = smem + base + (w ^ (2 * x_)) * 6144 + (x_ - 1) * 2048 + l * 16;
      s0 += *(LAS v4f*)X; s1 += *(LAS v4f*)(X + 1024);
    }
#pragma unroll
    for (int e = 0; e < 4; ++e) { o8[e] = s0[e]; o8[4 + e] = s1[e]; }
  };
  v4f xg[6];
  float ov[8], ex[8];
  auto sadd = [](float a, float b) { float r; asm("v_add_f32 %0, %1, %2" : "=v"(r) : "v"(a), "v"(b)); return r; };
  auto smul = [](float a, float b) { float r; asm("v_mul_f32 %0, %1, %2" : "=v"(r) : "v"(a), "v"(b)); return r; };
  auto silu_step = [&](int i) {
    if (i < 3) {
#pragma unroll
      for (int e = 0; e < 8; ++e) { ov[e] = sadd(i == 0 ? accG[0][e] : ov[e], xg[2 * i + (e >> 2)][e & 3]); PIN(ov[e]); }
    } else if (i == 3) {
#pragma unroll
      for (int e = 0; e < 8; e += 2) { u32 t = pkbf_o(smul(ov[e], rs2), smul(ov[e + 1], rs2)); ov[e] = bflo(t); ov[e + 1] = bfhi(t); PIN(ov[e]); PIN(ov[e + 1]); }
    } else if (i == 4) {
#pragma unroll
      for (int e = 0; e < 8; ++e) { ex[e] = __builtin_amdgcn_exp2f(ov[e] * -1.44269504088896f); PIN(ex[e]); }
    } else if (i == 5) {
#pragma unroll
      for (int e = 0; e < 8; ++e) { ex[e] = __builtin_amdgcn_rcpf(1.0f + ex[e]); PIN(ex[e]); }
    } else if (i == 6) {
#pragma unroll
      for (int e = 0; e < 8; e += 2) { u32 t = pkbf_o(ov[e] * ex[e], ov[e + 1] * ex[e + 1]); gv[e] = bflo(t); gv[e + 1] = bfhi(t); PIN(gv[e]); PIN(gv[e + 1]); }
    }
  };
  static_for<0, 10>([&](auto V) {
    constexpr int v = V;
    v8s(&cur)[8] = (v & 1) ? fb : fa;
    v8s(&prv)[8] = (v & 1) ? fa : fb;
    auto mmp = [&](int s) {
      if constexpr (v >= 8) {
        accS[0] = mfma32h(G[(v - 8) * 4 + s], prv[2 * s], accS[0]); accS[1] = mfma32h(G[(v - 8) * 4 + s], prv[2 * s + 1], accS[1]);
        SCHED();
        if ((v - 8) * 4 + s >= 1) silu_step((v - 8) * 4 + s - 1);
      } else if constexpr (v >= 1) {
        accG[0] = mfma32b(Wf[2 * (v - 1) + (s >> 1)][s & 1], prv[2 * s], accG[0]);
        accG[1] = mfma32b(Wf[2 * (v - 1) + (s >> 1)][s & 1], prv[2 * s + 1], accG[1]);
      }
      SCHED();
    };
    if constexpr (v < 9) {
      WAIT_VM(v <= 7 ? 4 : 0);
      if constexpr (v == 5) { if (w == 0) { PIN(fs); poll4(fs, (u32)(FL_S + l * 16)); } }
      BARRIER();
      if constexpr (v == 0 || v == 8) STAMPT(11 + v);
      if constexpr (v == 4) { if (w == 0) ld16c(fs, (u32)(FL_S + l * 16), FL); }
      if constexpr (v == 8) {
#pragma unroll
        for (int x_ = 1; x_ < 4; ++x_) {
          LAS char* X = smem + 49152 + (w ^ (2 * x_)) * 6144 + (x_ - 1) * 2048 + l * 16;
          xg[2 * x_ - 2] = *(LAS v4f*)X; xg[2 * x_ - 1] = *(LAS v4f*)(X + 1024);
        }
        SCHED();
      }
      mmp(0);
#pragma unroll
      for (int s = 0; s < 4; ++s) {
        if constexpr (v >= 7) {
          LAS char* sb = smem + aslot(2 * v + (s >> 1)) + (s & 1) * 2048;
          cur[2 * s] = cat8(trread(sb + trS), trread(sb + trS + 1024));
          cur[2 * s + 1] = cat8(trread(sb + (trS ^ 64)), trread(sb + (trS ^ 64) + 1024));
        } else {
          LAS char* sb = smem + aslot(2 * v + (s >> 1)) + (s & 1) * 1024;
          cur[2 * s] = *(LAS v8s*)(sb + hb);
          cur[2 * s + 1] = *(LAS v8s*)(sb + (hb ^ 8192));
        }
      }
      SCHED();
      if constexpr (v <= 6) issueP(2 * v + 4, IC<0>{});
      mmp(1);
      if constexpr (v <= 6) issueP(2 * v + 4, IC<1>{});
      mmp(2);
      if constexpr (v <= 6) issueP(2 * v + 5, IC<0>{});
      mmp(3);
      if constexpr (v <= 6) issueP(2 * v + 5, IC<1>{});
    } else {
      mmp(0); mmp(1); mmp(2); mmp(3);
    }
    if constexpr (v == 7) xchg_out(accG, 49152);
    SCHED();
  });
  xchg_out(accS, 0);
  BARRIER();
  {
    float o8[8], pv[8];
    xchg_in(accS, 0, o8);
#pragma unroll
    for (int e = 0; e < 8; e += 2) { u32 t = pkbf_o(o8[e] * oscale, o8[e + 1] * oscale); pv[e] = bflo(t) * gv[e]; pv[e + 1] = bfhi(t) * gv[e + 1]; }
    u32 a0 = pkbf(pv[0], pv[1]), a1 = pkbf(pv[2], pv[3]), b0 = pkbf(pv[4], pv[5]), b1 = pkbf(pv[6], pv[7]);
    auto r0 = __builtin_amdgcn_permlane32_swap(a0, b0, false, false);
    auto r1 = __builtin_amdgcn_permlane32_swap(a1, b1, false, false);
    v4u pk = {r0[0], r1[0], r0[1], r1[1]};
    v4u ov4;
#pragma unroll
    for (int e = 0; e < 4; ++e) ov4[e] = pkbf(bflo(xe[e]) + bflo(pk[e]), bfhi(xe[e]) + bfhi(pk[e]));
    *(v4u*)((char*)out + eoff) = ov4;
  }
  asm volatile("km_pf_b:");
  STAMPT(20);
  STAMPT(21);
  STAMP_OUT(ws);
}

extern "C" void kernel_launch(void* const* d_in, const int* in_sizes, int n_in, void* d_out, int out_size, void* d_ws,
                              size_t ws_size, hipStream_t stream) {
  kmain<<<256, 512, 0, stream>>>((const u16*)d_in[0], (const u16*)d_in[1], (const u16*)d_in[2], (const u16*)d_in[4],
                                 (const u16*)d_in[5], (const u16*)d_in[6], (char*)d_ws + WS_BASE(ws_size), (u16*)d_out);
}
```

```cpp
#include <hip/hip_runtime.h>
#include <stdint.h>

typedef unsigned short u16;
typedef unsigned int u32;
typedef unsigned long long u64;
typedef short v4s __attribute__((ext_vector_type(4)));
typedef short v8s __attribute__((ext_vector_type(8)));
typedef _Float16 v4h __attribute__((ext_vector_type(4)));
typedef _Float16 v8h __attribute__((ext_vector_type(8)));
typedef __bf16 v2b __attribute__((ext_vector_type(2)));
typedef __bf16 v8b __attribute__((ext_vector_type(8)));
typedef float v2f __attribute__((ext_vector_type(2)));
typedef float v4f __attribute__((ext_vector_type(4)));
typedef float v16f __attribute__((ext_vector_type(16)));
typedef u32 v2u __attribute__((ext_vector_type(2)));
typedef u32 v4u __attribute__((ext_vector_type(4)));

#define LAS __attribute__((address_space(3)))
#define DEVI __device__ __forceinline__

constexpr int D_ = 512, F_ = 1792, DF_ = 257;
constexpr size_t OFF_H = 0;
constexpr size_t OFF_S1 = (size_t)8 << 20;
constexpr size_t OFF_WT = (size_t)10 << 20;
constexpr size_t OFF_S2 = (size_t)13 << 20;
constexpr size_t OFF_M = (size_t)15 << 20;
constexpr size_t OFF_FL = OFF_M;
constexpr u32 FL_S = 0, FL_A = 1024, FL_H = 2048;
constexpr size_t OFF_BC = OFF_M + 49152;
constexpr size_t OFF_MB = OFF_M + 98304;
constexpr size_t OFF_RS2 = OFF_M + 106496;
constexpr size_t OFF_PQ = OFF_M + 65536;
constexpr size_t OFF_CSP = OFF_M + 131072;
constexpr int SPIN_CAP = 1 << 16;


#define WS_BASE(n) (((n) - ((size_t)16 << 20)) & ~(size_t)0x1fffff)
template <int I> struct IC { static constexpr int v = I; constexpr operator int() const { return I; } };
template <int B, int E, class Fn> DEVI void static_for(Fn&& f) {
  if constexpr (B < E) { f(IC<B>{}); static_for<B + 1, E>(f); }
}
#define WAIT_VM(n) asm volatile("s_waitcnt vmcnt(%0)" ::"n"(n) : "memory")
#define BARRIER() asm volatile("s_waitcnt lgkmcnt(0)\n\ts_barrier" ::: "memory")
#define SCHED() __builtin_amdgcn_sched_barrier(0)
#define PIN(x) asm volatile("" : "+v"(x))

DEVI int s_of_pi(int pi) {
  int RG = pi >> 6, i = pi & 63;
  return 128 * (4 * (RG & 3) + (i & 3)) + 8 * (i >> 2) + (RG >> 2);
}
DEVI void dma2(u32 voff0, u32 voff1, const char* sbase, u32 lds) {
  asm volatile("s_mov_b32 m0, %3\n\ts_nop 0\n\tglobal_load_lds_dwordx4 %0, %2\n\tglobal_load_lds_dwordx4 %1, %2 offset:1024" ::"v"(voff0), "v"(voff1), "s"(sbase), "s"(lds) : "memory");
}
DEVI void dma2c(u32 voff0, u32 voff1, const char* sbase, u32 lds) {
  asm volatile("s_mov_b32 m0, %3\n\ts_nop 0\n\tglobal_load_lds_dwordx4 %0, %2 sc1\n\tglobal_load_lds_dwordx4 %1, %2 offset:1024 sc1" ::"v"(voff0), "v"(voff1), "s"(sbase), "s"(lds) : "memory");
}
template <int OFF> DEVI void dma1(u32 voff, const char* sbase, u32 lds) {
  asm volatile("s_mov_b32 m0, %2\n\ts_nop 0\n\tglobal_load_lds_dwordx4 %0, %1 offset:%3" ::"v"(voff), "s"(sbase), "s"(lds), "n"(OFF) : "memory");
}
template <int OFF> DEVI void dma1s(u32 voff, const char* sbase, u32 lds) {
  asm volatile("s_mov_b32 m0, %2\n\ts_nop 0\n\tglobal_load_lds_dwordx4 %0, %1 offset:%3 sc1" ::"v"(voff), "s"(sbase), "s"(lds), "n"(OFF) : "memory");
}
DEVI void dma1c(u32 voff, const char* sbase, u32 lds) {
  asm volatile("s_mov_b32 m0, %2\n\ts_nop 0\n\tglobal_load_lds_dword %0, %1 sc1" ::"v"(voff), "s"(sbase), "s"(lds) : "memory");
}
DEVI v2u ld8c_sync(u32 voff, const char* sb) { v2u d; asm volatile("global_load_dwordx2 %0, %1, %2 sc1\n\ts_waitcnt vmcnt(0)" : "=&v"(d) : "v"(voff), "s"(sb) : "memory"); return d; }
DEVI u32 ld4c_sync(u32 voff, const char* sb) { u32 d; asm volatile("global_load_dword %0, %1, %2 sc1\n\ts_waitcnt vmcnt(0)" : "=&v"(d) : "v"(voff), "s"(sb) : "memory"); return d; }
DEVI v4u ld16c_sync(u32 voff, const char* sb) { v4u d; asm volatile("global_load_dwordx4 %0, %1, %2 sc1\n\ts_waitcnt vmcnt(0)" : "=&v"(d) : "v"(voff), "s"(sb) : "memory"); return d; }
template <class T> DEVI void ld16c(T& d, u32 voff, const char* sb) { asm volatile("global_load_dwordx4 %0, %1, %2 sc1" : "=&v"(d) : "v"(voff), "s"(sb) : "memory"); }
template <class T> DEVI void ld4c(T& d, u32 voff, const char* sb) { asm volatile("global_load_dword %0, %1, %2 sc1" : "=&v"(d) : "v"(voff), "s"(sb) : "memory"); }
template <class T> DEVI void ld16(T& d, u32 voff, const char* sb) { asm volatile("global_load_dwordx4 %0, %1, %2" : "=&v"(d) : "v"(voff), "s"(sb) : "memory"); }
DEVI void ld2(u32& d, u32 voff, const char* sb) { asm volatile("global_load_ushort %0, %1, %2" : "=&v"(d) : "v"(voff), "s"(sb) : "memory"); }
template <class T> DEVI void st16c(u32 voff, const char* sb, T v) { asm volatile("global_store_dwordx4 %0, %1, %2 sc1\n\ts_nop 1" ::"v"(voff), "v"(v), "s"(sb) : "memory"); }
template <class T> DEVI void st8c(u32 voff, const char* sb, T v) { asm volatile("global_store_dwordx2 %0, %1, %2 sc1" ::"v"(voff), "v"(v), "s"(sb) : "memory"); }
template <class T> DEVI void st4c(u32 voff, const char* sb, T v) { asm volatile("global_store_dword %0, %1, %2 sc1" ::"v"(voff), "v"(v), "s"(sb) : "memory"); }

DEVI v4s trread(LAS char* p) { return __builtin_amdgcn_ds_read_tr16_b64_v4i16((LAS v4s*)p); }
DEVI v8s cat8(v4s a, v4s b) { return __builtin_shufflevector(a, b, 0, 1, 2, 3, 4, 5, 6, 7); }
DEVI float bflo(u32 v) { return __builtin_bit_cast(float, v << 16); }
DEVI float bfhi(u32 v) { return __builtin_bit_cast(float, v & 0xffff0000u); }
DEVI float relu(float v) { return __builtin_bit_cast(float, max(__builtin_bit_cast(int, v), 0)); }
DEVI float rbf(float v) { return (float)(__bf16)v; }
DEVI u32 pkbf(float a, float b) { v2b p = {(__bf16)a, (__bf16)b}; return __builtin_bit_cast(u32, p); }
DEVI u32 pkbf_o(float a, float b) { u32 r = pkbf(a, b); asm("" : "+v"(r)); return r; }
DEVI float cosr(float rev) { return __builtin_amdgcn_cosf(rev); }
DEVI float sinr(float rev) { return __builtin_amdgcn_sinf(rev); }
DEVI v4f mfma16h(v8h a, v8h b, v4f c) { return __builtin_amdgcn_mfma_f32_16x16x32_f16(a, b, c, 0, 0, 0); }
DEVI v4f mfma16b(v8s a, v4u b, v4f c) { return __builtin_amdgcn_mfma_f32_16x16x32_bf16(__builtin_bit_cast(v8b, a), __builtin_bit_cast(v8b, b), c, 0, 0, 0); }
DEVI v16f mfma32b(v8s a, v8s b, v16f c) { return __builtin_amdgcn_mfma_f32_32x32x16_bf16(__builtin_bit_cast(v8b, a), __builtin_bit_cast(v8b, b), c, 0, 0, 0); }
DEVI v16f mfma32h(v8h a, v8s b, v16f c) { return __builtin_amdgcn_mfma_f32_32x32x16_f16(a, __builtin_bit_cast(v8h, b), c, 0, 0, 0); }
DEVI v8h pack_ri(v4f re, v4f im) {
  v8h r;
  r[0] = (_Float16)re[0]; r[1] = (_Float16)re[1]; r[2] = (_Float16)re[2]; r[3] = (_Float16)re[3];
  r[4] = (_Float16)im[0]; r[5] = (_Float16)im[1]; r[6] = (_Float16)im[2]; r[7] = (_Float16)im[3];
  return r;
}
DEVI v4h pack4h(v4f a) { v4h r; r[0] = (_Float16)a[0]; r[1] = (_Float16)a[1]; r[2] = (_Float16)a[2]; r[3] = (_Float16)a[3]; return r; }
DEVI void twid(v4f& re, v4f& im, const v4f& c, const v4f& s) {
#pragma unroll
  for (int r = 0; r < 4; ++r) { float a = re[r], b = im[r]; re[r] = a * c[r] + b * s[r]; im[r] = b * c[r] - a * s[r]; }
}
template <int CTRL> DEVI float dppadd(float v) {
  return v + __builtin_bit_cast(float, __builtin_amdgcn_update_dpp(0, __builtin_bit_cast(int, v), CTRL, 0xf, 0xf, true));
}
DEVI float wave_sum(float v) {
  v = dppadd<0xB1>(v); v = dppadd<0x4E>(v); v = dppadd<0x141>(v); v = dppadd<0x140>(v);
  int bb = __builtin_bit_cast(int, v);
  float a0 = __builtin_bit_cast(float, __builtin_amdgcn_readlane(bb, 0)), a1 = __builtin_bit_cast(float, __builtin_amdgcn_readlane(bb, 16));
  float a2 = __builtin_bit_cast(float, __builtin_amdgcn_readlane(bb, 32)), a3 = __builtin_bit_cast(float, __builtin_amdgcn_readlane(bb, 48));
  return (a0 + a1) + (a2 + a3);
}
template <int CTRL> DEVI u32 dppmax(u32 v) { return max(v, (u32)__builtin_amdgcn_update_dpp(0, (int)v, CTRL, 0xf, 0xf, true)); }
DEVI u32 wave_max(u32 v) {
  v = dppmax<0xB1>(v); v = dppmax<0x4E>(v); v = dppmax<0x141>(v); v = dppmax<0x140>(v);
  u32 a0 = __builtin_amdgcn_readlane(v, 0), a1 = __builtin_amdgcn_readlane(v, 16), a2 = __builtin_amdgcn_readlane(v, 32), a3 = __builtin_amdgcn_readlane(v, 48);
  return max(max(a0, a1), max(a2, a3));
}

struct Frag1 { v8s x0, x1, w1, w2; };

constexpr int L_SCR = 147456;
constexpr int L_RED = L_SCR + 1024, L_MX = L_SCR + 1088, L_BC0 = L_SCR + 2048, L_BC1 = L_SCR + 2304;
constexpr int L_TAB = L_SCR + 3072;
constexpr int L_T = L_SCR + 7168, L_FA = L_SCR, L_E1 = L_T, L_E2 = 81920, L_ZX = 81920;
constexpr int L_C1 = 98304, L_IMG = L_C1 + 9216;
constexpr int L_FR = 131072;
constexpr int E1P = 544, E2P = 1024 + 72;
DEVI constexpr int wslot(int n) {
  constexpr int u[14] = {6, 7, 8, 0, 1, 2, 3, 4, 6, 7, 8, 0, 1, 2};
  return u[n] * 16384;
}
DEVI constexpr int aslot(int n) {
  constexpr int u[18] = {3, 4, 5, 9, 2, 6, 7, 8, 0, 1, 3, 4, 5, 9, 2, 6, 7, 8};
  return u[n] * 16384;
}

__global__ __launch_bounds__(512, 2) void kmain(const u16* __restrict__ x, const u16* __restrict__ wr, const u16* __restrict__ wi,
                                                const u16* __restrict__ Wg, const u16* __restrict__ Wu, const u16* __restrict__ Wd,
                                                char* __restrict__ ws, u16* __restrict__ out) {
  __shared__ __attribute__((aligned(16))) char smem_[163840];
  LAS char* smem = (LAS char*)smem_;
  const u32 lbase = (u32)(size_t)smem;
  const int tid = threadIdx.x, l = tid & 63;
  const int w = __builtin_amdgcn_readfirstlane(tid >> 6);
  const int b = blockIdx.x, xcd = b & 7, idx = b >> 3;
  const bool ffn = idx < 28;
  const int g = l >> 4, o = l & 15;
  const int h = l >> 5, blk = (l >> 4) & 1, q = (l & 15) >> 2, pp = l & 3;
  const char* FL = ws + OFF_FL;
  const char* BC = ws + OFF_BC;
  asm volatile("" ::"s"(x), "s"(wr), "s"(wi), "s"(Wg), "s"(Wu), "s"(Wd), "s"(ws), "s"(out));

  const int pi = b * 8 + w;
  v4u xr;
  ld16(xr, (u32)(s_of_pi(pi) * 1024 + l * 16), (const char*)x);
  if (!ffn) {
    u64 pc; u32 len;
    asm volatile("s_getpc_b64 %0\nkm_pf_a:\n\ts_mov_b32 %1, km_pf_b-km_pf_a" : "=s"(pc), "=s"(len));
    dma1<0>(min((u32)(((idx & 3) * 8 + w) * 1024 + l * 16), len) & ~15u, (const char*)(pc & ~15ull), lbase + L_FA);
  }
  if (w == 0) dma1c((u32)((l & 1) * 4), BC, lbase + L_BC0);
  const int fkb = (b & 7) * 2 + (w >> 2), fcq = w & 3;
  {
    const u32 ks = (u32)((idx >> 1) + 16 * fkb + 256 * (4 * (idx & 1) + ((l >> 3) & 3)));
    const u32 base = ((ks * (DF_ * 2)) & ~15u) + fcq * 128;
    const u32 dlt = (u32)((const char*)wi - (const char*)wr);
    (void)dlt;
    const u32 c0 = ((l & 7) ^ (((l >> 4) & 1) << 2)) << 4;
    if (l < 32) dma1<0>(base + c0, (const char*)wr, lbase + L_FR + w * 2048);
    else dma1<0>(base + c0, (const char*)wi, lbase + L_FR + w * 2048);
    if (fcq == 3) {
      const u32 t0 = min(base + 128 + (c0 & 16), (u32)(2048 * DF_ * 2 - 16));
      if (l < 32) dma1<0>(t0, (const char*)wr, lbase + L_FR + w * 2048 + 1024);
      else dma1<0>(t0, (const char*)wi, lbase + L_FR + w * 2048 + 1024);
    }
  }
  const int p = (xcd >> 1) * 4 + idx / 7, j = (xcd & 1) * 7 + idx % 7;
  const int sh = w >> 2, fq = w & 3;
  u32 aoff[2];
#pragma unroll
  for (int i = 0; i < 2; ++i) {
    int row = (2 * w + i) * 8 + (l >> 3);
    aoff[i] = s_of_pi((p & 15) * 128 + row) * 1024 + (((l & 7) ^ ((row >> 1) & 7)) << 4) - i * 1024;
  }
  const u32 boff = (u32)(2 * w * 4 + (l >> 4)) * (F_ * 2) + j * 256 + (((l & 15) ^ ((l >> 4) << 2)) << 4);
  const u32 boff1 = boff + 4 * F_ * 2 - 1024;
  const int rot = p >> 1;
  auto issue = [&](int t, int slot) {
    const int kt = (t + rot) & 7;
    u32 dst = lbase + slot * 49152 + w * 2048;
    dma2(aoff[0], aoff[1], (const char*)x + kt * 128, dst);
    dma2(boff, boff1, (const char*)Wg + (size_t)kt * (64 * F_ * 2), dst + 16384);
    dma2(boff, boff1, (const char*)Wu + (size_t)kt * (64 * F_ * 2), dst + 32768);
  };
  const int sp = xcd * 4 + (idx - 28);
  if (ffn) issue(0, 0);

  u32 E = 0, tick = 0;
  const bool coord = (b == 224) && (w == 0);
  auto do_tick = [&]() { if (coord) { ++tick; if (l == 0) st8c(0u, BC, v2u{E, tick}); } };
  auto get_epoch = [&]() {
    const v2u b0 = *(LAS v2u*)(smem + L_BC0);
    v2u b1 = *(LAS v2u*)(smem + L_BC1);
    int spins = 0;
    while (__builtin_amdgcn_readfirstlane((b0[0] ^ b1[0]) | (b0[1] ^ b1[1])) == 0 && ++spins < SPIN_CAP) {
      __builtin_amdgcn_s_sleep(4);
      b1 = ld8c_sync(0u, BC);
    }
    E = __builtin_amdgcn_readfirstlane(b1[0]);
  };
  auto signal_a = [&]() {
    v4u r0 = *(LAS v4u*)(smem + L_RED), r1 = *(LAS v4u*)(smem + L_RED + 16);
    u32 m = max(max(max(r0[0], r0[1]), max(r0[2], r0[3])), max(max(r1[0], r1[1]), max(r1[2], r1[3])));
    if (l == 0) { st4c((u32)(FL_A + b * 4), FL, E | (m >> 7)); *(LAS u32*)(smem + L_MX + 4) = E; }
  };
  auto poll4 = [&](v4u f, u32 voff) -> v4u {
    int spins = 0;
    while (!__all((((f[0] ^ E) | (f[1] ^ E) | (f[2] ^ E) | (f[3] ^ E)) >> 8) == 0) && ++spins < SPIN_CAP) {
      do_tick();
      __builtin_amdgcn_s_sleep(12);
      f = ld16c_sync(voff, FL);
    }
    return f;
  };
  auto check_a = [&](v4u f) {
    f = poll4(f, (u32)(FL_A + l * 16));
    u32 m = wave_max(max(max(f[0], f[1]), max(f[2], f[3]))) & 0xffu;
    if (l == 0) *(LAS u32*)(smem + L_MX) = m;
  };
  if (coord) {
#pragma unroll
    for (int i = 0; i < 5; ++i) dma2c(i ? (u32)(FL_H + ((i - 1) * 128 + l) * 64) : (u32)(l * 16), i ? (u32)(FL_H + ((i - 1) * 128 + 64 + l) * 64 - 1024) : (u32)(l * 16), FL, lbase + i * 2048);
  }
  u32 sq[8];
  if (!ffn && w == 7) {
    const int kss = (sp >> 1) + 16 * (l >> 2) + 256 * (4 * (sp & 1) + (l & 3)), km = (2048 - kss) & 2047;
    const u32 o1 = (u32)(kss * DF_ * 2), o2 = (u32)(km * DF_ * 2);
    ld2(sq[0], o1, (const char*)wr); ld2(sq[1], o1, (const char*)wi); ld2(sq[2], o2, (const char*)wr); ld2(sq[3], o2, (const char*)wi);
    ld2(sq[4], o1 + 512, (const char*)wr); ld2(sq[5], o1 + 512, (const char*)wi); ld2(sq[6], o2 + 512, (const char*)wr); ld2(sq[7], o2 + 512, (const char*)wi);
  }

  {
    const float fo = (float)o, fg = (float)(4 * g);
    if (w < 2) {
      const float st = fo * (1.0f / 32.0f), a0 = fmaf(fg, st, w ? 0.25f : 0.f);
      v4u hi, lo;
#pragma unroll
      for (int e2 = 0; e2 < 4; ++e2) {
        float c0 = cosr(fmaf((float)(16 * (e2 >> 1) + 2 * (e2 & 1)), st, a0)), c1 = cosr(fmaf((float)(16 * (e2 >> 1) + 2 * (e2 & 1) + 1), st, a0));
        u32 ch = pkbf_o(c0, c1);
        hi[e2] = ch; lo[e2] = pkbf(c0 - bflo(ch), c1 - bfhi(ch));
      }
      *(LAS v4u*)(smem + L_C1 + w * 2048 + l * 16) = hi; *(LAS v4u*)(smem + L_C1 + w * 2048 + 1024 + l * 16) = lo;
    } else if (w < 5) {
      const float st = w == 2 ? (float)(o & 7) * (1.0f / 16.0f) : fo * (w == 3 ? 1.0f / 16.0f : 1.0f / 8.0f);
      const float a0 = (w == 2 && o >= 8) ? 0.25f : 0.f;
      const float sc = w == 2 ? 0.0625f : ((w == 4 && (g >> 1) != (o >> 3)) ? 0.f : 1.0f);
      v8h m;
#pragma unroll
      for (int e = 0; e < 4; ++e) { float a = fmaf(fg + (float)e, st, a0); m[e] = (_Float16)(sc * cosr(a)); m[4 + e] = (_Float16)(sc * sinr(a)); }
      *(LAS v8h*)(smem + (w == 2 ? L_C1 + 4096 : L_TAB + (w - 3) * 1024) + l * 16) = m;
    } else {
      const float st = w == 7 ? fo * (1.0f / 256.0f) : (fo + (w == 6 ? 16.f : 0.f)) * (1.0f / 512.0f);
      v4f c, sn;
#pragma unroll
      for (int e = 0; e < 4; ++e) { float a = (fg + (float)e) * st; c[e] = cosr(a); sn[e] = sinr(a); }
      LAS char* dst = smem + (w == 7 ? L_TAB + 2048 : L_C1 + 5120 + (w - 5) * 2048) + l * 16;
      *(LAS v4f*)dst = c; *(LAS v4f*)(dst + 1024) = sn;
    }
    BARRIER();
    LAS char* C1 = smem + L_C1 + l * 16;
    const v4u Fhr = *(LAS v4u*)C1, Flr = *(LAS v4u*)(C1 + 1024), Fhi = *(LAS v4u*)(C1 + 2048), Fli = *(LAS v4u*)(C1 + 3072);
    const v8h M2 = *(LAS v8h*)(C1 + 4096);
    v4f twc[2], tws[2];
#pragma unroll
    for (int u = 0; u < 2; ++u) { twc[u] = *(LAS v4f*)(C1 + 5120 + u * 2048); tws[u] = *(LAS v4f*)(C1 + 6144 + u * 2048); }
    if (ffn) WAIT_VM(8); else if (w == 7) WAIT_VM(11); else WAIT_VM(3);
    if (coord) {
      WAIT_VM(0);
      v4u f[10];
#pragma unroll
      for (int i = 0; i < 10; ++i) f[i] = *(LAS v4u*)(smem + i * 1024 + l * 16);
      u32 c = __builtin_amdgcn_readfirstlane(*(LAS u32*)(smem + L_BC0)) >> 8;
      bool hit;
      do {
        c = (c + 1) & 0xffffffu; hit = false;
#pragma unroll
        for (int i = 0; i < 10; ++i) hit |= ((f[i][0] >> 8) == c) | ((f[i][1] >> 8) == c) | ((f[i][2] >> 8) == c) | ((f[i][3] >> 8) == c);
      } while (__any(hit));
      E = c << 8;
      do_tick();
    }
    PIN(xr);
    const float c0 = bflo(__builtin_amdgcn_readfirstlane(xr[0]));
    v2f xv[4], s2 = {0.f, 0.f}, q2 = {0.f, 0.f};
#pragma unroll
    for (int e = 0; e < 4; ++e) {
      xv[e] = v2f{bflo(xr[e]), bfhi(xr[e])};
      v2f t = xv[e] - v2f{c0, c0};
      s2 += t; q2 = __builtin_elementwise_fma(t, t, q2);
    }
    const float dmean = wave_sum(s2[0] + s2[1]) * (1.0f / 512.0f), msq = wave_sum(q2[0] + q2[1]) * (1.0f / 512.0f);
    do_tick();
    const float mb = rbf(c0 + dmean);
    const float var = rbf(fmaxf(msq - dmean * dmean, 0.f));
    const float sd = rbf(__builtin_amdgcn_sqrtf(rbf(var + 1e-5f)));
    const float rs = __builtin_amdgcn_rcpf(sd);
#pragma unroll
    for (int e = 0; e < 4; ++e) {
      v2f t = xv[e] - v2f{mb, mb};
      u32 tb = pkbf_o(t[0], t[1]);
      t = v2f{bflo(tb), bfhi(tb)} * v2f{rs, rs};
      xr[e] = pkbf(t[0], t[1]);
    }
    *(LAS v4u*)(smem + L_IMG + w * 1024 + l * 16) = xr;
    if (l < 2) st4c((u32)(pi * 4 + l * 8192), ws + OFF_MB, l ? rs * rs : -mb);
    const v8s xf = cat8(trread(smem + L_IMG + w * 1024 + l * 8), trread(smem + L_IMG + w * 1024 + 512 + l * 8));
    const int kt1 = (1 + rot) & 7;
    const u32 dst1 = lbase + 49152 + w * 2048;
    if (ffn) dma2(aoff[0], aoff[1], (const char*)x + kt1 * 128, dst1);
    const v8s xg_ = __builtin_bit_cast(v8s, __builtin_bit_cast(v4u, xf) ^ v4u{0x80000000u, 0x80000000u, 0x80000000u, 0x80000000u});
    const v4f z = {0.f, 0.f, 0.f, 0.f};
    v4f dre[2], dim[2];
#pragma unroll
    for (int u = 0; u < 2; ++u) { dre[u] = mfma16b(u ? xg_ : xf, Flr, z); dim[u] = mfma16b(u ? xg_ : xf, Fli, z); }
#pragma unroll
    for (int u = 0; u < 2; ++u) { dre[u] = mfma16b(u ? xg_ : xf, Fhr, dre[u]); dim[u] = mfma16b(u ? xg_ : xf, Fhi, dim[u]); }
#pragma unroll
    for (int u = 0; u < 2; ++u) {
      twid(dre[u], dim[u], twc[u], tws[u]);
      const v8h bb = pack_ri(dre[u], dim[u]);
      v4f d2 = mfma16h(bb, M2, z);
      if (u == 0) {
        const u32 nv = o == 8 ? 0xac002c00u : 0u;
        v4f n = mfma16h(bb, __builtin_bit_cast(v8h, v4u{nv, nv, 0u, 0u}), z);
        if (l == 8) d2[0] = n[0];
      }
      *(LAS v4h*)(smem + L_T + ((o >> 3) * 8 + w) * 544 + (o & 7) * 64 + u * 32 + g * 8) = pack4h(d2);
    }
    if (ffn) {
      dma2(boff, boff1, (const char*)Wg + (size_t)kt1 * (64 * F_ * 2), dst1 + 16384);
      dma2(boff, boff1, (const char*)Wu + (size_t)kt1 * (64 * F_ * 2), dst1 + 32768);
    }
  }
  v8s su; v4u fu, tu;
  auto round1_read = [&]() {
    LAS char* rp = smem + L_T + (4 * (g & 1) + (o >> 2)) * 544 + (w * 32 + (g >> 1) * 16) * 2 + (o & 3) * 8;
    su = cat8(trread(rp), trread(rp + 8 * 544));
    LAS char* fp_ = smem + L_FR + w * 2048 + q * 128 + (((2 * g + (pp >> 1)) ^ ((q >> 1) << 2)) << 4) + (pp & 1) * 8;
    fu = __builtin_bit_cast(v4u, cat8(trread(fp_), trread(fp_ + 512)));
    LAS char* tp = smem + L_FR + w * 2048 + 1024 + q * 128 + (pp >> 1) * 16 + (pp & 1) * 8;
    tu = __builtin_bit_cast(v4u, cat8(trread(tp), trread(tp + 512)));
  };
  auto round1_max = [&]() {
    typedef unsigned short v2us __attribute__((ext_vector_type(2)));
    const int dl = (idx >> 1) & 7;
    const u32 msk = (fcq == 0 && l < dl) ? 0u : 0x7fff7fffu, msk2 = (fcq == 3 && o <= dl) ? 0x7fff7fffu : 0u;
    v2us m2 = {0, 0};
#pragma unroll
    for (int e = 0; e < 4; ++e) {
      u32 t = fu[e] & msk, t2 = tu[e] & msk2;
      m2 = __builtin_elementwise_max(m2, __builtin_elementwise_max(__builtin_bit_cast(v2us, t), __builtin_bit_cast(v2us, t2)));
    }
    const u32 mx = wave_max(max((u32)m2[0], (u32)m2[1]));
    if (l == 0) *(LAS u32*)(smem + L_RED + w * 4) = mx;
  };
  auto round1_store = [&]() {
    st16c((u32)(b * 8192 + (w * 32 + (g >> 1) * 16 + o) * 32 + (g & 1) * 16), ws + OFF_S1, su);
    st16c((u32)((idx * 16 + fkb) * 4608 + (fcq * 64 + l) * 16), ws + OFF_WT, fu);
    if (fcq == 3 && l < 16) st16c((u32)((idx * 16 + fkb) * 4608 + (256 + l) * 16), ws + OFF_WT, tu);
  };

  const int RG = xcd * 4 + (idx >> 3), ct = idx & 7;
  const int kd = xcd * 32 + ((idx + 3) & 31);
  const u32 swz = ((l & 7) ^ (((l >> 4) & 1) << 2)) << 4;
  const u32 ro0 = (u32)((l >> 3) * 1024) + swz, ro1 = ro0 + 8192 - 1024;
  const char* bsrc = (const char*)Wd + (size_t)(w * 16) * 1024 + ct * 128;
  auto issueW = [&](int n) { dma2(ro0, ro1, bsrc + (size_t)n * 131072, lbase + wslot(n) + w * 2048); };
  const int dh = w & 1, kq = w >> 1, ph = kq >> 1, kh = kq & 1;
  const int epi = RG * 64 + ph * 32 + (l & 31);
  const u32 eoff = (u32)(s_of_pi(epi) * D_ + ct * 64 + dh * 32 + kh * 16 + h * 8) * 2;
  v4u wt, s1v, xe; v4f pq[4]; float rs2;
  auto load_xe = [&]() { ld16(xe, eoff, (const char*)x); };
  auto load_s1 = [&]() { ld16c(s1v, (u32)((((4 * w + g) * 8 + (o >> 1)) * 256 + kd) * 32 + (o & 1) * 16), ws + OFF_S1); };
  auto load_wt = [&]() { ld16c(wt, (u32)((w * 64 + l) * 4608 + (kd + ((2 * w + (g >> 1)) & 7)) * 16), ws + OFF_WT); };
  auto load_rs = [&]() { ld4c(rs2, (u32)(epi * 4), ws + OFF_RS2); };
  v4f t2c, t2s, t3c, t3s;
  auto trig2 = [&]() {
    const float fw = (float)w, st2 = fw * (1.0f / 128.0f), a2 = fw * (float)(o + 64 * g) * (1.0f / 2048.0f);
#pragma unroll
    for (int e = 0; e < 4; ++e) { float b2 = fmaf((float)e, st2, a2); t2c[e] = cosr(b2); t2s[e] = sinr(b2); }
    PIN(t2c); PIN(t2s);
  };
  auto trig3 = [&]() {
    const float f7 = (float)(o & 7), st3 = f7 * (1.0f / 128.0f), a3 = f7 * (float)(2 * w + (o >> 3) + 64 * g) * (1.0f / 2048.0f);
#pragma unroll
    for (int e = 0; e < 4; ++e) { float b3 = fmaf((float)e, st3, a3); t3c[e] = cosr(b3); t3s[e] = sinr(b3); }
    PIN(t3c); PIN(t3s);
  };
  v4u f16; v4f t1c, t1s, re, im;
  const v4f z4 = {0.f, 0.f, 0.f, 0.f};
  auto imf = [](v4u f) { return __builtin_bit_cast(v8h, v4u{f[2] ^ 0x80008000u, f[3] ^ 0x80008000u, f[0], f[1]}); };
  auto segA0 = [&]() {
    f16 = *(LAS v4u*)(smem + L_TAB + l * 16);
    t1c = *(LAS v4f*)(smem + L_TAB + 2048 + l * 16); t1s = *(LAS v4f*)(smem + L_TAB + 3072 + l * 16);
  };
  auto segA1 = [&]() {
    PIN(s1v);
    const v8h a = __builtin_bit_cast(v8h, s1v);
    re = mfma16h(a, __builtin_bit_cast(v8h, f16), z4); im = mfma16h(a, imf(f16), z4);
  };
  auto segA2 = [&]() {
    twid(re, im, t1c, t1s);
    const v8h a = pack_ri(re, im);
    re = mfma16h(__builtin_bit_cast(v8h, f16), a, z4); im = mfma16h(imf(f16), a, z4);
  };
  auto segA3 = [&]() {
    twid(re, im, t2c, t2s);
    *(LAS v4h*)(smem + L_E1 + w * E1P + o * 32 + g * 8) = pack4h(re);
    *(LAS v4h*)(smem + L_E1 + (8 + w) * E1P + o * 32 + g * 8) = pack4h(im);
  };

  if (ffn) {
    v16f acc[2][2] = {};
    float mbr[2];
    const u32 wb0 = (u32)((8 * h + q) * 256 + ((fq ^ q) << 6) + blk * 32 + pp * 8);
    const u32 r1 = 16384 + sh * 16384, r2 = 32768 - sh * 16384;
    const int xrow = sh * 64 + (l & 31);
    const u32 xb0 = (u32)(xrow * 128 + ((h ^ ((xrow >> 1) & 7)) << 4));
    auto rd = [&](Frag1& f, LAS char* sb, int i) {
      LAS char* xa = sb + (xb0 ^ (i << 5));
      LAS char* wa_ = sb + wb0 + (i << 12);
      f.w1 = cat8(trread(wa_ + r1), trread(wa_ + r1 + 1024));
      f.x0 = *(LAS v8s*)xa; f.x1 = *(LAS v8s*)(xa + 4096);
      f.w2 = cat8(trread(wa_ + r2), trread(wa_ + r2 + 1024));
    };
    const u32 one = ((l & 15) == ((l >> 4) & 1)) ? 0x3f803f80u : 0u;
    const v8s sel = __builtin_bit_cast(v8s, v4u{one, one, one, one});
    v4f csa = {0.f, 0.f, 0.f, 0.f};
    Frag1 A, B;
    WAIT_VM(7);
    BARRIER();
    rd(A, smem, 0);
    round1_read();
    dma2(aoff[0], aoff[1], (const char*)x + ((2 + rot) & 7) * 128, lbase + 2 * 49152 + w * 2048);
    u32 c1[4], c2[4];
    static_for<0, 8>([&](auto T) {
      constexpr int t = T, slot = t % 3, nslot = (t + 1) % 3, pslot = (t + 2) % 3;
      LAS char* sb = smem + slot * 49152;
      {
        const int kt = (t + 2 + rot) & 7;
        const char* s1 = t < 6 ? (const char*)Wg + (size_t)kt * (64 * F_ * 2) : bsrc + (size_t)((t - 6) * 3 + 1) * 131072;
        const char* s2 = t < 6 ? (const char*)Wu + (size_t)kt * (64 * F_ * 2) : bsrc + (size_t)((t - 6) * 3 + 2) * 131072;
        const u32 o2 = t < 6 ? boff : ro0, o3 = t < 6 ? boff1 : ro1;
        const u32 dst = lbase + pslot * 49152 + w * 2048;
        rd(B, sb, 1); SCHED();
        acc[0][0] = mfma32b(A.w1, A.x0, acc[0][0]); acc[0][1] = mfma32b(A.w1, A.x1, acc[0][1]);
        if constexpr (t == 0) csa = mfma16b(sel, __builtin_bit_cast(v4u, A.w1), csa);
        SCHED();
        dma1<0>(o2, s1, dst + 16384);
        if constexpr (t == 2) load_xe();
        if constexpr (t == 6) load_wt();
        acc[1][0] = mfma32b(A.w2, A.x0, acc[1][0]); acc[1][1] = mfma32b(A.w2, A.x1, acc[1][1]); SCHED();
        if constexpr (t == 0) { round1_store(); round1_max(); SCHED(); }
        if constexpr (t == 3) { trig2(); SCHED(); }
        if constexpr (t == 4) { trig3(); SCHED(); }
        rd(A, sb, 2); SCHED();
        acc[0][0] = mfma32b(B.w1, B.x0, acc[0][0]); SCHED();
        dma1<1024>(o3, s1, dst + 16384);
        acc[0][1] = mfma32b(B.w1, B.x1, acc[0][1]);
        if constexpr (t == 0) csa = mfma16b(sel, __builtin_bit_cast(v4u, B.w1), csa);
        acc[1][0] = mfma32b(B.w2, B.x0, acc[1][0]); acc[1][1] = mfma32b(B.w2, B.x1, acc[1][1]); SCHED();
        dma1<0>(o2, s2, dst + 32768);
        if constexpr (t == 6) {
          load_rs();
        }
        if constexpr (t == 7) { WAIT_VM(13); segA1(); SCHED(); }
        rd(B, sb, 3); SCHED();
        if constexpr (t == 7) {
#pragma unroll
          for (int i = 0; i < 4; ++i) { PIN(c1[i]); PIN(c2[i]); }
          PIN(mbr[0]); PIN(mbr[1]);
          const v8s cs1 = __builtin_bit_cast(v8s, v4u{c1[0], c1[1], c1[2], c1[3]}), cs2 = __builtin_bit_cast(v8s, v4u{c2[0], c2[1], c2[2], c2[3]});
          const u32 m0_ = pkbf(mbr[0], mbr[0]), m1_ = pkbf(mbr[1], mbr[1]);
          const v8s mb0 = __builtin_bit_cast(v8s, v4u{m0_, m0_, m0_, m0_}), mb1 = __builtin_bit_cast(v8s, v4u{m1_, m1_, m1_, m1_});
          acc[0][0] = mfma32b(cs1, mb0, acc[0][0]); acc[1][0] = mfma32b(cs2, mb0, acc[1][0]);
          acc[0][1] = mfma32b(cs1, mb1, acc[0][1]); acc[1][1] = mfma32b(cs2, mb1, acc[1][1]); SCHED();
          segA2(); SCHED();
        }
        acc[0][0] = mfma32b(A.w1, A.x0, acc[0][0]); acc[0][1] = mfma32b(A.w1, A.x1, acc[0][1]);
        if constexpr (t == 0) csa = mfma16b(sel, __builtin_bit_cast(v4u, A.w1), csa);
        SCHED();
        dma1<1024>(o3, s2, dst + 32768);
        acc[1][0] = mfma32b(A.w2, A.x0, acc[1][0]); acc[1][1] = mfma32b(A.w2, A.x1, acc[1][1]); SCHED();
      }
      if constexpr (t < 7) {
        const char* s0 = t < 5 ? (const char*)x + ((t + 3 + rot) & 7) * 128 : bsrc + (size_t)((t - 5) * 3) * 131072;
        const u32 o0 = t < 5 ? aoff[0] : ro0, o1 = t < 5 ? aoff[1] : ro1;
        const u32 dst = lbase + slot * 49152 + w * 2048;
        if constexpr (t == 0) csa = mfma16b(sel, __builtin_bit_cast(v4u, B.w1), csa);
        WAIT_VM(t == 0 ? 8 : t == 2 ? 7 : t == 6 ? 19 : 6);
        if constexpr (t == 5) { if (w == 0) check_a(*(LAS v4u*)(smem + L_FA + l * 16)); }
        BARRIER();
        acc[0][0] = mfma32b(B.w1, B.x0, acc[0][0]); SCHED();
        rd(A, smem + nslot * 49152, 0);
        if constexpr (t == 6) segA0();
        SCHED();
        if constexpr (t == 0) {
          if (l < 16) {
#pragma unroll
            for (int i = 0; i < 2; ++i) {
              const float hi = rbf(csa[i]);
              st4c((u32)(((j * 16 + p) * 256 + sh * 128 + fq * 32 + i * 16 + l) * 4), ws + OFF_CSP, pkbf(hi, csa[i] - hi));
            }
          }
        }
        if constexpr (t == 0) { if (w == 0) dma1c((u32)((l & 1) * 4), BC, lbase + L_BC1); }
        if constexpr (t == 4) { if (w == 0) dma1s<0>((u32)(FL_A + l * 16), FL, lbase + L_FA); }
        if constexpr (t == 5) {
          const u32 co = (u32)(((j * 16 + (p & 1) + 8 * h) * 256 + fq * 32 + (l & 31)) * 4);
#pragma unroll
          for (int i = 0; i < 4; ++i) { ld4c(c1[i], co + sh * 512 + i * 2048, ws + OFF_CSP); ld4c(c2[i], co + (1 - sh) * 512 + i * 2048, ws + OFF_CSP); }
          ld4c(mbr[0], (u32)((p * 128 + xrow) * 4), ws + OFF_MB); ld4c(mbr[1], (u32)((p * 128 + xrow + 32) * 4), ws + OFF_MB);
          load_s1();
        }
        dma1<0>(o0, s0, dst);
        acc[0][1] = mfma32b(B.w1, B.x1, acc[0][1]);
        acc[1][0] = mfma32b(B.w2, B.x0, acc[1][0]);
        acc[1][1] = mfma32b(B.w2, B.x1, acc[1][1]); SCHED();
        dma1<1024>(o1, s0, dst);
        if constexpr (t == 1) { if (w == 0) { get_epoch(); signal_a(); } }
        SCHED();
      } else {
        acc[0][0] = mfma32b(B.w1, B.x0, acc[0][0]); acc[1][0] = mfma32b(B.w2, B.x0, acc[1][0]); SCHED();
        acc[0][1] = mfma32b(B.w1, B.x1, acc[0][1]); acc[1][1] = mfma32b(B.w2, B.x1, acc[1][1]); SCHED();
      }
    });
    auto fin = [&](const v16f& ag, const v16f& au, int rt) {
#pragma unroll
      for (int s2i = 0; s2i < 2; ++s2i) {
        v4u ov;
#pragma unroll
        for (int e = 0; e < 4; ++e) {
          const int r = s2i * 8 + 2 * e;
          v2f gg = v2f{relu(ag[r]), relu(ag[r + 1])} * v2f{au[r], au[r + 1]};
          ov[e] = pkbf(gg[0], gg[1]);
        }
        st16c((u32)(((p * 4 + sh * 2 + rt) * 112 + (j * 8 + fq * 2 + s2i)) * 1024 + l * 16), ws + OFF_H, ov);
      }
    };
    if (sh == 0) { fin(acc[0][0], acc[1][0], 0); fin(acc[0][1], acc[1][1], 1); }
    else { fin(acc[1][0], acc[0][0], 0); fin(acc[1][1], acc[0][1], 1); }
    segA3();
    WAIT_VM(4);
  } else {
    WAIT_VM(1);
    BARRIER();
    round1_read(); round1_store(); round1_max();
    if (w == 0 && !coord) dma1c((u32)((l & 1) * 4), BC, lbase + L_BC1);
    do_tick();
    if (w == 7) {
#pragma unroll
      for (int i = 0; i < 8; ++i) PIN(sq[i]);
      float war = bflo(sq[0]) + bflo(sq[2]), wai = bflo(sq[1]) - bflo(sq[3]);
      float wbr = bflo(sq[4]) + bflo(sq[6]), wbi = bflo(sq[5]) - bflo(sq[7]);
      v4f pv = {0.25f * (war + wbr), 0.25f * (wai + wbi), 0.25f * (war - wbr), 0.25f * (wai - wbi)};
      st16c((u32)((((sp >> 2) * 4 + (l & 3)) * 64 + (sp & 3) * 16 + (l >> 2)) * 16), ws + OFF_PQ, pv);
    }
    WAIT_VM(0);
    BARRIER();
    if (w == 0) { if (!coord) get_epoch(); signal_a(); do_tick(); WAIT_VM(0); }
    BARRIER();
    dma1c((u32)(sp * 57344 + (w * 112 + l) * 64), (const char*)Wd, lbase + 49152);
    dma1c((u32)(sp * 57344 + (w * 112 + 48 + l) * 64), (const char*)Wd, lbase + 49152);
    static_for<0, 6>([&](auto N) { issueW(N); });
    static_for<6, 14>([&](auto N) { dma2(ro0, ro1, bsrc - 512 + (size_t)N * 131072, lbase + 49152 + w * 2048); });
    load_xe();
    trig2(); trig3(); segA0();
    if (w == 0) {
      if (!coord) __builtin_amdgcn_s_sleep(16);
      check_a(v4u{~E, 0u, 0u, 0u});
    }
    BARRIER();
    load_s1(); load_wt(); load_rs();
    if (kd == 0) {
#pragma unroll
      for (int r = 0; r < 4; ++r) ld16c(pq[r], (u32)(w * 4096 + r * 1024 + l * 16), ws + OFF_PQ);
    }
    WAIT_VM(0);
    segA1(); segA2(); segA3();
  }
  PIN(wt); PIN(xe); PIN(rs2);
  BARRIER();

  const u32 trG = (u32)(kq * 4096 + (4 * h + q) * 128 + ((dh ^ ((q >> 1) & 1)) << 6) + (blk ^ kh) * 32 + pp * 8);
  v8s Wf[14][2];
  auto readW = [&](auto N) {
    constexpr int n = N;
    LAS char* sb = smem + wslot(n) + trG;
    Wf[n][0] = cat8(trread(sb), trread(sb + 1024)); Wf[n][1] = cat8(trread(sb + 2048), trread(sb + 3072));
  };
  const char* hsrc = ws + OFF_H + ((size_t)(RG * 2 + (w >> 2)) * 112 + (w & 3) * 2) * 1024;
  const u32 so0 = (u32)((l >> 3) * 4096) + swz, so1 = so0 + 32768 - 1024;
  const char* ssrc = ws + OFF_S2 + (size_t)(w * 16) * 4096 + RG * 128;
  auto issueP = [&](int n, auto HALF) {
    constexpr int hf = HALF;
    if (n < 14) dma1s<hf * 1024>((u32)(l * 16), hsrc + n * 8192, lbase + aslot(n) + w * 2048);
    else dma1s<hf * 1024>(hf ? so1 : so0, ssrc + (size_t)(n - 14) * (128 * 4096), lbase + aslot(n) + w * 2048);
  };
  const int d = ct * 64 + dh * 32 + ((l & 31) ^ (kh << 4));
  const float gst = (float)d * (1.0f / 512.0f), ga0 = (float)(16 * kq + 2 * h) * gst;
  v8h G[8];
  auto gen = [&](auto FI) {
    constexpr int f = FI;
#pragma unroll
    for (int e2 = 0; e2 < 4; ++e2) {
      float a = fmaf((float)(64 * (f >> 1) + 8 * (f & 1) + 4 * (e2 >> 1) + (e2 & 1)), gst, ga0);
      float c = cosr(a), sn = sinr(a);
      if (f == 0 && e2 == 0) { if (kq == 0 && h == 0) { c = 0.5f; sn = (d & 1) ? 0.5f : -0.5f; } }
      G[f][2 * e2] = (_Float16)c; G[f][2 * e2 + 1] = (_Float16)sn;
    }
    PIN(G[f]);
  };

  float oscale;
  const u32 fhoff = (u32)(FL_H + (RG * 16 + (min(l, 55) >> 2)) * 64 + (l & 3) * 4);
  u32 fp;
  {
    v8h a;
    {
      LAS char* rp = smem + L_E1 + (4 * (g & 1) + (o >> 2)) * E1P + (2 * w + (g >> 1)) * 32 + (o & 3) * 8;
      a = __builtin_bit_cast(v8h, cat8(trread(rp), trread(rp + 8 * E1P)));
    }
    const v4u f8 = *(LAS v4u*)(smem + L_TAB + 1024 + l * 16);
    const v2u me = *(LAS v2u*)(smem + L_MX);
    issueW(6);
    static_for<0, 3>(readW);
    const int Ex = (int)me[0] < 16 ? 16 : ((int)me[0] > 240 ? 240 : (int)me[0]);
    const float wscale = __builtin_bit_cast(float, (u32)(246 - Ex) << 23);
    oscale = __builtin_bit_cast(float, (u32)(Ex - 7) << 23);
    re = mfma16h(__builtin_bit_cast(v8h, f8), a, z4); im = mfma16h(imf(f8), a, z4);
    static_for<3, 6>(readW);
    WAIT_VM(2);
    if (ffn && l == 0) st4c((u32)(FL_H + ((p * 2 + sh) * 16 + j) * 64 + fq * 4), FL, me[1]);
    issueW(7);
    if (kd != 0) {
#pragma unroll
      for (int r = 0; r < 4; ++r) {
        float fr = ((r & 1) ? bfhi(wt[r >> 1]) : bflo(wt[r >> 1])) * wscale, fi = ((r & 1) ? bfhi(wt[2 + (r >> 1)]) : bflo(wt[2 + (r >> 1)])) * wscale;
        float zr = re[r], zi = im[r];
        re[r] = zr * fr - zi * fi;
        im[r] = -(zr * fi + zi * fr);
      }
    } else {
      const int ka = 2 * w + (g >> 1), kr = o + 64 * (g & 1);
#pragma unroll
      for (int r = 0; r < 4; ++r) { v2f t = {re[r], im[r]}; *(LAS v2f*)(smem + L_ZX + ka * 1024 + (kr + 16 * r) * 8) = t; }
      BARRIER();
#pragma unroll
      for (int r = 0; r < 4; ++r) {
        PIN(pq[r]);
        const int km = (2048 - (ka + 16 * (kr + 16 * r))) & 2047;
        v2f zm = *(LAS v2f*)(smem + L_ZX + (km & 15) * 1024 + (km >> 4) * 8);
        float zr = re[r], zi = im[r];
        re[r] = (zr * pq[r][0] - zi * pq[r][1] + zm[0] * pq[r][2] + zm[1] * pq[r][3]) * wscale;
        im[r] = -(zr * pq[r][1] + zi * pq[r][0] + zm[0] * pq[r][3] - zm[1] * pq[r][2]) * wscale;
      }
    }
    LAS char* E2 = smem + (kd == 0 ? L_E1 : L_E2);
    a = pack_ri(re, im);
    re = mfma16h(a, __builtin_bit_cast(v8h, f8), z4); im = mfma16h(a, imf(f8), z4);
    twid(re, im, t3c, t3s);
    *(LAS v4h*)(E2 + (o & 7) * E2P + (2 * w + (o >> 3)) * 32 + g * 8) = pack4h(re);
    *(LAS v4h*)(E2 + (o & 7) * E2P + 512 + (2 * w + (o >> 3)) * 32 + g * 8) = pack4h(im);
    BARRIER();
    {
      LAS char* rp = E2 + w * E2P + o * 32 + g * 8;
      a = __builtin_bit_cast(v8h, cat8(*(LAS v4s*)rp, *(LAS v4s*)(rp + 512)));
    }
    issueW(8);
    re = mfma16h(a, __builtin_bit_cast(v8h, f16), z4); im = mfma16h(a, imf(f16), z4);
    issueW(9);
    twid(re, im, t1c, t1s);
    a = pack_ri(re, im);
    re = mfma16h(__builtin_bit_cast(v8h, f16), a, z4); im = mfma16h(imf(f16), a, z4);
    issueW(10);
    st8c((u32)(2 * kd * 4096 + w * 512 + l * 8), ws + OFF_S2, pack4h(re));
    st8c((u32)((2 * kd + 1) * 4096 + w * 512 + l * 8), ws + OFF_S2, pack4h(im));
  }
  gen(IC<0>{}); issueW(11);
  WAIT_VM(8);
  BARRIER();
  static_for<6, 9>(readW);
  gen(IC<1>{}); issueW(12); gen(IC<2>{}); issueW(13);
  if (!ffn) {
    WAIT_VM(0);
    BARRIER();
    if (w == 0) {
      if (l == 0) st4c((u32)(FL_S + b * 4), FL, E);
      __builtin_amdgcn_s_sleep(16);
    }
  }
  if (w == 0) ld4c(fp, fhoff, FL);
  gen(IC<3>{}); gen(IC<4>{});
  WAIT_VM(0);
  if (w == 0) {
    PIN(fp);
    u32 f = fp;
    int spins = 0;
    while (!__all(f == E) && ++spins < SPIN_CAP) {
      __builtin_amdgcn_s_sleep(4);
      f = ld4c_sync(fhoff, FL);
    }
  }
  BARRIER();
  if (ffn && tid == 0) st4c((u32)(FL_S + b * 4), FL, E);
  issueP(0, IC<0>{}); issueP(0, IC<1>{}); issueP(1, IC<0>{}); issueP(1, IC<1>{});
  static_for<9, 14>(readW);
  gen(IC<5>{});
  issueP(2, IC<0>{}); issueP(2, IC<1>{});
  gen(IC<6>{});
  issueP(3, IC<0>{}); issueP(3, IC<1>{});
  gen(IC<7>{});

  const u32 trS = (u32)(kq * 4096 + (4 * h + q) * 128 + ((ph ^ ((q >> 1) & 1)) << 6) + blk * 32 + pp * 8);
  const u32 hb = (u32)(ph * 8192 + kq * 2048 + l * 16);
  v16f accG[2] = {}, accS[2] = {};
  v8s fa[8], fb[8];
  v4u fs;
  float gv[8];
  const u32 xw = (u32)(w * 6144 + l * 16);
  auto xchg_out = [&](const v16f (&ac)[2], int base) {
    LAS char* X = smem + base + xw;
#pragma unroll
    for (int i = 0; i < 6; ++i) {
      const v16f& a = ac[(i + 2) >> 2]; const int r = ((i + 2) & 3) * 4;
      *(LAS v4f*)(X + i * 1024) = v4f{a[r], a[r + 1], a[r + 2], a[r + 3]};
    }
  };
  auto xchg_in = [&](const v16f (&ac)[2], int base, float (&o8)[8]) {
    v4f s0 = {ac[0][0], ac[0][1], ac[0][2], ac[0][3]}, s1 = {ac[0][4], ac[0][5], ac[0][6], ac[0][7]};
#pragma unroll
    for (int x_ = 1; x_ < 4; ++x_) {
      LAS char* X = smem + base + (w ^ (2 * x_)) * 6144 + (x_ - 1) * 2048 + l * 16;
      s0 += *(LAS v4f*)X; s1 += *(LAS v4f*)(X + 1024);
    }
#pragma unroll
    for (int e = 0; e < 4; ++e) { o8[e] = s0[e]; o8[4 + e] = s1[e]; }
  };
  v4f xg[6];
  float ov[8], ex[8];
  auto sadd = [](float a, float b) { float r; asm("v_add_f32 %0, %1, %2" : "=v"(r) : "v"(a), "v"(b)); return r; };
  auto smul = [](float a, float b) { float r; asm("v_mul_f32 %0, %1, %2" : "=v"(r) : "v"(a), "v"(b)); return r; };
  auto silu_step = [&](int i) {
    if (i < 3) {
#pragma unroll
      for (int e = 0; e < 8; ++e) { ov[e] = sadd(i == 0 ? accG[0][e] : ov[e], xg[2 * i + (e >> 2)][e & 3]); PIN(ov[e]); }
    } else if (i == 3) {
#pragma unroll
      for (int e = 0; e < 8; e += 2) { u32 t = pkbf_o(smul(ov[e], rs2), smul(ov[e + 1], rs2)); ov[e] = bflo(t); ov[e + 1] = bfhi(t); PIN(ov[e]); PIN(ov[e + 1]); }
    } else if (i == 4) {
#pragma unroll
      for (int e = 0; e < 8; ++e) { ex[e] = __builtin_amdgcn_exp2f(ov[e] * -1.44269504088896f); PIN(ex[e]); }
    } else if (i == 5) {
#pragma unroll
      for (int e = 0; e < 8; ++e) { ex[e] = __builtin_amdgcn_rcpf(1.0f + ex[e]); PIN(ex[e]); }
    } else if (i == 6) {
#pragma unroll
      for (int e = 0; e < 8; e += 2) { u32 t = pkbf_o(ov[e] * ex[e], ov[e + 1] * ex[e + 1]); gv[e] = bflo(t); gv[e + 1] = bfhi(t); PIN(gv[e]); PIN(gv[e + 1]); }
    }
  };
  static_for<0, 10>([&](auto V) {
    constexpr int v = V;
    v8s(&cur)[8] = (v & 1) ? fb : fa;
    v8s(&prv)[8] = (v & 1) ? fa : fb;
    auto mmp = [&](int s) {
      if constexpr (v >= 8) {
        accS[0] = mfma32h(G[(v - 8) * 4 + s], prv[2 * s], accS[0]); accS[1] = mfma32h(G[(v - 8) * 4 + s], prv[2 * s + 1], accS[1]);
        SCHED();
        if ((v - 8) * 4 + s >= 1) silu_step((v - 8) * 4 + s - 1);
      } else if constexpr (v >= 1) {
        accG[0] = mfma32b(Wf[2 * (v - 1) + (s >> 1)][s & 1], prv[2 * s], accG[0]);
        accG[1] = mfma32b(Wf[2 * (v - 1) + (s >> 1)][s & 1], prv[2 * s + 1], accG[1]);
      }
      SCHED();
    };
    if constexpr (v < 9) {
      WAIT_VM(v <= 7 ? 4 : 0);
      if constexpr (v == 5) { if (w == 0) { PIN(fs); poll4(fs, (u32)(FL_S + l * 16)); } }
      BARRIER();
      if constexpr (v == 4) { if (w == 0) ld16c(fs, (u32)(FL_S + l * 16), FL); }
      if constexpr (v == 8) {
#pragma unroll
        for (int x_ = 1; x_ < 4; ++x_) {
          LAS char* X = smem + 49152 + (w ^ (2 * x_)) * 6144 + (x_ - 1) * 2048 + l * 16;
          xg[2 * x_ - 2] = *(LAS v4f*)X; xg[2 * x_ - 1] = *(LAS v4f*)(X + 1024);
        }
        SCHED();
      }
      mmp(0);
#pragma unroll
      for (int s = 0; s < 4; ++s) {
        if constexpr (v >= 7) {
          LAS char* sb = smem + aslot(2 * v + (s >> 1)) + (s & 1) * 2048;
          cur[2 * s] = cat8(trread(sb + trS), trread(sb + trS + 1024));
          cur[2 * s + 1] = cat8(trread(sb + (trS ^ 64)), trread(sb + (trS ^ 64) + 1024));
        } else {
          LAS char* sb = smem + aslot(2 * v + (s >> 1)) + (s & 1) * 1024;
          cur[2 * s] = *(LAS v8s*)(sb + hb);
          cur[2 * s + 1] = *(LAS v8s*)(sb + (hb ^ 8192));
        }
      }
      SCHED();
      if constexpr (v <= 6) issueP(2 * v + 4, IC<0>{});
      mmp(1);
      if constexpr (v <= 6) issueP(2 * v + 4, IC<1>{});
      mmp(2);
      if constexpr (v <= 6) issueP(2 * v + 5, IC<0>{});
      mmp(3);
      if constexpr (v <= 6) issueP(2 * v + 5, IC<1>{});
    } else {
      mmp(0); mmp(1); mmp(2); mmp(3);
    }
    if constexpr (v == 7) xchg_out(accG, 49152);
    SCHED();
  });
  xchg_out(accS, 0);
  BARRIER();
  {
    float o8[8], pv[8];
    xchg_in(accS, 0, o8);
#pragma unroll
    for (int e = 0; e < 8; e += 2) { u32 t = pkbf_o(o8[e] * oscale, o8[e + 1] * oscale); pv[e] = bflo(t) * gv[e]; pv[e + 1] = bfhi(t) * gv[e + 1]; }
    u32 a0 = pkbf(pv[0], pv[1]), a1 = pkbf(pv[2], pv[3]), b0 = pkbf(pv[4], pv[5]), b1 = pkbf(pv[6], pv[7]);
    auto r0 = __builtin_amdgcn_permlane32_swap(a0, b0, false, false);
    auto r1 = __builtin_amdgcn_permlane32_swap(a1, b1, false, false);
    v4u pk = {r0[0], r1[0], r0[1], r1[1]};
    v4u ov4;
#pragma unroll
    for (int e = 0; e < 4; ++e) ov4[e] = pkbf(bflo(xe[e]) + bflo(pk[e]), bfhi(xe[e]) + bfhi(pk[e]));
    *(v4u*)((char*)out + eoff) = ov4;
  }
  asm volatile("km_pf_b:");
}

extern "C" void kernel_launch(void* const* d_in, const int* in_sizes, int n_in, void* d_out, int out_size, void* d_ws,
                              size_t ws_size, hipStream_t stream) {
  kmain<<<256, 512, 0, stream>>>((const u16*)d_in[0], (const u16*)d_in[1], (const u16*)d_in[2], (const u16*)d_in[4],
                                 (const u16*)d_in[5], (const u16*)d_in[6], (char*)d_ws + WS_BASE(ws_size), (u16*)d_out);
}
```

```cpp
#include <hip/hip_runtime.h>
#include <stdint.h>

typedef unsigned short u16;
typedef unsigned int u32;
typedef unsigned long long u64;
typedef short v4s __attribute__((ext_vector_type(4)));
typedef short v8s __attribute__((ext_vector_type(8)));
typedef _Float16 v4h __attribute__((ext_vector_type(4)));
typedef _Float16 v8h __attribute__((ext_vector_type(8)));
typedef __bf16 v2b __attribute__((ext_vector_type(2)));
typedef __bf16 v8b __attribute__((ext_vector_type(8)));
typedef float v2f __attribute__((ext_vector_type(2)));
typedef float v4f __attribute__((ext_vector_type(4)));
typedef float v16f __attribute__((ext_vector_type(16)));
typedef u32 v2u __attribute__((ext_vector_type(2)));
typedef u32 v4u __attribute__((ext_vector_type(4)));

#define LAS __attribute__((address_space(3)))
#define GAS __attribute__((address_space(1)))
#define DEVI __device__ __forceinline__

constexpr int D_ = 512, F_ = 1792, DF_ = 257;
constexpr size_t OFF_H = (size_t)24 << 20;
constexpr size_t OFF_S1 = (size_t)32 << 20;
constexpr size_t OFF_WT = (size_t)34 << 20;
constexpr size_t OFF_S2 = (size_t)37 << 20;
constexpr size_t OFF_M = (size_t)39 << 20;
constexpr size_t OFF_FL = OFF_M;
constexpr u32 FL_S = 0, FL_A = 1024, FL_H = 2048;
constexpr size_t OFF_BC = OFF_M + 49152;
constexpr size_t OFF_MB = OFF_M + 98304;
constexpr size_t OFF_RS2 = OFF_M + 106496;
constexpr size_t OFF_PQ = OFF_M + 65536;
constexpr size_t OFF_CSP = OFF_M + 131072;
constexpr int SPIN_CAP = 1 << 16;
#define SLEEP_A 16

#define WS_BASE(n) ((((n) - ((size_t)40 << 20))) & ~(size_t)0x1fffff)
#define STAMP 1
#define PROBE 0
#define PROBE_B 45
constexpr size_t OFF_STAMP = 0;
#define STAMP_DECL u32 stamps_[32] = {}
#define STAMPT(id) do { u64 t_; asm volatile("s_memrealtime %0\n\ts_waitcnt lgkmcnt(0)" : "=s"(t_) :: "memory"); stamps_[id] = (u32)t_; } while (0)
#define STAMPC(id) do { u64 t_; asm volatile("s_memtime %0\n\ts_waitcnt lgkmcnt(0)" : "=s"(t_) :: "memory"); stamps_[id] = (u32)t_; } while (0)
#define STAMP_OUT(wsp) do { if (blockIdx.x == PROBE_B && threadIdx.x < 64) { u32 dl_ = 500u + (PROBE == 99 ? (stamps_[31] - stamps_[30]) / 20u : PROBE == 98 ? (stamps_[2] - stamps_[1]) : (stamps_[PROBE % 32] - stamps_[0])); u32 t1_; do { u64 t_; asm volatile("s_memrealtime %0\n\ts_waitcnt lgkmcnt(0)" : "=s"(t_) :: "memory"); t1_ = (u32)t_; __builtin_amdgcn_s_sleep(1); } while (t1_ - stamps_[21] < dl_); } } while (0)

template <int I> struct IC { static constexpr int v = I; constexpr operator int() const { return I; } };
template <int B, int E, class Fn> DEVI void static_for(Fn&& f) {
  if constexpr (B < E) { f(IC<B>{}); static_for<B + 1, E>(f); }
}
#define WAIT_VM(n) asm volatile("s_waitcnt vmcnt(%0)" ::"n"(n) : "memory")
#define BARRIER() asm volatile("s_waitcnt lgkmcnt(0)\n\ts_barrier" ::: "memory")
#define SCHED() __builtin_amdgcn_sched_barrier(0)
#define PIN(x) asm volatile("" : "+v"(x))

DEVI int s_of_pi(int pi) {
  int RG = pi >> 6, i = pi & 63;
  return 128 * (4 * (RG & 3) + (i & 3)) + 8 * (i >> 2) + (RG >> 2);
}
DEVI void dma2(u32 voff0, u32 voff1, const char* sbase, u32 lds) {
  asm volatile("s_mov_b32 m0, %3\n\ts_nop 0\n\tglobal_load_lds_dwordx4 %0, %2\n\tglobal_load_lds_dwordx4 %1, %2 offset:1024" ::"v"(voff0), "v"(voff1), "s"(sbase), "s"(lds) : "memory");
}
DEVI void dma2c(u32 voff0, u32 voff1, const char* sbase, u32 lds) {
  asm volatile("s_mov_b32 m0, %3\n\ts_nop 0\n\tglobal_load_lds_dwordx4 %0, %2 sc1\n\tglobal_load_lds_dwordx4 %1, %2 offset:1024 sc1" ::"v"(voff0), "v"(voff1), "s"(sbase), "s"(lds) : "memory");
}
template <int OFF> DEVI void dma1(u32 voff, const char* sbase, u32 lds) {
  asm volatile("s_mov_b32 m0, %2\n\ts_nop 0\n\tglobal_load_lds_dwordx4 %0, %1 offset:%3" ::"v"(voff), "s"(sbase), "s"(lds), "n"(OFF) : "memory");
}
template <int OFF> DEVI void dma1s(u32 voff, const char* sbase, u32 lds) {
  asm volatile("s_mov_b32 m0, %2\n\ts_nop 0\n\tglobal_load_lds_dwordx4 %0, %1 offset:%3 sc1" ::"v"(voff), "s"(sbase), "s"(lds), "n"(OFF) : "memory");
}
DEVI void dma1c(u32 voff, const char* sbase, u32 lds) {
  asm volatile("s_mov_b32 m0, %2\n\ts_nop 0\n\tglobal_load_lds_dword %0, %1 sc1" ::"v"(voff), "s"(sbase), "s"(lds) : "memory");
}
DEVI v2u ld8c_sync(u32 voff, const char* sb) { v2u d; asm volatile("global_load_dwordx2 %0, %1, %2 sc1\n\ts_waitcnt vmcnt(0)" : "=&v"(d) : "v"(voff), "s"(sb) : "memory"); return d; }
DEVI u32 ld4c_sync(u32 voff, const char* sb) { u32 d; asm volatile("global_load_dword %0, %1, %2 sc1\n\ts_waitcnt vmcnt(0)" : "=&v"(d) : "v"(voff), "s"(sb) : "memory"); return d; }
DEVI v4u ld16c_sync(u32 voff, const char* sb) { v4u d; asm volatile("global_load_dwordx4 %0, %1, %2 sc1\n\ts_waitcnt vmcnt(0)" : "=&v"(d) : "v"(voff), "s"(sb) : "memory"); return d; }
template <class T> DEVI void ld16c(T& d, u32 voff, const char* sb) { asm volatile("global_load_dwordx4 %0, %1, %2 sc1" : "=&v"(d) : "v"(voff), "s"(sb) : "memory"); }
template <class T> DEVI void ld8c(T& d, u32 voff, const char* sb) { asm volatile("global_load_dwordx2 %0, %1, %2 sc1" : "=&v"(d) : "v"(voff), "s"(sb) : "memory"); }
template <class T> DEVI void ld4c(T& d, u32 voff, const char* sb) { asm volatile("global_load_dword %0, %1, %2 sc1" : "=&v"(d) : "v"(voff), "s"(sb) : "memory"); }
template <class T> DEVI void ld16(T& d, u32 voff, const char* sb) { asm volatile("global_load_dwordx4 %0, %1, %2" : "=&v"(d) : "v"(voff), "s"(sb) : "memory"); }
DEVI void ld2(u32& d, u32 voff, const char* sb) { asm volatile("global_load_ushort %0, %1, %2" : "=&v"(d) : "v"(voff), "s"(sb) : "memory"); }
template <class T> DEVI void st16c(u32 voff, const char* sb, T v) { asm volatile("global_store_dwordx4 %0, %1, %2 sc1\n\ts_nop 1" ::"v"(voff), "v"(v), "s"(sb) : "memory"); }
template <class T> DEVI void st8c(u32 voff, const char* sb, T v) { asm volatile("global_store_dwordx2 %0, %1, %2 sc1" ::"v"(voff), "v"(v), "s"(sb) : "memory"); }
template <class T> DEVI void st4c(u32 voff, const char* sb, T v) { asm volatile("global_store_dword %0, %1, %2 sc1" ::"v"(voff), "v"(v), "s"(sb) : "memory"); }

DEVI v4s trread(LAS char* p) { return __builtin_amdgcn_ds_read_tr16_b64_v4i16((LAS v4s*)p); }
DEVI v8s cat8(v4s a, v4s b) { return __builtin_shufflevector(a, b, 0, 1, 2, 3, 4, 5, 6, 7); }
DEVI float bflo(u32 v) { return __builtin_bit_cast(float, v << 16); }
DEVI float bfhi(u32 v) { return __builtin_bit_cast(float, v & 0xffff0000u); }
DEVI float relu(float v) { return __builtin_bit_cast(float, max(__builtin_bit_cast(int, v), 0)); }
DEVI float rbf(float v) { return (float)(__bf16)v; }
DEVI u32 pkbf(float a, float b) { v2b p = {(__bf16)a, (__bf16)b}; return __builtin_bit_cast(u32, p); }
DEVI u32 pkbf_o(float a, float b) { u32 r = pkbf(a, b); asm("" : "+v"(r)); return r; }
DEVI float cosr(float rev) { return __builtin_amdgcn_cosf(rev); }
DEVI float sinr(float rev) { return __builtin_amdgcn_sinf(rev); }
DEVI v4f mfma16h(v8h a, v8h b, v4f c) { return __builtin_amdgcn_mfma_f32_16x16x32_f16(a, b, c, 0, 0, 0); }
DEVI v4f mfma16b(v8s a, v4u b, v4f c) { return __builtin_amdgcn_mfma_f32_16x16x32_bf16(__builtin_bit_cast(v8b, a), __builtin_bit_cast(v8b, b), c, 0, 0, 0); }
DEVI v16f mfma32b(v8s a, v8s b, v16f c) { return __builtin_amdgcn_mfma_f32_32x32x16_bf16(__builtin_bit_cast(v8b, a), __builtin_bit_cast(v8b, b), c, 0, 0, 0); }
DEVI v16f mfma32h(v8h a, v8s b, v16f c) { return __builtin_amdgcn_mfma_f32_32x32x16_f16(a, __builtin_bit_cast(v8h, b), c, 0, 0, 0); }
DEVI v8h pack_ri(v4f re, v4f im) {
  v8h r;
  r[0] = (_Float16)re[0]; r[1] = (_Float16)re[1]; r[2] = (_Float16)re[2]; r[3] = (_Float16)re[3];
  r[4] = (_Float16)im[0]; r[5] = (_Float16)im[1]; r[6] = (_Float16)im[2]; r[7] = (_Float16)im[3];
  return r;
}
DEVI v4h pack4h(v4f a) { v4h r; r[0] = (_Float16)a[0]; r[1] = (_Float16)a[1]; r[2] = (_Float16)a[2]; r[3] = (_Float16)a[3]; return r; }
DEVI void twid(v4f& re, v4f& im, const v4f& c, const v4f& s) {
#pragma unroll
  for (int r = 0; r < 4; ++r) { float a = re[r], b = im[r]; re[r] = a * c[r] + b * s[r]; im[r] = b * c[r] - a * s[r]; }
}
template <int CTRL> DEVI float dppadd(float v) {
  return v + __builtin_bit_cast(float, __builtin_amdgcn_update_dpp(0, __builtin_bit_cast(int, v), CTRL, 0xf, 0xf, true));
}
DEVI float wave_sum(float v) {
  v = dppadd<0xB1>(v); v = dppadd<0x4E>(v); v = dppadd<0x141>(v); v = dppadd<0x140>(v);
  int bb = __builtin_bit_cast(int, v);
  float a0 = __builtin_bit_cast(float, __builtin_amdgcn_readlane(bb, 0)), a1 = __builtin_bit_cast(float, __builtin_amdgcn_readlane(bb, 16));
  float a2 = __builtin_bit_cast(float, __builtin_amdgcn_readlane(bb, 32)), a3 = __builtin_bit_cast(float, __builtin_amdgcn_readlane(bb, 48));
  return (a0 + a1) + (a2 + a3);
}
DEVI float dot2ones(u32 v, float acc) {
  const v2b ones = {(__bf16)1.0f, (__bf16)1.0f};
  return __builtin_amdgcn_fdot2_f32_bf16(__builtin_bit_cast(v2b, v), ones, acc, false);
}
template <int CTRL> DEVI u32 dppmax(u32 v) { return max(v, (u32)__builtin_amdgcn_update_dpp(0, (int)v, CTRL, 0xf, 0xf, true)); }
DEVI u32 wave_max(u32 v) {
  v = dppmax<0xB1>(v); v = dppmax<0x4E>(v); v = dppmax<0x141>(v); v = dppmax<0x140>(v);
  u32 a0 = __builtin_amdgcn_readlane(v, 0), a1 = __builtin_amdgcn_readlane(v, 16), a2 = __builtin_amdgcn_readlane(v, 32), a3 = __builtin_amdgcn_readlane(v, 48);
  return max(max(a0, a1), max(a2, a3));
}

struct Frag1 { v8s x0, x1, w1, w2; };

constexpr int L_SCR = 147456;
constexpr int L_CS = L_SCR, L_RED = L_SCR + 1024, L_MX = L_SCR + 1088, L_BC0 = L_SCR + 2048, L_BC1 = L_SCR + 2304;
constexpr int L_TAB = L_SCR + 3072;
constexpr int L_T = L_SCR + 7168, L_FA = L_SCR, L_E1 = L_T, L_E2 = 81920, L_ZX = 81920;
constexpr int L_C1 = 98304, L_IMG = L_C1 + 9216;
constexpr int L_FR = 131072;
constexpr int E1P = 544, E2P = 1024 + 72;
DEVI constexpr int wslot(int n) {
  constexpr int u[14] = {6, 7, 8, 0, 1, 2, 3, 4, 6, 7, 8, 0, 1, 2};
  return u[n] * 16384;
}
DEVI constexpr int aslot(int n) {
  constexpr int u[18] = {3, 4, 5, 9, 2, 6, 7, 8, 0, 1, 3, 4, 5, 9, 2, 6, 7, 8};
  return u[n] * 16384;
}

__global__ __launch_bounds__(512, 2) void kmain(const u16* __restrict__ x, const u16* __restrict__ wr, const u16* __restrict__ wi,
                                                const u16* __restrict__ Wg, const u16* __restrict__ Wu, const u16* __restrict__ Wd,
                                                char* __restrict__ ws, u16* __restrict__ out) {
  __shared__ __attribute__((aligned(16))) char smem_[163840];
  LAS char* smem = (LAS char*)smem_;
  const u32 lbase = (u32)(size_t)smem;
  const int tid = threadIdx.x, l = tid & 63;
  const int w = __builtin_amdgcn_readfirstlane(tid >> 6);
  const int b = blockIdx.x, xcd = b & 7, idx = b >> 3;
  const bool ffn = idx < 28;
  const int g = l >> 4, o = l & 15;
  const int h = l >> 5, blk = (l >> 4) & 1, q = (l & 15) >> 2, pp = l & 3;
  const char* FL = ws + OFF_FL;
  const char* BC = ws + OFF_BC;
  STAMP_DECL; STAMPT(0);
  asm volatile("" ::"s"(x), "s"(wr), "s"(wi), "s"(Wg), "s"(Wu), "s"(Wd), "s"(ws), "s"(out));

  const int pi = b * 8 + w;
  v4u xr;
  ld16(xr, (u32)(s_of_pi(pi) * 1024 + l * 16), (const char*)x);
  if (!ffn) {
    u64 pc; u32 len;
    asm volatile("s_getpc_b64 %0\nkm_pf_a:\n\ts_mov_b32 %1, km_pf_b-km_pf_a" : "=s"(pc), "=s"(len));
    dma1<0>(min((u32)(((idx & 3) * 8 + w) * 1024 + l * 16), len) & ~15u, (const char*)(pc & ~15ull), lbase + L_FA);
  }
  if (w == 0) dma1c((u32)((l & 1) * 4), BC, lbase + L_BC0);
  const int fkb = (b & 7) * 2 + (w >> 2), fcq = w & 3;
  {
    const u32 ks = (u32)((idx >> 1) + 16 * fkb + 256 * (4 * (idx & 1) + ((l >> 3) & 3)));
    const u32 base = ((ks * (DF_ * 2)) & ~15u) + fcq * 128;
    const u32 dlt = (u32)((const char*)wi - (const char*)wr);
    (void)dlt;
    const u32 c0 = ((l & 7) ^ (((l >> 4) & 1) << 2)) << 4;
    if (l < 32) dma1<0>(base + c0, (const char*)wr, lbase + L_FR + w * 2048);
    else dma1<0>(base + c0, (const char*)wi, lbase + L_FR + w * 2048);
    if (fcq == 3) {
      const u32 t0 = min(base + 128 + (c0 & 16), (u32)(2048 * DF_ * 2 - 16));
      if (l < 32) dma1<0>(t0, (const char*)wr, lbase + L_FR + w * 2048 + 1024);
      else dma1<0>(t0, (const char*)wi, lbase + L_FR + w * 2048 + 1024);
    }
  }
  const int p = (xcd >> 1) * 4 + idx / 7, j = (xcd & 1) * 7 + idx % 7;
  const int sh = w >> 2, fq = w & 3;
  u32 aoff[2];
#pragma unroll
  for (int i = 0; i < 2; ++i) {
    int row = (2 * w + i) * 8 + (l >> 3);
    aoff[i] = s_of_pi((p & 15) * 128 + row) * 1024 + (((l & 7) ^ ((row >> 1) & 7)) << 4) - i * 1024;
  }
  const u32 boff = (u32)(2 * w * 4 + (l >> 4)) * (F_ * 2) + j * 256 + (((l & 15) ^ ((l >> 4) << 2)) << 4);
  const u32 boff1 = boff + 4 * F_ * 2 - 1024;
  const int rot = p >> 1;
  auto issue = [&](int t, int slot) {
    const int kt = (t + rot) & 7;
    u32 dst = lbase + slot * 49152 + w * 2048;
    dma2(aoff[0], aoff[1], (const char*)x + kt * 128, dst);
    dma2(boff, boff1, (const char*)Wg + (size_t)kt * (64 * F_ * 2), dst + 16384);
    dma2(boff, boff1, (const char*)Wu + (size_t)kt * (64 * F_ * 2), dst + 32768);
  };
  const int sp = xcd * 4 + (idx - 28);
  if (ffn) issue(0, 0);

  u32 E = 0, tick = 0;
  const bool coord = (b == 224) && (w == 0);
  auto do_tick = [&]() { if (coord) { ++tick; if (l == 0) st8c(0u, BC, v2u{E, tick}); } };
  auto get_epoch = [&]() {
    const v2u b0 = *(LAS v2u*)(smem + L_BC0);
    v2u b1 = *(LAS v2u*)(smem + L_BC1);
    int spins = 0;
    while (__builtin_amdgcn_readfirstlane((b0[0] ^ b1[0]) | (b0[1] ^ b1[1])) == 0 && ++spins < SPIN_CAP) {
      __builtin_amdgcn_s_sleep(4);
      b1 = ld8c_sync(0u, BC);
    }
    E = __builtin_amdgcn_readfirstlane(b1[0]);
  };
  auto signal_a = [&]() {
    v4u r0 = *(LAS v4u*)(smem + L_RED), r1 = *(LAS v4u*)(smem + L_RED + 16);
    u32 m = max(max(max(r0[0], r0[1]), max(r0[2], r0[3])), max(max(r1[0], r1[1]), max(r1[2], r1[3])));
    if (l == 0) { st4c((u32)(FL_A + b * 4), FL, E | (m >> 7)); *(LAS u32*)(smem + L_MX + 4) = E; }
  };
  auto poll4 = [&](v4u f, u32 voff) -> v4u {
    int spins = 0;
    while (!__all((((f[0] ^ E) | (f[1] ^ E) | (f[2] ^ E) | (f[3] ^ E)) >> 8) == 0) && ++spins < SPIN_CAP) {
      do_tick();
      __builtin_amdgcn_s_sleep(12);
      f = ld16c_sync(voff, FL);
    }
    return f;
  };
  auto check_a = [&](v4u f) {
    f = poll4(f, (u32)(FL_A + l * 16));
    u32 m = wave_max(max(max(f[0], f[1]), max(f[2], f[3]))) & 0xffu;
    if (l == 0) *(LAS u32*)(smem + L_MX) = m;
  };
  if (coord) {
#pragma unroll
    for (int i = 0; i < 5; ++i) dma2c(i ? (u32)(FL_H + ((i - 1) * 128 + l) * 64) : (u32)(l * 16), i ? (u32)(FL_H + ((i - 1) * 128 + 64 + l) * 64 - 1024) : (u32)(l * 16), FL, lbase + i * 2048);
  }
  u32 sq[8];
  if (!ffn && w == 7) {
    const int kss = (sp >> 1) + 16 * (l >> 2) + 256 * (4 * (sp & 1) + (l & 3)), km = (2048 - kss) & 2047;
    const u32 o1 = (u32)(kss * DF_ * 2), o2 = (u32)(km * DF_ * 2);
    ld2(sq[0], o1, (const char*)wr); ld2(sq[1], o1, (const char*)wi); ld2(sq[2], o2, (const char*)wr); ld2(sq[3], o2, (const char*)wi);
    ld2(sq[4], o1 + 512, (const char*)wr); ld2(sq[5], o1 + 512, (const char*)wi); ld2(sq[6], o2 + 512, (const char*)wr); ld2(sq[7], o2 + 512, (const char*)wi);
  }

  {
    const float fo = (float)o, fg = (float)(4 * g);
    if (w < 2) {
      const float st = fo * (1.0f / 32.0f), a0 = fmaf(fg, st, w ? 0.25f : 0.f);
      v4u hi, lo;
#pragma unroll
      for (int e2 = 0; e2 < 4; ++e2) {
        float c0 = cosr(fmaf((float)(16 * (e2 >> 1) + 2 * (e2 & 1)), st, a0)), c1 = cosr(fmaf((float)(16 * (e2 >> 1) + 2 * (e2 & 1) + 1), st, a0));
        u32 ch = pkbf_o(c0, c1);
        hi[e2] = ch; lo[e2] = pkbf(c0 - bflo(ch), c1 - bfhi(ch));
      }
      *(LAS v4u*)(smem + L_C1 + w * 2048 + l * 16) = hi; *(LAS v4u*)(smem + L_C1 + w * 2048 + 1024 + l * 16) = lo;
    } else if (w < 5) {
      const float st = w == 2 ? (float)(o & 7) * (1.0f / 16.0f) : fo * (w == 3 ? 1.0f / 16.0f : 1.0f / 8.0f);
      const float a0 = (w == 2 && o >= 8) ? 0.25f : 0.f;
      const float sc = w == 2 ? 0.0625f : ((w == 4 && (g >> 1) != (o >> 3)) ? 0.f : 1.0f);
      v8h m;
#pragma unroll
      for (int e = 0; e < 4; ++e) { float a = fmaf(fg + (float)e, st, a0); m[e] = (_Float16)(sc * cosr(a)); m[4 + e] = (_Float16)(sc * sinr(a)); }
      *(LAS v8h*)(smem + (w == 2 ? L_C1 + 4096 : L_TAB + (w - 3) * 1024) + l * 16) = m;
    } else {
      const float st = w == 7 ? fo * (1.0f / 256.0f) : (fo + (w == 6 ? 16.f : 0.f)) * (1.0f / 512.0f);
      v4f c, sn;
#pragma unroll
      for (int e = 0; e < 4; ++e) { float a = (fg + (float)e) * st; c[e] = cosr(a); sn[e] = sinr(a); }
      LAS char* dst = smem + (w == 7 ? L_TAB + 2048 : L_C1 + 5120 + (w - 5) * 2048) + l * 16;
      *(LAS v4f*)dst = c; *(LAS v4f*)(dst + 1024) = sn;
    }
    BARRIER();
    LAS char* C1 = smem + L_C1 + l * 16;
    const v4u Fhr = *(LAS v4u*)C1, Flr = *(LAS v4u*)(C1 + 1024), Fhi = *(LAS v4u*)(C1 + 2048), Fli = *(LAS v4u*)(C1 + 3072);
    const v8h M2 = *(LAS v8h*)(C1 + 4096);
    v4f twc[2], tws[2];
#pragma unroll
    for (int u = 0; u < 2; ++u) { twc[u] = *(LAS v4f*)(C1 + 5120 + u * 2048); tws[u] = *(LAS v4f*)(C1 + 6144 + u * 2048); }
    if (ffn) WAIT_VM(8); else if (w == 7) WAIT_VM(11); else WAIT_VM(3);
    if (coord) {
      WAIT_VM(0);
      v4u f[10];
#pragma unroll
      for (int i = 0; i < 10; ++i) f[i] = *(LAS v4u*)(smem + i * 1024 + l * 16);
      u32 c = __builtin_amdgcn_readfirstlane(*(LAS u32*)(smem + L_BC0)) >> 8;
      bool hit;
      do {
        c = (c + 1) & 0xffffffu; hit = false;
#pragma unroll
        for (int i = 0; i < 10; ++i) hit |= ((f[i][0] >> 8) == c) | ((f[i][1] >> 8) == c) | ((f[i][2] >> 8) == c) | ((f[i][3] >> 8) == c);
      } while (__any(hit));
      E = c << 8;
      do_tick();
    }
    PIN(xr);
    STAMPT(3);
    const float c0 = bflo(__builtin_amdgcn_readfirstlane(xr[0]));
    v2f xv[4], s2 = {0.f, 0.f}, q2 = {0.f, 0.f};
#pragma unroll
    for (int e = 0; e < 4; ++e) {
      xv[e] = v2f{bflo(xr[e]), bfhi(xr[e])};
      v2f t = xv[e] - v2f{c0, c0};
      s2 += t; q2 = __builtin_elementwise_fma(t, t, q2);
    }
    const float dmean = wave_sum(s2[0] + s2[1]) * (1.0f / 512.0f), msq = wave_sum(q2[0] + q2[1]) * (1.0f / 512.0f);
    do_tick();
    const float mb = rbf(c0 + dmean);
    const float var = rbf(fmaxf(msq - dmean * dmean, 0.f));
    const float sd = rbf(__builtin_amdgcn_sqrtf(rbf(var + 1e-5f)));
    const float rs = __builtin_amdgcn_rcpf(sd);
#pragma unroll
    for (int e = 0; e < 4; ++e) {
      v2f t = xv[e] - v2f{mb, mb};
      u32 tb = pkbf_o(t[0], t[1]);
      t = v2f{bflo(tb), bfhi(tb)} * v2f{rs, rs};
      xr[e] = pkbf(t[0], t[1]);
    }
    *(LAS v4u*)(smem + L_IMG + w * 1024 + l * 16) = xr;
    if (l < 2) st4c((u32)(pi * 4 + l * 8192), ws + OFF_MB, l ? rs * rs : -mb);
    const v8s xf = cat8(trread(smem + L_IMG + w * 1024 + l * 8), trread(smem + L_IMG + w * 1024 + 512 + l * 8));
    const int kt1 = (1 + rot) & 7;
    const u32 dst1 = lbase + 49152 + w * 2048;
    if (ffn) dma2(aoff[0], aoff[1], (const char*)x + kt1 * 128, dst1);
    const v8s xg_ = __builtin_bit_cast(v8s, __builtin_bit_cast(v4u, xf) ^ v4u{0x80000000u, 0x80000000u, 0x80000000u, 0x80000000u});
    const v4f z = {0.f, 0.f, 0.f, 0.f};
    v4f dre[2], dim[2];
#pragma unroll
    for (int u = 0; u < 2; ++u) { dre[u] = mfma16b(u ? xg_ : xf, Flr, z); dim[u] = mfma16b(u ? xg_ : xf, Fli, z); }
#pragma unroll
    for (int u = 0; u < 2; ++u) { dre[u] = mfma16b(u ? xg_ : xf, Fhr, dre[u]); dim[u] = mfma16b(u ? xg_ : xf, Fhi, dim[u]); }
#pragma unroll
    for (int u = 0; u < 2; ++u) {
      twid(dre[u], dim[u], twc[u], tws[u]);
      const v8h bb = pack_ri(dre[u], dim[u]);
      v4f d2 = mfma16h(bb, M2, z);
      if (u == 0) {
        const u32 nv = o == 8 ? 0xac002c00u : 0u;
        v4f n = mfma16h(bb, __builtin_bit_cast(v8h, v4u{nv, nv, 0u, 0u}), z);
        if (l == 8) d2[0] = n[0];
      }
      *(LAS v4h*)(smem + L_T + ((o >> 3) * 8 + w) * 544 + (o & 7) * 64 + u * 32 + g * 8) = pack4h(d2);
    }
    if (ffn) {
      dma2(boff, boff1, (const char*)Wg + (size_t)kt1 * (64 * F_ * 2), dst1 + 16384);
      dma2(boff, boff1, (const char*)Wu + (size_t)kt1 * (64 * F_ * 2), dst1 + 32768);
    }
    STAMPT(4);
  }
  v8s su; v4u fu, tu;
  auto round1_read = [&]() {
    LAS char* rp = smem + L_T + (4 * (g & 1) + (o >> 2)) * 544 + (w * 32 + (g >> 1) * 16) * 2 + (o & 3) * 8;
    su = cat8(trread(rp), trread(rp + 8 * 544));
    LAS char* fp_ = smem + L_FR + w * 2048 + q * 128 + (((2 * g + (pp >> 1)) ^ ((q >> 1) << 2)) << 4) + (pp & 1) * 8;
    fu = __builtin_bit_cast(v4u, cat8(trread(fp_), trread(fp_ + 512)));
    LAS char* tp = smem + L_FR + w * 2048 + 1024 + q * 128 + (pp >> 1) * 16 + (pp & 1) * 8;
    tu = __builtin_bit_cast(v4u, cat8(trread(tp), trread(tp + 512)));
  };
  auto round1_max = [&]() {
    typedef unsigned short v2us __attribute__((ext_vector_type(2)));
    const int dl = (idx >> 1) & 7;
    const u32 msk = (fcq == 0 && l < dl) ? 0u : 0x7fff7fffu, msk2 = (fcq == 3 && o <= dl) ? 0x7fff7fffu : 0u;
    v2us m2 = {0, 0};
#pragma unroll
    for (int e = 0; e < 4; ++e) {
      u32 t = fu[e] & msk, t2 = tu[e] & msk2;
      m2 = __builtin_elementwise_max(m2, __builtin_elementwise_max(__builtin_bit_cast(v2us, t), __builtin_bit_cast(v2us, t2)));
    }
    const u32 mx = wave_max(max((u32)m2[0], (u32)m2[1]));
    if (l == 0) *(LAS u32*)(smem + L_RED + w * 4) = mx;
  };
  auto round1_store = [&]() {
    st16c((u32)(b * 8192 + (w * 32 + (g >> 1) * 16 + o) * 32 + (g & 1) * 16), ws + OFF_S1, su);
    st16c((u32)((idx * 16 + fkb) * 4608 + (fcq * 64 + l) * 16), ws + OFF_WT, fu);
    if (fcq == 3 && l < 16) st16c((u32)((idx * 16 + fkb) * 4608 + (256 + l) * 16), ws + OFF_WT, tu);
  };

  const int RG = xcd * 4 + (idx >> 3), ct = idx & 7;
  const int kd = xcd * 32 + ((idx + 3) & 31);
  const u32 swz = ((l & 7) ^ (((l >> 4) & 1) << 2)) << 4;
  const u32 ro0 = (u32)((l >> 3) * 1024) + swz, ro1 = ro0 + 8192 - 1024;
  const char* bsrc = (const char*)Wd + (size_t)(w * 16) * 1024 + ct * 128;
  auto issueW = [&](int n) { dma2(ro0, ro1, bsrc + (size_t)n * 131072, lbase + wslot(n) + w * 2048); };
  const int dh = w & 1, kq = w >> 1, ph = kq >> 1, kh = kq & 1;
  const int epi = RG * 64 + ph * 32 + (l & 31);
  const u32 eoff = (u32)(s_of_pi(epi) * D_ + ct * 64 + dh * 32 + kh * 16 + h * 8) * 2;
  v4u wt, s1v, xe; v4f pq[4]; float rs2;
  auto load_xe = [&]() { ld16(xe, eoff, (const char*)x); };
  auto load_s1 = [&]() { ld16c(s1v, (u32)((((4 * w + g) * 8 + (o >> 1)) * 256 + kd) * 32 + (o & 1) * 16), ws + OFF_S1); };
  auto load_wt = [&]() { ld16c(wt, (u32)((w * 64 + l) * 4608 + (kd + ((2 * w + (g >> 1)) & 7)) * 16), ws + OFF_WT); };
  auto load_rs = [&]() { ld4c(rs2, (u32)(epi * 4), ws + OFF_RS2); };
  v4f t2c, t2s, t3c, t3s;
  auto trig2 = [&]() {
    const float fw = (float)w, st2 = fw * (1.0f / 128.0f), a2 = fw * (float)(o + 64 * g) * (1.0f / 2048.0f);
#pragma unroll
    for (int e = 0; e < 4; ++e) { float b2 = fmaf((float)e, st2, a2); t2c[e] = cosr(b2); t2s[e] = sinr(b2); }
    PIN(t2c); PIN(t2s);
  };
  auto trig3 = [&]() {
    const float f7 = (float)(o & 7), st3 = f7 * (1.0f / 128.0f), a3 = f7 * (float)(2 * w + (o >> 3) + 64 * g) * (1.0f / 2048.0f);
#pragma unroll
    for (int e = 0; e < 4; ++e) { float b3 = fmaf((float)e, st3, a3); t3c[e] = cosr(b3); t3s[e] = sinr(b3); }
    PIN(t3c); PIN(t3s);
  };
  v4u f16; v4f t1c, t1s, re, im;
  const v4f z4 = {0.f, 0.f, 0.f, 0.f};
  auto imf = [](v4u f) { return __builtin_bit_cast(v8h, v4u{f[2] ^ 0x80008000u, f[3] ^ 0x80008000u, f[0], f[1]}); };
  auto segA0 = [&]() {
    f16 = *(LAS v4u*)(smem + L_TAB + l * 16);
    t1c = *(LAS v4f*)(smem + L_TAB + 2048 + l * 16); t1s = *(LAS v4f*)(smem + L_TAB + 3072 + l * 16);
  };
  auto segA1 = [&]() {
    PIN(s1v);
    const v8h a = __builtin_bit_cast(v8h, s1v);
    re = mfma16h(a, __builtin_bit_cast(v8h, f16), z4); im = mfma16h(a, imf(f16), z4);
  };
  auto segA2 = [&]() {
    twid(re, im, t1c, t1s);
    const v8h a = pack_ri(re, im);
    re = mfma16h(__builtin_bit_cast(v8h, f16), a, z4); im = mfma16h(imf(f16), a, z4);
  };
  auto segA3 = [&]() {
    twid(re, im, t2c, t2s);
    *(LAS v4h*)(smem + L_E1 + w * E1P + o * 32 + g * 8) = pack4h(re);
    *(LAS v4h*)(smem + L_E1 + (8 + w) * E1P + o * 32 + g * 8) = pack4h(im);
  };

  if (ffn) {
    v16f acc[2][2] = {};
    float mbr[2];
    const u32 wb0 = (u32)((8 * h + q) * 256 + ((fq ^ q) << 6) + blk * 32 + pp * 8);
    const u32 r1 = 16384 + sh * 16384, r2 = 32768 - sh * 16384;
    const int xrow = sh * 64 + (l & 31);
    const u32 xb0 = (u32)(xrow * 128 + ((h ^ ((xrow >> 1) & 7)) << 4));
    auto rd = [&](Frag1& f, LAS char* sb, int i) {
      LAS char* xa = sb + (xb0 ^ (i << 5));
      LAS char* wa_ = sb + wb0 + (i << 12);
      f.w1 = cat8(trread(wa_ + r1), trread(wa_ + r1 + 1024));
      f.x0 = *(LAS v8s*)xa; f.x1 = *(LAS v8s*)(xa + 4096);
      f.w2 = cat8(trread(wa_ + r2), trread(wa_ + r2 + 1024));
    };
    const u32 one = ((l & 15) == ((l >> 4) & 1)) ? 0x3f803f80u : 0u;
    const v8s sel = __builtin_bit_cast(v8s, v4u{one, one, one, one});
    v4f csa = {0.f, 0.f, 0.f, 0.f};
    Frag1 A, B;
    WAIT_VM(7);
    BARRIER();
    STAMPT(1); STAMPC(30);
    rd(A, smem, 0);
    round1_read();
    dma2(aoff[0], aoff[1], (const char*)x + ((2 + rot) & 7) * 128, lbase + 2 * 49152 + w * 2048);
    u32 c1[4], c2[4];
    static_for<0, 8>([&](auto T) {
      constexpr int t = T, slot = t % 3, nslot = (t + 1) % 3, pslot = (t + 2) % 3;
      LAS char* sb = smem + slot * 49152;
      {
        const int kt = (t + 2 + rot) & 7;
        const char* s1 = t < 6 ? (const char*)Wg + (size_t)kt * (64 * F_ * 2) : bsrc + (size_t)((t - 6) * 3 + 1) * 131072;
        const char* s2 = t < 6 ? (const char*)Wu + (size_t)kt * (64 * F_ * 2) : bsrc + (size_t)((t - 6) * 3 + 2) * 131072;
        const u32 o2 = t < 6 ? boff : ro0, o3 = t < 6 ? boff1 : ro1;
        const u32 dst = lbase + pslot * 49152 + w * 2048;
        rd(B, sb, 1); SCHED();
        acc[0][0] = mfma32b(A.w1, A.x0, acc[0][0]); acc[0][1] = mfma32b(A.w1, A.x1, acc[0][1]);
        if constexpr (t == 0) csa = mfma16b(sel, __builtin_bit_cast(v4u, A.w1), csa);
        SCHED();
        dma1<0>(o2, s1, dst + 16384);
        if constexpr (t == 2) load_xe();
        if constexpr (t == 6) load_wt();
        acc[1][0] = mfma32b(A.w2, A.x0, acc[1][0]); acc[1][1] = mfma32b(A.w2, A.x1, acc[1][1]); SCHED();
        if constexpr (t == 0) { round1_store(); round1_max(); SCHED(); }
        if constexpr (t == 3) { trig2(); SCHED(); }
        if constexpr (t == 4) { trig3(); SCHED(); }
        rd(A, sb, 2); SCHED();
        acc[0][0] = mfma32b(B.w1, B.x0, acc[0][0]); SCHED();
        dma1<1024>(o3, s1, dst + 16384);
        acc[0][1] = mfma32b(B.w1, B.x1, acc[0][1]);
        if constexpr (t == 0) csa = mfma16b(sel, __builtin_bit_cast(v4u, B.w1), csa);
        acc[1][0] = mfma32b(B.w2, B.x0, acc[1][0]); acc[1][1] = mfma32b(B.w2, B.x1, acc[1][1]); SCHED();
        dma1<0>(o2, s2, dst + 32768);
        if constexpr (t == 6) {
          load_rs();
        }
        if constexpr (t == 7) { WAIT_VM(13); segA1(); SCHED(); }
        rd(B, sb, 3); SCHED();
        if constexpr (t == 7) {
#pragma unroll
          for (int i = 0; i < 4; ++i) { PIN(c1[i]); PIN(c2[i]); }
          PIN(mbr[0]); PIN(mbr[1]);
          const v8s cs1 = __builtin_bit_cast(v8s, v4u{c1[0], c1[1], c1[2], c1[3]}), cs2 = __builtin_bit_cast(v8s, v4u{c2[0], c2[1], c2[2], c2[3]});
          const u32 m0_ = pkbf(mbr[0], mbr[0]), m1_ = pkbf(mbr[1], mbr[1]);
          const v8s mb0 = __builtin_bit_cast(v8s, v4u{m0_, m0_, m0_, m0_}), mb1 = __builtin_bit_cast(v8s, v4u{m1_, m1_, m1_, m1_});
          acc[0][0] = mfma32b(cs1, mb0, acc[0][0]); acc[1][0] = mfma32b(cs2, mb0, acc[1][0]);
          acc[0][1] = mfma32b(cs1, mb1, acc[0][1]); acc[1][1] = mfma32b(cs2, mb1, acc[1][1]); SCHED();
          segA2(); SCHED();
        }
        acc[0][0] = mfma32b(A.w1, A.x0, acc[0][0]); acc[0][1] = mfma32b(A.w1, A.x1, acc[0][1]);
        if constexpr (t == 0) csa = mfma16b(sel, __builtin_bit_cast(v4u, A.w1), csa);
        SCHED();
        dma1<1024>(o3, s2, dst + 32768);
        acc[1][0] = mfma32b(A.w2, A.x0, acc[1][0]); acc[1][1] = mfma32b(A.w2, A.x1, acc[1][1]); SCHED();
      }
      if constexpr (t < 7) {
        const char* s0 = t < 5 ? (const char*)x + ((t + 3 + rot) & 7) * 128 : bsrc + (size_t)((t - 5) * 3) * 131072;
        const u32 o0 = t < 5 ? aoff[0] : ro0, o1 = t < 5 ? aoff[1] : ro1;
        const u32 dst = lbase + slot * 49152 + w * 2048;
        if constexpr (t == 0) csa = mfma16b(sel, __builtin_bit_cast(v4u, B.w1), csa);
        WAIT_VM(t == 0 ? 8 : t == 2 ? 7 : t == 6 ? 19 : 6);
        if constexpr (t == 5) { if (w == 0) check_a(*(LAS v4u*)(smem + L_FA + l * 16)); }
        BARRIER();
        STAMPT(22 + t);
        acc[0][0] = mfma32b(B.w1, B.x0, acc[0][0]); SCHED();
        rd(A, smem + nslot * 49152, 0);
        if constexpr (t == 6) segA0();
        SCHED();
        if constexpr (t == 0) {
          if (l < 16) {
#pragma unroll
            for (int i = 0; i < 2; ++i) {
              const float hi = rbf(csa[i]);
              st4c((u32)(((j * 16 + p) * 256 + sh * 128 + fq * 32 + i * 16 + l) * 4), ws + OFF_CSP, pkbf(hi, csa[i] - hi));
            }
          }
        }
        if constexpr (t == 0) { if (w == 0) dma1c((u32)((l & 1) * 4), BC, lbase + L_BC1); }
        if constexpr (t == 4) { if (w == 0) dma1s<0>((u32)(FL_A + l * 16), FL, lbase + L_FA); }
        if constexpr (t == 5) {
          const u32 co = (u32)(((j * 16 + (p & 1) + 8 * h) * 256 + fq * 32 + (l & 31)) * 4);
#pragma unroll
          for (int i = 0; i < 4; ++i) { ld4c(c1[i], co + sh * 512 + i * 2048, ws + OFF_CSP); ld4c(c2[i], co + (1 - sh) * 512 + i * 2048, ws + OFF_CSP); }
          ld4c(mbr[0], (u32)((p * 128 + xrow) * 4), ws + OFF_MB); ld4c(mbr[1], (u32)((p * 128 + xrow + 32) * 4), ws + OFF_MB);
          load_s1();
        }
        dma1<0>(o0, s0, dst);
        acc[0][1] = mfma32b(B.w1, B.x1, acc[0][1]);
        acc[1][0] = mfma32b(B.w2, B.x0, acc[1][0]);
        acc[1][1] = mfma32b(B.w2, B.x1, acc[1][1]); SCHED();
        dma1<1024>(o1, s0, dst);
        if constexpr (t == 1) { if (w == 0) { get_epoch(); signal_a(); } }
        SCHED();
      } else {
        acc[0][0] = mfma32b(B.w1, B.x0, acc[0][0]); acc[1][0] = mfma32b(B.w2, B.x0, acc[1][0]); SCHED();
        acc[0][1] = mfma32b(B.w1, B.x1, acc[0][1]); acc[1][1] = mfma32b(B.w2, B.x1, acc[1][1]); SCHED();
      }
    });
    STAMPT(2); STAMPC(31);
    auto fin = [&](const v16f& ag, const v16f& au, int rt) {
#pragma unroll
      for (int s2i = 0; s2i < 2; ++s2i) {
        v4u ov;
#pragma unroll
        for (int e = 0; e < 4; ++e) {
          const int r = s2i * 8 + 2 * e;
          v2f gg = v2f{relu(ag[r]), relu(ag[r + 1])} * v2f{au[r], au[r + 1]};
          ov[e] = pkbf(gg[0], gg[1]);
        }
        st16c((u32)(((p * 4 + sh * 2 + rt) * 112 + (j * 8 + fq * 2 + s2i)) * 1024 + l * 16), ws + OFF_H, ov);
      }
    };
    if (sh == 0) { fin(acc[0][0], acc[1][0], 0); fin(acc[0][1], acc[1][1], 1); }
    else { fin(acc[1][0], acc[0][0], 0); fin(acc[1][1], acc[0][1], 1); }
    STAMPT(6);
    segA3();
    WAIT_VM(4);
  } else {
    WAIT_VM(1);
    BARRIER();
    round1_read(); round1_store(); round1_max();
    if (w == 0 && !coord) dma1c((u32)((l & 1) * 4), BC, lbase + L_BC1);
    do_tick();
    if (w == 7) {
#pragma unroll
      for (int i = 0; i < 8; ++i) PIN(sq[i]);
      float war = bflo(sq[0]) + bflo(sq[2]), wai = bflo(sq[1]) - bflo(sq[3]);
      float wbr = bflo(sq[4]) + bflo(sq[6]), wbi = bflo(sq[5]) - bflo(sq[7]);
      v4f pv = {0.25f * (war + wbr), 0.25f * (wai + wbi), 0.25f * (war - wbr), 0.25f * (wai - wbi)};
      st16c((u32)((((sp >> 2) * 4 + (l & 3)) * 64 + (sp & 3) * 16 + (l >> 2)) * 16), ws + OFF_PQ, pv);
    }
    WAIT_VM(0);
    BARRIER();
    STAMPT(1); STAMPC(30);
    if (w == 0) { if (!coord) get_epoch(); signal_a(); do_tick(); WAIT_VM(0); }
    BARRIER();
    STAMPT(24);
    dma1c((u32)(sp * 57344 + (w * 112 + l) * 64), (const char*)Wd, lbase + 49152);
    dma1c((u32)(sp * 57344 + (w * 112 + 48 + l) * 64), (const char*)Wd, lbase + 49152);
    static_for<0, 6>([&](auto N) { issueW(N); });
    static_for<6, 14>([&](auto N) { dma2(ro0, ro1, bsrc - 512 + (size_t)N * 131072, lbase + 49152 + w * 2048); });
    load_xe();
    trig2(); trig3(); segA0();
    STAMPT(25);
    if (w == 0) {
      if (!coord) __builtin_amdgcn_s_sleep(SLEEP_A);
      check_a(v4u{~E, 0u, 0u, 0u});
    }
    STAMPT(26);
    BARRIER();
    load_s1(); load_wt(); load_rs();
    if (kd == 0) {
#pragma unroll
      for (int r = 0; r < 4; ++r) ld16c(pq[r], (u32)(w * 4096 + r * 1024 + l * 16), ws + OFF_PQ);
    }
    WAIT_VM(0);
    segA1(); segA2(); segA3();
    STAMPT(2); STAMPC(31);
  }
  PIN(wt); PIN(xe); PIN(rs2);
  BARRIER();
  STAMPT(5);

  const u32 trG = (u32)(kq * 4096 + (4 * h + q) * 128 + ((dh ^ ((q >> 1) & 1)) << 6) + (blk ^ kh) * 32 + pp * 8);
  v8s Wf[14][2];
  auto readW = [&](auto N) {
    constexpr int n = N;
    LAS char* sb = smem + wslot(n) + trG;
    Wf[n][0] = cat8(trread(sb), trread(sb + 1024)); Wf[n][1] = cat8(trread(sb + 2048), trread(sb + 3072));
  };
  const char* hsrc = ws + OFF_H + ((size_t)(RG * 2 + (w >> 2)) * 112 + (w & 3) * 2) * 1024;
  const u32 so0 = (u32)((l >> 3) * 4096) + swz, so1 = so0 + 32768 - 1024;
  const char* ssrc = ws + OFF_S2 + (size_t)(w * 16) * 4096 + RG * 128;
  auto issueP = [&](int n, auto HALF) {
    constexpr int hf = HALF;
    if (n < 14) dma1s<hf * 1024>((u32)(l * 16), hsrc + n * 8192, lbase + aslot(n) + w * 2048);
    else dma1s<hf * 1024>(hf ? so1 : so0, ssrc + (size_t)(n - 14) * (128 * 4096), lbase + aslot(n) + w * 2048);
  };
  const int d = ct * 64 + dh * 32 + ((l & 31) ^ (kh << 4));
  const float gst = (float)d * (1.0f / 512.0f), ga0 = (float)(16 * kq + 2 * h) * gst;
  v8h G[8];
  auto gen = [&](auto FI) {
    constexpr int f = FI;
#pragma unroll
    for (int e2 = 0; e2 < 4; ++e2) {
      float a = fmaf((float)(64 * (f >> 1) + 8 * (f & 1) + 4 * (e2 >> 1) + (e2 & 1)), gst, ga0);
      float c = cosr(a), sn = sinr(a);
      if (f == 0 && e2 == 0) { if (kq == 0 && h == 0) { c = 0.5f; sn = (d & 1) ? 0.5f : -0.5f; } }
      G[f][2 * e2] = (_Float16)c; G[f][2 * e2 + 1] = (_Float16)sn;
    }
    PIN(G[f]);
  };

  float oscale;
  const u32 fhoff = (u32)(FL_H + (RG * 16 + (min(l, 55) >> 2)) * 64 + (l & 3) * 4);
  u32 fp;
  {
    v8h a;
    {
      LAS char* rp = smem + L_E1 + (4 * (g & 1) + (o >> 2)) * E1P + (2 * w + (g >> 1)) * 32 + (o & 3) * 8;
      a = __builtin_bit_cast(v8h, cat8(trread(rp), trread(rp + 8 * E1P)));
    }
    const v4u f8 = *(LAS v4u*)(smem + L_TAB + 1024 + l * 16);
    const v2u me = *(LAS v2u*)(smem + L_MX);
    issueW(6);
    static_for<0, 3>(readW);
    const int Ex = (int)me[0] < 16 ? 16 : ((int)me[0] > 240 ? 240 : (int)me[0]);
    const float wscale = __builtin_bit_cast(float, (u32)(246 - Ex) << 23);
    oscale = __builtin_bit_cast(float, (u32)(Ex - 7) << 23);
    re = mfma16h(__builtin_bit_cast(v8h, f8), a, z4); im = mfma16h(imf(f8), a, z4);
    static_for<3, 6>(readW);
    WAIT_VM(2);
    STAMPT(7);
    if (ffn && l == 0) st4c((u32)(FL_H + ((p * 2 + sh) * 16 + j) * 64 + fq * 4), FL, me[1]);
    issueW(7);
    if (kd != 0) {
#pragma unroll
      for (int r = 0; r < 4; ++r) {
        float fr = ((r & 1) ? bfhi(wt[r >> 1]) : bflo(wt[r >> 1])) * wscale, fi = ((r & 1) ? bfhi(wt[2 + (r >> 1)]) : bflo(wt[2 + (r >> 1)])) * wscale;
        float zr = re[r], zi = im[r];
        re[r] = zr * fr - zi * fi;
        im[r] = -(zr * fi + zi * fr);
      }
    } else {
      const int ka = 2 * w + (g >> 1), kr = o + 64 * (g & 1);
#pragma unroll
      for (int r = 0; r < 4; ++r) { v2f t = {re[r], im[r]}; *(LAS v2f*)(smem + L_ZX + ka * 1024 + (kr + 16 * r) * 8) = t; }
      BARRIER();
#pragma unroll
      for (int r = 0; r < 4; ++r) {
        PIN(pq[r]);
        const int km = (2048 - (ka + 16 * (kr + 16 * r))) & 2047;
        v2f zm = *(LAS v2f*)(smem + L_ZX + (km & 15) * 1024 + (km >> 4) * 8);
        float zr = re[r], zi = im[r];
        re[r] = (zr * pq[r][0] - zi * pq[r][1] + zm[0] * pq[r][2] + zm[1] * pq[r][3]) * wscale;
        im[r] = -(zr * pq[r][1] + zi * pq[r][0] + zm[0] * pq[r][3] - zm[1] * pq[r][2]) * wscale;
      }
    }
    LAS char* E2 = smem + (kd == 0 ? L_E1 : L_E2);
    a = pack_ri(re, im);
    re = mfma16h(a, __builtin_bit_cast(v8h, f8), z4); im = mfma16h(a, imf(f8), z4);
    twid(re, im, t3c, t3s);
    *(LAS v4h*)(E2 + (o & 7) * E2P + (2 * w + (o >> 3)) * 32 + g * 8) = pack4h(re);
    *(LAS v4h*)(E2 + (o & 7) * E2P + 512 + (2 * w + (o >> 3)) * 32 + g * 8) = pack4h(im);
    BARRIER();
    STAMPT(8);
    {
      LAS char* rp = E2 + w * E2P + o * 32 + g * 8;
      a = __builtin_bit_cast(v8h, cat8(*(LAS v4s*)rp, *(LAS v4s*)(rp + 512)));
    }
    issueW(8);
    re = mfma16h(a, __builtin_bit_cast(v8h, f16), z4); im = mfma16h(a, imf(f16), z4);
    issueW(9);
    twid(re, im, t1c, t1s);
    a = pack_ri(re, im);
    re = mfma16h(__builtin_bit_cast(v8h, f16), a, z4); im = mfma16h(imf(f16), a, z4);
    issueW(10);
    st8c((u32)(2 * kd * 4096 + w * 512 + l * 8), ws + OFF_S2, pack4h(re));
    st8c((u32)((2 * kd + 1) * 4096 + w * 512 + l * 8), ws + OFF_S2, pack4h(im));
  }
  STAMPT(9);
  gen(IC<0>{}); issueW(11);
  WAIT_VM(8);
  BARRIER();
  static_for<6, 9>(readW);
  gen(IC<1>{}); issueW(12); gen(IC<2>{}); issueW(13);
  if (!ffn) {
    WAIT_VM(0);
    BARRIER();
    if (w == 0) {
      if (l == 0) st4c((u32)(FL_S + b * 4), FL, E);
      __builtin_amdgcn_s_sleep(16);
    }
  }
  if (w == 0) ld4c(fp, fhoff, FL);
  gen(IC<3>{}); gen(IC<4>{});
  WAIT_VM(0);
  if (w == 0) {
    PIN(fp);
    u32 f = fp;
    int spins = 0;
    while (!__all(f == E) && ++spins < SPIN_CAP) {
      __builtin_amdgcn_s_sleep(4);
      f = ld4c_sync(fhoff, FL);
    }
  }
  BARRIER();
  STAMPT(10);
  if (ffn && tid == 0) st4c((u32)(FL_S + b * 4), FL, E);
  issueP(0, IC<0>{}); issueP(0, IC<1>{}); issueP(1, IC<0>{}); issueP(1, IC<1>{});
  static_for<9, 14>(readW);
  gen(IC<5>{});
  issueP(2, IC<0>{}); issueP(2, IC<1>{});
  gen(IC<6>{});
  issueP(3, IC<0>{}); issueP(3, IC<1>{});
  gen(IC<7>{});

  const u32 trS = (u32)(kq * 4096 + (4 * h + q) * 128 + ((ph ^ ((q >> 1) & 1)) << 6) + blk * 32 + pp * 8);
  const u32 hb = (u32)(ph * 8192 + kq * 2048 + l * 16);
  v16f accG[2] = {}, accS[2] = {};
  v8s fa[8], fb[8];
  v4u fs;
  float gv[8];
  const u32 xw = (u32)(w * 6144 + l * 16);
  auto xchg_out = [&](const v16f (&ac)[2], int base) {
    LAS char* X = smem + base + xw;
#pragma unroll
    for (int i = 0; i < 6; ++i) {
      const v16f& a = ac[(i + 2) >> 2]; const int r = ((i + 2) & 3) * 4;
      *(LAS v4f*)(X + i * 1024) = v4f{a[r], a[r + 1], a[r + 2], a[r + 3]};
    }
  };
  auto xchg_in = [&](const v16f (&ac)[2], int base, float (&o8)[8]) {
    v4f s0 = {ac[0][0], ac[0][1], ac[0][2], ac[0][3]}, s1 = {ac[0][4], ac[0][5], ac[0][6], ac[0][7]};
#pragma unroll
    for (int x_ = 1; x_ < 4; ++x_) {
      LAS char* X = smem + base + (w ^ (2 * x_)) * 6144 + (x_ - 1) * 2048 + l * 16;
      s0 += *(LAS v4f*)X; s1 += *(LAS v4f*)(X + 1024);
    }
#pragma unroll
    for (int e = 0; e < 4; ++e) { o8[e] = s0[e]; o8[4 + e] = s1[e]; }
  };
  v4f xg[6];
  float ov[8], ex[8];
  auto sadd = [](float a, float b) { float r; asm("v_add_f32 %0, %1, %2" : "=v"(r) : "v"(a), "v"(b)); return r; };
  auto smul = [](float a, float b) { float r; asm("v_mul_f32 %0, %1, %2" : "=v"(r) : "v"(a), "v"(b)); return r; };
  auto silu_step = [&](int i) {
    if (i < 3) {
#pragma unroll
      for (int e = 0; e < 8; ++e) { ov[e] = sadd(i == 0 ? accG[0][e] : ov[e], xg[2 * i + (e >> 2)][e & 3]); PIN(ov[e]); }
    } else if (i == 3) {
#pragma unroll
      for (int e = 0; e < 8; e += 2) { u32 t = pkbf_o(smul(ov[e], rs2), smul(ov[e + 1], rs2)); ov[e] = bflo(t); ov[e + 1] = bfhi(t); PIN(ov[e]); PIN(ov[e + 1]); }
    } else if (i == 4) {
#pragma unroll
      for (int e = 0; e < 8; ++e) { ex[e] = __builtin_amdgcn_exp2f(ov[e] * -1.44269504088896f); PIN(ex[e]); }
    } else if (i == 5) {
#pragma unroll
      for (int e = 0; e < 8; ++e) { ex[e] = __builtin_amdgcn_rcpf(1.0f + ex[e]); PIN(ex[e]); }
    } else if (i == 6) {
#pragma unroll
      for (int e = 0; e < 8; e += 2) { u32 t = pkbf_o(ov[e] * ex[e], ov[e + 1] * ex[e + 1]); gv[e] = bflo(t); gv[e + 1] = bfhi(t); PIN(gv[e]); PIN(gv[e + 1]); }
    }
  };
  static_for<0, 10>([&](auto V) {
    constexpr int v = V;
    v8s(&cur)[8] = (v & 1) ? fb : fa;
    v8s(&prv)[8] = (v & 1) ? fa : fb;
    auto mmp = [&](int s) {
      if constexpr (v >= 8) {
        accS[0] = mfma32h(G[(v - 8) * 4 + s], prv[2 * s], accS[0]); accS[1] = mfma32h(G[(v - 8) * 4 + s], prv[2 * s + 1], accS[1]);
        SCHED();
        if ((v - 8) * 4 + s >= 1) silu_step((v - 8) * 4 + s - 1);
      } else if constexpr (v >= 1) {
        accG[0] = mfma32b(Wf[2 * (v - 1) + (s >> 1)][s & 1], prv[2 * s], accG[0]);
        accG[1] = mfma32b(Wf[2 * (v - 1) + (s >> 1)][s & 1], prv[2 * s + 1], accG[1]);
      }
      SCHED();
    };
    if constexpr (v < 9) {
      WAIT_VM(v <= 7 ? 4 : 0);
      if constexpr (v == 5) { if (w == 0) { PIN(fs); poll4(fs, (u32)(FL_S + l * 16)); } }
      BARRIER();
      if constexpr (v == 0 || v == 8) STAMPT(11 + v);
      if constexpr (v == 4) { if (w == 0) ld16c(fs, (u32)(FL_S + l * 16), FL); }
      if constexpr (v == 8) {
#pragma unroll
        for (int x_ = 1; x_ < 4; ++x_) {
          LAS char* X = smem + 49152 + (w ^ (2 * x_)) * 6144 + (x_ - 1) * 2048 + l * 16;
          xg[2 * x_ - 2] = *(LAS v4f*)X; xg[2 * x_ - 1] = *(LAS v4f*)(X + 1024);
        }
        SCHED();
      }
      mmp(0);
#pragma unroll
      for (int s = 0; s < 4; ++s) {
        if constexpr (v >= 7) {
          LAS char* sb = smem + aslot(2 * v + (s >> 1)) + (s & 1) * 2048;
          cur[2 * s] = cat8(trread(sb + trS), trread(sb + trS + 1024));
          cur[2 * s + 1] = cat8(trread(sb + (trS ^ 64)), trread(sb + (trS ^ 64) + 1024));
        } else {
          LAS char* sb = smem + aslot(2 * v + (s >> 1)) + (s & 1) * 1024;
          cur[2 * s] = *(LAS v8s*)(sb + hb);
          cur[2 * s + 1] = *(LAS v8s*)(sb + (hb ^ 8192));
        }
      }
      SCHED();
      if constexpr (v <= 6) issueP(2 * v + 4, IC<0>{});
      mmp(1);
      if constexpr (v <= 6) issueP(2 * v + 4, IC<1>{});
      mmp(2);
      if constexpr (v <= 6) issueP(2 * v + 5, IC<0>{});
      mmp(3);
      if constexpr (v <= 6) issueP(2 * v + 5, IC<1>{});
    } else {
      mmp(0); mmp(1); mmp(2); mmp(3);
    }
    if constexpr (v == 7) xchg_out(accG, 49152);
    SCHED();
  });
  xchg_out(accS, 0);
  BARRIER();
  {
    float o8[8], pv[8];
    xchg_in(accS, 0, o8);
#pragma unroll
    for (int e = 0; e < 8; e += 2) { u32 t = pkbf_o(o8[e] * oscale, o8[e + 1] * oscale); pv[e] = bflo(t) * gv[e]; pv[e + 1] = bfhi(t) * gv[e + 1]; }
    u32 a0 = pkbf(pv[0], pv[1]), a1 = pkbf(pv[2], pv[3]), b0 = pkbf(pv[4], pv[5]), b1 = pkbf(pv[6], pv[7]);
    auto r0 = __builtin_amdgcn_permlane32_swap(a0, b0, false, false);
    auto r1 = __builtin_amdgcn_permlane32_swap(a1, b1, false, false);
    v4u pk = {r0[0], r1[0], r0[1], r1[1]};
    v4u ov4;
#pragma unroll
    for (int e = 0; e < 4; ++e) ov4[e] = pkbf(bflo(xe[e]) + bflo(pk[e]), bfhi(xe[e]) + bfhi(pk[e]));
    *(v4u*)((char*)out + eoff) = ov4;
  }
  asm volatile("km_pf_b:");
  STAMPT(20);
  WAIT_VM(0);
  STAMPT(21);
  STAMP_OUT(ws);
}

extern "C" void kernel_launch(void* const* d_in, const int* in_sizes, int n_in, void* d_out, int out_size, void* d_ws,
                              size_t ws_size, hipStream_t stream) {
  kmain<<<256, 512, 0, stream>>>((const u16*)d_in[0], (const u16*)d_in[1], (const u16*)d_in[2], (const u16*)d_in[4],
                                 (const u16*)d_in[5], (const u16*)d_in[6], (char*)d_ws + WS_BASE(ws_size), (u16*)d_out);
}
```

```cpp
#include <hip/hip_runtime.h>
#include <stdint.h>

typedef unsigned short u16;
typedef unsigned int u32;
typedef unsigned long long u64;
typedef short v4s __attribute__((ext_vector_type(4)));
typedef short v8s __attribute__((ext_vector_type(8)));
typedef _Float16 v4h __attribute__((ext_vector_type(4)));
typedef _Float16 v8h __attribute__((ext_vector_type(8)));
typedef __bf16 v2b __attribute__((ext_vector_type(2)));
typedef __bf16 v8b __attribute__((ext_vector_type(8)));
typedef float v2f __attribute__((ext_vector_type(2)));
typedef float v4f __attribute__((ext_vector_type(4)));
typedef float v16f __attribute__((ext_vector_type(16)));
typedef u32 v2u __attribute__((ext_vector_type(2)));
typedef u32 v4u __attribute__((ext_vector_type(4)));

#define LAS __attribute__((address_space(3)))
#define GAS __attribute__((address_space(1)))
#define DEVI __device__ __forceinline__

constexpr int D_ = 512, F_ = 1792, DF_ = 257;
constexpr size_t OFF_H = (size_t)24 << 20;
constexpr size_t OFF_S1 = (size_t)32 << 20;
constexpr size_t OFF_WT = (size_t)34 << 20;
constexpr size_t OFF_S2 = (size_t)37 << 20;
constexpr size_t OFF_M = (size_t)39 << 20;
constexpr size_t OFF_FL = OFF_M;
constexpr u32 FL_S = 0, FL_A = 1024, FL_H = 2048;
constexpr size_t OFF_BC = OFF_M + 49152;
constexpr size_t OFF_MB = OFF_M + 98304;
constexpr size_t OFF_RS2 = OFF_M + 106496;
constexpr size_t OFF_PQ = OFF_M + 65536;
constexpr size_t OFF_CSP = OFF_M + 131072;
constexpr int SPIN_CAP = 1 << 16;
#define SLEEP_A 16

#define WS_BASE(n) ((((n) - ((size_t)40 << 20))) & ~(size_t)0x1fffff)
#define STAMP_DECL
#define STAMPT(id)
#define STAMPC(id)
#define STAMP_OUT(wsp)

template <int I> struct IC { static constexpr int v = I; constexpr operator int() const { return I; } };
template <int B, int E, class Fn> DEVI void static_for(Fn&& f) {
  if constexpr (B < E) { f(IC<B>{}); static_for<B + 1, E>(f); }
}
#define WAIT_VM(n) asm volatile("s_waitcnt vmcnt(%0)" ::"n"(n) : "memory")
#define BARRIER() asm volatile("s_waitcnt lgkmcnt(0)\n\ts_barrier" ::: "memory")
#define SCHED() __builtin_amdgcn_sched_barrier(0)
#define PIN(x) asm volatile("" : "+v"(x))

DEVI int s_of_pi(int pi) {
  int RG = pi >> 6, i = pi & 63;
  return 128 * (4 * (RG & 3) + (i & 3)) + 8 * (i >> 2) + (RG >> 2);
}
DEVI void dma2(u32 voff0, u32 voff1, const char* sbase, u32 lds) {
  asm volatile("s_mov_b32 m0, %3\n\ts_nop 0\n\tglobal_load_lds_dwordx4 %0, %2\n\tglobal_load_lds_dwordx4 %1, %2 offset:1024" ::"v"(voff0), "v"(voff1), "s"(sbase), "s"(lds) : "memory");
}
DEVI void dma2c(u32 voff0, u32 voff1, const char* sbase, u32 lds) {
  asm volatile("s_mov_b32 m0, %3\n\ts_nop 0\n\tglobal_load_lds_dwordx4 %0, %2 sc1\n\tglobal_load_lds_dwordx4 %1, %2 offset:1024 sc1" ::"v"(voff0), "v"(voff1), "s"(sbase), "s"(lds) : "memory");
}
template <int OFF> DEVI void dma1(u32 voff, const char* sbase, u32 lds) {
  asm volatile("s_mov_b32 m0, %2\n\ts_nop 0\n\tglobal_load_lds_dwordx4 %0, %1 offset:%3" ::"v"(voff), "s"(sbase), "s"(lds), "n"(OFF) : "memory");
}
template <int OFF> DEVI void dma1s(u32 voff, const char* sbase, u32 lds) {
  asm volatile("s_mov_b32 m0, %2\n\ts_nop 0\n\tglobal_load_lds_dwordx4 %0, %1 offset:%3 sc1" ::"v"(voff), "s"(sbase), "s"(lds), "n"(OFF) : "memory");
}
DEVI void dma1c(u32 voff, const char* sbase, u32 lds) {
  asm volatile("s_mov_b32 m0, %2\n\ts_nop 0\n\tglobal_load_lds_dword %0, %1 sc1" ::"v"(voff), "s"(sbase), "s"(lds) : "memory");
}
DEVI v2u ld8c_sync(u32 voff, const char* sb) { v2u d; asm volatile("global_load_dwordx2 %0, %1, %2 sc1\n\ts_waitcnt vmcnt(0)" : "=&v"(d) : "v"(voff), "s"(sb) : "memory"); return d; }
DEVI u32 ld4c_sync(u32 voff, const char* sb) { u32 d; asm volatile("global_load_dword %0, %1, %2 sc1\n\ts_waitcnt vmcnt(0)" : "=&v"(d) : "v"(voff), "s"(sb) : "memory"); return d; }
DEVI v4u ld16c_sync(u32 voff, const char* sb) { v4u d; asm volatile("global_load_dwordx4 %0, %1, %2 sc1\n\ts_waitcnt vmcnt(0)" : "=&v"(d) : "v"(voff), "s"(sb) : "memory"); return d; }
template <class T> DEVI void ld16c(T& d, u32 voff, const char* sb) { asm volatile("global_load_dwordx4 %0, %1, %2 sc1" : "=&v"(d) : "v"(voff), "s"(sb) : "memory"); }
template <class T> DEVI void ld8c(T& d, u32 voff, const char* sb) { asm volatile("global_load_dwordx2 %0, %1, %2 sc1" : "=&v"(d) : "v"(voff), "s"(sb) : "memory"); }
template <class T> DEVI void ld4c(T& d, u32 voff, const char* sb) { asm volatile("global_load_dword %0, %1, %2 sc1" : "=&v"(d) : "v"(voff), "s"(sb) : "memory"); }
template <class T> DEVI void ld16(T& d, u32 voff, const char* sb) { asm volatile("global_load_dwordx4 %0, %1, %2" : "=&v"(d) : "v"(voff), "s"(sb) : "memory"); }
DEVI void ld2(u32& d, u32 voff, const char* sb) { asm volatile("global_load_ushort %0, %1, %2" : "=&v"(d) : "v"(voff), "s"(sb) : "memory"); }
template <class T> DEVI void st16c(u32 voff, const char* sb, T v) { asm volatile("global_store_dwordx4 %0, %1, %2 sc1\n\ts_nop 1" ::"v"(voff), "v"(v), "s"(sb) : "memory"); }
template <class T> DEVI void st8c(u32 voff, const char* sb, T v) { asm volatile("global_store_dwordx2 %0, %1, %2 sc1" ::"v"(voff), "v"(v), "s"(sb) : "memory"); }
template <class T> DEVI void st4c(u32 voff, const char* sb, T v) { asm volatile("global_store_dword %0, %1, %2 sc1" ::"v"(voff), "v"(v), "s"(sb) : "memory"); }

DEVI v4s trread(LAS char* p) { return __builtin_amdgcn_ds_read_tr16_b64_v4i16((LAS v4s*)p); }
DEVI v8s cat8(v4s a, v4s b) { return __builtin_shufflevector(a, b, 0, 1, 2, 3, 4, 5, 6, 7); }
DEVI float bflo(u32 v) { return __builtin_bit_cast(float, v << 16); }
DEVI float bfhi(u32 v) { return __builtin_bit_cast(float, v & 0xffff0000u); }
DEVI float relu(float v) { return __builtin_bit_cast(float, max(__builtin_bit_cast(int, v), 0)); }
DEVI float rbf(float v) { return (float)(__bf16)v; }
DEVI u32 pkbf(float a, float b) { v2b p = {(__bf16)a, (__bf16)b}; return __builtin_bit_cast(u32, p); }
DEVI u32 pkbf_o(float a, float b) { u32 r = pkbf(a, b); asm("" : "+v"(r)); return r; }
DEVI float cosr(float rev) { return __builtin_amdgcn_cosf(rev); }
DEVI float sinr(float rev) { return __builtin_amdgcn_sinf(rev); }
DEVI v4f mfma16h(v8h a, v8h b, v4f c) { return __builtin_amdgcn_mfma_f32_16x16x32_f16(a, b, c, 0, 0, 0); }
DEVI v4f mfma16b(v8s a, v4u b, v4f c) { return __builtin_amdgcn_mfma_f32_16x16x32_bf16(__builtin_bit_cast(v8b, a), __builtin_bit_cast(v8b, b), c, 0, 0, 0); }
DEVI v16f mfma32b(v8s a, v8s b, v16f c) { return __builtin_amdgcn_mfma_f32_32x32x16_bf16(__builtin_bit_cast(v8b, a), __builtin_bit_cast(v8b, b), c, 0, 0, 0); }
DEVI v16f mfma32h(v8h a, v8s b, v16f c) { return __builtin_amdgcn_mfma_f32_32x32x16_f16(a, __builtin_bit_cast(v8h, b), c, 0, 0, 0); }
DEVI v8h pack_ri(v4f re, v4f im) {
  v8h r;
  r[0] = (_Float16)re[0]; r[1] = (_Float16)re[1]; r[2] = (_Float16)re[2]; r[3] = (_Float16)re[3];
  r[4] = (_Float16)im[0]; r[5] = (_Float16)im[1]; r[6] = (_Float16)im[2]; r[7] = (_Float16)im[3];
  return r;
}
DEVI v4h pack4h(v4f a) { v4h r; r[0] = (_Float16)a[0]; r[1] = (_Float16)a[1]; r[2] = (_Float16)a[2]; r[3] = (_Float16)a[3]; return r; }
DEVI void twid(v4f& re, v4f& im, const v4f& c, const v4f& s) {
#pragma unroll
  for (int r = 0; r < 4; ++r) { float a = re[r], b = im[r]; re[r] = a * c[r] + b * s[r]; im[r] = b * c[r] - a * s[r]; }
}
template <int CTRL> DEVI float dppadd(float v) {
  return v + __builtin_bit_cast(float, __builtin_amdgcn_update_dpp(0, __builtin_bit_cast(int, v), CTRL, 0xf, 0xf, true));
}
DEVI float wave_sum(float v) {
  v = dppadd<0xB1>(v); v = dppadd<0x4E>(v); v = dppadd<0x141>(v); v = dppadd<0x140>(v);
  int bb = __builtin_bit_cast(int, v);
  float a0 = __builtin_bit_cast(float, __builtin_amdgcn_readlane(bb, 0)), a1 = __builtin_bit_cast(float, __builtin_amdgcn_readlane(bb, 16));
  float a2 = __builtin_bit_cast(float, __builtin_amdgcn_readlane(bb, 32)), a3 = __builtin_bit_cast(float, __builtin_amdgcn_readlane(bb, 48));
  return (a0 + a1) + (a2 + a3);
}
DEVI float dot2ones(u32 v, float acc) {
  const v2b ones = {(__bf16)1.0f, (__bf16)1.0f};
  return __builtin_amdgcn_fdot2_f32_bf16(__builtin_bit_cast(v2b, v), ones, acc, false);
}
template <int CTRL> DEVI u32 dppmax(u32 v) { return max(v, (u32)__builtin_amdgcn_update_dpp(0, (int)v, CTRL, 0xf, 0xf, true)); }
DEVI u32 wave_max(u32 v) {
  v = dppmax<0xB1>(v); v = dppmax<0x4E>(v); v = dppmax<0x141>(v); v = dppmax<0x140>(v);
  u32 a0 = __builtin_amdgcn_readlane(v, 0), a1 = __builtin_amdgcn_readlane(v, 16), a2 = __builtin_amdgcn_readlane(v, 32), a3 = __builtin_amdgcn_readlane(v, 48);
  return max(max(a0, a1), max(a2, a3));
}

struct Frag1 { v8s x0, x1, w1, w2; };

constexpr int L_SCR = 147456;
constexpr int L_CS = L_SCR, L_RED = L_SCR + 1024, L_MX = L_SCR + 1088, L_BC0 = L_SCR + 2048, L_BC1 = L_SCR + 2304;
constexpr int L_TAB = L_SCR + 3072;
constexpr int L_T = L_SCR + 7168, L_FA = L_SCR, L_E1 = L_T, L_E2 = 81920, L_ZX = 81920;
constexpr int L_C1 = 98304, L_IMG = L_C1 + 9216;
constexpr int L_FR = 131072;
constexpr int E1P = 544, E2P = 1024 + 72;
DEVI constexpr int wslot(int n) {
  constexpr int u[14] = {6, 7, 8, 0, 1, 2, 3, 4, 6, 7, 8, 0, 1, 2};
  return u[n] * 16384;
}
DEVI constexpr int aslot(int n) {
  constexpr int u[18] = {3, 4, 5, 9, 2, 6, 7, 8, 0, 1, 3, 4, 5, 9, 2, 6, 7, 8};
  return u[n] * 16384;
}

__global__ __launch_bounds__(512, 2) void kmain(const u16* __restrict__ x, const u16* __restrict__ wr, const u16* __restrict__ wi,
                                                const u16* __restrict__ Wg, const u16* __restrict__ Wu, const u16* __restrict__ Wd,
                                                char* __restrict__ ws, u16* __restrict__ out) {
  __shared__ __attribute__((aligned(16))) char smem_[163840];
  LAS char* smem = (LAS char*)smem_;
  const u32 lbase = (u32)(size_t)smem;
  const int tid = threadIdx.x, l = tid & 63;
  const int w = __builtin_amdgcn_readfirstlane(tid >> 6);
  const int b = blockIdx.x, xcd = b & 7, idx = b >> 3;
  const bool ffn = idx < 28;
  const int g = l >> 4, o = l & 15;
  const int h = l >> 5, blk = (l >> 4) & 1, q = (l & 15) >> 2, pp = l & 3;
  const char* FL = ws + OFF_FL;
  const char* BC = ws + OFF_BC;
  STAMP_DECL; STAMPT(0);
  asm volatile("" ::"s"(x), "s"(wr), "s"(wi), "s"(Wg), "s"(Wu), "s"(Wd), "s"(ws), "s"(out));

  const int pi = b * 8 + w;
  v4u xr;
  ld16(xr, (u32)(s_of_pi(pi) * 1024 + l * 16), (const char*)x);
  if (!ffn) {
    u64 pc; u32 len;
    asm volatile("s_getpc_b64 %0\nkm_pf_a:\n\ts_mov_b32 %1, km_pf_b-km_pf_a" : "=s"(pc), "=s"(len));
    dma1<0>(min((u32)(((idx & 3) * 8 + w) * 1024 + l * 16), len) & ~15u, (const char*)(pc & ~15ull), lbase + L_FA);
  }
  if (w == 0) dma1c((u32)((l & 1) * 4), BC, lbase + L_BC0);
  const int fkb = (b & 7) * 2 + (w >> 2), fcq = w & 3;
  {
    const u32 ks = (u32)((idx >> 1) + 16 * fkb + 256 * (4 * (idx & 1) + ((l >> 3) & 3)));
    const u32 base = ((ks * (DF_ * 2)) & ~15u) + fcq * 128;
    const u32 dlt = (u32)((const char*)wi - (const char*)wr);
    (void)dlt;
    const u32 c0 = ((l & 7) ^ (((l >> 4) & 1) << 2)) << 4;
    if (l < 32) dma1<0>(base + c0, (const char*)wr, lbase + L_FR + w * 2048);
    else dma1<0>(base + c0, (const char*)wi, lbase + L_FR + w * 2048);
    if (fcq == 3) {
      const u32 t0 = min(base + 128 + (c0 & 16), (u32)(2048 * DF_ * 2 - 16));
      if (l < 32) dma1<0>(t0, (const char*)wr, lbase + L_FR + w * 2048 + 1024);
      else dma1<0>(t0, (const char*)wi, lbase + L_FR + w * 2048 + 1024);
    }
  }
  const int p = (xcd >> 1) * 4 + idx / 7, j = (xcd & 1) * 7 + idx % 7;
  const int sh = w >> 2, fq = w & 3;
  u32 aoff[2];
#pragma unroll
  for (int i = 0; i < 2; ++i) {
    int row = (2 * w + i) * 8 + (l >> 3);
    aoff[i] = s_of_pi((p & 15) * 128 + row) * 1024 + (((l & 7) ^ ((row >> 1) & 7)) << 4) - i * 1024;
  }
  const u32 boff = (u32)(2 * w * 4 + (l >> 4)) * (F_ * 2) + j * 256 + (((l & 15) ^ ((l >> 4) << 2)) << 4);
  const u32 boff1 = boff + 4 * F_ * 2 - 1024;
  const int rot = p >> 1;
  auto issue = [&](int t, int slot) {
    const int kt = (t + rot) & 7;
    u32 dst = lbase + slot * 49152 + w * 2048;
    dma2(aoff[0], aoff[1], (const char*)x + kt * 128, dst);
    dma2(boff, boff1, (const char*)Wg + (size_t)kt * (64 * F_ * 2), dst + 16384);
    dma2(boff, boff1, (const char*)Wu + (size_t)kt * (64 * F_ * 2), dst + 32768);
  };
  const int sp = xcd * 4 + (idx - 28);
  if (ffn) issue(0, 0);

  u32 E = 0, tick = 0;
  const bool coord = (b == 224) && (w == 0);
  auto do_tick = [&]() { if (coord) { ++tick; if (l == 0) st8c(0u, BC, v2u{E, tick}); } };
  auto get_epoch = [&]() {
    const v2u b0 = *(LAS v2u*)(smem + L_BC0);
    v2u b1 = *(LAS v2u*)(smem + L_BC1);
    int spins = 0;
    while (__builtin_amdgcn_readfirstlane((b0[0] ^ b1[0]) | (b0[1] ^ b1[1])) == 0 && ++spins < SPIN_CAP) {
      __builtin_amdgcn_s_sleep(4);
      b1 = ld8c_sync(0u, BC);
    }
    E = __builtin_amdgcn_readfirstlane(b1[0]);
  };
  auto signal_a = [&]() {
    v4u r0 = *(LAS v4u*)(smem + L_RED), r1 = *(LAS v4u*)(smem + L_RED + 16);
    u32 m = max(max(max(r0[0], r0[1]), max(r0[2], r0[3])), max(max(r1[0], r1[1]), max(r1[2], r1[3])));
    if (l == 0) { st4c((u32)(FL_A + b * 4), FL, E | (m >> 7)); *(LAS u32*)(smem + L_MX + 4) = E; }
  };
  auto poll4 = [&](v4u f, u32 voff) -> v4u {
    int spins = 0;
    while (!__all((((f[0] ^ E) | (f[1] ^ E) | (f[2] ^ E) | (f[3] ^ E)) >> 8) == 0) && ++spins < SPIN_CAP) {
      do_tick();
      __builtin_amdgcn_s_sleep(12);
      f = ld16c_sync(voff, FL);
    }
    return f;
  };
  auto check_a = [&](v4u f) {
    f = poll4(f, (u32)(FL_A + l * 16));
    u32 m = wave_max(max(max(f[0], f[1]), max(f[2], f[3]))) & 0xffu;
    if (l == 0) *(LAS u32*)(smem + L_MX) = m;
  };
  if (coord) {
#pragma unroll
    for (int i = 0; i < 5; ++i) dma2c(i ? (u32)(FL_H + ((i - 1) * 128 + l) * 64) : (u32)(l * 16), i ? (u32)(FL_H + ((i - 1) * 128 + 64 + l) * 64 - 1024) : (u32)(l * 16), FL, lbase + i * 2048);
  }
  u32 sq[8];
  if (!ffn && w == 7) {
    const int kss = (sp >> 1) + 16 * (l >> 2) + 256 * (4 * (sp & 1) + (l & 3)), km = (2048 - kss) & 2047;
    const u32 o1 = (u32)(kss * DF_ * 2), o2 = (u32)(km * DF_ * 2);
    ld2(sq[0], o1, (const char*)wr); ld2(sq[1], o1, (const char*)wi); ld2(sq[2], o2, (const char*)wr); ld2(sq[3], o2, (const char*)wi);
    ld2(sq[4], o1 + 512, (const char*)wr); ld2(sq[5], o1 + 512, (const char*)wi); ld2(sq[6], o2 + 512, (const char*)wr); ld2(sq[7], o2 + 512, (const char*)wi);
  }

  {
    const float fo = (float)o, fg = (float)(4 * g);
    if (w < 2) {
      const float st = fo * (1.0f / 32.0f), a0 = fmaf(fg, st, w ? 0.25f : 0.f);
      v4u hi, lo;
#pragma unroll
      for (int e2 = 0; e2 < 4; ++e2) {
        float c0 = cosr(fmaf((float)(16 * (e2 >> 1) + 2 * (e2 & 1)), st, a0)), c1 = cosr(fmaf((float)(16 * (e2 >> 1) + 2 * (e2 & 1) + 1), st, a0));
        u32 ch = pkbf_o(c0, c1);
        hi[e2] = ch; lo[e2] = pkbf(c0 - bflo(ch), c1 - bfhi(ch));
      }
      *(LAS v4u*)(smem + L_C1 + w * 2048 + l * 16) = hi; *(LAS v4u*)(smem + L_C1 + w * 2048 + 1024 + l * 16) = lo;
    } else if (w < 5) {
      const float st = w == 2 ? (float)(o & 7) * (1.0f / 16.0f) : fo * (w == 3 ? 1.0f / 16.0f : 1.0f / 8.0f);
      const float a0 = (w == 2 && o >= 8) ? 0.25f : 0.f;
      const float sc = w == 2 ? 0.0625f : ((w == 4 && (g >> 1) != (o >> 3)) ? 0.f : 1.0f);
      v8h m;
#pragma unroll
      for (int e = 0; e < 4; ++e) { float a = fmaf(fg + (float)e, st, a0); m[e] = (_Float16)(sc * cosr(a)); m[4 + e] = (_Float16)(sc * sinr(a)); }
      *(LAS v8h*)(smem + (w == 2 ? L_C1 + 4096 : L_TAB + (w - 3) * 1024) + l * 16) = m;
    } else {
      const float st = w == 7 ? fo * (1.0f / 256.0f) : (fo + (w == 6 ? 16.f : 0.f)) * (1.0f / 512.0f);
      v4f c, sn;
#pragma unroll
      for (int e = 0; e < 4; ++e) { float a = (fg + (float)e) * st; c[e] = cosr(a); sn[e] = sinr(a); }
      LAS char* dst = smem + (w == 7 ? L_TAB + 2048 : L_C1 + 5120 + (w - 5) * 2048) + l * 16;
      *(LAS v4f*)dst = c; *(LAS v4f*)(dst + 1024) = sn;
    }
    BARRIER();
    LAS char* C1 = smem + L_C1 + l * 16;
    const v4u Fhr = *(LAS v4u*)C1, Flr = *(LAS v4u*)(C1 + 1024), Fhi = *(LAS v4u*)(C1 + 2048), Fli = *(LAS v4u*)(C1 + 3072);
    const v8h M2 = *(LAS v8h*)(C1 + 4096);
    v4f twc[2], tws[2];
#pragma unroll
    for (int u = 0; u < 2; ++u) { twc[u] = *(LAS v4f*)(C1 + 5120 + u * 2048); tws[u] = *(LAS v4f*)(C1 + 6144 + u * 2048); }
    if (ffn) WAIT_VM(8); else if (w == 7) WAIT_VM(11); else WAIT_VM(3);
    if (coord) {
      WAIT_VM(0);
      v4u f[10];
#pragma unroll
      for (int i = 0; i < 10; ++i) f[i] = *(LAS v4u*)(smem + i * 1024 + l * 16);
      u32 c = __builtin_amdgcn_readfirstlane(*(LAS u32*)(smem + L_BC0)) >> 8;
      bool hit;
      do {
        c = (c + 1) & 0xffffffu; hit = false;
#pragma unroll
        for (int i = 0; i < 10; ++i) hit |= ((f[i][0] >> 8) == c) | ((f[i][1] >> 8) == c) | ((f[i][2] >> 8) == c) | ((f[i][3] >> 8) == c);
      } while (__any(hit));
      E = c << 8;
      do_tick();
    }
    PIN(xr);
    STAMPT(3);
    const float c0 = bflo(__builtin_amdgcn_readfirstlane(xr[0]));
    v2f xv[4], s2 = {0.f, 0.f}, q2 = {0.f, 0.f};
#pragma unroll
    for (int e = 0; e < 4; ++e) {
      xv[e] = v2f{bflo(xr[e]), bfhi(xr[e])};
      v2f t = xv[e] - v2f{c0, c0};
      s2 += t; q2 = __builtin_elementwise_fma(t, t, q2);
    }
    const float dmean = wave_sum(s2[0] + s2[1]) * (1.0f / 512.0f), msq = wave_sum(q2[0] + q2[1]) * (1.0f / 512.0f);
    do_tick();
    const float mb = rbf(c0 + dmean);
    const float var = rbf(fmaxf(msq - dmean * dmean, 0.f));
    const float sd = rbf(__builtin_amdgcn_sqrtf(rbf(var + 1e-5f)));
    const float rs = __builtin_amdgcn_rcpf(sd);
#pragma unroll
    for (int e = 0; e < 4; ++e) {
      v2f t = xv[e] - v2f{mb, mb};
      u32 tb = pkbf_o(t[0], t[1]);
      t = v2f{bflo(tb), bfhi(tb)} * v2f{rs, rs};
      xr[e] = pkbf(t[0], t[1]);
    }
    *(LAS v4u*)(smem + L_IMG + w * 1024 + l * 16) = xr;
    if (l < 2) st4c((u32)(pi * 4 + l * 8192), ws + OFF_MB, l ? rs * rs : -mb);
    const v8s xf = cat8(trread(smem + L_IMG + w * 1024 + l * 8), trread(smem + L_IMG + w * 1024 + 512 + l * 8));
    const int kt1 = (1 + rot) & 7;
    const u32 dst1 = lbase + 49152 + w * 2048;
    if (ffn) dma2(aoff[0], aoff[1], (const char*)x + kt1 * 128, dst1);
    const v8s xg_ = __builtin_bit_cast(v8s, __builtin_bit_cast(v4u, xf) ^ v4u{0x80000000u, 0x80000000u, 0x80000000u, 0x80000000u});
    const v4f z = {0.f, 0.f, 0.f, 0.f};
    v4f dre[2], dim[2];
#pragma unroll
    for (int u = 0; u < 2; ++u) { dre[u] = mfma16b(u ? xg_ : xf, Flr, z); dim[u] = mfma16b(u ? xg_ : xf, Fli, z); }
#pragma unroll
    for (int u = 0; u < 2; ++u) { dre[u] = mfma16b(u ? xg_ : xf, Fhr, dre[u]); dim[u] = mfma16b(u ? xg_ : xf, Fhi, dim[u]); }
#pragma unroll
    for (int u = 0; u < 2; ++u) {
      twid(dre[u], dim[u], twc[u], tws[u]);
      const v8h bb = pack_ri(dre[u], dim[u]);
      v4f d2 = mfma16h(bb, M2, z);
      if (u == 0) {
        const u32 nv = o == 8 ? 0xac002c00u : 0u;
        v4f n = mfma16h(bb, __builtin_bit_cast(v8h, v4u{nv, nv, 0u, 0u}), z);
        if (l == 8) d2[0] = n[0];
      }
      *(LAS v4h*)(smem + L_T + ((o >> 3) * 8 + w) * 544 + (o & 7) * 64 + u * 32 + g * 8) = pack4h(d2);
    }
    if (ffn) {
      dma2(boff, boff1, (const char*)Wg + (size_t)kt1 * (64 * F_ * 2), dst1 + 16384);
      dma2(boff, boff1, (const char*)Wu + (size_t)kt1 * (64 * F_ * 2), dst1 + 32768);
    }
    STAMPT(4);
  }
  v8s su; v4u fu, tu;
  auto round1_read = [&]() {
    LAS char* rp = smem + L_T + (4 * (g & 1) + (o >> 2)) * 544 + (w * 32 + (g >> 1) * 16) * 2 + (o & 3) * 8;
    su = cat8(trread(rp), trread(rp + 8 * 544));
    LAS char* fp_ = smem + L_FR + w * 2048 + q * 128 + (((2 * g + (pp >> 1)) ^ ((q >> 1) << 2)) << 4) + (pp & 1) * 8;
    fu = __builtin_bit_cast(v4u, cat8(trread(fp_), trread(fp_ + 512)));
    LAS char* tp = smem + L_FR + w * 2048 + 1024 + q * 128 + (pp >> 1) * 16 + (pp & 1) * 8;
    tu = __builtin_bit_cast(v4u, cat8(trread(tp), trread(tp + 512)));
  };
  auto round1_max = [&]() {
    typedef unsigned short v2us __attribute__((ext_vector_type(2)));
    const int dl = (idx >> 1) & 7;
    const u32 msk = (fcq == 0 && l < dl) ? 0u : 0x7fff7fffu, msk2 = (fcq == 3 && o <= dl) ? 0x7fff7fffu : 0u;
    v2us m2 = {0, 0};
#pragma unroll
    for (int e = 0; e < 4; ++e) {
      u32 t = fu[e] & msk, t2 = tu[e] & msk2;
      m2 = __builtin_elementwise_max(m2, __builtin_elementwise_max(__builtin_bit_cast(v2us, t), __builtin_bit_cast(v2us, t2)));
    }
    const u32 mx = wave_max(max((u32)m2[0], (u32)m2[1]));
    if (l == 0) *(LAS u32*)(smem + L_RED + w * 4) = mx;
  };
  auto round1_store = [&]() {
    st16c((u32)(b * 8192 + (w * 32 + (g >> 1) * 16 + o) * 32 + (g & 1) * 16), ws + OFF_S1, su);
    st16c((u32)((idx * 16 + fkb) * 4608 + (fcq * 64 + l) * 16), ws + OFF_WT, fu);
    if (fcq == 3 && l < 16) st16c((u32)((idx * 16 + fkb) * 4608 + (256 + l) * 16), ws + OFF_WT, tu);
  };

  const int RG = xcd * 4 + (idx >> 3), ct = idx & 7;
  const int kd = xcd * 32 + ((idx + 3) & 31);
  const u32 swz = ((l & 7) ^ (((l >> 4) & 1) << 2)) << 4;
  const u32 ro0 = (u32)((l >> 3) * 1024) + swz, ro1 = ro0 + 8192 - 1024;
  const char* bsrc = (const char*)Wd + (size_t)(w * 16) * 1024 + ct * 128;
  auto issueW = [&](int n) { dma2(ro0, ro1, bsrc + (size_t)n * 131072, lbase + wslot(n) + w * 2048); };
  const int dh = w & 1, kq = w >> 1, ph = kq >> 1, kh = kq & 1;
  const int epi = RG * 64 + ph * 32 + (l & 31);
  const u32 eoff = (u32)(s_of_pi(epi) * D_ + ct * 64 + dh * 32 + kh * 16 + h * 8) * 2;
  v4u wt, s1v, xe; v4f pq[4]; float rs2;
  auto load_xe = [&]() { ld16(xe, eoff, (const char*)x); };
  auto load_s1 = [&]() { ld16c(s1v, (u32)((((4 * w + g) * 8 + (o >> 1)) * 256 + kd) * 32 + (o & 1) * 16), ws + OFF_S1); };
  auto load_wt = [&]() { ld16c(wt, (u32)((w * 64 + l) * 4608 + (kd + ((2 * w + (g >> 1)) & 7)) * 16), ws + OFF_WT); };
  auto load_rs = [&]() { ld4c(rs2, (u32)(epi * 4), ws + OFF_RS2); };
  v4f t2c, t2s, t3c, t3s;
  auto trig2 = [&]() {
    const float fw = (float)w, st2 = fw * (1.0f / 128.0f), a2 = fw * (float)(o + 64 * g) * (1.0f / 2048.0f);
#pragma unroll
    for (int e = 0; e < 4; ++e) { float b2 = fmaf((float)e, st2, a2); t2c[e] = cosr(b2); t2s[e] = sinr(b2); }
    PIN(t2c); PIN(t2s);
  };
  auto trig3 = [&]() {
    const float f7 = (float)(o & 7), st3 = f7 * (1.0f / 128.0f), a3 = f7 * (float)(2 * w + (o >> 3) + 64 * g) * (1.0f / 2048.0f);
#pragma unroll
    for (int e = 0; e < 4; ++e) { float b3 = fmaf((float)e, st3, a3); t3c[e] = cosr(b3); t3s[e] = sinr(b3); }
    PIN(t3c); PIN(t3s);
  };
  v4u f16; v4f t1c, t1s, re, im;
  const v4f z4 = {0.f, 0.f, 0.f, 0.f};
  auto imf = [](v4u f) { return __builtin_bit_cast(v8h, v4u{f[2] ^ 0x80008000u, f[3] ^ 0x80008000u, f[0], f[1]}); };
  auto segA0 = [&]() {
    f16 = *(LAS v4u*)(smem + L_TAB + l * 16);
    t1c = *(LAS v4f*)(smem + L_TAB + 2048 + l * 16); t1s = *(LAS v4f*)(smem + L_TAB + 3072 + l * 16);
  };
  auto segA1 = [&]() {
    PIN(s1v);
    const v8h a = __builtin_bit_cast(v8h, s1v);
    re = mfma16h(a, __builtin_bit_cast(v8h, f16), z4); im = mfma16h(a, imf(f16), z4);
  };
  auto segA2 = [&]() {
    twid(re, im, t1c, t1s);
    const v8h a = pack_ri(re, im);
    re = mfma16h(__builtin_bit_cast(v8h, f16), a, z4); im = mfma16h(imf(f16), a, z4);
  };
  auto segA3 = [&]() {
    twid(re, im, t2c, t2s);
    *(LAS v4h*)(smem + L_E1 + w * E1P + o * 32 + g * 8) = pack4h(re);
    *(LAS v4h*)(smem + L_E1 + (8 + w) * E1P + o * 32 + g * 8) = pack4h(im);
  };

  if (ffn) {
    v16f acc[2][2] = {};
    float mbr[2];
    const u32 wb0 = (u32)((8 * h + q) * 256 + ((fq ^ q) << 6) + blk * 32 + pp * 8);
    const u32 r1 = 16384 + sh * 16384, r2 = 32768 - sh * 16384;
    const int xrow = sh * 64 + (l & 31);
    const u32 xb0 = (u32)(xrow * 128 + ((h ^ ((xrow >> 1) & 7)) << 4));
    auto rd = [&](Frag1& f, LAS char* sb, int i) {
      LAS char* xa = sb + (xb0 ^ (i << 5));
      LAS char* wa_ = sb + wb0 + (i << 12);
      f.w1 = cat8(trread(wa_ + r1), trread(wa_ + r1 + 1024));
      f.x0 = *(LAS v8s*)xa; f.x1 = *(LAS v8s*)(xa + 4096);
      f.w2 = cat8(trread(wa_ + r2), trread(wa_ + r2 + 1024));
    };
    const u32 one = ((l & 15) == ((l >> 4) & 1)) ? 0x3f803f80u : 0u;
    const v8s sel = __builtin_bit_cast(v8s, v4u{one, one, one, one});
    v4f csa = {0.f, 0.f, 0.f, 0.f};
    Frag1 A, B;
    WAIT_VM(7);
    BARRIER();
    STAMPT(1); STAMPC(30);
    rd(A, smem, 0);
    round1_read();
    dma2(aoff[0], aoff[1], (const char*)x + ((2 + rot) & 7) * 128, lbase + 2 * 49152 + w * 2048);
    u32 c1[4], c2[4];
    static_for<0, 8>([&](auto T) {
      constexpr int t = T, slot = t % 3, nslot = (t + 1) % 3, pslot = (t + 2) % 3;
      LAS char* sb = smem + slot * 49152;
      {
        const int kt = (t + 2 + rot) & 7;
        const char* s1 = t < 6 ? (const char*)Wg + (size_t)kt * (64 * F_ * 2) : bsrc + (size_t)((t - 6) * 3 + 1) * 131072;
        const char* s2 = t < 6 ? (const char*)Wu + (size_t)kt * (64 * F_ * 2) : bsrc + (size_t)((t - 6) * 3 + 2) * 131072;
        const u32 o2 = t < 6 ? boff : ro0, o3 = t < 6 ? boff1 : ro1;
        const u32 dst = lbase + pslot * 49152 + w * 2048;
        rd(B, sb, 1); SCHED();
        acc[0][0] = mfma32b(A.w1, A.x0, acc[0][0]); acc[0][1] = mfma32b(A.w1, A.x1, acc[0][1]);
        if constexpr (t == 0) csa = mfma16b(sel, __builtin_bit_cast(v4u, A.w1), csa);
        SCHED();
        dma1<0>(o2, s1, dst + 16384);
        if constexpr (t == 2) load_xe();
        if constexpr (t == 7) load_wt();
        acc[1][0] = mfma32b(A.w2, A.x0, acc[1][0]); acc[1][1] = mfma32b(A.w2, A.x1, acc[1][1]); SCHED();
        if constexpr (t == 0) { round1_store(); round1_max(); SCHED(); }
        if constexpr (t == 3) { trig2(); SCHED(); }
        if constexpr (t == 4) { trig3(); SCHED(); }
        rd(A, sb, 2); SCHED();
        acc[0][0] = mfma32b(B.w1, B.x0, acc[0][0]); SCHED();
        dma1<1024>(o3, s1, dst + 16384);
        acc[0][1] = mfma32b(B.w1, B.x1, acc[0][1]);
        if constexpr (t == 0) csa = mfma16b(sel, __builtin_bit_cast(v4u, B.w1), csa);
        acc[1][0] = mfma32b(B.w2, B.x0, acc[1][0]); acc[1][1] = mfma32b(B.w2, B.x1, acc[1][1]); SCHED();
        dma1<0>(o2, s2, dst + 32768);
        if constexpr (t == 6) {
          load_rs();
        }
        if constexpr (t == 7) { WAIT_VM(13); segA1(); SCHED(); }
        rd(B, sb, 3); SCHED();
        if constexpr (t == 7) {
#pragma unroll
          for (int i = 0; i < 4; ++i) { PIN(c1[i]); PIN(c2[i]); }
          PIN(mbr[0]); PIN(mbr[1]);
          const v8s cs1 = __builtin_bit_cast(v8s, v4u{c1[0], c1[1], c1[2], c1[3]}), cs2 = __builtin_bit_cast(v8s, v4u{c2[0], c2[1], c2[2], c2[3]});
          const u32 m0_ = pkbf(mbr[0], mbr[0]), m1_ = pkbf(mbr[1], mbr[1]);
          const v8s mb0 = __builtin_bit_cast(v8s, v4u{m0_, m0_, m0_, m0_}), mb1 = __builtin_bit_cast(v8s, v4u{m1_, m1_, m1_, m1_});
          acc[0][0] = mfma32b(cs1, mb0, acc[0][0]); acc[1][0] = mfma32b(cs2, mb0, acc[1][0]);
          acc[0][1] = mfma32b(cs1, mb1, acc[0][1]); acc[1][1] = mfma32b(cs2, mb1, acc[1][1]); SCHED();
          segA2(); SCHED();
        }
        acc[0][0] = mfma32b(A.w1, A.x0, acc[0][0]); acc[0][1] = mfma32b(A.w1, A.x1, acc[0][1]);
        if constexpr (t == 0) csa = mfma16b(sel, __builtin_bit_cast(v4u, A.w1), csa);
        SCHED();
        dma1<1024>(o3, s2, dst + 32768);
        acc[1][0] = mfma32b(A.w2, A.x0, acc[1][0]); acc[1][1] = mfma32b(A.w2, A.x1, acc[1][1]); SCHED();
      }
      if constexpr (t < 7) {
        const char* s0 = t < 5 ? (const char*)x + ((t + 3 + rot) & 7) * 128 : bsrc + (size_t)((t - 5) * 3) * 131072;
        const u32 o0 = t < 5 ? aoff[0] : ro0, o1 = t < 5 ? aoff[1] : ro1;
        const u32 dst = lbase + slot * 49152 + w * 2048;
        if constexpr (t == 0) csa = mfma16b(sel, __builtin_bit_cast(v4u, B.w1), csa);
        WAIT_VM(t == 0 ? 8 : t == 2 ? 7 : t == 6 ? 18 : 6);
        if constexpr (t == 5) { if (w == 0) check_a(*(LAS v4u*)(smem + L_FA + l * 16)); }
        BARRIER();
        STAMPT(22 + t);
        acc[0][0] = mfma32b(B.w1, B.x0, acc[0][0]); SCHED();
        rd(A, smem + nslot * 49152, 0);
        if constexpr (t == 6) segA0();
        SCHED();
        if constexpr (t == 0) {
          if (l < 16) {
#pragma unroll
            for (int i = 0; i < 2; ++i) {
              const float hi = rbf(csa[i]);
              st4c((u32)(((j * 16 + p) * 256 + sh * 128 + fq * 32 + i * 16 + l) * 4), ws + OFF_CSP, pkbf(hi, csa[i] - hi));
            }
          }
        }
        if constexpr (t == 0) { if (w == 0) dma1c((u32)((l & 1) * 4), BC, lbase + L_BC1); }
        if constexpr (t == 4) { if (w == 0) dma1s<0>((u32)(FL_A + l * 16), FL, lbase + L_FA); }
        if constexpr (t == 5) {
          const u32 co = (u32)(((j * 16 + (p & 1) + 8 * h) * 256 + fq * 32 + (l & 31)) * 4);
#pragma unroll
          for (int i = 0; i < 4; ++i) { ld4c(c1[i], co + sh * 512 + i * 2048, ws + OFF_CSP); ld4c(c2[i], co + (1 - sh) * 512 + i * 2048, ws + OFF_CSP); }
          ld4c(mbr[0], (u32)((p * 128 + xrow) * 4), ws + OFF_MB); ld4c(mbr[1], (u32)((p * 128 + xrow + 32) * 4), ws + OFF_MB);
          load_s1();
        }
        dma1<0>(o0, s0, dst);
        acc[0][1] = mfma32b(B.w1, B.x1, acc[0][1]);
        acc[1][0] = mfma32b(B.w2, B.x0, acc[1][0]);
        acc[1][1] = mfma32b(B.w2, B.x1, acc[1][1]); SCHED();
        dma1<1024>(o1, s0, dst);
        if constexpr (t == 1) { if (w == 0) { get_epoch(); signal_a(); } }
        SCHED();
      } else {
        acc[0][0] = mfma32b(B.w1, B.x0, acc[0][0]); acc[1][0] = mfma32b(B.w2, B.x0, acc[1][0]); SCHED();
        acc[0][1] = mfma32b(B.w1, B.x1, acc[0][1]); acc[1][1] = mfma32b(B.w2, B.x1, acc[1][1]); SCHED();
      }
    });
    STAMPT(2); STAMPC(31);
    auto fin = [&](const v16f& ag, const v16f& au, int rt) {
#pragma unroll
      for (int s2i = 0; s2i < 2; ++s2i) {
        v4u ov;
#pragma unroll
        for (int e = 0; e < 4; ++e) {
          const int r = s2i * 8 + 2 * e;
          v2f gg = v2f{relu(ag[r]), relu(ag[r + 1])} * v2f{au[r], au[r + 1]};
          ov[e] = pkbf(gg[0], gg[1]);
        }
        st16c((u32)(((p * 4 + sh * 2 + rt) * 112 + (j * 8 + fq * 2 + s2i)) * 1024 + l * 16), ws + OFF_H, ov);
      }
    };
    if (sh == 0) { fin(acc[0][0], acc[1][0], 0); fin(acc[0][1], acc[1][1], 1); }
    else { fin(acc[1][0], acc[0][0], 0); fin(acc[1][1], acc[0][1], 1); }
    STAMPT(6);
    segA3();
    WAIT_VM(4);
  } else {
    WAIT_VM(1);
    BARRIER();
    round1_read(); round1_store(); round1_max();
    if (w == 0 && !coord) dma1c((u32)((l & 1) * 4), BC, lbase + L_BC1);
    do_tick();
    if (w == 7) {
#pragma unroll
      for (int i = 0; i < 8; ++i) PIN(sq[i]);
      float war = bflo(sq[0]) + bflo(sq[2]), wai = bflo(sq[1]) - bflo(sq[3]);
      float wbr = bflo(sq[4]) + bflo(sq[6]), wbi = bflo(sq[5]) - bflo(sq[7]);
      v4f pv = {0.25f * (war + wbr), 0.25f * (wai + wbi), 0.25f * (war - wbr), 0.25f * (wai - wbi)};
      st16c((u32)((((sp >> 2) * 4 + (l & 3)) * 64 + (sp & 3) * 16 + (l >> 2)) * 16), ws + OFF_PQ, pv);
    }
    WAIT_VM(0);
    BARRIER();
    STAMPT(1); STAMPC(30);
    if (w == 0) { if (!coord) get_epoch(); signal_a(); do_tick(); WAIT_VM(0); }
    BARRIER();
    STAMPT(24);
    dma1c((u32)(sp * 57344 + (w * 112 + l) * 64), (const char*)Wd, lbase + 49152);
    dma1c((u32)(sp * 57344 + (w * 112 + 48 + l) * 64), (const char*)Wd, lbase + 49152);
    static_for<0, 6>([&](auto N) { issueW(N); });
    static_for<6, 14>([&](auto N) { dma2(ro0, ro1, bsrc - 512 + (size_t)N * 131072, lbase + 49152 + w * 2048); });
    load_xe();
    trig2(); trig3(); segA0();
    STAMPT(25);
    if (w == 0) {
      if (!coord) __builtin_amdgcn_s_sleep(SLEEP_A);
      check_a(v4u{~E, 0u, 0u, 0u});
    }
    STAMPT(26);
    BARRIER();
    load_s1(); load_wt(); load_rs();
    if (kd == 0) {
#pragma unroll
      for (int r = 0; r < 4; ++r) ld16c(pq[r], (u32)(w * 4096 + r * 1024 + l * 16), ws + OFF_PQ);
    }
    WAIT_VM(0);
    segA1(); segA2(); segA3();
    STAMPT(2); STAMPC(31);
  }
  PIN(wt); PIN(xe); PIN(rs2);
  BARRIER();
  STAMPT(5);

  const u32 trG = (u32)(kq * 4096 + (4 * h + q) * 128 + ((dh ^ ((q >> 1) & 1)) << 6) + (blk ^ kh) * 32 + pp * 8);
  v8s Wf[14][2];
  auto readW = [&](auto N) {
    constexpr int n = N;
    LAS char* sb = smem + wslot(n) + trG;
    Wf[n][0] = cat8(trread(sb), trread(sb + 1024)); Wf[n][1] = cat8(trread(sb + 2048), trread(sb + 3072));
  };
  const char* hsrc = ws + OFF_H + ((size_t)(RG * 2 + (w >> 2)) * 112 + (w & 3) * 2) * 1024;
  const u32 so0 = (u32)((l >> 3) * 4096) + swz, so1 = so0 + 32768 - 1024;
  const char* ssrc = ws + OFF_S2 + (size_t)(w * 16) * 4096 + RG * 128;
  auto issueP = [&](int n, auto HALF) {
    constexpr int hf = HALF;
    if (n < 14) dma1s<hf * 1024>((u32)(l * 16), hsrc + n * 8192, lbase + aslot(n) + w * 2048);
    else dma1s<hf * 1024>(hf ? so1 : so0, ssrc + (size_t)(n - 14) * (128 * 4096), lbase + aslot(n) + w * 2048);
  };
  const int d = ct * 64 + dh * 32 + ((l & 31) ^ (kh << 4));
  const float gst = (float)d * (1.0f / 512.0f), ga0 = (float)(16 * kq + 2 * h) * gst;
  v8h G[8];
  auto gen = [&](auto FI) {
    constexpr int f = FI;
#pragma unroll
    for (int e2 = 0; e2 < 4; ++e2) {
      float a = fmaf((float)(64 * (f >> 1) + 8 * (f & 1) + 4 * (e2 >> 1) + (e2 & 1)), gst, ga0);
      float c = cosr(a), sn = sinr(a);
      if (f == 0 && e2 == 0) { if (kq == 0 && h == 0) { c = 0.5f; sn = (d & 1) ? 0.5f : -0.5f; } }
      G[f][2 * e2] = (_Float16)c; G[f][2 * e2 + 1] = (_Float16)sn;
    }
    PIN(G[f]);
  };

  float oscale;
  const u32 fhoff = (u32)(FL_H + (RG * 16 + (min(l, 55) >> 2)) * 64 + (l & 3) * 4);
  u32 fp;
  {
    v8h a;
    {
      LAS char* rp = smem + L_E1 + (4 * (g & 1) + (o >> 2)) * E1P + (2 * w + (g >> 1)) * 32 + (o & 3) * 8;
      a = __builtin_bit_cast(v8h, cat8(trread(rp), trread(rp + 8 * E1P)));
    }
    const v4u f8 = *(LAS v4u*)(smem + L_TAB + 1024 + l * 16);
    const v2u me = *(LAS v2u*)(smem + L_MX);
    issueW(6);
    static_for<0, 3>(readW);
    const int Ex = (int)me[0] < 16 ? 16 : ((int)me[0] > 240 ? 240 : (int)me[0]);
    const float wscale = __builtin_bit_cast(float, (u32)(246 - Ex) << 23);
    oscale = __builtin_bit_cast(float, (u32)(Ex - 7) << 23);
    re = mfma16h(__builtin_bit_cast(v8h, f8), a, z4); im = mfma16h(imf(f8), a, z4);
    static_for<3, 6>(readW);
    WAIT_VM(2);
    STAMPT(7);
    if (ffn && l == 0) st4c((u32)(FL_H + ((p * 2 + sh) * 16 + j) * 64 + fq * 4), FL, me[1]);
    issueW(7);
    if (kd != 0) {
#pragma unroll
      for (int r = 0; r < 4; ++r) {
        float fr = ((r & 1) ? bfhi(wt[r >> 1]) : bflo(wt[r >> 1])) * wscale, fi = ((r & 1) ? bfhi(wt[2 + (r >> 1)]) : bflo(wt[2 + (r >> 1)])) * wscale;
        float zr = re[r], zi = im[r];
        re[r] = zr * fr - zi * fi;
        im[r] = -(zr * fi + zi * fr);
      }
    } else {
      const int ka = 2 * w + (g >> 1), kr = o + 64 * (g & 1);
#pragma unroll
      for (int r = 0; r < 4; ++r) { v2f t = {re[r], im[r]}; *(LAS v2f*)(smem + L_ZX + ka * 1024 + (kr + 16 * r) * 8) = t; }
      BARRIER();
#pragma unroll
      for (int r = 0; r < 4; ++r) {
        PIN(pq[r]);
        const int km = (2048 - (ka + 16 * (kr + 16 * r))) & 2047;
        v2f zm = *(LAS v2f*)(smem + L_ZX + (km & 15) * 1024 + (km >> 4) * 8);
        float zr = re[r], zi = im[r];
        re[r] = (zr * pq[r][0] - zi * pq[r][1] + zm[0] * pq[r][2] + zm[1] * pq[r][3]) * wscale;
        im[r] = -(zr * pq[r][1] + zi * pq[r][0] + zm[0] * pq[r][3] - zm[1] * pq[r][2]) * wscale;
      }
    }
    LAS char* E2 = smem + (kd == 0 ? L_E1 : L_E2);
    a = pack_ri(re, im);
    re = mfma16h(a, __builtin_bit_cast(v8h, f8), z4); im = mfma16h(a, imf(f8), z4);
    twid(re, im, t3c, t3s);
    *(LAS v4h*)(E2 + (o & 7) * E2P + (2 * w + (o >> 3)) * 32 + g * 8) = pack4h(re);
    *(LAS v4h*)(E2 + (o & 7) * E2P + 512 + (2 * w + (o >> 3)) * 32 + g * 8) = pack4h(im);
    BARRIER();
    STAMPT(8);
    {
      LAS char* rp = E2 + w * E2P + o * 32 + g * 8;
      a = __builtin_bit_cast(v8h, cat8(*(LAS v4s*)rp, *(LAS v4s*)(rp + 512)));
    }
    issueW(8);
    re = mfma16h(a, __builtin_bit_cast(v8h, f16), z4); im = mfma16h(a, imf(f16), z4);
    issueW(9);
    twid(re, im, t1c, t1s);
    a = pack_ri(re, im);
    re = mfma16h(__builtin_bit_cast(v8h, f16), a, z4); im = mfma16h(imf(f16), a, z4);
    issueW(10);
    st8c((u32)(2 * kd * 4096 + w * 512 + l * 8), ws + OFF_S2, pack4h(re));
    st8c((u32)((2 * kd + 1) * 4096 + w * 512 + l * 8), ws + OFF_S2, pack4h(im));
  }
  STAMPT(9);
  gen(IC<0>{}); issueW(11);
  WAIT_VM(8);
  BARRIER();
  static_for<6, 9>(readW);
  gen(IC<1>{}); issueW(12); gen(IC<2>{}); issueW(13);
  if (!ffn) {
    WAIT_VM(0);
    BARRIER();
    if (w == 0) {
      if (l == 0) st4c((u32)(FL_S + b * 4), FL, E);
      __builtin_amdgcn_s_sleep(16);
    }
  }
  if (w == 0) ld4c(fp, fhoff, FL);
  gen(IC<3>{}); gen(IC<4>{});
  WAIT_VM(0);
  if (w == 0) {
    PIN(fp);
    u32 f = fp;
    int spins = 0;
    while (!__all(f == E) && ++spins < SPIN_CAP) {
      __builtin_amdgcn_s_sleep(4);
      f = ld4c_sync(fhoff, FL);
    }
  }
  BARRIER();
  STAMPT(10);
  if (ffn && tid == 0) st4c((u32)(FL_S + b * 4), FL, E);
  issueP(0, IC<0>{}); issueP(0, IC<1>{}); issueP(1, IC<0>{}); issueP(1, IC<1>{});
  static_for<9, 14>(readW);
  gen(IC<5>{});
  issueP(2, IC<0>{}); issueP(2, IC<1>{});
  gen(IC<6>{});
  issueP(3, IC<0>{}); issueP(3, IC<1>{});
  gen(IC<7>{});

  const u32 trS = (u32)(kq * 4096 + (4 * h + q) * 128 + ((ph ^ ((q >> 1) & 1)) << 6) + blk * 32 + pp * 8);
  const u32 hb = (u32)(ph * 8192 + kq * 2048 + l * 16);
  v16f accG[2] = {}, accS[2] = {};
  v8s fa[8], fb[8];
  v4u fs;
  float gv[8];
  const u32 xw = (u32)(w * 6144 + l * 16);
  auto xchg_out = [&](const v16f (&ac)[2], int base) {
    LAS char* X = smem + base + xw;
#pragma unroll
    for (int i = 0; i < 6; ++i) {
      const v16f& a = ac[(i + 2) >> 2]; const int r = ((i + 2) & 3) * 4;
      *(LAS v4f*)(X + i * 1024) = v4f{a[r], a[r + 1], a[r + 2], a[r + 3]};
    }
  };
  auto xchg_in = [&](const v16f (&ac)[2], int base, float (&o8)[8]) {
    v4f s0 = {ac[0][0], ac[0][1], ac[0][2], ac[0][3]}, s1 = {ac[0][4], ac[0][5], ac[0][6], ac[0][7]};
#pragma unroll
    for (int x_ = 1; x_ < 4; ++x_) {
      LAS char* X = smem + base + (w ^ (2 * x_)) * 6144 + (x_ - 1) * 2048 + l * 16;
      s0 += *(LAS v4f*)X; s1 += *(LAS v4f*)(X + 1024);
    }
#pragma unroll
    for (int e = 0; e < 4; ++e) { o8[e] = s0[e]; o8[4 + e] = s1[e]; }
  };
  v4f xg[6];
  float ov[8], ex[8];
  auto sadd = [](float a, float b) { float r; asm("v_add_f32 %0, %1, %2" : "=v"(r) : "v"(a), "v"(b)); return r; };
  auto smul = [](float a, float b) { float r; asm("v_mul_f32 %0, %1, %2" : "=v"(r) : "v"(a), "v"(b)); return r; };
  auto silu_step = [&](int i) {
    if (i < 3) {
#pragma unroll
      for (int e = 0; e < 8; ++e) { ov[e] = sadd(i == 0 ? accG[0][e] : ov[e], xg[2 * i + (e >> 2)][e & 3]); PIN(ov[e]); }
    } else if (i == 3) {
#pragma unroll
      for (int e = 0; e < 8; e += 2) { u32 t = pkbf_o(smul(ov[e], rs2), smul(ov[e + 1], rs2)); ov[e] = bflo(t); ov[e + 1] = bfhi(t); PIN(ov[e]); PIN(ov[e + 1]); }
    } else if (i == 4) {
#pragma unroll
      for (int e = 0; e < 8; ++e) { ex[e] = __builtin_amdgcn_exp2f(ov[e] * -1.44269504088896f); PIN(ex[e]); }
    } else if (i == 5) {
#pragma unroll
      for (int e = 0; e < 8; ++e) { ex[e] = __builtin_amdgcn_rcpf(1.0f + ex[e]); PIN(ex[e]); }
    } else if (i == 6) {
#pragma unroll
      for (int e = 0; e < 8; e += 2) { u32 t = pkbf_o(ov[e] * ex[e], ov[e + 1] * ex[e + 1]); gv[e] = bflo(t); gv[e + 1] = bfhi(t); PIN(gv[e]); PIN(gv[e + 1]); }
    }
  };
  static_for<0, 10>([&](auto V) {
    constexpr int v = V;
    v8s(&cur)[8] = (v & 1) ? fb : fa;
    v8s(&prv)[8] = (v & 1) ? fa : fb;
    auto mm2 = [&](auto U, v8s (&fr)[8], int s) {
      constexpr int u = U;
      if constexpr (u >= 7) { accS[0] = mfma32h(G[(u - 7) * 4 + s], fr[2 * s], accS[0]); accS[1] = mfma32h(G[(u - 7) * 4 + s], fr[2 * s + 1], accS[1]); }
      else { accG[0] = mfma32b(Wf[2 * u + (s >> 1)][s & 1], fr[2 * s], accG[0]); accG[1] = mfma32b(Wf[2 * u + (s >> 1)][s & 1], fr[2 * s + 1], accG[1]); }
      SCHED();
    };
    if constexpr (v < 9) {
      WAIT_VM(v <= 7 ? 4 : 0);
      if constexpr (v == 5) { if (w == 0) { PIN(fs); poll4(fs, (u32)(FL_S + l * 16)); } }
      BARRIER();
      if constexpr (v == 0 || v == 8) STAMPT(11 + v);
      if constexpr (v == 4) { if (w == 0) ld16c(fs, (u32)(FL_S + l * 16), FL); }
      if constexpr (v >= 1) mm2(IC<(v >= 1 ? v - 1 : 0)>{}, prv, 2);
#pragma unroll
      for (int s = 0; s < 4; ++s) {
        if constexpr (v >= 7) {
          LAS char* sb = smem + aslot(2 * v + (s >> 1)) + (s & 1) * 2048;
          cur[2 * s] = cat8(trread(sb + trS), trread(sb + trS + 1024));
          cur[2 * s + 1] = cat8(trread(sb + (trS ^ 64)), trread(sb + (trS ^ 64) + 1024));
        } else {
          LAS char* sb = smem + aslot(2 * v + (s >> 1)) + (s & 1) * 1024;
          cur[2 * s] = *(LAS v8s*)(sb + hb);
          cur[2 * s + 1] = *(LAS v8s*)(sb + (hb ^ 8192));
        }
      }
      if constexpr (v == 8) {
#pragma unroll
        for (int x_ = 1; x_ < 4; ++x_) {
          LAS char* X = smem + 49152 + (w ^ (2 * x_)) * 6144 + (x_ - 1) * 2048 + l * 16;
          xg[2 * x_ - 2] = *(LAS v4f*)X; xg[2 * x_ - 1] = *(LAS v4f*)(X + 1024);
        }
      }
      SCHED();
      if constexpr (v <= 6) issueP(2 * v + 4, IC<0>{});
      if constexpr (v >= 1) mm2(IC<(v >= 1 ? v - 1 : 0)>{}, prv, 3);
      if constexpr (v <= 6) issueP(2 * v + 4, IC<1>{});
      if constexpr (v == 8) { silu_step(0); silu_step(1); SCHED(); }
      mm2(V, cur, 0);
      if constexpr (v <= 6) issueP(2 * v + 5, IC<0>{});
      if constexpr (v == 7) { xchg_out(accG, 49152); SCHED(); }
      if constexpr (v == 8) { silu_step(2); silu_step(3); SCHED(); }
      mm2(V, cur, 1);
      if constexpr (v <= 6) issueP(2 * v + 5, IC<1>{});
      if constexpr (v == 8) { silu_step(4); SCHED(); }
    } else {
      mm2(IC<8>{}, prv, 2);
      silu_step(5); SCHED();
      mm2(IC<8>{}, prv, 3);
      silu_step(6); SCHED();
    }
  });
  xchg_out(accS, 0);
  BARRIER();
  {
    float o8[8], pv[8];
    xchg_in(accS, 0, o8);
#pragma unroll
    for (int e = 0; e < 8; e += 2) { u32 t = pkbf_o(o8[e] * oscale, o8[e + 1] * oscale); pv[e] = bflo(t) * gv[e]; pv[e + 1] = bfhi(t) * gv[e + 1]; }
    u32 a0 = pkbf(pv[0], pv[1]), a1 = pkbf(pv[2], pv[3]), b0 = pkbf(pv[4], pv[5]), b1 = pkbf(pv[6], pv[7]);
    auto r0 = __builtin_amdgcn_permlane32_swap(a0, b0, false, false);
    auto r1 = __builtin_amdgcn_permlane32_swap(a1, b1, false, false);
    v4u pk = {r0[0], r1[0], r0[1], r1[1]};
    v4u ov4;
#pragma unroll
    for (int e = 0; e < 4; ++e) ov4[e] = pkbf(bflo(xe[e]) + bflo(pk[e]), bfhi(xe[e]) + bfhi(pk[e]));
    *(v4u*)((char*)out + eoff) = ov4;
  }
  asm volatile("km_pf_b:");
  STAMPT(20);
  STAMPT(21);
  STAMP_OUT(ws);
}

extern "C" void kernel_launch(void* const* d_in, const int* in_sizes, int n_in, void* d_out, int out_size, void* d_ws,
                              size_t ws_size, hipStream_t stream) {
  kmain<<<256, 512, 0, stream>>>((const u16*)d_in[0], (const u16*)d_in[1], (const u16*)d_in[2], (const u16*)d_in[4],
                                 (const u16*)d_in[5], (const u16*)d_in[6], (char*)d_ws + WS_BASE(ws_size), (u16*)d_out);
}
```

```cpp
#include <hip/hip_runtime.h>
#include <stdint.h>

typedef unsigned short u16;
typedef unsigned int u32;
typedef unsigned long long u64;
typedef short v4s __attribute__((ext_vector_type(4)));
typedef short v8s __attribute__((ext_vector_type(8)));
typedef _Float16 v4h __attribute__((ext_vector_type(4)));
typedef _Float16 v8h __attribute__((ext_vector_type(8)));
typedef __bf16 v2b __attribute__((ext_vector_type(2)));
typedef __bf16 v8b __attribute__((ext_vector_type(8)));
typedef float v2f __attribute__((ext_vector_type(2)));
typedef float v4f __attribute__((ext_vector_type(4)));
typedef float v16f __attribute__((ext_vector_type(16)));
typedef u32 v2u __attribute__((ext_vector_type(2)));
typedef u32 v4u __attribute__((ext_vector_type(4)));

#define LAS __attribute__((address_space(3)))
#define GAS __attribute__((address_space(1)))
#define DEVI __device__ __forceinline__

constexpr int D_ = 512, F_ = 1792, DF_ = 257;
constexpr size_t OFF_H = (size_t)24 << 20;
constexpr size_t OFF_S1 = (size_t)32 << 20;
constexpr size_t OFF_WT = ((size_t)34 << 20) + (256 << 10);
constexpr size_t OFF_S2 = ((size_t)36 << 20) + (768 << 10);
constexpr size_t OFF_M = (size_t)39 << 20;
constexpr size_t OFF_FL = OFF_M;
constexpr u32 FL_S = 0, FL_A = 1024, FL_H = 2048;
constexpr size_t OFF_BC = OFF_M + 49152;
constexpr size_t OFF_MB = OFF_M + 98304;
constexpr size_t OFF_RS2 = OFF_M + 106496;
constexpr size_t OFF_PQ = OFF_M + 65536;
constexpr size_t OFF_CSP = OFF_M + 131072;
constexpr int S1P = 8192 + 128, WTP = 4608 + 128, S2P = 4096 + 128;
constexpr int SPIN_CAP = 1 << 16;
#define SLEEP_A 16

#define WS_BASE(n) ((((n) - ((size_t)40 << 20))) & ~(size_t)0x1fffff)
#define STAMP_DECL
#define STAMPT(id)
#define STAMPC(id)
#define STAMP_OUT(wsp)

template <int I> struct IC { static constexpr int v = I; constexpr operator int() const { return I; } };
template <int B, int E, class Fn> DEVI void static_for(Fn&& f) {
  if constexpr (B < E) { f(IC<B>{}); static_for<B + 1, E>(f); }
}
#define WAIT_VM(n) asm volatile("s_waitcnt vmcnt(%0)" ::"n"(n) : "memory")
#define BARRIER() asm volatile("s_waitcnt lgkmcnt(0)\n\ts_barrier" ::: "memory")
#define SCHED() __builtin_amdgcn_sched_barrier(0)
#define PIN(x) asm volatile("" : "+v"(x))

DEVI int s_of_pi(int pi) {
  int RG = pi >> 6, i = pi & 63;
  return 128 * (4 * (RG & 3) + (i & 3)) + 8 * (i >> 2) + (RG >> 2);
}
DEVI void dma2(u32 voff0, u32 voff1, const char* sbase, u32 lds) {
  asm volatile("s_mov_b32 m0, %3\n\ts_nop 0\n\tglobal_load_lds_dwordx4 %0, %2\n\tglobal_load_lds_dwordx4 %1, %2 offset:1024" ::"v"(voff0), "v"(voff1), "s"(sbase), "s"(lds) : "memory");
}
DEVI void dma2c(u32 voff0, u32 voff1, const char* sbase, u32 lds) {
  asm volatile("s_mov_b32 m0, %3\n\ts_nop 0\n\tglobal_load_lds_dwordx4 %0, %2 sc1\n\tglobal_load_lds_dwordx4 %1, %2 offset:1024 sc1" ::"v"(voff0), "v"(voff1), "s"(sbase), "s"(lds) : "memory");
}
template <int OFF> DEVI void dma1(u32 voff, const char* sbase, u32 lds) {
  asm volatile("s_mov_b32 m0, %2\n\ts_nop 0\n\tglobal_load_lds_dwordx4 %0, %1 offset:%3" ::"v"(voff), "s"(sbase), "s"(lds), "n"(OFF) : "memory");
}
template <int OFF> DEVI void dma1s(u32 voff, const char* sbase, u32 lds) {
  asm volatile("s_mov_b32 m0, %2\n\ts_nop 0\n\tglobal_load_lds_dwordx4 %0, %1 offset:%3 sc1" ::"v"(voff), "s"(sbase), "s"(lds), "n"(OFF) : "memory");
}
DEVI void dma1c(u32 voff, const char* sbase, u32 lds) {
  asm volatile("s_mov_b32 m0, %2\n\ts_nop 0\n\tglobal_load_lds_dword %0, %1 sc1" ::"v"(voff), "s"(sbase), "s"(lds) : "memory");
}
DEVI v2u ld8c_sync(u32 voff, const char* sb) { v2u d; asm volatile("global_load_dwordx2 %0, %1, %2 sc1\n\ts_waitcnt vmcnt(0)" : "=&v"(d) : "v"(voff), "s"(sb) : "memory"); return d; }
DEVI u32 ld4c_sync(u32 voff, const char* sb) { u32 d; asm volatile("global_load_dword %0, %1, %2 sc1\n\ts_waitcnt vmcnt(0)" : "=&v"(d) : "v"(voff), "s"(sb) : "memory"); return d; }
DEVI v4u ld16c_sync(u32 voff, const char* sb) { v4u d; asm volatile("global_load_dwordx4 %0, %1, %2 sc1\n\ts_waitcnt vmcnt(0)" : "=&v"(d) : "v"(voff), "s"(sb) : "memory"); return d; }
template <class T> DEVI void ld16c(T& d, u32 voff, const char* sb) { asm volatile("global_load_dwordx4 %0, %1, %2 sc1" : "=&v"(d) : "v"(voff), "s"(sb) : "memory"); }
template <class T> DEVI void ld8c(T& d, u32 voff, const char* sb) { asm volatile("global_load_dwordx2 %0, %1, %2 sc1" : "=&v"(d) : "v"(voff), "s"(sb) : "memory"); }
template <class T> DEVI void ld4c(T& d, u32 voff, const char* sb) { asm volatile("global_load_dword %0, %1, %2 sc1" : "=&v"(d) : "v"(voff), "s"(sb) : "memory"); }
template <class T> DEVI void ld16(T& d, u32 voff, const char* sb) { asm volatile("global_load_dwordx4 %0, %1, %2" : "=&v"(d) : "v"(voff), "s"(sb) : "memory"); }
DEVI void ld2(u32& d, u32 voff, const char* sb) { asm volatile("global_load_ushort %0, %1, %2" : "=&v"(d) : "v"(voff), "s"(sb) : "memory"); }
template <class T> DEVI void st16c(u32 voff, const char* sb, T v) { asm volatile("global_store_dwordx4 %0, %1, %2 sc1\n\ts_nop 1" ::"v"(voff), "v"(v), "s"(sb) : "memory"); }
template <class T> DEVI void st8c(u32 voff, const char* sb, T v) { asm volatile("global_store_dwordx2 %0, %1, %2 sc1" ::"v"(voff), "v"(v), "s"(sb) : "memory"); }
template <class T> DEVI void st4c(u32 voff, const char* sb, T v) { asm volatile("global_store_dword %0, %1, %2 sc1" ::"v"(voff), "v"(v), "s"(sb) : "memory"); }

DEVI v4s trread(LAS char* p) { return __builtin_amdgcn_ds_read_tr16_b64_v4i16((LAS v4s*)p); }
DEVI v8s cat8(v4s a, v4s b) { return __builtin_shufflevector(a, b, 0, 1, 2, 3, 4, 5, 6, 7); }
DEVI float bflo(u32 v) { return __builtin_bit_cast(float, v << 16); }
DEVI float bfhi(u32 v) { return __builtin_bit_cast(float, v & 0xffff0000u); }
DEVI float relu(float v) { return __builtin_bit_cast(float, max(__builtin_bit_cast(int, v), 0)); }
DEVI float rbf(float v) { return (float)(__bf16)v; }
DEVI u32 pkbf(float a, float b) { v2b p = {(__bf16)a, (__bf16)b}; return __builtin_bit_cast(u32, p); }
DEVI u32 pkbf_o(float a, float b) { u32 r = pkbf(a, b); asm("" : "+v"(r)); return r; }
DEVI float cosr(float rev) { return __builtin_amdgcn_cosf(rev); }
DEVI float sinr(float rev) { return __builtin_amdgcn_sinf(rev); }
DEVI v4f mfma16h(v8h a, v8h b, v4f c) { return __builtin_amdgcn_mfma_f32_16x16x32_f16(a, b, c, 0, 0, 0); }
DEVI v4f mfma16b(v8s a, v4u b, v4f c) { return __builtin_amdgcn_mfma_f32_16x16x32_bf16(__builtin_bit_cast(v8b, a), __builtin_bit_cast(v8b, b), c, 0, 0, 0); }
DEVI v16f mfma32b(v8s a, v8s b, v16f c) { return __builtin_amdgcn_mfma_f32_32x32x16_bf16(__builtin_bit_cast(v8b, a), __builtin_bit_cast(v8b, b), c, 0, 0, 0); }
DEVI v16f mfma32h(v8h a, v8s b, v16f c) { return __builtin_amdgcn_mfma_f32_32x32x16_f16(a, __builtin_bit_cast(v8h, b), c, 0, 0, 0); }
DEVI v8h pack_ri(v4f re, v4f im) {
  v8h r;
  r[0] = (_Float16)re[0]; r[1] = (_Float16)re[1]; r[2] = (_Float16)re[2]; r[3] = (_Float16)re[3];
  r[4] = (_Float16)im[0]; r[5] = (_Float16)im[1]; r[6] = (_Float16)im[2]; r[7] = (_Float16)im[3];
  return r;
}
DEVI v4h pack4h(v4f a) { v4h r; r[0] = (_Float16)a[0]; r[1] = (_Float16)a[1]; r[2] = (_Float16)a[2]; r[3] = (_Float16)a[3]; return r; }
DEVI void twid(v4f& re, v4f& im, const v4f& c, const v4f& s) {
#pragma unroll
  for (int r = 0; r < 4; ++r) { float a = re[r], b = im[r]; re[r] = a * c[r] + b * s[r]; im[r] = b * c[r] - a * s[r]; }
}
template <int CTRL> DEVI float dppadd(float v) {
  return v + __builtin_bit_cast(float, __builtin_amdgcn_update_dpp(0, __builtin_bit_cast(int, v), CTRL, 0xf, 0xf, true));
}
DEVI float wave_sum(float v) {
  v = dppadd<0xB1>(v); v = dppadd<0x4E>(v); v = dppadd<0x141>(v); v = dppadd<0x140>(v);
  int bb = __builtin_bit_cast(int, v);
  float a0 = __builtin_bit_cast(float, __builtin_amdgcn_readlane(bb, 0)), a1 = __builtin_bit_cast(float, __builtin_amdgcn_readlane(bb, 16));
  float a2 = __builtin_bit_cast(float, __builtin_amdgcn_readlane(bb, 32)), a3 = __builtin_bit_cast(float, __builtin_amdgcn_readlane(bb, 48));
  return (a0 + a1) + (a2 + a3);
}
DEVI float dot2ones(u32 v, float acc) {
  const v2b ones = {(__bf16)1.0f, (__bf16)1.0f};
  return __builtin_amdgcn_fdot2_f32_bf16(__builtin_bit_cast(v2b, v), ones, acc, false);
}
template <int CTRL> DEVI u32 dppmax(u32 v) { return max(v, (u32)__builtin_amdgcn_update_dpp(0, (int)v, CTRL, 0xf, 0xf, true)); }
DEVI u32 wave_max(u32 v) {
  v = dppmax<0xB1>(v); v = dppmax<0x4E>(v); v = dppmax<0x141>(v); v = dppmax<0x140>(v);
  u32 a0 = __builtin_amdgcn_readlane(v, 0), a1 = __builtin_amdgcn_readlane(v, 16), a2 = __builtin_amdgcn_readlane(v, 32), a3 = __builtin_amdgcn_readlane(v, 48);
  return max(max(a0, a1), max(a2, a3));
}

struct Frag1 { v8s x0, x1, w1, w2; };

constexpr int L_SCR = 147456;
constexpr int L_CS = L_SCR, L_RED = L_SCR + 1024, L_MX = L_SCR + 1088, L_BC0 = L_SCR + 2048, L_BC1 = L_SCR + 2304;
constexpr int L_TAB = L_SCR + 3072;
constexpr int L_T = L_SCR + 7168, L_FA = L_SCR, L_E1 = L_T, L_E2 = 81920, L_ZX = 81920;
constexpr int L_C1 = 98304, L_IMG = L_C1 + 9216;
constexpr int L_FR = 131072;
constexpr int E1P = 544, E2P = 1024 + 72;
DEVI constexpr int wslot(int n) {
  constexpr int u[14] = {6, 7, 8, 0, 1, 2, 3, 4, 6, 7, 8, 0, 1, 2};
  return u[n] * 16384;
}
DEVI constexpr int aslot(int n) {
  constexpr int u[18] = {3, 4, 5, 9, 2, 6, 7, 8, 0, 1, 3, 4, 5, 9, 2, 6, 7, 8};
  return u[n] * 16384;
}

__global__ __launch_bounds__(512, 2) void kmain(const u16* __restrict__ x, const u16* __restrict__ wr, const u16* __restrict__ wi,
                                                const u16* __restrict__ Wg, const u16* __restrict__ Wu, const u16* __restrict__ Wd,
                                                char* __restrict__ ws, u16* __restrict__ out) {
  __shared__ __attribute__((aligned(16))) char smem_[163840];
  LAS char* smem = (LAS char*)smem_;
  const u32 lbase = (u32)(size_t)smem;
  const int tid = threadIdx.x, l = tid & 63;
  const int w = __builtin_amdgcn_readfirstlane(tid >> 6);
  const int b = blockIdx.x, xcd = b & 7, idx = b >> 3;
  const bool ffn = idx < 28;
  const int g = l >> 4, o = l & 15;
  const int h = l >> 5, blk = (l >> 4) & 1, q = (l & 15) >> 2, pp = l & 3;
  const char* FL = ws + OFF_FL;
  const char* BC = ws + OFF_BC;
  STAMP_DECL; STAMPT(0);
  asm volatile("" ::"s"(x), "s"(wr), "s"(wi), "s"(Wg), "s"(Wu), "s"(Wd), "s"(ws), "s"(out));

  const int pi = b * 8 + w;
  v4u xr;
  ld16(xr, (u32)(s_of_pi(pi) * 1024 + l * 16), (const char*)x);
  u64 kpc; u32 klen;
  asm volatile("s_getpc_b64 %0\nkm_pf_a:\n\ts_mov_b32 %1, km_pf_b-km_pf_a-16" : "=s"(kpc), "=s"(klen));
  auto pf_code = [&]() { dma1<0>(min((u32)(((idx & 3) * 8 + w) * 1024 + l * 16), klen) & ~15u, (const char*)(kpc & ~15ull), lbase + L_FA); };
  if (!ffn) pf_code();
  if (w == 0) dma1c((u32)((l & 1) * 4), BC, lbase + L_BC0);
  const int fkb = (b & 7) * 2 + (w >> 2), fcq = w & 3;
  auto filt_dma = [&]() {
    const u32 ks = (u32)((idx >> 1) + 16 * fkb + 256 * (4 * (idx & 1) + ((l >> 3) & 3)));
    const u32 base = ((ks * (DF_ * 2)) & ~15u) + fcq * 128;
    const u32 c0 = ((l & 7) ^ (((l >> 4) & 1) << 2)) << 4;
    if (l < 32) dma1<0>(base + c0, (const char*)wr, lbase + L_FR + w * 2048);
    else dma1<0>(base + c0, (const char*)wi, lbase + L_FR + w * 2048);
    if (fcq == 3) {
      const u32 t0 = min(base + 128 + (c0 & 16), (u32)(2048 * DF_ * 2 - 16));
      if (l < 32) dma1<0>(t0, (const char*)wr, lbase + L_FR + w * 2048 + 1024);
      else dma1<0>(t0, (const char*)wi, lbase + L_FR + w * 2048 + 1024);
    }
  };
  const int p = (xcd >> 1) * 4 + idx / 7, j = (xcd & 1) * 7 + idx % 7;
  const int sh = w >> 2, fq = w & 3;
  u32 aoff[2];
#pragma unroll
  for (int i = 0; i < 2; ++i) {
    int row = (2 * w + i) * 8 + (l >> 3);
    aoff[i] = s_of_pi((p & 15) * 128 + row) * 1024 + (((l & 7) ^ ((row >> 1) & 7)) << 4) - i * 1024;
  }
  const u32 boff = (u32)(2 * w * 4 + (l >> 4)) * (F_ * 2) + j * 256 + (((l & 15) ^ ((l >> 4) << 2)) << 4);
  const u32 boff1 = boff + 4 * F_ * 2 - 1024;
  const int rot = p >> 1;
  auto issue = [&](int t, int slot) {
    const int kt = (t + rot) & 7;
    u32 dst = lbase + slot * 49152 + w * 2048;
    dma2(aoff[0], aoff[1], (const char*)x + kt * 128, dst);
    dma2(boff, boff1, (const char*)Wg + (size_t)kt * (64 * F_ * 2), dst + 16384);
    dma2(boff, boff1, (const char*)Wu + (size_t)kt * (64 * F_ * 2), dst + 32768);
  };
  const int sp = xcd * 4 + (idx - 28);
  if (ffn) issue(0, 0); else filt_dma();

  u32 E = 0, tick = 0;
  const bool coord = (b == 224) && (w == 0);
  auto do_tick = [&]() { if (coord) { ++tick; if (l == 0) st8c(0u, BC, v2u{E, tick}); } };
  auto get_epoch = [&]() {
    const v2u b0 = *(LAS v2u*)(smem + L_BC0);
    v2u b1 = *(LAS v2u*)(smem + L_BC1);
    int spins = 0;
    while (__builtin_amdgcn_readfirstlane((b0[0] ^ b1[0]) | (b0[1] ^ b1[1])) == 0 && ++spins < SPIN_CAP) {
      __builtin_amdgcn_s_sleep(4);
      b1 = ld8c_sync(0u, BC);
    }
    E = __builtin_amdgcn_readfirstlane(b1[0]);
  };
  auto signal_a = [&]() {
    v4u r0 = *(LAS v4u*)(smem + L_RED), r1 = *(LAS v4u*)(smem + L_RED + 16);
    u32 m = max(max(max(r0[0], r0[1]), max(r0[2], r0[3])), max(max(r1[0], r1[1]), max(r1[2], r1[3])));
    if (l == 0) { st4c((u32)(FL_A + b * 4), FL, E | (m >> 7)); *(LAS u32*)(smem + L_MX + 4) = E; }
  };
  auto poll4 = [&](v4u f, u32 voff) -> v4u {
    int spins = 0;
    while (!__all((((f[0] ^ E) | (f[1] ^ E) | (f[2] ^ E) | (f[3] ^ E)) >> 8) == 0) && ++spins < SPIN_CAP) {
      do_tick();
      __builtin_amdgcn_s_sleep(12);
      f = ld16c_sync(voff, FL);
    }
    return f;
  };
  auto check_a = [&](v4u f) {
    f = poll4(f, (u32)(FL_A + l * 16));
    u32 m = wave_max(max(max(f[0], f[1]), max(f[2], f[3]))) & 0xffu;
    if (l == 0) *(LAS u32*)(smem + L_MX) = m;
  };
  if (coord) {
#pragma unroll
    for (int i = 0; i < 5; ++i) dma2c(i ? (u32)(FL_H + ((i - 1) * 128 + l) * 64) : (u32)(l * 16), i ? (u32)(FL_H + ((i - 1) * 128 + 64 + l) * 64 - 1024) : (u32)(l * 16), FL, lbase + i * 2048);
  }
  u32 sq[8];
  if (!ffn && w == 7) {
    const int kss = (sp >> 1) + 16 * (l >> 2) + 256 * (4 * (sp & 1) + (l & 3)), km = (2048 - kss) & 2047;
    const u32 o1 = (u32)(kss * DF_ * 2), o2 = (u32)(km * DF_ * 2);
    ld2(sq[0], o1, (const char*)wr); ld2(sq[1], o1, (const char*)wi); ld2(sq[2], o2, (const char*)wr); ld2(sq[3], o2, (const char*)wi);
    ld2(sq[4], o1 + 512, (const char*)wr); ld2(sq[5], o1 + 512, (const char*)wi); ld2(sq[6], o2 + 512, (const char*)wr); ld2(sq[7], o2 + 512, (const char*)wi);
  }

  {
    const float fo = (float)o, fg = (float)(4 * g);
    if (w < 2) {
      const float st = fo * (1.0f / 32.0f), a0 = fmaf(fg, st, w ? 0.25f : 0.f);
      v4u hi, lo;
#pragma unroll
      for (int e2 = 0; e2 < 4; ++e2) {
        float c0 = cosr(fmaf((float)(16 * (e2 >> 1) + 2 * (e2 & 1)), st, a0)), c1 = cosr(fmaf((float)(16 * (e2 >> 1) + 2 * (e2 & 1) + 1), st, a0));
        u32 ch = pkbf_o(c0, c1);
        hi[e2] = ch; lo[e2] = pkbf(c0 - bflo(ch), c1 - bfhi(ch));
      }
      *(LAS v4u*)(smem + L_C1 + w * 2048 + l * 16) = hi; *(LAS v4u*)(smem + L_C1 + w * 2048 + 1024 + l * 16) = lo;
    } else if (w < 5) {
      const float st = w == 2 ? (float)(o & 7) * (1.0f / 16.0f) : fo * (w == 3 ? 1.0f / 16.0f : 1.0f / 8.0f);
      const float a0 = (w == 2 && o >= 8) ? 0.25f : 0.f;
      const float sc = w == 2 ? 0.0625f : ((w == 4 && (g >> 1) != (o >> 3)) ? 0.f : 1.0f);
      v8h m;
#pragma unroll
      for (int e = 0; e < 4; ++e) { float a = fmaf(fg + (float)e, st, a0); m[e] = (_Float16)(sc * cosr(a)); m[4 + e] = (_Float16)(sc * sinr(a)); }
      *(LAS v8h*)(smem + (w == 2 ? L_C1 + 4096 : L_TAB + (w - 3) * 1024) + l * 16) = m;
    } else {
      const float st = w == 7 ? fo * (1.0f / 256.0f) : (fo + (w == 6 ? 16.f : 0.f)) * (1.0f / 512.0f);
      v4f c, sn;
#pragma unroll
      for (int e = 0; e < 4; ++e) { float a = (fg + (float)e) * st; c[e] = cosr(a); sn[e] = sinr(a); }
      LAS char* dst = smem + (w == 7 ? L_TAB + 2048 : L_C1 + 5120 + (w - 5) * 2048) + l * 16;
      *(LAS v4f*)dst = c; *(LAS v4f*)(dst + 1024) = sn;
    }
    BARRIER();
    if (ffn) filt_dma();
    LAS char* C1 = smem + L_C1 + l * 16;
    const v4u Fhr = *(LAS v4u*)C1, Flr = *(LAS v4u*)(C1 + 1024), Fhi = *(LAS v4u*)(C1 + 2048), Fli = *(LAS v4u*)(C1 + 3072);
    const v8h M2 = *(LAS v8h*)(C1 + 4096);
    v4f twc[2], tws[2];
#pragma unroll
    for (int u = 0; u < 2; ++u) { twc[u] = *(LAS v4f*)(C1 + 5120 + u * 2048); tws[u] = *(LAS v4f*)(C1 + 6144 + u * 2048); }
    if (ffn) WAIT_VM(8); else if (w == 7) WAIT_VM(11); else WAIT_VM(3);
    if (coord) {
      WAIT_VM(0);
      v4u f[10];
#pragma unroll
      for (int i = 0; i < 10; ++i) f[i] = *(LAS v4u*)(smem + i * 1024 + l * 16);
      u32 c = __builtin_amdgcn_readfirstlane(*(LAS u32*)(smem + L_BC0)) >> 8;
      bool hit;
      do {
        c = (c + 1) & 0xffffffu; hit = false;
#pragma unroll
        for (int i = 0; i < 10; ++i) hit |= ((f[i][0] >> 8) == c) | ((f[i][1] >> 8) == c) | ((f[i][2] >> 8) == c) | ((f[i][3] >> 8) == c);
      } while (__any(hit));
      E = c << 8;
      do_tick();
    }
    PIN(xr);
    STAMPT(3);
    const float c0 = bflo(__builtin_amdgcn_readfirstlane(xr[0]));
    v2f xv[4], s2 = {0.f, 0.f}, q2 = {0.f, 0.f};
#pragma unroll
    for (int e = 0; e < 4; ++e) {
      xv[e] = v2f{bflo(xr[e]), bfhi(xr[e])};
      v2f t = xv[e] - v2f{c0, c0};
      s2 += t; q2 = __builtin_elementwise_fma(t, t, q2);
    }
    const float dmean = wave_sum(s2[0] + s2[1]) * (1.0f / 512.0f), msq = wave_sum(q2[0] + q2[1]) * (1.0f / 512.0f);
    do_tick();
    const float mb = rbf(c0 + dmean);
    const float var = rbf(fmaxf(msq - dmean * dmean, 0.f));
    const float sd = rbf(__builtin_amdgcn_sqrtf(rbf(var + 1e-5f)));
    const float rs = __builtin_amdgcn_rcpf(sd);
#pragma unroll
    for (int e = 0; e < 4; ++e) {
      v2f t = xv[e] - v2f{mb, mb};
      u32 tb = pkbf_o(t[0], t[1]);
      t = v2f{bflo(tb), bfhi(tb)} * v2f{rs, rs};
      xr[e] = pkbf(t[0], t[1]);
    }
    *(LAS v4u*)(smem + L_IMG + w * 1024 + l * 16) = xr;
    if (l < 2) st4c((u32)(pi * 4 + l * 8192), ws + OFF_MB, l ? rs * rs : -mb);
    const v8s xf = cat8(trread(smem + L_IMG + w * 1024 + l * 8), trread(smem + L_IMG + w * 1024 + 512 + l * 8));
    const int kt1 = (1 + rot) & 7;
    const u32 dst1 = lbase + 49152 + w * 2048;
    if (ffn) dma2(aoff[0], aoff[1], (const char*)x + kt1 * 128, dst1);
    const v8s xg_ = __builtin_bit_cast(v8s, __builtin_bit_cast(v4u, xf) ^ v4u{0x80000000u, 0x80000000u, 0x80000000u, 0x80000000u});
    const v4f z = {0.f, 0.f, 0.f, 0.f};
    v4f dre[2], dim[2];
#pragma unroll
    for (int u = 0; u < 2; ++u) { dre[u] = mfma16b(u ? xg_ : xf, Flr, z); dim[u] = mfma16b(u ? xg_ : xf, Fli, z); }
#pragma unroll
    for (int u = 0; u < 2; ++u) { dre[u] = mfma16b(u ? xg_ : xf, Fhr, dre[u]); dim[u] = mfma16b(u ? xg_ : xf, Fhi, dim[u]); }
#pragma unroll
    for (int u = 0; u < 2; ++u) {
      twid(dre[u], dim[u], twc[u], tws[u]);
      const v8h bb = pack_ri(dre[u], dim[u]);
      v4f d2 = mfma16h(bb, M2, z);
      if (u == 0) {
        const u32 nv = o == 8 ? 0xac002c00u : 0u;
        v4f n = mfma16h(bb, __builtin_bit_cast(v8h, v4u{nv, nv, 0u, 0u}), z);
        if (l == 8) d2[0] = n[0];
      }
      *(LAS v4h*)(smem + L_T + ((o >> 3) * 8 + w) * 544 + (o & 7) * 64 + u * 32 + g * 8) = pack4h(d2);
    }
    if (ffn) {
      dma2(boff, boff1, (const char*)Wg + (size_t)kt1 * (64 * F_ * 2), dst1 + 16384);
      dma2(boff, boff1, (const char*)Wu + (size_t)kt1 * (64 * F_ * 2), dst1 + 32768);
    }
    STAMPT(4);
  }
  v8s su; v4u fu, tu;
  auto round1_read = [&]() {
    LAS char* rp = smem + L_T + (4 * (g & 1) + (o >> 2)) * 544 + (w * 32 + (g >> 1) * 16) * 2 + (o & 3) * 8;
    su = cat8(trread(rp), trread(rp + 8 * 544));
    LAS char* fp_ = smem + L_FR + w * 2048 + q * 128 + (((2 * g + (pp >> 1)) ^ ((q >> 1) << 2)) << 4) + (pp & 1) * 8;
    fu = __builtin_bit_cast(v4u, cat8(trread(fp_), trread(fp_ + 512)));
    LAS char* tp = smem + L_FR + w * 2048 + 1024 + q * 128 + (pp >> 1) * 16 + (pp & 1) * 8;
    tu = __builtin_bit_cast(v4u, cat8(trread(tp), trread(tp + 512)));
  };
  auto round1_max = [&]() {
    typedef unsigned short v2us __attribute__((ext_vector_type(2)));
    const int dl = (idx >> 1) & 7;
    const u32 msk = (fcq == 0 && l < dl) ? 0u : 0x7fff7fffu, msk2 = (fcq == 3 && o <= dl) ? 0x7fff7fffu : 0u;
    v2us m2 = {0, 0};
#pragma unroll
    for (int e = 0; e < 4; ++e) {
      u32 t = fu[e] & msk, t2 = tu[e] & msk2;
      m2 = __builtin_elementwise_max(m2, __builtin_elementwise_max(__builtin_bit_cast(v2us, t), __builtin_bit_cast(v2us, t2)));
    }
    const u32 mx = wave_max(max((u32)m2[0], (u32)m2[1]));
    if (l == 0) *(LAS u32*)(smem + L_RED + w * 4) = mx;
  };
  auto round1_store = [&]() {
    st16c((u32)(b * S1P + (w * 32 + (g >> 1) * 16 + o) * 32 + (g & 1) * 16), ws + OFF_S1, su);
    st16c((u32)((idx * 16 + fkb) * WTP + (fcq * 64 + l) * 16), ws + OFF_WT, fu);
    if (fcq == 3 && l < 16) st16c((u32)((idx * 16 + fkb) * WTP + (256 + l) * 16), ws + OFF_WT, tu);
  };

  const int RG = xcd * 4 + (idx >> 3), ct = idx & 7;
  const int kd = xcd * 32 + ((idx + 3) & 31);
  const u32 swz = ((l & 7) ^ (((l >> 4) & 1) << 2)) << 4;
  const u32 ro0 = (u32)((l >> 3) * 1024) + swz, ro1 = ro0 + 8192 - 1024;
  const char* bsrc = (const char*)Wd + (size_t)(w * 16) * 1024 + ct * 128;
  auto issueW = [&](int n) { dma2(ro0, ro1, bsrc + (size_t)n * 131072, lbase + wslot(n) + w * 2048); };
  auto pfl = [&](int n) {
    const char* src = (const char*)Wd + (size_t)n * 131072 + ((idx & 3) * 8 + w) * 4096;
    dma1<0>((u32)((l >> 5) * 1024 + (l & 31) * 16), src, lbase + L_FA); dma1<0>((u32)((l >> 5) * 1024 + (l & 31) * 16), src + 2048, lbase + L_FA);
  };
  const int dh = w & 1, kq = w >> 1, ph = kq >> 1, kh = kq & 1;
  const int epi = RG * 64 + ph * 32 + (l & 31);
  const u32 eoff = (u32)(s_of_pi(epi) * D_ + ct * 64 + dh * 32 + kh * 16 + h * 8) * 2;
  v4u wt, s1v, xe; v4f pq[4]; float rs2;
  auto load_xe = [&]() { ld16(xe, eoff, (const char*)x); };
  auto load_s1 = [&]() { ld16c(s1v, (u32)(((4 * w + g) * 8 + (o >> 1)) * S1P + kd * 32 + (o & 1) * 16), ws + OFF_S1); };
  auto load_wt = [&]() { ld16c(wt, (u32)((w * 64 + l) * WTP + (kd + ((2 * w + (g >> 1)) & 7)) * 16), ws + OFF_WT); };
  auto load_rs = [&]() { ld4c(rs2, (u32)(epi * 4), ws + OFF_RS2); };
  v4f t2c, t2s, t3c, t3s;
  auto trig2 = [&]() {
    const float fw = (float)w, st2 = fw * (1.0f / 128.0f), a2 = fw * (float)(o + 64 * g) * (1.0f / 2048.0f);
#pragma unroll
    for (int e = 0; e < 4; ++e) { float b2 = fmaf((float)e, st2, a2); t2c[e] = cosr(b2); t2s[e] = sinr(b2); }
    PIN(t2c); PIN(t2s);
  };
  auto trig3 = [&]() {
    const float f7 = (float)(o & 7), st3 = f7 * (1.0f / 128.0f), a3 = f7 * (float)(2 * w + (o >> 3) + 64 * g) * (1.0f / 2048.0f);
#pragma unroll
    for (int e = 0; e < 4; ++e) { float b3 = fmaf((float)e, st3, a3); t3c[e] = cosr(b3); t3s[e] = sinr(b3); }
    PIN(t3c); PIN(t3s);
  };
  v4u f16; v4f t1c, t1s, re, im;
  const v4f z4 = {0.f, 0.f, 0.f, 0.f};
  auto imf = [](v4u f) { return __builtin_bit_cast(v8h, v4u{f[2] ^ 0x80008000u, f[3] ^ 0x80008000u, f[0], f[1]}); };
  auto segA0 = [&]() {
    f16 = *(LAS v4u*)(smem + L_TAB + l * 16);
    t1c = *(LAS v4f*)(smem + L_TAB + 2048 + l * 16); t1s = *(LAS v4f*)(smem + L_TAB + 3072 + l * 16);
  };
  auto segA1 = [&]() {
    PIN(s1v);
    const v8h a = __builtin_bit_cast(v8h, s1v);
    re = mfma16h(a, __builtin_bit_cast(v8h, f16), z4); im = mfma16h(a, imf(f16), z4);
  };
  auto segA2 = [&]() {
    twid(re, im, t1c, t1s);
    const v8h a = pack_ri(re, im);
    re = mfma16h(__builtin_bit_cast(v8h, f16), a, z4); im = mfma16h(imf(f16), a, z4);
  };
  auto segA3 = [&]() {
    twid(re, im, t2c, t2s);
    *(LAS v4h*)(smem + L_E1 + w * E1P + o * 32 + g * 8) = pack4h(re);
    *(LAS v4h*)(smem + L_E1 + (8 + w) * E1P + o * 32 + g * 8) = pack4h(im);
  };

  if (ffn) {
    v16f acc[2][2] = {};
    float mbr[2];
    const u32 wb0 = (u32)((8 * h + q) * 256 + ((fq ^ q) << 6) + blk * 32 + pp * 8);
    const u32 r1 = 16384 + sh * 16384, r2 = 32768 - sh * 16384;
    const int xrow = sh * 64 + (l & 31);
    const u32 xb0 = (u32)(xrow * 128 + ((h ^ ((xrow >> 1) & 7)) << 4));
    auto rd = [&](Frag1& f, LAS char* sb, int i) {
      LAS char* xa = sb + (xb0 ^ (i << 5));
      LAS char* wa_ = sb + wb0 + (i << 12);
      f.w1 = cat8(trread(wa_ + r1), trread(wa_ + r1 + 1024));
      f.x0 = *(LAS v8s*)xa; f.x1 = *(LAS v8s*)(xa + 4096);
      f.w2 = cat8(trread(wa_ + r2), trread(wa_ + r2 + 1024));
    };
    const u32 one = ((l & 15) == ((l >> 4) & 1)) ? 0x3f803f80u : 0u;
    const v8s sel = __builtin_bit_cast(v8s, v4u{one, one, one, one});
    v4f csa = {0.f, 0.f, 0.f, 0.f};
    Frag1 A, B;
    WAIT_VM(7);
    BARRIER();
    STAMPT(1); STAMPC(30);
    rd(A, smem, 0);
    round1_read();
    dma2(aoff[0], aoff[1], (const char*)x + ((2 + rot) & 7) * 128, lbase + 2 * 49152 + w * 2048);
    u32 c1[4], c2[4];
    static_for<0, 8>([&](auto T) {
      constexpr int t = T, slot = t % 3, nslot = (t + 1) % 3, pslot = (t + 2) % 3;
      LAS char* sb = smem + slot * 49152;
      {
        const int kt = (t + 2 + rot) & 7;
        const char* s1 = t < 6 ? (const char*)Wg + (size_t)kt * (64 * F_ * 2) : bsrc + (size_t)((t - 6) * 3 + 1) * 131072;
        const char* s2 = t < 6 ? (const char*)Wu + (size_t)kt * (64 * F_ * 2) : bsrc + (size_t)((t - 6) * 3 + 2) * 131072;
        const u32 o2 = t < 6 ? boff : ro0, o3 = t < 6 ? boff1 : ro1;
        const u32 dst = lbase + pslot * 49152 + w * 2048;
        rd(B, sb, 1); SCHED();
        acc[0][0] = mfma32b(A.w1, A.x0, acc[0][0]); acc[0][1] = mfma32b(A.w1, A.x1, acc[0][1]);
        if constexpr (t == 0) csa = mfma16b(sel, __builtin_bit_cast(v4u, A.w1), csa);
        SCHED();
        dma1<0>(o2, s1, dst + 16384);
        if constexpr (t == 2) load_xe();
        if constexpr (t == 7) load_wt();
        acc[1][0] = mfma32b(A.w2, A.x0, acc[1][0]); acc[1][1] = mfma32b(A.w2, A.x1, acc[1][1]); SCHED();
        if constexpr (t == 0) { round1_store(); round1_max(); SCHED(); }
        if constexpr (t == 3) { trig2(); SCHED(); }
        if constexpr (t == 4) { trig3(); SCHED(); }
        rd(A, sb, 2); SCHED();
        acc[0][0] = mfma32b(B.w1, B.x0, acc[0][0]); SCHED();
        dma1<1024>(o3, s1, dst + 16384);
        acc[0][1] = mfma32b(B.w1, B.x1, acc[0][1]);
        if constexpr (t == 0) csa = mfma16b(sel, __builtin_bit_cast(v4u, B.w1), csa);
        acc[1][0] = mfma32b(B.w2, B.x0, acc[1][0]); acc[1][1] = mfma32b(B.w2, B.x1, acc[1][1]); SCHED();
        dma1<0>(o2, s2, dst + 32768);
        if constexpr (t == 6) {
          load_rs();
        }
        if constexpr (t == 7) { WAIT_VM(13); segA1(); SCHED(); }
        rd(B, sb, 3); SCHED();
        if constexpr (t == 7) {
#pragma unroll
          for (int i = 0; i < 4; ++i) { PIN(c1[i]); PIN(c2[i]); }
          PIN(mbr[0]); PIN(mbr[1]);
          const v8s cs1 = __builtin_bit_cast(v8s, v4u{c1[0], c1[1], c1[2], c1[3]}), cs2 = __builtin_bit_cast(v8s, v4u{c2[0], c2[1], c2[2], c2[3]});
          const u32 m0_ = pkbf(mbr[0], mbr[0]), m1_ = pkbf(mbr[1], mbr[1]);
          const v8s mb0 = __builtin_bit_cast(v8s, v4u{m0_, m0_, m0_, m0_}), mb1 = __builtin_bit_cast(v8s, v4u{m1_, m1_, m1_, m1_});
          acc[0][0] = mfma32b(cs1, mb0, acc[0][0]); acc[1][0] = mfma32b(cs2, mb0, acc[1][0]);
          acc[0][1] = mfma32b(cs1, mb1, acc[0][1]); acc[1][1] = mfma32b(cs2, mb1, acc[1][1]); SCHED();
          segA2(); SCHED();
        }
        acc[0][0] = mfma32b(A.w1, A.x0, acc[0][0]); acc[0][1] = mfma32b(A.w1, A.x1, acc[0][1]);
        if constexpr (t == 0) csa = mfma16b(sel, __builtin_bit_cast(v4u, A.w1), csa);
        SCHED();
        dma1<1024>(o3, s2, dst + 32768);
        acc[1][0] = mfma32b(A.w2, A.x0, acc[1][0]); acc[1][1] = mfma32b(A.w2, A.x1, acc[1][1]); SCHED();
      }
      if constexpr (t < 7) {
        const char* s0 = t < 5 ? (const char*)x + ((t + 3 + rot) & 7) * 128 : bsrc + (size_t)((t - 5) * 3) * 131072;
        const u32 o0 = t < 5 ? aoff[0] : ro0, o1 = t < 5 ? aoff[1] : ro1;
        const u32 dst = lbase + slot * 49152 + w * 2048;
        if constexpr (t == 0) csa = mfma16b(sel, __builtin_bit_cast(v4u, B.w1), csa);
        WAIT_VM(t == 0 ? 8 : t == 2 ? 7 : t == 6 ? 18 : 6);
        if constexpr (t == 5) { if (w == 0) check_a(*(LAS v4u*)(smem + L_FA + l * 16)); }
        BARRIER();
        STAMPT(22 + t);
        acc[0][0] = mfma32b(B.w1, B.x0, acc[0][0]); SCHED();
        rd(A, smem + nslot * 49152, 0);
        if constexpr (t == 6) segA0();
        SCHED();
        if constexpr (t == 0) {
          if (l < 16) {
#pragma unroll
            for (int i = 0; i < 2; ++i) {
              const float hi = rbf(csa[i]);
              st4c((u32)(((j * 16 + p) * 256 + sh * 128 + fq * 32 + i * 16 + l) * 4), ws + OFF_CSP, pkbf(hi, csa[i] - hi));
            }
          }
        }
        if constexpr (t == 0) { if (w == 0) dma1c((u32)((l & 1) * 4), BC, lbase + L_BC1); }
        if constexpr (t == 4) { if (w == 0) dma1s<0>((u32)(FL_A + l * 16), FL, lbase + L_FA); }
        if constexpr (t == 5) {
          const u32 co = (u32)(((j * 16 + (p & 1) + 8 * h) * 256 + fq * 32 + (l & 31)) * 4);
#pragma unroll
          for (int i = 0; i < 4; ++i) { ld4c(c1[i], co + sh * 512 + i * 2048, ws + OFF_CSP); ld4c(c2[i], co + (1 - sh) * 512 + i * 2048, ws + OFF_CSP); }
          ld4c(mbr[0], (u32)((p * 128 + xrow) * 4), ws + OFF_MB); ld4c(mbr[1], (u32)((p * 128 + xrow + 32) * 4), ws + OFF_MB);
          load_s1();
        }
        dma1<0>(o0, s0, dst);
        acc[0][1] = mfma32b(B.w1, B.x1, acc[0][1]);
        acc[1][0] = mfma32b(B.w2, B.x0, acc[1][0]);
        acc[1][1] = mfma32b(B.w2, B.x1, acc[1][1]); SCHED();
        dma1<1024>(o1, s0, dst);
        if constexpr (t == 1) { if (w == 0) { get_epoch(); signal_a(); } }
        SCHED();
      } else {
        acc[0][0] = mfma32b(B.w1, B.x0, acc[0][0]); acc[1][0] = mfma32b(B.w2, B.x0, acc[1][0]); SCHED();
        acc[0][1] = mfma32b(B.w1, B.x1, acc[0][1]); acc[1][1] = mfma32b(B.w2, B.x1, acc[1][1]); SCHED();
      }
    });
    STAMPT(2); STAMPC(31);
    auto fin = [&](const v16f& ag, const v16f& au, int rt) {
#pragma unroll
      for (int s2i = 0; s2i < 2; ++s2i) {
        v4u ov;
#pragma unroll
        for (int e = 0; e < 4; ++e) {
          const int r = s2i * 8 + 2 * e;
          v2f gg = v2f{relu(ag[r]), relu(ag[r + 1])} * v2f{au[r], au[r + 1]};
          ov[e] = pkbf(gg[0], gg[1]);
        }
        st16c((u32)(((p * 4 + sh * 2 + rt) * 112 + (j * 8 + fq * 2 + s2i)) * 1024 + l * 16), ws + OFF_H, ov);
      }
    };
    if (sh == 0) { fin(acc[0][0], acc[1][0], 0); fin(acc[0][1], acc[1][1], 1); }
    else { fin(acc[1][0], acc[0][0], 0); fin(acc[1][1], acc[0][1], 1); }
    STAMPT(6);
    segA3();
    WAIT_VM(4);
  } else {
    WAIT_VM(1);
    BARRIER();
    round1_read(); round1_store(); round1_max();
    if (w == 0 && !coord) dma1c((u32)((l & 1) * 4), BC, lbase + L_BC1);
    do_tick();
    if (w == 7) {
#pragma unroll
      for (int i = 0; i < 8; ++i) PIN(sq[i]);
      float war = bflo(sq[0]) + bflo(sq[2]), wai = bflo(sq[1]) - bflo(sq[3]);
      float wbr = bflo(sq[4]) + bflo(sq[6]), wbi = bflo(sq[5]) - bflo(sq[7]);
      v4f pv = {0.25f * (war + wbr), 0.25f * (wai + wbi), 0.25f * (war - wbr), 0.25f * (wai - wbi)};
      st16c((u32)((((sp >> 2) * 4 + (l & 3)) * 64 + (sp & 3) * 16 + (l >> 2)) * 16), ws + OFF_PQ, pv);
    }
    WAIT_VM(0);
    BARRIER();
    STAMPT(1); STAMPC(30);
    if (w == 0) { if (!coord) get_epoch(); signal_a(); do_tick(); WAIT_VM(0); }
    BARRIER();
    STAMPT(24);
    dma1c((u32)(sp * 57344 + (w * 112 + l) * 64), (const char*)Wd, lbase + 49152);
    dma1c((u32)(sp * 57344 + (w * 112 + 48 + l) * 64), (const char*)Wd, lbase + 49152);
    static_for<0, 6>([&](auto N) { pfl(N); });
    static_for<0, 6>([&](auto N) { issueW(N); });
    load_xe();
    trig2(); trig3(); segA0();
    STAMPT(25);
    if (w == 0) {
      check_a(v4u{~E, 0u, 0u, 0u});
    }
    STAMPT(26);
    BARRIER();
    load_s1(); load_wt(); load_rs();
    if (kd == 0) {
#pragma unroll
      for (int r = 0; r < 4; ++r) ld16c(pq[r], (u32)(w * 4096 + r * 1024 + l * 16), ws + OFF_PQ);
      pf_code();
      WAIT_VM(1);
    } else {
      pf_code();
      static_for<6, 14>([&](auto N) { pfl(N); });
      WAIT_VM(17);
    }
    segA1(); segA2(); segA3();
    STAMPT(2); STAMPC(31);
  }
  PIN(wt); PIN(xe); PIN(rs2);
  BARRIER();
  STAMPT(5);

  const u32 trG = (u32)(kq * 4096 + (4 * h + q) * 128 + ((dh ^ ((q >> 1) & 1)) << 6) + (blk ^ kh) * 32 + pp * 8);
  v8s Wf[14][2];
  auto readW = [&](auto N) {
    constexpr int n = N;
    LAS char* sb = smem + wslot(n) + trG;
    Wf[n][0] = cat8(trread(sb), trread(sb + 1024)); Wf[n][1] = cat8(trread(sb + 2048), trread(sb + 3072));
  };
  const char* hsrc = ws + OFF_H + ((size_t)(RG * 2 + (w >> 2)) * 112 + (w & 3) * 2) * 1024;
  const u32 so0 = (u32)((l >> 3) * S2P) + swz, so1 = so0 + 8 * S2P - 1024;
  const char* ssrc = ws + OFF_S2 + (size_t)(w * 16) * S2P + RG * 128;
  auto issueP = [&](int n, auto HALF) {
    constexpr int hf = HALF;
    if (n < 14) dma1s<hf * 1024>((u32)(l * 16), hsrc + n * 8192, lbase + aslot(n) + w * 2048);
    else dma1s<hf * 1024>(hf ? so1 : so0, ssrc + (size_t)(n - 14) * (128 * S2P), lbase + aslot(n) + w * 2048);
  };
  const int d = ct * 64 + dh * 32 + ((l & 31) ^ (kh << 4));
  const float gst = (float)d * (1.0f / 512.0f), ga0 = (float)(16 * kq + 2 * h) * gst;
  v8h G[8];
  auto gen = [&](auto FI) {
    constexpr int f = FI;
#pragma unroll
    for (int e2 = 0; e2 < 4; ++e2) {
      float a = fmaf((float)(64 * (f >> 1) + 8 * (f & 1) + 4 * (e2 >> 1) + (e2 & 1)), gst, ga0);
      float c = cosr(a), sn = sinr(a);
      if (f == 0 && e2 == 0) { if (kq == 0 && h == 0) { c = 0.5f; sn = (d & 1) ? 0.5f : -0.5f; } }
      G[f][2 * e2] = (_Float16)c; G[f][2 * e2 + 1] = (_Float16)sn;
    }
    PIN(G[f]);
  };

  float oscale;
  const u32 fhoff = (u32)(FL_H + (RG * 16 + (min(l, 55) >> 2)) * 64 + (l & 3) * 4);
  u32 fp;
  {
    v8h a;
    {
      LAS char* rp = smem + L_E1 + (4 * (g & 1) + (o >> 2)) * E1P + (2 * w + (g >> 1)) * 32 + (o & 3) * 8;
      a = __builtin_bit_cast(v8h, cat8(trread(rp), trread(rp + 8 * E1P)));
    }
    const v4u f8 = *(LAS v4u*)(smem + L_TAB + 1024 + l * 16);
    const v2u me = *(LAS v2u*)(smem + L_MX);
    issueW(6);
    static_for<0, 3>(readW);
    const int Ex = (int)me[0] < 16 ? 16 : ((int)me[0] > 240 ? 240 : (int)me[0]);
    const float wscale = __builtin_bit_cast(float, (u32)(246 - Ex) << 23);
    oscale = __builtin_bit_cast(float, (u32)(Ex - 7) << 23);
    re = mfma16h(__builtin_bit_cast(v8h, f8), a, z4); im = mfma16h(imf(f8), a, z4);
    static_for<3, 6>(readW);
    WAIT_VM(2);
    STAMPT(7);
    if (ffn && l == 0) st4c((u32)(FL_H + ((p * 2 + sh) * 16 + j) * 64 + fq * 4), FL, me[1]);
    issueW(7);
    if (kd != 0) {
#pragma unroll
      for (int r = 0; r < 4; ++r) {
        float fr = ((r & 1) ? bfhi(wt[r >> 1]) : bflo(wt[r >> 1])) * wscale, fi = ((r & 1) ? bfhi(wt[2 + (r >> 1)]) : bflo(wt[2 + (r >> 1)])) * wscale;
        float zr = re[r], zi = im[r];
        re[r] = zr * fr - zi * fi;
        im[r] = -(zr * fi + zi * fr);
      }
    } else {
      const int ka = 2 * w + (g >> 1), kr = o + 64 * (g & 1);
#pragma unroll
      for (int r = 0; r < 4; ++r) { v2f t = {re[r], im[r]}; *(LAS v2f*)(smem + L_ZX + ka * 1024 + (kr + 16 * r) * 8) = t; }
      BARRIER();
#pragma unroll
      for (int r = 0; r < 4; ++r) {
        PIN(pq[r]);
        const int km = (2048 - (ka + 16 * (kr + 16 * r))) & 2047;
        v2f zm = *(LAS v2f*)(smem + L_ZX + (km & 15) * 1024 + (km >> 4) * 8);
        float zr = re[r], zi = im[r];
        re[r] = (zr * pq[r][0] - zi * pq[r][1] + zm[0] * pq[r][2] + zm[1] * pq[r][3]) * wscale;
        im[r] = -(zr * pq[r][1] + zi * pq[r][0] + zm[0] * pq[r][3] - zm[1] * pq[r][2]) * wscale;
      }
    }
    LAS char* E2 = smem + (kd == 0 ? L_E1 : L_E2);
    a = pack_ri(re, im);
    re = mfma16h(a, __builtin_bit_cast(v8h, f8), z4); im = mfma16h(a, imf(f8), z4);
    twid(re, im, t3c, t3s);
    *(LAS v4h*)(E2 + (o & 7) * E2P + (2 * w + (o >> 3)) * 32 + g * 8) = pack4h(re);
    *(LAS v4h*)(E2 + (o & 7) * E2P + 512 + (2 * w + (o >> 3)) * 32 + g * 8) = pack4h(im);
    BARRIER();
    STAMPT(8);
    {
      LAS char* rp = E2 + w * E2P + o * 32 + g * 8;
      a = __builtin_bit_cast(v8h, cat8(*(LAS v4s*)rp, *(LAS v4s*)(rp + 512)));
    }
    issueW(8);
    re = mfma16h(a, __builtin_bit_cast(v8h, f16), z4); im = mfma16h(a, imf(f16), z4);
    issueW(9);
    twid(re, im, t1c, t1s);
    a = pack_ri(re, im);
    re = mfma16h(__builtin_bit_cast(v8h, f16), a, z4); im = mfma16h(imf(f16), a, z4);
    issueW(10);
    st8c((u32)(2 * kd * S2P + w * 512 + l * 8), ws + OFF_S2, pack4h(re));
    st8c((u32)((2 * kd + 1) * S2P + w * 512 + l * 8), ws + OFF_S2, pack4h(im));
  }
  STAMPT(9);
  gen(IC<0>{}); issueW(11);
  WAIT_VM(8);
  BARRIER();
  static_for<6, 9>(readW);
  gen(IC<1>{}); issueW(12); gen(IC<2>{}); issueW(13);
  if (!ffn) {
    WAIT_VM(0);
    BARRIER();
    if (tid == 0) st4c((u32)(FL_S + b * 4), FL, E);
  }
  if (w == 0) ld4c(fp, fhoff, FL);
  gen(IC<3>{}); gen(IC<4>{});
  WAIT_VM(0);
  if (w == 0) {
    PIN(fp);
    u32 f = fp;
    int spins = 0;
    while (!__all(f == E) && ++spins < SPIN_CAP) {
      __builtin_amdgcn_s_sleep(4);
      f = ld4c_sync(fhoff, FL);
    }
  }
  BARRIER();
  STAMPT(10);
  if (ffn && tid == 0) st4c((u32)(FL_S + b * 4), FL, E);
  issueP(0, IC<0>{}); issueP(0, IC<1>{}); issueP(1, IC<0>{}); issueP(1, IC<1>{});
  static_for<9, 14>(readW);
  gen(IC<5>{});
  issueP(2, IC<0>{}); issueP(2, IC<1>{});
  gen(IC<6>{});
  issueP(3, IC<0>{}); issueP(3, IC<1>{});
  gen(IC<7>{});

  const u32 trS = (u32)(kq * 4096 + (4 * h + q) * 128 + ((ph ^ ((q >> 1) & 1)) << 6) + blk * 32 + pp * 8);
  const u32 hb = (u32)(ph * 8192 + kq * 2048 + l * 16);
  v16f accG[2] = {}, accS[2] = {};
  v8s fa[8], fb[8];
  v4u fs;
  float gv[8];
  const u32 xw = (u32)(w * 6144 + l * 16);
  auto xchg_out = [&](const v16f (&ac)[2], int base) {
    LAS char* X = smem + base + xw;
#pragma unroll
    for (int i = 0; i < 6; ++i) {
      const v16f& a = ac[(i + 2) >> 2]; const int r = ((i + 2) & 3) * 4;
      *(LAS v4f*)(X + i * 1024) = v4f{a[r], a[r + 1], a[r + 2], a[r + 3]};
    }
  };
  auto xchg_in = [&](const v16f (&ac)[2], int base, float (&o8)[8]) {
    v4f s0 = {ac[0][0], ac[0][1], ac[0][2], ac[0][3]}, s1 = {ac[0][4], ac[0][5], ac[0][6], ac[0][7]};
#pragma unroll
    for (int x_ = 1; x_ < 4; ++x_) {
      LAS char* X = smem + base + (w ^ (2 * x_)) * 6144 + (x_ - 1) * 2048 + l * 16;
      s0 += *(LAS v4f*)X; s1 += *(LAS v4f*)(X + 1024);
    }
#pragma unroll
    for (int e = 0; e < 4; ++e) { o8[e] = s0[e]; o8[4 + e] = s1[e]; }
  };
  v4f xg[6];
  float ov[8], ex[8];
  auto sadd = [](float a, float b) { float r; asm("v_add_f32 %0, %1, %2" : "=v"(r) : "v"(a), "v"(b)); return r; };
  auto smul = [](float a, float b) { float r; asm("v_mul_f32 %0, %1, %2" : "=v"(r) : "v"(a), "v"(b)); return r; };
  auto silu_step = [&](int i) {
    if (i < 3) {
#pragma unroll
      for (int e = 0; e < 8; ++e) { ov[e] = sadd(i == 0 ? accG[0][e] : ov[e], xg[2 * i + (e >> 2)][e & 3]); PIN(ov[e]); }
    } else if (i == 3) {
#pragma unroll
      for (int e = 0; e < 8; e += 2) { u32 t = pkbf_o(smul(ov[e], rs2), smul(ov[e + 1], rs2)); ov[e] = bflo(t); ov[e + 1] = bfhi(t); PIN(ov[e]); PIN(ov[e + 1]); }
    } else if (i == 4) {
#pragma unroll
      for (int e = 0; e < 8; ++e) { ex[e] = __builtin_amdgcn_exp2f(ov[e] * -1.44269504088896f); PIN(ex[e]); }
    } else if (i == 5) {
#pragma unroll
      for (int e = 0; e < 8; ++e) { ex[e] = __builtin_amdgcn_rcpf(1.0f + ex[e]); PIN(ex[e]); }
    } else if (i == 6) {
#pragma unroll
      for (int e = 0; e < 8; e += 2) { u32 t = pkbf_o(ov[e] * ex[e], ov[e + 1] * ex[e + 1]); gv[e] = bflo(t); gv[e + 1] = bfhi(t); PIN(gv[e]); PIN(gv[e + 1]); }
    }
  };
  static_for<0, 10>([&](auto V) {
    constexpr int v = V;
    v8s(&cur)[8] = (v & 1) ? fb : fa;
    v8s(&prv)[8] = (v & 1) ? fa : fb;
    auto mm2 = [&](auto U, v8s (&fr)[8], int s) {
      constexpr int u = U;
      if constexpr (u >= 7) { accS[0] = mfma32h(G[(u - 7) * 4 + s], fr[2 * s], accS[0]); accS[1] = mfma32h(G[(u - 7) * 4 + s], fr[2 * s + 1], accS[1]); }
      else { accG[0] = mfma32b(Wf[2 * u + (s >> 1)][s & 1], fr[2 * s], accG[0]); accG[1] = mfma32b(Wf[2 * u + (s >> 1)][s & 1], fr[2 * s + 1], accG[1]); }
      SCHED();
    };
    if constexpr (v < 9) {
      WAIT_VM(v <= 7 ? 4 : 0);
      if constexpr (v == 5) { if (w == 0) { PIN(fs); poll4(fs, (u32)(FL_S + l * 16)); } }
      BARRIER();
      if constexpr (v == 0 || v == 8) STAMPT(11 + v);
      if constexpr (v == 4) { if (w == 0) ld16c(fs, (u32)(FL_S + l * 16), FL); }
      if constexpr (v >= 1) mm2(IC<(v >= 1 ? v - 1 : 0)>{}, prv, 2);
#pragma unroll
      for (int s = 0; s < 4; ++s) {
        if constexpr (v >= 7) {
          LAS char* sb = smem + aslot(2 * v + (s >> 1)) + (s & 1) * 2048;
          cur[2 * s] = cat8(trread(sb + trS), trread(sb + trS + 1024));
          cur[2 * s + 1] = cat8(trread(sb + (trS ^ 64)), trread(sb + (trS ^ 64) + 1024));
        } else {
          LAS char* sb = smem + aslot(2 * v + (s >> 1)) + (s & 1) * 1024;
          cur[2 * s] = *(LAS v8s*)(sb + hb);
          cur[2 * s + 1] = *(LAS v8s*)(sb + (hb ^ 8192));
        }
      }
      if constexpr (v == 8) {
#pragma unroll
        for (int x_ = 1; x_ < 4; ++x_) {
          LAS char* X = smem + 49152 + (w ^ (2 * x_)) * 6144 + (x_ - 1) * 2048 + l * 16;
          xg[2 * x_ - 2] = *(LAS v4f*)X; xg[2 * x_ - 1] = *(LAS v4f*)(X + 1024);
        }
      }
      SCHED();
      if constexpr (v <= 6) issueP(2 * v + 4, IC<0>{});
      if constexpr (v >= 1) mm2(IC<(v >= 1 ? v - 1 : 0)>{}, prv, 3);
      if constexpr (v <= 6) issueP(2 * v + 4, IC<1>{});
      if constexpr (v == 8) { silu_step(0); silu_step(1); SCHED(); }
      mm2(V, cur, 0);
      if constexpr (v <= 6) issueP(2 * v + 5, IC<0>{});
      if constexpr (v == 7) { xchg_out(accG, 49152); SCHED(); }
      if constexpr (v == 8) { silu_step(2); silu_step(3); SCHED(); }
      mm2(V, cur, 1);
      if constexpr (v <= 6) issueP(2 * v + 5, IC<1>{});
      if constexpr (v == 8) { silu_step(4); SCHED(); }
    } else {
      mm2(IC<8>{}, prv, 2);
      silu_step(5); SCHED();
      mm2(IC<8>{}, prv, 3);
      silu_step(6); SCHED();
    }
  });
  xchg_out(accS, 0);
  BARRIER();
  {
    float o8[8], pv[8];
    xchg_in(accS, 0, o8);
#pragma unroll
    for (int e = 0; e < 8; e += 2) { u32 t = pkbf_o(o8[e] * oscale, o8[e + 1] * oscale); pv[e] = bflo(t) * gv[e]; pv[e + 1] = bfhi(t) * gv[e + 1]; }
    u32 a0 = pkbf(pv[0], pv[1]), a1 = pkbf(pv[2], pv[3]), b0 = pkbf(pv[4], pv[5]), b1 = pkbf(pv[6], pv[7]);
    auto r0 = __builtin_amdgcn_permlane32_swap(a0, b0, false, false);
    auto r1 = __builtin_amdgcn_permlane32_swap(a1, b1, false, false);
    v4u pk = {r0[0], r1[0], r0[1], r1[1]};
    v4u ov4;
#pragma unroll
    for (int e = 0; e < 4; ++e) ov4[e] = pkbf(bflo(xe[e]) + bflo(pk[e]), bfhi(xe[e]) + bfhi(pk[e]));
    *(v4u*)((char*)out + eoff) = ov4;
  }
  STAMPT(20);
  STAMPT(21);
  STAMP_OUT(ws);
}

__global__ void kmain_end() { asm volatile("km_pf_b:"); }

extern "C" void kernel_launch(void* const* d_in, const int* in_sizes, int n_in, void* d_out, int out_size, void* d_ws,
                              size_t ws_size, hipStream_t stream) {
  kmain<<<256, 512, 0, stream>>>((const u16*)d_in[0], (const u16*)d_in[1], (const u16*)d_in[2], (const u16*)d_in[4],
                                 (const u16*)d_in[5], (const u16*)d_in[6], (char*)d_ws + WS_BASE(ws_size), (u16*)d_out);
}
```

```cpp
#include <hip/hip_runtime.h>
#include <stdint.h>

typedef unsigned short u16;
typedef unsigned int u32;
typedef unsigned long long u64;
typedef short v4s __attribute__((ext_vector_type(4)));
typedef short v8s __attribute__((ext_vector_type(8)));
typedef _Float16 v4h __attribute__((ext_vector_type(4)));
typedef _Float16 v8h __attribute__((ext_vector_type(8)));
typedef __bf16 v2b __attribute__((ext_vector_type(2)));
typedef __bf16 v8b __attribute__((ext_vector_type(8)));
typedef float v2f __attribute__((ext_vector_type(2)));
typedef float v4f __attribute__((ext_vector_type(4)));
typedef float v16f __attribute__((ext_vector_type(16)));
typedef u32 v2u __attribute__((ext_vector_type(2)));
typedef u32 v4u __attribute__((ext_vector_type(4)));

#define LAS __attribute__((address_space(3)))
#define DEVI __device__ __forceinline__

constexpr int D_ = 512, F_ = 1792, DF_ = 257;
constexpr size_t OFF_H = 0;
constexpr size_t OFF_S1 = (size_t)8 << 20;
constexpr size_t OFF_WT = ((size_t)10 << 20) + (256 << 10);
constexpr size_t OFF_S2 = ((size_t)12 << 20) + (768 << 10);
constexpr size_t OFF_M = (size_t)15 << 20;
constexpr size_t OFF_FL = OFF_M;
constexpr u32 FL_S = 0, FL_A = 1024, FL_H = 2048;
constexpr size_t OFF_BC = OFF_M + 49152;
constexpr size_t OFF_MB = OFF_M + 98304;
constexpr size_t OFF_RS2 = OFF_M + 106496;
constexpr size_t OFF_PQ = OFF_M + 65536;
constexpr size_t OFF_CSP = OFF_M + 131072;
constexpr int S1P = 8192 + 128, WTP = 4608 + 128, S2P = 4096 + 128;
constexpr int SPIN_CAP = 1 << 16;


#define WS_BASE(n) (((n) - ((size_t)16 << 20)) & ~(size_t)0x1fffff)
template <int I> struct IC { static constexpr int v = I; constexpr operator int() const { return I; } };
template <int B, int E, class Fn> DEVI void static_for(Fn&& f) {
  if constexpr (B < E) { f(IC<B>{}); static_for<B + 1, E>(f); }
}
#define WAIT_VM(n) asm volatile("s_waitcnt vmcnt(%0)" ::"n"(n) : "memory")
#define BARRIER() asm volatile("s_waitcnt lgkmcnt(0)\n\ts_barrier" ::: "memory")
#define SCHED() __builtin_amdgcn_sched_barrier(0)
#define PIN(x) asm volatile("" : "+v"(x))

DEVI int s_of_pi(int pi) {
  int RG = pi >> 6, i = pi & 63;
  return 128 * (4 * (RG & 3) + (i & 3)) + 8 * (i >> 2) + (RG >> 2);
}
DEVI void dma2(u32 voff0, u32 voff1, const char* sbase, u32 lds) {
  asm volatile("s_mov_b32 m0, %3\n\ts_nop 0\n\tglobal_load_lds_dwordx4 %0, %2\n\tglobal_load_lds_dwordx4 %1, %2 offset:1024" ::"v"(voff0), "v"(voff1), "s"(sbase), "s"(lds) : "memory");
}
DEVI void dma2c(u32 voff0, u32 voff1, const char* sbase, u32 lds) {
  asm volatile("s_mov_b32 m0, %3\n\ts_nop 0\n\tglobal_load_lds_dwordx4 %0, %2 sc1\n\tglobal_load_lds_dwordx4 %1, %2 offset:1024 sc1" ::"v"(voff0), "v"(voff1), "s"(sbase), "s"(lds) : "memory");
}
template <int OFF> DEVI void dma1(u32 voff, const char* sbase, u32 lds) {
  asm volatile("s_mov_b32 m0, %2\n\ts_nop 0\n\tglobal_load_lds_dwordx4 %0, %1 offset:%3" ::"v"(voff), "s"(sbase), "s"(lds), "n"(OFF) : "memory");
}
template <int OFF> DEVI void dma1s(u32 voff, const char* sbase, u32 lds) {
  asm volatile("s_mov_b32 m0, %2\n\ts_nop 0\n\tglobal_load_lds_dwordx4 %0, %1 offset:%3 sc1" ::"v"(voff), "s"(sbase), "s"(lds), "n"(OFF) : "memory");
}
DEVI void dma1c(u32 voff, const char* sbase, u32 lds) {
  asm volatile("s_mov_b32 m0, %2\n\ts_nop 0\n\tglobal_load_lds_dword %0, %1 sc1" ::"v"(voff), "s"(sbase), "s"(lds) : "memory");
}
DEVI v2u ld8c_sync(u32 voff, const char* sb) { v2u d; asm volatile("global_load_dwordx2 %0, %1, %2 sc1\n\ts_waitcnt vmcnt(0)" : "=&v"(d) : "v"(voff), "s"(sb) : "memory"); return d; }
DEVI u32 ld4c_sync(u32 voff, const char* sb) { u32 d; asm volatile("global_load_dword %0, %1, %2 sc1\n\ts_waitcnt vmcnt(0)" : "=&v"(d) : "v"(voff), "s"(sb) : "memory"); return d; }
DEVI v4u ld16c_sync(u32 voff, const char* sb) { v4u d; asm volatile("global_load_dwordx4 %0, %1, %2 sc1\n\ts_waitcnt vmcnt(0)" : "=&v"(d) : "v"(voff), "s"(sb) : "memory"); return d; }
template <class T> DEVI void ld16c(T& d, u32 voff, const char* sb) { asm volatile("global_load_dwordx4 %0, %1, %2 sc1" : "=&v"(d) : "v"(voff), "s"(sb) : "memory"); }
template <class T> DEVI void ld4c(T& d, u32 voff, const char* sb) { asm volatile("global_load_dword %0, %1, %2 sc1" : "=&v"(d) : "v"(voff), "s"(sb) : "memory"); }
template <class T> DEVI void ld16(T& d, u32 voff, const char* sb) { asm volatile("global_load_dwordx4 %0, %1, %2" : "=&v"(d) : "v"(voff), "s"(sb) : "memory"); }
DEVI void ld2(u32& d, u32 voff, const char* sb) { asm volatile("global_load_ushort %0, %1, %2" : "=&v"(d) : "v"(voff), "s"(sb) : "memory"); }
template <class T> DEVI void st16c(u32 voff, const char* sb, T v) { asm volatile("global_store_dwordx4 %0, %1, %2 sc1\n\ts_nop 1" ::"v"(voff), "v"(v), "s"(sb) : "memory"); }
template <class T> DEVI void st8c(u32 voff, const char* sb, T v) { asm volatile("global_store_dwordx2 %0, %1, %2 sc1" ::"v"(voff), "v"(v), "s"(sb) : "memory"); }
template <class T> DEVI void st4c(u32 voff, const char* sb, T v) { asm volatile("global_store_dword %0, %1, %2 sc1" ::"v"(voff), "v"(v), "s"(sb) : "memory"); }

DEVI v4s trread(LAS char* p) { return __builtin_amdgcn_ds_read_tr16_b64_v4i16((LAS v4s*)p); }
DEVI v8s cat8(v4s a, v4s b) { return __builtin_shufflevector(a, b, 0, 1, 2, 3, 4, 5, 6, 7); }
DEVI float bflo(u32 v) { return __builtin_bit_cast(float, v << 16); }
DEVI float bfhi(u32 v) { return __builtin_bit_cast(float, v & 0xffff0000u); }
DEVI float relu(float v) { return __builtin_bit_cast(float, max(__builtin_bit_cast(int, v), 0)); }
DEVI float rbf(float v) { return (float)(__bf16)v; }
DEVI u32 pkbf(float a, float b) { v2b p = {(__bf16)a, (__bf16)b}; return __builtin_bit_cast(u32, p); }
DEVI u32 pkbf_o(float a, float b) { u32 r = pkbf(a, b); asm("" : "+v"(r)); return r; }
DEVI float cosr(float rev) { return __builtin_amdgcn_cosf(rev); }
DEVI float sinr(float rev) { return __builtin_amdgcn_sinf(rev); }
DEVI v4f mfma16h(v8h a, v8h b, v4f c) { return __builtin_amdgcn_mfma_f32_16x16x32_f16(a, b, c, 0, 0, 0); }
DEVI v4f mfma16b(v8s a, v4u b, v4f c) { return __builtin_amdgcn_mfma_f32_16x16x32_bf16(__builtin_bit_cast(v8b, a), __builtin_bit_cast(v8b, b), c, 0, 0, 0); }
DEVI v16f mfma32b(v8s a, v8s b, v16f c) { return __builtin_amdgcn_mfma_f32_32x32x16_bf16(__builtin_bit_cast(v8b, a), __builtin_bit_cast(v8b, b), c, 0, 0, 0); }
DEVI v16f mfma32h(v8h a, v8s b, v16f c) { return __builtin_amdgcn_mfma_f32_32x32x16_f16(a, __builtin_bit_cast(v8h, b), c, 0, 0, 0); }
DEVI v8h pack_ri(v4f re, v4f im) {
  v8h r;
  r[0] = (_Float16)re[0]; r[1] = (_Float16)re[1]; r[2] = (_Float16)re[2]; r[3] = (_Float16)re[3];
  r[4] = (_Float16)im[0]; r[5] = (_Float16)im[1]; r[6] = (_Float16)im[2]; r[7] = (_Float16)im[3];
  return r;
}
DEVI v4h pack4h(v4f a) { v4h r; r[0] = (_Float16)a[0]; r[1] = (_Float16)a[1]; r[2] = (_Float16)a[2]; r[3] = (_Float16)a[3]; return r; }
DEVI void twid(v4f& re, v4f& im, const v4f& c, const v4f& s) {
#pragma unroll
  for (int r = 0; r < 4; ++r) { float a = re[r], b = im[r]; re[r] = a * c[r] + b * s[r]; im[r] = b * c[r] - a * s[r]; }
}
template <int CTRL> DEVI float dppadd(float v) {
  return v + __builtin_bit_cast(float, __builtin_amdgcn_update_dpp(0, __builtin_bit_cast(int, v), CTRL, 0xf, 0xf, true));
}
DEVI float wave_sum(float v) {
  v = dppadd<0xB1>(v); v = dppadd<0x4E>(v); v = dppadd<0x141>(v); v = dppadd<0x140>(v);
  int bb = __builtin_bit_cast(int, v);
  float a0 = __builtin_bit_cast(float, __builtin_amdgcn_readlane(bb, 0)), a1 = __builtin_bit_cast(float, __builtin_amdgcn_readlane(bb, 16));
  float a2 = __builtin_bit_cast(float, __builtin_amdgcn_readlane(bb, 32)), a3 = __builtin_bit_cast(float, __builtin_amdgcn_readlane(bb, 48));
  return (a0 + a1) + (a2 + a3);
}
template <int CTRL> DEVI u32 dppmax(u32 v) { return max(v, (u32)__builtin_amdgcn_update_dpp(0, (int)v, CTRL, 0xf, 0xf, true)); }
DEVI u32 wave_max(u32 v) {
  v = dppmax<0xB1>(v); v = dppmax<0x4E>(v); v = dppmax<0x141>(v); v = dppmax<0x140>(v);
  u32 a0 = __builtin_amdgcn_readlane(v, 0), a1 = __builtin_amdgcn_readlane(v, 16), a2 = __builtin_amdgcn_readlane(v, 32), a3 = __builtin_amdgcn_readlane(v, 48);
  return max(max(a0, a1), max(a2, a3));
}

struct Frag1 { v8s x0, x1, w1, w2; };

constexpr int L_SCR = 147456;
constexpr int L_RED = L_SCR + 1024, L_MX = L_SCR + 1088, L_BC0 = L_SCR + 2048, L_BC1 = L_SCR + 2304;
constexpr int L_TAB = L_SCR + 3072;
constexpr int L_T = L_SCR + 7168, L_FA = L_SCR, L_E1 = L_T, L_E2 = 81920, L_ZX = 81920;
constexpr int L_C1 = 98304, L_IMG = L_C1 + 9216;
constexpr int L_FR = 131072;
constexpr int E1P = 544, E2P = 1024 + 72;
DEVI constexpr int wslot(int n) {
  constexpr int u[14] = {6, 7, 8, 0, 1, 2, 3, 4, 6, 7, 8, 0, 1, 2};
  return u[n] * 16384;
}
DEVI constexpr int aslot(int n) {
  constexpr int u[18] = {3, 4, 5, 9, 2, 6, 7, 8, 0, 1, 3, 4, 5, 9, 2, 6, 7, 8};
  return u[n] * 16384;
}

__global__ __launch_bounds__(512, 2) void kmain(const u16* __restrict__ x, const u16* __restrict__ wr, const u16* __restrict__ wi,
                                                const u16* __restrict__ Wg, const u16* __restrict__ Wu, const u16* __restrict__ Wd,
                                                char* __restrict__ ws, u16* __restrict__ out) {
  __shared__ __attribute__((aligned(16))) char smem_[163840];
  LAS char* smem = (LAS char*)smem_;
  const u32 lbase = (u32)(size_t)smem;
  const int tid = threadIdx.x, l = tid & 63;
  const int w = __builtin_amdgcn_readfirstlane(tid >> 6);
  const int b = blockIdx.x, xcd = b & 7, idx = b >> 3;
  const bool ffn = idx < 28;
  const int g = l >> 4, o = l & 15;
  const int h = l >> 5, blk = (l >> 4) & 1, q = (l & 15) >> 2, pp = l & 3;
  const char* FL = ws + OFF_FL;
  const char* BC = ws + OFF_BC;
  asm volatile("" ::"s"(x), "s"(wr), "s"(wi), "s"(Wg), "s"(Wu), "s"(Wd), "s"(ws), "s"(out));

  const int pi = b * 8 + w;
  v4u xr;
  ld16(xr, (u32)(s_of_pi(pi) * 1024 + l * 16), (const char*)x);
  u64 kpc; u32 klen;
  asm volatile("s_getpc_b64 %0\nkm_pf_a:\n\ts_mov_b32 %1, km_pf_b-km_pf_a-16" : "=s"(kpc), "=s"(klen));
  auto pf_code = [&]() { dma1<0>(min((u32)(((idx & 3) * 8 + w) * 1024 + l * 16), klen) & ~15u, (const char*)(kpc & ~15ull), lbase + L_FA); };
  if (!ffn) pf_code();
  if (w == 0) dma1c((u32)((l & 1) * 4), BC, lbase + L_BC0);
  const int fkb = (b & 7) * 2 + (w >> 2), fcq = w & 3;
  auto filt_dma = [&]() {
    const u32 ks = (u32)((idx >> 1) + 16 * fkb + 256 * (4 * (idx & 1) + ((l >> 3) & 3)));
    const u32 base = ((ks * (DF_ * 2)) & ~15u) + fcq * 128;
    const u32 c0 = ((l & 7) ^ (((l >> 4) & 1) << 2)) << 4;
    if (l < 32) dma1<0>(base + c0, (const char*)wr, lbase + L_FR + w * 2048);
    else dma1<0>(base + c0, (const char*)wi, lbase + L_FR + w * 2048);
    if (fcq == 3) {
      const u32 t0 = min(base + 128 + (c0 & 16), (u32)(2048 * DF_ * 2 - 16));
      if (l < 32) dma1<0>(t0, (const char*)wr, lbase + L_FR + w * 2048 + 1024);
      else dma1<0>(t0, (const char*)wi, lbase + L_FR + w * 2048 + 1024);
    }
  };
  const int p = (xcd >> 1) * 4 + idx / 7, j = (xcd & 1) * 7 + idx % 7;
  const int sh = w >> 2, fq = w & 3;
  u32 aoff[2];
#pragma unroll
  for (int i = 0; i < 2; ++i) {
    int row = (2 * w + i) * 8 + (l >> 3);
    aoff[i] = s_of_pi((p & 15) * 128 + row) * 1024 + (((l & 7) ^ ((row >> 1) & 7)) << 4) - i * 1024;
  }
  const u32 boff = (u32)(2 * w * 4 + (l >> 4)) * (F_ * 2) + j * 256 + (((l & 15) ^ ((l >> 4) << 2)) << 4);
  const u32 boff1 = boff + 4 * F_ * 2 - 1024;
  const int rot = p >> 1;
  auto issue = [&](int t, int slot) {
    const int kt = (t + rot) & 7;
    u32 dst = lbase + slot * 49152 + w * 2048;
    dma2(aoff[0], aoff[1], (const char*)x + kt * 128, dst);
    dma2(boff, boff1, (const char*)Wg + (size_t)kt * (64 * F_ * 2), dst + 16384);
    dma2(boff, boff1, (const char*)Wu + (size_t)kt * (64 * F_ * 2), dst + 32768);
  };
  const int sp = xcd * 4 + (idx - 28);
  if (ffn) issue(0, 0); else filt_dma();

  u32 E = 0, tick = 0;
  const bool coord = (b == 224) && (w == 0);
  auto do_tick = [&]() { if (coord) { ++tick; if (l == 0) st8c(0u, BC, v2u{E, tick}); } };
  auto get_epoch = [&]() {
    const v2u b0 = *(LAS v2u*)(smem + L_BC0);
    v2u b1 = *(LAS v2u*)(smem + L_BC1);
    int spins = 0;
    while (__builtin_amdgcn_readfirstlane((b0[0] ^ b1[0]) | (b0[1] ^ b1[1])) == 0 && ++spins < SPIN_CAP) {
      __builtin_amdgcn_s_sleep(4);
      b1 = ld8c_sync(0u, BC);
    }
    E = __builtin_amdgcn_readfirstlane(b1[0]);
  };
  auto signal_a = [&]() {
    v4u r0 = *(LAS v4u*)(smem + L_RED), r1 = *(LAS v4u*)(smem + L_RED + 16);
    u32 m = max(max(max(r0[0], r0[1]), max(r0[2], r0[3])), max(max(r1[0], r1[1]), max(r1[2], r1[3])));
    if (l == 0) { st4c((u32)(FL_A + b * 4), FL, E | (m >> 7)); *(LAS u32*)(smem + L_MX + 4) = E; }
  };
  auto poll4 = [&](v4u f, u32 voff) -> v4u {
    int spins = 0;
    while (!__all((((f[0] ^ E) | (f[1] ^ E) | (f[2] ^ E) | (f[3] ^ E)) >> 8) == 0) && ++spins < SPIN_CAP) {
      do_tick();
      __builtin_amdgcn_s_sleep(12);
      f = ld16c_sync(voff, FL);
    }
    return f;
  };
  auto check_a = [&](v4u f) {
    f = poll4(f, (u32)(FL_A + l * 16));
    u32 m = wave_max(max(max(f[0], f[1]), max(f[2], f[3]))) & 0xffu;
    if (l == 0) *(LAS u32*)(smem + L_MX) = m;
  };
  if (coord) {
#pragma unroll
    for (int i = 0; i < 5; ++i) dma2c(i ? (u32)(FL_H + ((i - 1) * 128 + l) * 64) : (u32)(l * 16), i ? (u32)(FL_H + ((i - 1) * 128 + 64 + l) * 64 - 1024) : (u32)(l * 16), FL, lbase + i * 2048);
  }
  u32 sq[8];
  if (!ffn && w == 7) {
    const int kss = (sp >> 1) + 16 * (l >> 2) + 256 * (4 * (sp & 1) + (l & 3)), km = (2048 - kss) & 2047;
    const u32 o1 = (u32)(kss * DF_ * 2), o2 = (u32)(km * DF_ * 2);
    ld2(sq[0], o1, (const char*)wr); ld2(sq[1], o1, (const char*)wi); ld2(sq[2], o2, (const char*)wr); ld2(sq[3], o2, (const char*)wi);
    ld2(sq[4], o1 + 512, (const char*)wr); ld2(sq[5], o1 + 512, (const char*)wi); ld2(sq[6], o2 + 512, (const char*)wr); ld2(sq[7], o2 + 512, (const char*)wi);
  }

  {
    const float fo = (float)o, fg = (float)(4 * g);
    if (w < 2) {
      const float st = fo * (1.0f / 32.0f), a0 = fmaf(fg, st, w ? 0.25f : 0.f);
      v4u hi, lo;
#pragma unroll
      for (int e2 = 0; e2 < 4; ++e2) {
        float c0 = cosr(fmaf((float)(16 * (e2 >> 1) + 2 * (e2 & 1)), st, a0)), c1 = cosr(fmaf((float)(16 * (e2 >> 1) + 2 * (e2 & 1) + 1), st, a0));
        u32 ch = pkbf_o(c0, c1);
        hi[e2] = ch; lo[e2] = pkbf(c0 - bflo(ch), c1 - bfhi(ch));
      }
      *(LAS v4u*)(smem + L_C1 + w * 2048 + l * 16) = hi; *(LAS v4u*)(smem + L_C1 + w * 2048 + 1024 + l * 16) = lo;
    } else if (w < 5) {
      const float st = w == 2 ? (float)(o & 7) * (1.0f / 16.0f) : fo * (w == 3 ? 1.0f / 16.0f : 1.0f / 8.0f);
      const float a0 = (w == 2 && o >= 8) ? 0.25f : 0.f;
      const float sc = w == 2 ? 0.0625f : ((w == 4 && (g >> 1) != (o >> 3)) ? 0.f : 1.0f);
      v8h m;
#pragma unroll
      for (int e = 0; e < 4; ++e) { float a = fmaf(fg + (float)e, st, a0); m[e] = (_Float16)(sc * cosr(a)); m[4 + e] = (_Float16)(sc * sinr(a)); }
      *(LAS v8h*)(smem + (w == 2 ? L_C1 + 4096 : L_TAB + (w - 3) * 1024) + l * 16) = m;
    } else {
      const float st = w == 7 ? fo * (1.0f / 256.0f) : (fo + (w == 6 ? 16.f : 0.f)) * (1.0f / 512.0f);
      v4f c, sn;
#pragma unroll
      for (int e = 0; e < 4; ++e) { float a = (fg + (float)e) * st; c[e] = cosr(a); sn[e] = sinr(a); }
      LAS char* dst = smem + (w == 7 ? L_TAB + 2048 : L_C1 + 5120 + (w - 5) * 2048) + l * 16;
      *(LAS v4f*)dst = c; *(LAS v4f*)(dst + 1024) = sn;
    }
    BARRIER();
    if (ffn) filt_dma();
    LAS char* C1 = smem + L_C1 + l * 16;
    const v4u Fhr = *(LAS v4u*)C1, Flr = *(LAS v4u*)(C1 + 1024), Fhi = *(LAS v4u*)(C1 + 2048), Fli = *(LAS v4u*)(C1 + 3072);
    const v8h M2 = *(LAS v8h*)(C1 + 4096);
    v4f twc[2], tws[2];
#pragma unroll
    for (int u = 0; u < 2; ++u) { twc[u] = *(LAS v4f*)(C1 + 5120 + u * 2048); tws[u] = *(LAS v4f*)(C1 + 6144 + u * 2048); }
    if (ffn) WAIT_VM(8); else if (w == 7) WAIT_VM(11); else WAIT_VM(3);
    if (coord) {
      WAIT_VM(0);
      v4u f[10];
#pragma unroll
      for (int i = 0; i < 10; ++i) f[i] = *(LAS v4u*)(smem + i * 1024 + l * 16);
      u32 c = __builtin_amdgcn_readfirstlane(*(LAS u32*)(smem + L_BC0)) >> 8;
      bool hit;
      do {
        c = (c + 1) & 0xffffffu; hit = false;
#pragma unroll
        for (int i = 0; i < 10; ++i) hit |= ((f[i][0] >> 8) == c) | ((f[i][1] >> 8) == c) | ((f[i][2] >> 8) == c) | ((f[i][3] >> 8) == c);
      } while (__any(hit));
      E = c << 8;
      do_tick();
    }
    PIN(xr);
    const float c0 = bflo(__builtin_amdgcn_readfirstlane(xr[0]));
    v2f xv[4], s2 = {0.f, 0.f}, q2 = {0.f, 0.f};
#pragma unroll
    for (int e = 0; e < 4; ++e) {
      xv[e] = v2f{bflo(xr[e]), bfhi(xr[e])};
      v2f t = xv[e] - v2f{c0, c0};
      s2 += t; q2 = __builtin_elementwise_fma(t, t, q2);
    }
    const float dmean = wave_sum(s2[0] + s2[1]) * (1.0f / 512.0f), msq = wave_sum(q2[0] + q2[1]) * (1.0f / 512.0f);
    do_tick();
    const float mb = rbf(c0 + dmean);
    const float var = rbf(fmaxf(msq - dmean * dmean, 0.f));
    const float sd = rbf(__builtin_amdgcn_sqrtf(rbf(var + 1e-5f)));
    const float rs = __builtin_amdgcn_rcpf(sd);
#pragma unroll
    for (int e = 0; e < 4; ++e) {
      v2f t = xv[e] - v2f{mb, mb};
      u32 tb = pkbf_o(t[0], t[1]);
      t = v2f{bflo(tb), bfhi(tb)} * v2f{rs, rs};
      xr[e] = pkbf(t[0], t[1]);
    }
    *(LAS v4u*)(smem + L_IMG + w * 1024 + l * 16) = xr;
    if (l < 2) st4c((u32)(pi * 4 + l * 8192), ws + OFF_MB, l ? rs * rs : -mb);
    const v8s xf = cat8(trread(smem + L_IMG + w * 1024 + l * 8), trread(smem + L_IMG + w * 1024 + 512 + l * 8));
    const int kt1 = (1 + rot) & 7;
    const u32 dst1 = lbase + 49152 + w * 2048;
    if (ffn) dma2(aoff[0], aoff[1], (const char*)x + kt1 * 128, dst1);
    const v8s xg_ = __builtin_bit_cast(v8s, __builtin_bit_cast(v4u, xf) ^ v4u{0x80000000u, 0x80000000u, 0x80000000u, 0x80000000u});
    const v4f z = {0.f, 0.f, 0.f, 0.f};
    v4f dre[2], dim[2];
#pragma unroll
    for (int u = 0; u < 2; ++u) { dre[u] = mfma16b(u ? xg_ : xf, Flr, z); dim[u] = mfma16b(u ? xg_ : xf, Fli, z); }
#pragma unroll
    for (int u = 0; u < 2; ++u) { dre[u] = mfma16b(u ? xg_ : xf, Fhr, dre[u]); dim[u] = mfma16b(u ? xg_ : xf, Fhi, dim[u]); }
#pragma unroll
    for (int u = 0; u < 2; ++u) {
      twid(dre[u], dim[u], twc[u], tws[u]);
      const v8h bb = pack_ri(dre[u], dim[u]);
      v4f d2 = mfma16h(bb, M2, z);
      if (u == 0) {
        const u32 nv = o == 8 ? 0xac002c00u : 0u;
        v4f n = mfma16h(bb, __builtin_bit_cast(v8h, v4u{nv, nv, 0u, 0u}), z);
        if (l == 8) d2[0] = n[0];
      }
      *(LAS v4h*)(smem + L_T + ((o >> 3) * 8 + w) * 544 + (o & 7) * 64 + u * 32 + g * 8) = pack4h(d2);
    }
    if (ffn) {
      dma2(boff, boff1, (const char*)Wg + (size_t)kt1 * (64 * F_ * 2), dst1 + 16384);
      dma2(boff, boff1, (const char*)Wu + (size_t)kt1 * (64 * F_ * 2), dst1 + 32768);
    }
  }
  v8s su; v4u fu, tu;
  auto round1_read = [&]() {
    LAS char* rp = smem + L_T + (4 * (g & 1) + (o >> 2)) * 544 + (w * 32 + (g >> 1) * 16) * 2 + (o & 3) * 8;
    su = cat8(trread(rp), trread(rp + 8 * 544));
    LAS char* fp_ = smem + L_FR + w * 2048 + q * 128 + (((2 * g + (pp >> 1)) ^ ((q >> 1) << 2)) << 4) + (pp & 1) * 8;
    fu = __builtin_bit_cast(v4u, cat8(trread(fp_), trread(fp_ + 512)));
    LAS char* tp = smem + L_FR + w * 2048 + 1024 + q * 128 + (pp >> 1) * 16 + (pp & 1) * 8;
    tu = __builtin_bit_cast(v4u, cat8(trread(tp), trread(tp + 512)));
  };
  auto round1_max = [&]() {
    typedef unsigned short v2us __attribute__((ext_vector_type(2)));
    const int dl = (idx >> 1) & 7;
    const u32 msk = (fcq == 0 && l < dl) ? 0u : 0x7fff7fffu, msk2 = (fcq == 3 && o <= dl) ? 0x7fff7fffu : 0u;
    v2us m2 = {0, 0};
#pragma unroll
    for (int e = 0; e < 4; ++e) {
      u32 t = fu[e] & msk, t2 = tu[e] & msk2;
      m2 = __builtin_elementwise_max(m2, __builtin_elementwise_max(__builtin_bit_cast(v2us, t), __builtin_bit_cast(v2us, t2)));
    }
    const u32 mx = wave_max(max((u32)m2[0], (u32)m2[1]));
    if (l == 0) *(LAS u32*)(smem + L_RED + w * 4) = mx;
  };
  auto round1_store = [&]() {
    st16c((u32)(b * S1P + (w * 32 + (g >> 1) * 16 + o) * 32 + (g & 1) * 16), ws + OFF_S1, su);
    st16c((u32)((idx * 16 + fkb) * WTP + (fcq * 64 + l) * 16), ws + OFF_WT, fu);
    if (fcq == 3 && l < 16) st16c((u32)((idx * 16 + fkb) * WTP + (256 + l) * 16), ws + OFF_WT, tu);
  };

  const int RG = xcd * 4 + (idx >> 3), ct = idx & 7;
  const int kd = xcd * 32 + ((idx + 3) & 31);
  const u32 swz = ((l & 7) ^ (((l >> 4) & 1) << 2)) << 4;
  const u32 ro0 = (u32)((l >> 3) * 1024) + swz, ro1 = ro0 + 8192 - 1024;
  const char* bsrc = (const char*)Wd + (size_t)(w * 16) * 1024 + ct * 128;
  auto issueW = [&](int n) { dma2(ro0, ro1, bsrc + (size_t)n * 131072, lbase + wslot(n) + w * 2048); };
  auto pfl = [&](int n) {
    const char* src = (const char*)Wd + (size_t)n * 131072 + ((idx & 3) * 8 + w) * 4096;
    dma1<0>((u32)((l >> 5) * 1024 + (l & 31) * 16), src, lbase + L_FA); dma1<0>((u32)((l >> 5) * 1024 + (l & 31) * 16), src + 2048, lbase + L_FA);
  };
  const int dh = w & 1, kq = w >> 1, ph = kq >> 1, kh = kq & 1;
  const int epi = RG * 64 + ph * 32 + (l & 31);
  const u32 eoff = (u32)(s_of_pi(epi) * D_ + ct * 64 + dh * 32 + kh * 16 + h * 8) * 2;
  v4u wt, s1v, xe; v4f pq[4]; float rs2;
  auto load_xe = [&]() { ld16(xe, eoff, (const char*)x); };
  auto load_s1 = [&]() { ld16c(s1v, (u32)(((4 * w + g) * 8 + (o >> 1)) * S1P + kd * 32 + (o & 1) * 16), ws + OFF_S1); };
  auto load_wt = [&]() { ld16c(wt, (u32)((w * 64 + l) * WTP + (kd + ((2 * w + (g >> 1)) & 7)) * 16), ws + OFF_WT); };
  auto load_rs = [&]() { ld4c(rs2, (u32)(epi * 4), ws + OFF_RS2); };
  v4f t2c, t2s, t3c, t3s;
  auto trig2 = [&]() {
    const float fw = (float)w, st2 = fw * (1.0f / 128.0f), a2 = fw * (float)(o + 64 * g) * (1.0f / 2048.0f);
#pragma unroll
    for (int e = 0; e < 4; ++e) { float b2 = fmaf((float)e, st2, a2); t2c[e] = cosr(b2); t2s[e] = sinr(b2); }
    PIN(t2c); PIN(t2s);
  };
  auto trig3 = [&]() {
    const float f7 = (float)(o & 7), st3 = f7 * (1.0f / 128.0f), a3 = f7 * (float)(2 * w + (o >> 3) + 64 * g) * (1.0f / 2048.0f);
#pragma unroll
    for (int e = 0; e < 4; ++e) { float b3 = fmaf((float)e, st3, a3); t3c[e] = cosr(b3); t3s[e] = sinr(b3); }
    PIN(t3c); PIN(t3s);
  };
  v4u f16; v4f t1c, t1s, re, im;
  const v4f z4 = {0.f, 0.f, 0.f, 0.f};
  auto imf = [](v4u f) { return __builtin_bit_cast(v8h, v4u{f[2] ^ 0x80008000u, f[3] ^ 0x80008000u, f[0], f[1]}); };
  auto segA0 = [&]() {
    f16 = *(LAS v4u*)(smem + L_TAB + l * 16);
    t1c = *(LAS v4f*)(smem + L_TAB + 2048 + l * 16); t1s = *(LAS v4f*)(smem + L_TAB + 3072 + l * 16);
  };
  auto segA1 = [&]() {
    PIN(s1v);
    const v8h a = __builtin_bit_cast(v8h, s1v);
    re = mfma16h(a, __builtin_bit_cast(v8h, f16), z4); im = mfma16h(a, imf(f16), z4);
  };
  auto segA2 = [&]() {
    twid(re, im, t1c, t1s);
    const v8h a = pack_ri(re, im);
    re = mfma16h(__builtin_bit_cast(v8h, f16), a, z4); im = mfma16h(imf(f16), a, z4);
  };
  auto segA3 = [&]() {
    twid(re, im, t2c, t2s);
    *(LAS v4h*)(smem + L_E1 + w * E1P + o * 32 + g * 8) = pack4h(re);
    *(LAS v4h*)(smem + L_E1 + (8 + w) * E1P + o * 32 + g * 8) = pack4h(im);
  };

  if (ffn) {
    v16f acc[2][2] = {};
    float mbr[2];
    const u32 wb0 = (u32)((8 * h + q) * 256 + ((fq ^ q) << 6) + blk * 32 + pp * 8);
    const u32 r1 = 16384 + sh * 16384, r2 = 32768 - sh * 16384;
    const int xrow = sh * 64 + (l & 31);
    const u32 xb0 = (u32)(xrow * 128 + ((h ^ ((xrow >> 1) & 7)) << 4));
    auto rd = [&](Frag1& f, LAS char* sb, int i) {
      LAS char* xa = sb + (xb0 ^ (i << 5));
      LAS char* wa_ = sb + wb0 + (i << 12);
      f.w1 = cat8(trread(wa_ + r1), trread(wa_ + r1 + 1024));
      f.x0 = *(LAS v8s*)xa; f.x1 = *(LAS v8s*)(xa + 4096);
      f.w2 = cat8(trread(wa_ + r2), trread(wa_ + r2 + 1024));
    };
    const u32 one = ((l & 15) == ((l >> 4) & 1)) ? 0x3f803f80u : 0u;
    const v8s sel = __builtin_bit_cast(v8s, v4u{one, one, one, one});
    v4f csa = {0.f, 0.f, 0.f, 0.f};
    Frag1 A, B;
    WAIT_VM(7);
    BARRIER();
    rd(A, smem, 0);
    round1_read();
    dma2(aoff[0], aoff[1], (const char*)x + ((2 + rot) & 7) * 128, lbase + 2 * 49152 + w * 2048);
    u32 c1[4], c2[4];
    static_for<0, 8>([&](auto T) {
      constexpr int t = T, slot = t % 3, nslot = (t + 1) % 3, pslot = (t + 2) % 3;
      LAS char* sb = smem + slot * 49152;
      {
        const int kt = (t + 2 + rot) & 7;
        const char* s1 = t < 6 ? (const char*)Wg + (size_t)kt * (64 * F_ * 2) : bsrc + (size_t)((t - 6) * 3 + 1) * 131072;
        const char* s2 = t < 6 ? (const char*)Wu + (size_t)kt * (64 * F_ * 2) : bsrc + (size_t)((t - 6) * 3 + 2) * 131072;
        const u32 o2 = t < 6 ? boff : ro0, o3 = t < 6 ? boff1 : ro1;
        const u32 dst = lbase + pslot * 49152 + w * 2048;
        rd(B, sb, 1); SCHED();
        acc[0][0] = mfma32b(A.w1, A.x0, acc[0][0]); acc[0][1] = mfma32b(A.w1, A.x1, acc[0][1]);
        if constexpr (t == 0) csa = mfma16b(sel, __builtin_bit_cast(v4u, A.w1), csa);
        SCHED();
        dma1<0>(o2, s1, dst + 16384);
        if constexpr (t == 2) load_xe();
        if constexpr (t == 7) load_wt();
        acc[1][0] = mfma32b(A.w2, A.x0, acc[1][0]); acc[1][1] = mfma32b(A.w2, A.x1, acc[1][1]); SCHED();
        if constexpr (t == 0) { round1_store(); round1_max(); SCHED(); }
        if constexpr (t == 3) { trig2(); SCHED(); }
        if constexpr (t == 4) { trig3(); SCHED(); }
        rd(A, sb, 2); SCHED();
        acc[0][0] = mfma32b(B.w1, B.x0, acc[0][0]); SCHED();
        dma1<1024>(o3, s1, dst + 16384);
        acc[0][1] = mfma32b(B.w1, B.x1, acc[0][1]);
        if constexpr (t == 0) csa = mfma16b(sel, __builtin_bit_cast(v4u, B.w1), csa);
        acc[1][0] = mfma32b(B.w2, B.x0, acc[1][0]); acc[1][1] = mfma32b(B.w2, B.x1, acc[1][1]); SCHED();
        dma1<0>(o2, s2, dst + 32768);
        if constexpr (t == 6) {
          load_rs();
        }
        if constexpr (t == 7) { WAIT_VM(13); segA1(); SCHED(); }
        rd(B, sb, 3); SCHED();
        if constexpr (t == 7) {
#pragma unroll
          for (int i = 0; i < 4; ++i) { PIN(c1[i]); PIN(c2[i]); }
          PIN(mbr[0]); PIN(mbr[1]);
          const v8s cs1 = __builtin_bit_cast(v8s, v4u{c1[0], c1[1], c1[2], c1[3]}), cs2 = __builtin_bit_cast(v8s, v4u{c2[0], c2[1], c2[2], c2[3]});
          const u32 m0_ = pkbf(mbr[0], mbr[0]), m1_ = pkbf(mbr[1], mbr[1]);
          const v8s mb0 = __builtin_bit_cast(v8s, v4u{m0_, m0_, m0_, m0_}), mb1 = __builtin_bit_cast(v8s, v4u{m1_, m1_, m1_, m1_});
          acc[0][0] = mfma32b(cs1, mb0, acc[0][0]); acc[1][0] = mfma32b(cs2, mb0, acc[1][0]);
          acc[0][1] = mfma32b(cs1, mb1, acc[0][1]); acc[1][1] = mfma32b(cs2, mb1, acc[1][1]); SCHED();
          segA2(); SCHED();
        }
        acc[0][0] = mfma32b(A.w1, A.x0, acc[0][0]); acc[0][1] = mfma32b(A.w1, A.x1, acc[0][1]);
        if constexpr (t == 0) csa = mfma16b(sel, __builtin_bit_cast(v4u, A.w1), csa);
        SCHED();
        dma1<1024>(o3, s2, dst + 32768);
        acc[1][0] = mfma32b(A.w2, A.x0, acc[1][0]); acc[1][1] = mfma32b(A.w2, A.x1, acc[1][1]); SCHED();
      }
      if constexpr (t < 7) {
        const char* s0 = t < 5 ? (const char*)x + ((t + 3 + rot) & 7) * 128 : bsrc + (size_t)((t - 5) * 3) * 131072;
        const u32 o0 = t < 5 ? aoff[0] : ro0, o1 = t < 5 ? aoff[1] : ro1;
        const u32 dst = lbase + slot * 49152 + w * 2048;
        if constexpr (t == 0) csa = mfma16b(sel, __builtin_bit_cast(v4u, B.w1), csa);
        WAIT_VM(t == 0 ? 8 : t == 2 ? 7 : t == 6 ? 18 : 6);
        if constexpr (t == 5) { if (w == 0) check_a(*(LAS v4u*)(smem + L_FA + l * 16)); }
        BARRIER();
        acc[0][0] = mfma32b(B.w1, B.x0, acc[0][0]); SCHED();
        rd(A, smem + nslot * 49152, 0);
        if constexpr (t == 6) segA0();
        SCHED();
        if constexpr (t == 0) {
          if (l < 16) {
#pragma unroll
            for (int i = 0; i < 2; ++i) {
              const float hi = rbf(csa[i]);
              st4c((u32)(((j * 16 + p) * 256 + sh * 128 + fq * 32 + i * 16 + l) * 4), ws + OFF_CSP, pkbf(hi, csa[i] - hi));
            }
          }
        }
        if constexpr (t == 0) { if (w == 0) dma1c((u32)((l & 1) * 4), BC, lbase + L_BC1); }
        if constexpr (t == 4) { if (w == 0) dma1s<0>((u32)(FL_A + l * 16), FL, lbase + L_FA); }
        if constexpr (t == 5) {
          const u32 co = (u32)(((j * 16 + (p & 1) + 8 * h) * 256 + fq * 32 + (l & 31)) * 4);
#pragma unroll
          for (int i = 0; i < 4; ++i) { ld4c(c1[i], co + sh * 512 + i * 2048, ws + OFF_CSP); ld4c(c2[i], co + (1 - sh) * 512 + i * 2048, ws + OFF_CSP); }
          ld4c(mbr[0], (u32)((p * 128 + xrow) * 4), ws + OFF_MB); ld4c(mbr[1], (u32)((p * 128 + xrow + 32) * 4), ws + OFF_MB);
          load_s1();
        }
        dma1<0>(o0, s0, dst);
        acc[0][1] = mfma32b(B.w1, B.x1, acc[0][1]);
        acc[1][0] = mfma32b(B.w2, B.x0, acc[1][0]);
        acc[1][1] = mfma32b(B.w2, B.x1, acc[1][1]); SCHED();
        dma1<1024>(o1, s0, dst);
        if constexpr (t == 1) { if (w == 0) { get_epoch(); signal_a(); } }
        SCHED();
      } else {
        acc[0][0] = mfma32b(B.w1, B.x0, acc[0][0]); acc[1][0] = mfma32b(B.w2, B.x0, acc[1][0]); SCHED();
        acc[0][1] = mfma32b(B.w1, B.x1, acc[0][1]); acc[1][1] = mfma32b(B.w2, B.x1, acc[1][1]); SCHED();
      }
    });
    auto fin = [&](const v16f& ag, const v16f& au, int rt) {
#pragma unroll
      for (int s2i = 0; s2i < 2; ++s2i) {
        v4u ov;
#pragma unroll
        for (int e = 0; e < 4; ++e) {
          const int r = s2i * 8 + 2 * e;
          v2f gg = v2f{relu(ag[r]), relu(ag[r + 1])} * v2f{au[r], au[r + 1]};
          ov[e] = pkbf(gg[0], gg[1]);
        }
        st16c((u32)(((p * 4 + sh * 2 + rt) * 112 + (j * 8 + fq * 2 + s2i)) * 1024 + l * 16), ws + OFF_H, ov);
      }
    };
    if (sh == 0) { fin(acc[0][0], acc[1][0], 0); fin(acc[0][1], acc[1][1], 1); }
    else { fin(acc[1][0], acc[0][0], 0); fin(acc[1][1], acc[0][1], 1); }
    segA3();
    WAIT_VM(4);
  } else {
    WAIT_VM(1);
    BARRIER();
    round1_read(); round1_store(); round1_max();
    if (w == 0 && !coord) dma1c((u32)((l & 1) * 4), BC, lbase + L_BC1);
    do_tick();
    if (w == 7) {
#pragma unroll
      for (int i = 0; i < 8; ++i) PIN(sq[i]);
      float war = bflo(sq[0]) + bflo(sq[2]), wai = bflo(sq[1]) - bflo(sq[3]);
      float wbr = bflo(sq[4]) + bflo(sq[6]), wbi = bflo(sq[5]) - bflo(sq[7]);
      v4f pv = {0.25f * (war + wbr), 0.25f * (wai + wbi), 0.25f * (war - wbr), 0.25f * (wai - wbi)};
      st16c((u32)((((sp >> 2) * 4 + (l & 3)) * 64 + (sp & 3) * 16 + (l >> 2)) * 16), ws + OFF_PQ, pv);
    }
    WAIT_VM(0);
    BARRIER();
    if (w == 0) { if (!coord) get_epoch(); signal_a(); do_tick(); WAIT_VM(0); }
    BARRIER();
    dma1c((u32)(sp * 57344 + (w * 112 + l) * 64), (const char*)Wd, lbase + 49152);
    dma1c((u32)(sp * 57344 + (w * 112 + 48 + l) * 64), (const char*)Wd, lbase + 49152);
    static_for<0, 6>([&](auto N) { pfl(N); });
    static_for<0, 6>([&](auto N) { issueW(N); });
    load_xe();
    trig2(); trig3(); segA0();
    if (w == 0) {
      check_a(v4u{~E, 0u, 0u, 0u});
    }
    BARRIER();
    load_s1(); load_wt(); load_rs();
    if (kd == 0) {
#pragma unroll
      for (int r = 0; r < 4; ++r) ld16c(pq[r], (u32)(w * 4096 + r * 1024 + l * 16), ws + OFF_PQ);
      pf_code();
      WAIT_VM(1);
    } else {
      pf_code();
      static_for<6, 14>([&](auto N) { pfl(N); });
      WAIT_VM(17);
    }
    segA1(); segA2(); segA3();
  }
  PIN(wt); PIN(xe); PIN(rs2);
  BARRIER();

  const u32 trG = (u32)(kq * 4096 + (4 * h + q) * 128 + ((dh ^ ((q >> 1) & 1)) << 6) + (blk ^ kh) * 32 + pp * 8);
  v8s Wf[14][2];
  auto readW = [&](auto N) {
    constexpr int n = N;
    LAS char* sb = smem + wslot(n) + trG;
    Wf[n][0] = cat8(trread(sb), trread(sb + 1024)); Wf[n][1] = cat8(trread(sb + 2048), trread(sb + 3072));
  };
  const char* hsrc = ws + OFF_H + ((size_t)(RG * 2 + (w >> 2)) * 112 + (w & 3) * 2) * 1024;
  const u32 so0 = (u32)((l >> 3) * S2P) + swz, so1 = so0 + 8 * S2P - 1024;
  const char* ssrc = ws + OFF_S2 + (size_t)(w * 16) * S2P + RG * 128;
  auto issueP = [&](int n, auto HALF) {
    constexpr int hf = HALF;
    if (n < 14) dma1s<hf * 1024>((u32)(l * 16), hsrc + n * 8192, lbase + aslot(n) + w * 2048);
    else dma1s<hf * 1024>(hf ? so1 : so0, ssrc + (size_t)(n - 14) * (128 * S2P), lbase + aslot(n) + w * 2048);
  };
  const int d = ct * 64 + dh * 32 + ((l & 31) ^ (kh << 4));
  const float gst = (float)d * (1.0f / 512.0f), ga0 = (float)(16 * kq + 2 * h) * gst;
  v8h G[8];
  auto gen = [&](auto FI) {
    constexpr int f = FI;
#pragma unroll
    for (int e2 = 0; e2 < 4; ++e2) {
      float a = fmaf((float)(64 * (f >> 1) + 8 * (f & 1) + 4 * (e2 >> 1) + (e2 & 1)), gst, ga0);
      float c = cosr(a), sn = sinr(a);
      if (f == 0 && e2 == 0) { if (kq == 0 && h == 0) { c = 0.5f; sn = (d & 1) ? 0.5f : -0.5f; } }
      G[f][2 * e2] = (_Float16)c; G[f][2 * e2 + 1] = (_Float16)sn;
    }
    PIN(G[f]);
  };

  float oscale;
  const u32 fhoff = (u32)(FL_H + (RG * 16 + (min(l, 55) >> 2)) * 64 + (l & 3) * 4);
  u32 fp;
  {
    v8h a;
    {
      LAS char* rp = smem + L_E1 + (4 * (g & 1) + (o >> 2)) * E1P + (2 * w + (g >> 1)) * 32 + (o & 3) * 8;
      a = __builtin_bit_cast(v8h, cat8(trread(rp), trread(rp + 8 * E1P)));
    }
    const v4u f8 = *(LAS v4u*)(smem + L_TAB + 1024 + l * 16);
    const v2u me = *(LAS v2u*)(smem + L_MX);
    issueW(6);
    static_for<0, 3>(readW);
    const int Ex = (int)me[0] < 16 ? 16 : ((int)me[0] > 240 ? 240 : (int)me[0]);
    const float wscale = __builtin_bit_cast(float, (u32)(246 - Ex) << 23);
    oscale = __builtin_bit_cast(float, (u32)(Ex - 7) << 23);
    re = mfma16h(__builtin_bit_cast(v8h, f8), a, z4); im = mfma16h(imf(f8), a, z4);
    static_for<3, 6>(readW);
    WAIT_VM(2);
    if (ffn && l == 0) st4c((u32)(FL_H + ((p * 2 + sh) * 16 + j) * 64 + fq * 4), FL, me[1]);
    issueW(7);
    if (kd != 0) {
#pragma unroll
      for (int r = 0; r < 4; ++r) {
        float fr = ((r & 1) ? bfhi(wt[r >> 1]) : bflo(wt[r >> 1])) * wscale, fi = ((r & 1) ? bfhi(wt[2 + (r >> 1)]) : bflo(wt[2 + (r >> 1)])) * wscale;
        float zr = re[r], zi = im[r];
        re[r] = zr * fr - zi * fi;
        im[r] = -(zr * fi + zi * fr);
      }
    } else {
      const int ka = 2 * w + (g >> 1), kr = o + 64 * (g & 1);
#pragma unroll
      for (int r = 0; r < 4; ++r) { v2f t = {re[r], im[r]}; *(LAS v2f*)(smem + L_ZX + ka * 1024 + (kr + 16 * r) * 8) = t; }
      BARRIER();
#pragma unroll
      for (int r = 0; r < 4; ++r) {
        PIN(pq[r]);
        const int km = (2048 - (ka + 16 * (kr + 16 * r))) & 2047;
        v2f zm = *(LAS v2f*)(smem + L_ZX + (km & 15) * 1024 + (km >> 4) * 8);
        float zr = re[r], zi = im[r];
        re[r] = (zr * pq[r][0] - zi * pq[r][1] + zm[0] * pq[r][2] + zm[1] * pq[r][3]) * wscale;
        im[r] = -(zr * pq[r][1] + zi * pq[r][0] + zm[0] * pq[r][3] - zm[1] * pq[r][2]) * wscale;
      }
    }
    LAS char* E2 = smem + (kd == 0 ? L_E1 : L_E2);
    a = pack_ri(re, im);
    re = mfma16h(a, __builtin_bit_cast(v8h, f8), z4); im = mfma16h(a, imf(f8), z4);
    twid(re, im, t3c, t3s);
    *(LAS v4h*)(E2 + (o & 7) * E2P + (2 * w + (o >> 3)) * 32 + g * 8) = pack4h(re);
    *(LAS v4h*)(E2 + (o & 7) * E2P + 512 + (2 * w + (o >> 3)) * 32 + g * 8) = pack4h(im);
    BARRIER();
    {
      LAS char* rp = E2 + w * E2P + o * 32 + g * 8;
      a = __builtin_bit_cast(v8h, cat8(*(LAS v4s*)rp, *(LAS v4s*)(rp + 512)));
    }
    issueW(8);
    re = mfma16h(a, __builtin_bit_cast(v8h, f16), z4); im = mfma16h(a, imf(f16), z4);
    issueW(9);
    twid(re, im, t1c, t1s);
    a = pack_ri(re, im);
    re = mfma16h(__builtin_bit_cast(v8h, f16), a, z4); im = mfma16h(imf(f16), a, z4);
    issueW(10);
    st8c((u32)(2 * kd * S2P + w * 512 + l * 8), ws + OFF_S2, pack4h(re));
    st8c((u32)((2 * kd + 1) * S2P + w * 512 + l * 8), ws + OFF_S2, pack4h(im));
  }
  gen(IC<0>{}); issueW(11);
  WAIT_VM(8);
  BARRIER();
  static_for<6, 9>(readW);
  gen(IC<1>{}); issueW(12); gen(IC<2>{}); issueW(13);
  WAIT_VM(6);
  BARRIER();
  if (tid == 0) st4c((u32)(FL_S + b * 4), FL, E);
  if (w == 0) ld4c(fp, fhoff, FL);
  gen(IC<3>{}); gen(IC<4>{});
  WAIT_VM(0);
  if (w == 0) {
    PIN(fp);
    u32 f = fp;
    int spins = 0;
    while (!__all(f == E) && ++spins < SPIN_CAP) {
      __builtin_amdgcn_s_sleep(4);
      f = ld4c_sync(fhoff, FL);
    }
  }
  BARRIER();
  issueP(0, IC<0>{}); issueP(0, IC<1>{}); issueP(1, IC<0>{}); issueP(1, IC<1>{});
  static_for<9, 14>(readW);
  gen(IC<5>{});
  issueP(2, IC<0>{}); issueP(2, IC<1>{});
  gen(IC<6>{});
  issueP(3, IC<0>{}); issueP(3, IC<1>{});
  gen(IC<7>{});

  const u32 trS = (u32)(kq * 4096 + (4 * h + q) * 128 + ((ph ^ ((q >> 1) & 1)) << 6) + blk * 32 + pp * 8);
  const u32 hb = (u32)(ph * 8192 + kq * 2048 + l * 16);
  v16f accG[2] = {}, accS[2] = {};
  v8s fa[8], fb[8];
  v4u fs;
  float gv[8];
  const u32 xw = (u32)(w * 6144 + l * 16);
  auto xchg_out = [&](const v16f (&ac)[2], int base) {
    LAS char* X = smem + base + xw;
#pragma unroll
    for (int i = 0; i < 6; ++i) {
      const v16f& a = ac[(i + 2) >> 2]; const int r = ((i + 2) & 3) * 4;
      *(LAS v4f*)(X + i * 1024) = v4f{a[r], a[r + 1], a[r + 2], a[r + 3]};
    }
  };
  auto xchg_in = [&](const v16f (&ac)[2], int base, float (&o8)[8]) {
    v4f s0 = {ac[0][0], ac[0][1], ac[0][2], ac[0][3]}, s1 = {ac[0][4], ac[0][5], ac[0][6], ac[0][7]};
#pragma unroll
    for (int x_ = 1; x_ < 4; ++x_) {
      LAS char* X = smem + base + (w ^ (2 * x_)) * 6144 + (x_ - 1) * 2048 + l * 16;
      s0 += *(LAS v4f*)X; s1 += *(LAS v4f*)(X + 1024);
    }
#pragma unroll
    for (int e = 0; e < 4; ++e) { o8[e] = s0[e]; o8[4 + e] = s1[e]; }
  };
  v4f xg[6];
  float ov[8], ex[8];
  auto sadd = [](float a, float b) { float r; asm("v_add_f32 %0, %1, %2" : "=v"(r) : "v"(a), "v"(b)); return r; };
  auto smul = [](float a, float b) { float r; asm("v_mul_f32 %0, %1, %2" : "=v"(r) : "v"(a), "v"(b)); return r; };
  auto silu_step = [&](int i) {
    if (i < 3) {
#pragma unroll
      for (int e = 0; e < 8; ++e) { ov[e] = sadd(i == 0 ? accG[0][e] : ov[e], xg[2 * i + (e >> 2)][e & 3]); PIN(ov[e]); }
    } else if (i == 3) {
#pragma unroll
      for (int e = 0; e < 8; e += 2) { u32 t = pkbf_o(smul(ov[e], rs2), smul(ov[e + 1], rs2)); ov[e] = bflo(t); ov[e + 1] = bfhi(t); PIN(ov[e]); PIN(ov[e + 1]); }
    } else if (i == 4) {
#pragma unroll
      for (int e = 0; e < 8; ++e) { ex[e] = __builtin_amdgcn_exp2f(ov[e] * -1.44269504088896f); PIN(ex[e]); }
    } else if (i == 5) {
#pragma unroll
      for (int e = 0; e < 8; ++e) { ex[e] = __builtin_amdgcn_rcpf(1.0f + ex[e]); PIN(ex[e]); }
    } else if (i == 6) {
#pragma unroll
      for (int e = 0; e < 8; e += 2) { u32 t = pkbf_o(ov[e] * ex[e], ov[e + 1] * ex[e + 1]); gv[e] = bflo(t); gv[e + 1] = bfhi(t); PIN(gv[e]); PIN(gv[e + 1]); }
    }
  };
  static_for<0, 10>([&](auto V) {
    constexpr int v = V;
    v8s(&cur)[8] = (v & 1) ? fb : fa;
    v8s(&prv)[8] = (v & 1) ? fa : fb;
    auto mm2 = [&](auto U, v8s (&fr)[8], int s) {
      constexpr int u = U;
      if constexpr (u >= 7) { accS[0] = mfma32h(G[(u - 7) * 4 + s], fr[2 * s], accS[0]); accS[1] = mfma32h(G[(u - 7) * 4 + s], fr[2 * s + 1], accS[1]); }
      else { accG[0] = mfma32b(Wf[2 * u + (s >> 1)][s & 1], fr[2 * s], accG[0]); accG[1] = mfma32b(Wf[2 * u + (s >> 1)][s & 1], fr[2 * s + 1], accG[1]); }
      SCHED();
    };
    if constexpr (v < 9) {
      WAIT_VM(v <= 7 ? 4 : 0);
      if constexpr (v == 5) { if (w == 0) { PIN(fs); poll4(fs, (u32)(FL_S + l * 16)); } }
      BARRIER();
      if constexpr (v == 4) { if (w == 0) ld16c(fs, (u32)(FL_S + l * 16), FL); }
      if constexpr (v >= 1) mm2(IC<(v >= 1 ? v - 1 : 0)>{}, prv, 2);
#pragma unroll
      for (int s = 0; s < 4; ++s) {
        if constexpr (v >= 7) {
          LAS char* sb = smem + aslot(2 * v + (s >> 1)) + (s & 1) * 2048;
          cur[2 * s] = cat8(trread(sb + trS), trread(sb + trS + 1024));
          cur[2 * s + 1] = cat8(trread(sb + (trS ^ 64)), trread(sb + (trS ^ 64) + 1024));
        } else {
          LAS char* sb = smem + aslot(2 * v + (s >> 1)) + (s & 1) * 1024;
          cur[2 * s] = *(LAS v8s*)(sb + hb);
          cur[2 * s + 1] = *(LAS v8s*)(sb + (hb ^ 8192));
        }
      }
      if constexpr (v == 8) {
#pragma unroll
        for (int x_ = 1; x_ < 4; ++x_) {
          LAS char* X = smem + 49152 + (w ^ (2 * x_)) * 6144 + (x_ - 1) * 2048 + l * 16;
          xg[2 * x_ - 2] = *(LAS v4f*)X; xg[2 * x_ - 1] = *(LAS v4f*)(X + 1024);
        }
      }
      SCHED();
      if constexpr (v <= 6) issueP(2 * v + 4, IC<0>{});
      if constexpr (v >= 1) mm2(IC<(v >= 1 ? v - 1 : 0)>{}, prv, 3);
      if constexpr (v <= 6) issueP(2 * v + 4, IC<1>{});
      if constexpr (v == 8) { silu_step(0); silu_step(1); SCHED(); }
      mm2(V, cur, 0);
      if constexpr (v <= 6) issueP(2 * v + 5, IC<0>{});
      if constexpr (v == 7) { xchg_out(accG, 49152); SCHED(); }
      if constexpr (v == 8) { silu_step(2); silu_step(3); SCHED(); }
      mm2(V, cur, 1);
      if constexpr (v <= 6) issueP(2 * v + 5, IC<1>{});
      if constexpr (v == 8) { silu_step(4); SCHED(); }
    } else {
      mm2(IC<8>{}, prv, 2);
      silu_step(5); SCHED();
      mm2(IC<8>{}, prv, 3);
      silu_step(6); SCHED();
    }
  });
  xchg_out(accS, 0);
  BARRIER();
  {
    float o8[8], pv[8];
    xchg_in(accS, 0, o8);
#pragma unroll
    for (int e = 0; e < 8; e += 2) { u32 t = pkbf_o(o8[e] * oscale, o8[e + 1] * oscale); pv[e] = bflo(t) * gv[e]; pv[e + 1] = bfhi(t) * gv[e + 1]; }
    u32 a0 = pkbf(pv[0], pv[1]), a1 = pkbf(pv[2], pv[3]), b0 = pkbf(pv[4], pv[5]), b1 = pkbf(pv[6], pv[7]);
    auto r0 = __builtin_amdgcn_permlane32_swap(a0, b0, false, false);
    auto r1 = __builtin_amdgcn_permlane32_swap(a1, b1, false, false);
    v4u pk = {r0[0], r1[0], r0[1], r1[1]};
    v4u ov4;
#pragma unroll
    for (int e = 0; e < 4; ++e) ov4[e] = pkbf(bflo(xe[e]) + bflo(pk[e]), bfhi(xe[e]) + bfhi(pk[e]));
    *(v4u*)((char*)out + eoff) = ov4;
  }
}

__global__ void kmain_end() { asm volatile("km_pf_b:"); }

extern "C" void kernel_launch(void* const* d_in, const int* in_sizes, int n_in, void* d_out, int out_size, void* d_ws,
                              size_t ws_size, hipStream_t stream) {
  kmain<<<256, 512, 0, stream>>>((const u16*)d_in[0], (const u16*)d_in[1], (const u16*)d_in[2], (const u16*)d_in[4],
                                 (const u16*)d_in[5], (const u16*)d_in[6], (char*)d_ws + WS_BASE(ws_size), (u16*)d_out);
}
```

```cpp
#include <hip/hip_runtime.h>
#include <stdint.h>

typedef unsigned short u16;
typedef unsigned int u32;
typedef unsigned long long u64;
typedef short v4s __attribute__((ext_vector_type(4)));
typedef short v8s __attribute__((ext_vector_type(8)));
typedef _Float16 v4h __attribute__((ext_vector_type(4)));
typedef _Float16 v8h __attribute__((ext_vector_type(8)));
typedef __bf16 v2b __attribute__((ext_vector_type(2)));
typedef __bf16 v8b __attribute__((ext_vector_type(8)));
typedef float v2f __attribute__((ext_vector_type(2)));
typedef float v4f __attribute__((ext_vector_type(4)));
typedef float v16f __attribute__((ext_vector_type(16)));
typedef u32 v2u __attribute__((ext_vector_type(2)));
typedef u32 v4u __attribute__((ext_vector_type(4)));

#define LAS __attribute__((address_space(3)))
#define DEVI __device__ __forceinline__

constexpr int D_ = 512, F_ = 1792, DF_ = 257;
constexpr size_t OFF_H = 0;
constexpr size_t OFF_S1 = (size_t)8 << 20;
constexpr size_t OFF_WT = ((size_t)10 << 20) + (256 << 10);
constexpr size_t OFF_S2 = ((size_t)12 << 20) + (768 << 10);
constexpr size_t OFF_M = (size_t)15 << 20;
constexpr size_t OFF_FL = OFF_M;
constexpr u32 FL_S = 0, FL_A = 1024, FL_H = 2048;
constexpr size_t OFF_BC = OFF_M + 49152;
constexpr size_t OFF_MB = OFF_M + 98304;
constexpr size_t OFF_RS2 = OFF_M + 106496;
constexpr size_t OFF_PQ = OFF_M + 65536;
constexpr size_t OFF_CSP = OFF_M + 131072;
constexpr int S1P = 8192 + 128, WTP = 4608 + 128, S2P = 4096 + 128;
constexpr int SPIN_CAP = 1 << 16;


#define WS_BASE(n) (((n) - ((size_t)16 << 20)) & ~(size_t)0x1fffff)
template <int I> struct IC { static constexpr int v = I; constexpr operator int() const { return I; } };
template <int B, int E, class Fn> DEVI void static_for(Fn&& f) {
  if constexpr (B < E) { f(IC<B>{}); static_for<B + 1, E>(f); }
}
#define WAIT_VM(n) asm volatile("s_waitcnt vmcnt(%0)" ::"n"(n) : "memory")
#define BARRIER() asm volatile("s_waitcnt lgkmcnt(0)\n\ts_barrier" ::: "memory")
#define SCHED() __builtin_amdgcn_sched_barrier(0)
#define PIN(x) asm volatile("" : "+v"(x))

DEVI int s_of_pi(int pi) {
  int RG = pi >> 6, i = pi & 63;
  return 128 * (4 * (RG & 3) + (i & 3)) + 8 * (i >> 2) + (RG >> 2);
}
DEVI void dma2(u32 voff0, u32 voff1, const char* sbase, u32 lds) {
  asm volatile("s_mov_b32 m0, %3\n\ts_nop 0\n\tglobal_load_lds_dwordx4 %0, %2\n\tglobal_load_lds_dwordx4 %1, %2 offset:1024" ::"v"(voff0), "v"(voff1), "s"(sbase), "s"(lds) : "memory");
}
DEVI void dma2c(u32 voff0, u32 voff1, const char* sbase, u32 lds) {
  asm volatile("s_mov_b32 m0, %3\n\ts_nop 0\n\tglobal_load_lds_dwordx4 %0, %2 sc1\n\tglobal_load_lds_dwordx4 %1, %2 offset:1024 sc1" ::"v"(voff0), "v"(voff1), "s"(sbase), "s"(lds) : "memory");
}
template <int OFF> DEVI void dma1(u32 voff, const char* sbase, u32 lds) {
  asm volatile("s_mov_b32 m0, %2\n\ts_nop 0\n\tglobal_load_lds_dwordx4 %0, %1 offset:%3" ::"v"(voff), "s"(sbase), "s"(lds), "n"(OFF) : "memory");
}
template <int OFF> DEVI void dma1s(u32 voff, const char* sbase, u32 lds) {
  asm volatile("s_mov_b32 m0, %2\n\ts_nop 0\n\tglobal_load_lds_dwordx4 %0, %1 offset:%3 sc1" ::"v"(voff), "s"(sbase), "s"(lds), "n"(OFF) : "memory");
}
DEVI void dma1c(u32 voff, const char* sbase, u32 lds) {
  asm volatile("s_mov_b32 m0, %2\n\ts_nop 0\n\tglobal_load_lds_dword %0, %1 sc1" ::"v"(voff), "s"(sbase), "s"(lds) : "memory");
}
DEVI v2u ld8c_sync(u32 voff, const char* sb) { v2u d; asm volatile("global_load_dwordx2 %0, %1, %2 sc1\n\ts_waitcnt vmcnt(0)" : "=&v"(d) : "v"(voff), "s"(sb) : "memory"); return d; }
DEVI u32 ld4c_sync(u32 voff, const char* sb) { u32 d; asm volatile("global_load_dword %0, %1, %2 sc1\n\ts_waitcnt vmcnt(0)" : "=&v"(d) : "v"(voff), "s"(sb) : "memory"); return d; }
DEVI v4u ld16c_sync(u32 voff, const char* sb) { v4u d; asm volatile("global_load_dwordx4 %0, %1, %2 sc1\n\ts_waitcnt vmcnt(0)" : "=&v"(d) : "v"(voff), "s"(sb) : "memory"); return d; }
template <class T> DEVI void ld16c(T& d, u32 voff, const char* sb) { asm volatile("global_load_dwordx4 %0, %1, %2 sc1" : "=&v"(d) : "v"(voff), "s"(sb) : "memory"); }
template <class T> DEVI void ld4c(T& d, u32 voff, const char* sb) { asm volatile("global_load_dword %0, %1, %2 sc1" : "=&v"(d) : "v"(voff), "s"(sb) : "memory"); }
template <class T> DEVI void ld16(T& d, u32 voff, const char* sb) { asm volatile("global_load_dwordx4 %0, %1, %2" : "=&v"(d) : "v"(voff), "s"(sb) : "memory"); }
DEVI void ld2(u32& d, u32 voff, const char* sb) { asm volatile("global_load_ushort %0, %1, %2" : "=&v"(d) : "v"(voff), "s"(sb) : "memory"); }
template <class T> DEVI void st16c(u32 voff, const char* sb, T v) { asm volatile("global_store_dwordx4 %0, %1, %2 sc1\n\ts_nop 1" ::"v"(voff), "v"(v), "s"(sb) : "memory"); }
template <class T> DEVI void st8c(u32 voff, const char* sb, T v) { asm volatile("global_store_dwordx2 %0, %1, %2 sc1" ::"v"(voff), "v"(v), "s"(sb) : "memory"); }
template <class T> DEVI void st4c(u32 voff, const char* sb, T v) { asm volatile("global_store_dword %0, %1, %2 sc1" ::"v"(voff), "v"(v), "s"(sb) : "memory"); }

DEVI v4s trread(LAS char* p) { return __builtin_amdgcn_ds_read_tr16_b64_v4i16((LAS v4s*)p); }
DEVI v8s cat8(v4s a, v4s b) { return __builtin_shufflevector(a, b, 0, 1, 2, 3, 4, 5, 6, 7); }
DEVI float bflo(u32 v) { return __builtin_bit_cast(float, v << 16); }
DEVI float bfhi(u32 v) { return __builtin_bit_cast(float, v & 0xffff0000u); }
DEVI float relu(float v) { return __builtin_bit_cast(float, max(__builtin_bit_cast(int, v), 0)); }
DEVI float rbf(float v) { return (float)(__bf16)v; }
DEVI u32 pkbf(float a, float b) { v2b p = {(__bf16)a, (__bf16)b}; return __builtin_bit_cast(u32, p); }
DEVI u32 pkbf_o(float a, float b) { u32 r = pkbf(a, b); asm("" : "+v"(r)); return r; }
DEVI float cosr(float rev) { return __builtin_amdgcn_cosf(rev); }
DEVI float sinr(float rev) { return __builtin_amdgcn_sinf(rev); }
DEVI v4f mfma16h(v8h a, v8h b, v4f c) { return __builtin_amdgcn_mfma_f32_16x16x32_f16(a, b, c, 0, 0, 0); }
DEVI v4f mfma16b(v8s a, v4u b, v4f c) { return __builtin_amdgcn_mfma_f32_16x16x32_bf16(__builtin_bit_cast(v8b, a), __builtin_bit_cast(v8b, b), c, 0, 0, 0); }
DEVI v16f mfma32b(v8s a, v8s b, v16f c) { return __builtin_amdgcn_mfma_f32_32x32x16_bf16(__builtin_bit_cast(v8b, a), __builtin_bit_cast(v8b, b), c, 0, 0, 0); }
DEVI v16f mfma32h(v8h a, v8s b, v16f c) { return __builtin_amdgcn_mfma_f32_32x32x16_f16(a, __builtin_bit_cast(v8h, b), c, 0, 0, 0); }
DEVI v8h pack_ri(v4f re, v4f im) {
  v8h r;
  r[0] = (_Float16)re[0]; r[1] = (_Float16)re[1]; r[2] = (_Float16)re[2]; r[3] = (_Float16)re[3];
  r[4] = (_Float16)im[0]; r[5] = (_Float16)im[1]; r[6] = (_Float16)im[2]; r[7] = (_Float16)im[3];
  return r;
}
DEVI v4h pack4h(v4f a) { v4h r; r[0] = (_Float16)a[0]; r[1] = (_Float16)a[1]; r[2] = (_Float16)a[2]; r[3] = (_Float16)a[3]; return r; }
DEVI void twid(v4f& re, v4f& im, const v4f& c, const v4f& s) {
#pragma unroll
  for (int r = 0; r < 4; ++r) { float a = re[r], b = im[r]; re[r] = a * c[r] + b * s[r]; im[r] = b * c[r] - a * s[r]; }
}
template <int CTRL> DEVI float dppadd(float v) {
  return v + __builtin_bit_cast(float, __builtin_amdgcn_update_dpp(0, __builtin_bit_cast(int, v), CTRL, 0xf, 0xf, true));
}
DEVI float wave_sum(float v) {
  v = dppadd<0xB1>(v); v = dppadd<0x4E>(v); v = dppadd<0x141>(v); v = dppadd<0x140>(v);
  int bb = __builtin_bit_cast(int, v);
  float a0 = __builtin_bit_cast(float, __builtin_amdgcn_readlane(bb, 0)), a1 = __builtin_bit_cast(float, __builtin_amdgcn_readlane(bb, 16));
  float a2 = __builtin_bit_cast(float, __builtin_amdgcn_readlane(bb, 32)), a3 = __builtin_bit_cast(float, __builtin_amdgcn_readlane(bb, 48));
  return (a0 + a1) + (a2 + a3);
}
template <int CTRL> DEVI u32 dppmax(u32 v) { return max(v, (u32)__builtin_amdgcn_update_dpp(0, (int)v, CTRL, 0xf, 0xf, true)); }
DEVI u32 wave_max(u32 v) {
  v = dppmax<0xB1>(v); v = dppmax<0x4E>(v); v = dppmax<0x141>(v); v = dppmax<0x140>(v);
  u32 a0 = __builtin_amdgcn_readlane(v, 0), a1 = __builtin_amdgcn_readlane(v, 16), a2 = __builtin_amdgcn_readlane(v, 32), a3 = __builtin_amdgcn_readlane(v, 48);
  return max(max(a0, a1), max(a2, a3));
}

struct Frag1 { v8s x0, x1, w1, w2; };

constexpr int L_SCR = 147456;
constexpr int L_RED = L_SCR + 1024, L_MX = L_SCR + 1088, L_BC0 = L_SCR + 2048, L_BC1 = L_SCR + 2304;
constexpr int L_TAB = L_SCR + 3072;
constexpr int L_T = L_SCR + 7168, L_FA = L_SCR, L_E1 = L_T, L_E2 = 81920, L_ZX = 81920;
constexpr int L_C1 = 98304, L_IMG = L_C1 + 9216;
constexpr int L_FR = 131072;
constexpr int E1P = 544, E2P = 1024 + 72;
DEVI constexpr int wslot(int n) {
  constexpr int u[14] = {6, 7, 8, 0, 1, 2, 3, 4, 6, 7, 8, 0, 1, 2};
  return u[n] * 16384;
}
DEVI constexpr int aslot(int n) {
  constexpr int u[18] = {3, 4, 5, 9, 2, 6, 7, 8, 0, 1, 3, 4, 5, 9, 2, 6, 7, 8};
  return u[n] * 16384;
}

__global__ __launch_bounds__(512, 2) void kmain(const u16* __restrict__ x, const u16* __restrict__ wr, const u16* __restrict__ wi,
                                                const u16* __restrict__ Wg, const u16* __restrict__ Wu, const u16* __restrict__ Wd,
                                                char* __restrict__ ws, u16* __restrict__ out) {
  __shared__ __attribute__((aligned(16))) char smem_[163840];
  LAS char* smem = (LAS char*)smem_;
  const u32 lbase = (u32)(size_t)smem;
  const int tid = threadIdx.x, l = tid & 63;
  const int w = __builtin_amdgcn_readfirstlane(tid >> 6);
  const int b = blockIdx.x, xcd = b & 7, idx = b >> 3;
  const bool ffn = idx < 28;
  const int g = l >> 4, o = l & 15;
  const int h = l >> 5, blk = (l >> 4) & 1, q = (l & 15) >> 2, pp = l & 3;
  const char* FL = ws + OFF_FL;
  const char* BC = ws + OFF_BC;
  asm volatile("" ::"s"(x), "s"(wr), "s"(wi), "s"(Wg), "s"(Wu), "s"(Wd), "s"(ws), "s"(out));

  const int pi = b * 8 + w;
  v4u xr;
  ld16(xr, (u32)(s_of_pi(pi) * 1024 + l * 16), (const char*)x);
  u64 kpc; u32 klen;
  asm volatile("s_getpc_b64 %0\nkm_pf_a:\n\ts_mov_b32 %1, km_pf_b-km_pf_a-16" : "=s"(kpc), "=s"(klen));
  auto pf_code = [&]() { dma1<0>(min((u32)(((idx & 3) * 8 + w) * 1024 + l * 16), klen) & ~15u, (const char*)(kpc & ~15ull), lbase + L_FA); };
  if (!ffn) pf_code();
  if (tid == 0) dma1s<0>(0u, BC, lbase + L_BC0);
  const int fkb = (b & 7) * 2 + (w >> 2), fcq = w & 3;
  auto filt_dma = [&]() {
    const u32 ks = (u32)((idx >> 1) + 16 * fkb + 256 * (4 * (idx & 1) + ((l >> 3) & 3)));
    const u32 base = ((ks * (DF_ * 2)) & ~15u) + fcq * 128;
    const u32 c0 = ((l & 7) ^ (((l >> 4) & 1) << 2)) << 4;
    if (l < 32) dma1<0>(base + c0, (const char*)wr, lbase + L_FR + w * 2048);
    else dma1<0>(base + c0, (const char*)wi, lbase + L_FR + w * 2048);
    if (fcq == 3) {
      const u32 t0 = min(base + 128 + (c0 & 16), (u32)(2048 * DF_ * 2 - 16));
      if (l < 32) dma1<0>(t0, (const char*)wr, lbase + L_FR + w * 2048 + 1024);
      else dma1<0>(t0, (const char*)wi, lbase + L_FR + w * 2048 + 1024);
    }
  };
  const int p = (xcd >> 1) * 4 + idx / 7, j = (xcd & 1) * 7 + idx % 7;
  const int sh = w >> 2, fq = w & 3;
  u32 aoff[2];
#pragma unroll
  for (int i = 0; i < 2; ++i) {
    int row = (2 * w + i) * 8 + (l >> 3);
    aoff[i] = s_of_pi((p & 15) * 128 + row) * 1024 + (((l & 7) ^ ((row >> 1) & 7)) << 4) - i * 1024;
  }
  const u32 boff = (u32)(2 * w * 4 + (l >> 4)) * (F_ * 2) + j * 256 + (((l & 15) ^ ((l >> 4) << 2)) << 4);
  const u32 boff1 = boff + 4 * F_ * 2 - 1024;
  const int rot = p >> 1;
  auto issue = [&](int t, int slot) {
    const int kt = (t + rot) & 7;
    u32 dst = lbase + slot * 49152 + w * 2048;
    dma2(aoff[0], aoff[1], (const char*)x + kt * 128, dst);
    dma2(boff, boff1, (const char*)Wg + (size_t)kt * (64 * F_ * 2), dst + 16384);
    dma2(boff, boff1, (const char*)Wu + (size_t)kt * (64 * F_ * 2), dst + 32768);
  };
  const int sp = xcd * 4 + (idx - 28);
  if (ffn) issue(0, 0); else filt_dma();

  u32 E = 0, tick = 0;
  const bool coord = (b == 224) && (w == 0);
  auto do_tick = [&]() { if (coord) { ++tick; if (l == 0) st8c(0u, BC, v2u{E, tick}); } };
  auto get_epoch = [&]() {
    const v2u b0 = *(LAS v2u*)(smem + L_BC0);
    v2u b1 = *(LAS v2u*)(smem + L_BC1);
    int spins = 0;
    while (__builtin_amdgcn_readfirstlane((b0[0] ^ b1[0]) | (b0[1] ^ b1[1])) == 0 && ++spins < SPIN_CAP) {
      __builtin_amdgcn_s_sleep(4);
      b1 = ld8c_sync(0u, BC);
    }
    E = __builtin_amdgcn_readfirstlane(b1[0]);
  };
  auto signal_a = [&]() {
    v4u r0 = *(LAS v4u*)(smem + L_RED), r1 = *(LAS v4u*)(smem + L_RED + 16);
    u32 m = max(max(max(r0[0], r0[1]), max(r0[2], r0[3])), max(max(r1[0], r1[1]), max(r1[2], r1[3])));
    if (l == 0) { st4c((u32)(FL_A + b * 4), FL, E | (m >> 7)); *(LAS u32*)(smem + L_MX + 4) = E; }
  };
  auto poll4 = [&](v4u f, u32 voff) -> v4u {
    int spins = 0;
    while (!__all((((f[0] ^ E) | (f[1] ^ E) | (f[2] ^ E) | (f[3] ^ E)) >> 8) == 0) && ++spins < SPIN_CAP) {
      do_tick();
      __builtin_amdgcn_s_sleep(12);
      f = ld16c_sync(voff, FL);
    }
    return f;
  };
  auto check_a = [&](v4u f) {
    f = poll4(f, (u32)(FL_A + l * 16));
    u32 m = wave_max(max(max(f[0], f[1]), max(f[2], f[3]))) & 0xffu;
    if (l == 0) *(LAS u32*)(smem + L_MX) = m;
  };
  if (coord) {
#pragma unroll
    for (int i = 0; i < 5; ++i) dma2c(i ? (u32)(FL_H + ((i - 1) * 128 + l) * 64) : (u32)(l * 16), i ? (u32)(FL_H + ((i - 1) * 128 + 64 + l) * 64 - 1024) : (u32)(l * 16), FL, lbase + i * 2048);
  }
  u32 sq[8];
  if (!ffn && w == 7) {
    const int kss = (sp >> 1) + 16 * (l >> 2) + 256 * (4 * (sp & 1) + (l & 3)), km = (2048 - kss) & 2047;
    const u32 o1 = (u32)(kss * DF_ * 2), o2 = (u32)(km * DF_ * 2);
    ld2(sq[0], o1, (const char*)wr); ld2(sq[1], o1, (const char*)wi); ld2(sq[2], o2, (const char*)wr); ld2(sq[3], o2, (const char*)wi);
    ld2(sq[4], o1 + 512, (const char*)wr); ld2(sq[5], o1 + 512, (const char*)wi); ld2(sq[6], o2 + 512, (const char*)wr); ld2(sq[7], o2 + 512, (const char*)wi);
  }

  {
    const float fo = (float)o, fg = (float)(4 * g);
    if (w < 2) {
      const float st = fo * (1.0f / 32.0f), a0 = fmaf(fg, st, w ? 0.25f : 0.f);
      v4u hi, lo;
#pragma unroll
      for (int e2 = 0; e2 < 4; ++e2) {
        float c0 = cosr(fmaf((float)(16 * (e2 >> 1) + 2 * (e2 & 1)), st, a0)), c1 = cosr(fmaf((float)(16 * (e2 >> 1) + 2 * (e2 & 1) + 1), st, a0));
        u32 ch = pkbf_o(c0, c1);
        hi[e2] = ch; lo[e2] = pkbf(c0 - bflo(ch), c1 - bfhi(ch));
      }
      *(LAS v4u*)(smem + L_C1 + w * 2048 + l * 16) = hi; *(LAS v4u*)(smem + L_C1 + w * 2048 + 1024 + l * 16) = lo;
    } else if (w < 5) {
      const float st = w == 2 ? (float)(o & 7) * (1.0f / 16.0f) : fo * (w == 3 ? 1.0f / 16.0f : 1.0f / 8.0f);
      const float a0 = (w == 2 && o >= 8) ? 0.25f : 0.f;
      const float sc = w == 2 ? 0.0625f : ((w == 4 && (g >> 1) != (o >> 3)) ? 0.f : 1.0f);
      v8h m;
#pragma unroll
      for (int e = 0; e < 4; ++e) { float a = fmaf(fg + (float)e, st, a0); m[e] = (_Float16)(sc * cosr(a)); m[4 + e] = (_Float16)(sc * sinr(a)); }
      *(LAS v8h*)(smem + (w == 2 ? L_C1 + 4096 : L_TAB + (w - 3) * 1024) + l * 16) = m;
    } else {
      const float st = w == 7 ? fo * (1.0f / 256.0f) : (fo + (w == 6 ? 16.f : 0.f)) * (1.0f / 512.0f);
      v4f c, sn;
#pragma unroll
      for (int e = 0; e < 4; ++e) { float a = (fg + (float)e) * st; c[e] = cosr(a); sn[e] = sinr(a); }
      LAS char* dst = smem + (w == 7 ? L_TAB + 2048 : L_C1 + 5120 + (w - 5) * 2048) + l * 16;
      *(LAS v4f*)dst = c; *(LAS v4f*)(dst + 1024) = sn;
    }
    BARRIER();
    if (ffn) filt_dma();
    LAS char* C1 = smem + L_C1 + l * 16;
    const v4u Fhr = *(LAS v4u*)C1, Flr = *(LAS v4u*)(C1 + 1024), Fhi = *(LAS v4u*)(C1 + 2048), Fli = *(LAS v4u*)(C1 + 3072);
    const v8h M2 = *(LAS v8h*)(C1 + 4096);
    v4f twc[2], tws[2];
#pragma unroll
    for (int u = 0; u < 2; ++u) { twc[u] = *(LAS v4f*)(C1 + 5120 + u * 2048); tws[u] = *(LAS v4f*)(C1 + 6144 + u * 2048); }
    if (ffn) WAIT_VM(8); else if (w == 7) WAIT_VM(11); else WAIT_VM(3);
    if (coord) {
      WAIT_VM(0);
      v4u f[10];
#pragma unroll
      for (int i = 0; i < 10; ++i) f[i] = *(LAS v4u*)(smem + i * 1024 + l * 16);
      u32 c = __builtin_amdgcn_readfirstlane(*(LAS u32*)(smem + L_BC0)) >> 8;
      bool hit;
      do {
        c = (c + 1) & 0xffffffu; hit = false;
#pragma unroll
        for (int i = 0; i < 10; ++i) hit |= ((f[i][0] >> 8) == c) | ((f[i][1] >> 8) == c) | ((f[i][2] >> 8) == c) | ((f[i][3] >> 8) == c);
      } while (__any(hit));
      E = c << 8;
      do_tick();
    }
    PIN(xr);
    const float c0 = bflo(__builtin_amdgcn_readfirstlane(xr[0]));
    v2f xv[4], s2 = {0.f, 0.f}, q2 = {0.f, 0.f};
#pragma unroll
    for (int e = 0; e < 4; ++e) {
      xv[e] = v2f{bflo(xr[e]), bfhi(xr[e])};
      v2f t = xv[e] - v2f{c0, c0};
      s2 += t; q2 = __builtin_elementwise_fma(t, t, q2);
    }
    const float dmean = wave_sum(s2[0] + s2[1]) * (1.0f / 512.0f), msq = wave_sum(q2[0] + q2[1]) * (1.0f / 512.0f);
    do_tick();
    const float mb = rbf(c0 + dmean);
    const float var = rbf(fmaxf(msq - dmean * dmean, 0.f));
    const float sd = rbf(__builtin_amdgcn_sqrtf(rbf(var + 1e-5f)));
    const float rs = __builtin_amdgcn_rcpf(sd);
#pragma unroll
    for (int e = 0; e < 4; ++e) {
      v2f t = xv[e] - v2f{mb, mb};
      u32 tb = pkbf_o(t[0], t[1]);
      t = v2f{bflo(tb), bfhi(tb)} * v2f{rs, rs};
      xr[e] = pkbf(t[0], t[1]);
    }
    *(LAS v4u*)(smem + L_IMG + w * 1024 + l * 16) = xr;
    if (l < 2) st4c((u32)(pi * 4 + l * 8192), ws + OFF_MB, l ? rs * rs : -mb);
    const v8s xf = cat8(trread(smem + L_IMG + w * 1024 + l * 8), trread(smem + L_IMG + w * 1024 + 512 + l * 8));
    const int kt1 = (1 + rot) & 7;
    const u32 dst1 = lbase + 49152 + w * 2048;
    if (ffn) dma2(aoff[0], aoff[1], (const char*)x + kt1 * 128, dst1);
    const v8s xg_ = __builtin_bit_cast(v8s, __builtin_bit_cast(v4u, xf) ^ v4u{0x80000000u, 0x80000000u, 0x80000000u, 0x80000000u});
    const v4f z = {0.f, 0.f, 0.f, 0.f};
    v4f dre[2], dim[2];
#pragma unroll
    for (int u = 0; u < 2; ++u) { dre[u] = mfma16b(u ? xg_ : xf, Flr, z); dim[u] = mfma16b(u ? xg_ : xf, Fli, z); }
#pragma unroll
    for (int u = 0; u < 2; ++u) { dre[u] = mfma16b(u ? xg_ : xf, Fhr, dre[u]); dim[u] = mfma16b(u ? xg_ : xf, Fhi, dim[u]); }
#pragma unroll
    for (int u = 0; u < 2; ++u) {
      twid(dre[u], dim[u], twc[u], tws[u]);
      const v8h bb = pack_ri(dre[u], dim[u]);
      v4f d2 = mfma16h(bb, M2, z);
      if (u == 0) {
        const u32 nv = o == 8 ? 0xac002c00u : 0u;
        v4f n = mfma16h(bb, __builtin_bit_cast(v8h, v4u{nv, nv, 0u, 0u}), z);
        if (l == 8) d2[0] = n[0];
      }
      *(LAS v4h*)(smem + L_T + ((o >> 3) * 8 + w) * 544 + (o & 7) * 64 + u * 32 + g * 8) = pack4h(d2);
    }
    if (ffn) {
      dma2(boff, boff1, (const char*)Wg + (size_t)kt1 * (64 * F_ * 2), dst1 + 16384);
      dma2(boff, boff1, (const char*)Wu + (size_t)kt1 * (64 * F_ * 2), dst1 + 32768);
    }
  }
  v8s su; v4u fu, tu;
  auto round1_read = [&]() {
    LAS char* rp = smem + L_T + (4 * (g & 1) + (o >> 2)) * 544 + (w * 32 + (g >> 1) * 16) * 2 + (o & 3) * 8;
    su = cat8(trread(rp), trread(rp + 8 * 544));
    LAS char* fp_ = smem + L_FR + w * 2048 + q * 128 + (((2 * g + (pp >> 1)) ^ ((q >> 1) << 2)) << 4) + (pp & 1) * 8;
    fu = __builtin_bit_cast(v4u, cat8(trread(fp_), trread(fp_ + 512)));
    LAS char* tp = smem + L_FR + w * 2048 + 1024 + q * 128 + (pp >> 1) * 16 + (pp & 1) * 8;
    tu = __builtin_bit_cast(v4u, cat8(trread(tp), trread(tp + 512)));
  };
  auto round1_max = [&]() {
    typedef unsigned short v2us __attribute__((ext_vector_type(2)));
    const int dl = (idx >> 1) & 7;
    const u32 msk = (fcq == 0 && l < dl) ? 0u : 0x7fff7fffu, msk2 = (fcq == 3 && o <= dl) ? 0x7fff7fffu : 0u;
    v2us m2 = {0, 0};
#pragma unroll
    for (int e = 0; e < 4; ++e) {
      u32 t = fu[e] & msk, t2 = tu[e] & msk2;
      m2 = __builtin_elementwise_max(m2, __builtin_elementwise_max(__builtin_bit_cast(v2us, t), __builtin_bit_cast(v2us, t2)));
    }
    const u32 mx = wave_max(max((u32)m2[0], (u32)m2[1]));
    if (l == 0) *(LAS u32*)(smem + L_RED + w * 4) = mx;
  };
  auto round1_store = [&]() {
    st16c((u32)(b * S1P + (w * 32 + (g >> 1) * 16 + o) * 32 + (g & 1) * 16), ws + OFF_S1, su);
    st16c((u32)((idx * 16 + fkb) * WTP + (fcq * 64 + l) * 16), ws + OFF_WT, fu);
    if (fcq == 3 && l < 16) st16c((u32)((idx * 16 + fkb) * WTP + (256 + l) * 16), ws + OFF_WT, tu);
  };

  const int RG = xcd * 4 + (idx >> 3), ct = idx & 7;
  const int kd = xcd * 32 + ((idx + 3) & 31);
  const u32 swz = ((l & 7) ^ (((l >> 4) & 1) << 2)) << 4;
  const u32 ro0 = (u32)((l >> 3) * 1024) + swz, ro1 = ro0 + 8192 - 1024;
  const char* bsrc = (const char*)Wd + (size_t)(w * 16) * 1024 + ct * 128;
  auto issueW = [&](int n) { dma2(ro0, ro1, bsrc + (size_t)n * 131072, lbase + wslot(n) + w * 2048); };
  auto pfl = [&](int n) {
    const char* src = (const char*)Wd + (size_t)n * 131072 + ((idx & 3) * 8 + w) * 4096;
    dma1<0>((u32)((l >> 5) * 1024 + (l & 31) * 16), src, lbase + L_FA); dma1<0>((u32)((l >> 5) * 1024 + (l & 31) * 16), src + 2048, lbase + L_FA);
  };
  const int dh = w & 1, kq = w >> 1, ph = kq >> 1, kh = kq & 1;
  const int epi = RG * 64 + ph * 32 + (l & 31);
  const u32 eoff = (u32)(s_of_pi(epi) * D_ + ct * 64 + dh * 32 + kh * 16 + h * 8) * 2;
  v4u wt, s1v, xe; v4f pq[4]; float rs2;
  auto load_xe = [&]() { ld16(xe, eoff, (const char*)x); };
  auto load_s1 = [&]() { ld16c(s1v, (u32)(((4 * w + g) * 8 + (o >> 1)) * S1P + kd * 32 + (o & 1) * 16), ws + OFF_S1); };
  auto load_wt = [&]() { ld16c(wt, (u32)((w * 64 + l) * WTP + (kd + ((2 * w + (g >> 1)) & 7)) * 16), ws + OFF_WT); };
  auto load_rs = [&]() { ld4c(rs2, (u32)(epi * 4), ws + OFF_RS2); };
  v4f t2c, t2s, t3c, t3s;
  auto trig2 = [&]() {
    const float fw = (float)w, st2 = fw * (1.0f / 128.0f), a2 = fw * (float)(o + 64 * g) * (1.0f / 2048.0f);
#pragma unroll
    for (int e = 0; e < 4; ++e) { float b2 = fmaf((float)e, st2, a2); t2c[e] = cosr(b2); t2s[e] = sinr(b2); }
    PIN(t2c); PIN(t2s);
  };
  auto trig3 = [&]() {
    const float f7 = (float)(o & 7), st3 = f7 * (1.0f / 128.0f), a3 = f7 * (float)(2 * w + (o >> 3) + 64 * g) * (1.0f / 2048.0f);
#pragma unroll
    for (int e = 0; e < 4; ++e) { float b3 = fmaf((float)e, st3, a3); t3c[e] = cosr(b3); t3s[e] = sinr(b3); }
    PIN(t3c); PIN(t3s);
  };
  v4u f16; v4f t1c, t1s, re, im;
  const v4f z4 = {0.f, 0.f, 0.f, 0.f};
  auto imf = [](v4u f) { return __builtin_bit_cast(v8h, v4u{f[2] ^ 0x80008000u, f[3] ^ 0x80008000u, f[0], f[1]}); };
  auto segA0 = [&]() {
    f16 = *(LAS v4u*)(smem + L_TAB + l * 16);
    t1c = *(LAS v4f*)(smem + L_TAB + 2048 + l * 16); t1s = *(LAS v4f*)(smem + L_TAB + 3072 + l * 16);
  };
  auto segA1 = [&]() {
    PIN(s1v);
    const v8h a = __builtin_bit_cast(v8h, s1v);
    re = mfma16h(a, __builtin_bit_cast(v8h, f16), z4); im = mfma16h(a, imf(f16), z4);
  };
  auto segA2 = [&]() {
    twid(re, im, t1c, t1s);
    const v8h a = pack_ri(re, im);
    re = mfma16h(__builtin_bit_cast(v8h, f16), a, z4); im = mfma16h(imf(f16), a, z4);
  };
  auto segA3 = [&]() {
    twid(re, im, t2c, t2s);
    *(LAS v4h*)(smem + L_E1 + w * E1P + o * 32 + g * 8) = pack4h(re);
    *(LAS v4h*)(smem + L_E1 + (8 + w) * E1P + o * 32 + g * 8) = pack4h(im);
  };

  if (ffn) {
    v16f acc[2][2] = {};
    float mbr[2];
    const u32 wb0 = (u32)((8 * h + q) * 256 + ((fq ^ q) << 6) + blk * 32 + pp * 8);
    const u32 r1 = 16384 + sh * 16384, r2 = 32768 - sh * 16384;
    const int xrow = sh * 64 + (l & 31);
    const u32 xb0 = (u32)(xrow * 128 + ((h ^ ((xrow >> 1) & 7)) << 4));
    auto rd = [&](Frag1& f, LAS char* sb, int i) {
      LAS char* xa = sb + (xb0 ^ (i << 5));
      LAS char* wa_ = sb + wb0 + (i << 12);
      f.w1 = cat8(trread(wa_ + r1), trread(wa_ + r1 + 1024));
      f.x0 = *(LAS v8s*)xa; f.x1 = *(LAS v8s*)(xa + 4096);
      f.w2 = cat8(trread(wa_ + r2), trread(wa_ + r2 + 1024));
    };
    const u32 one = ((l & 15) == ((l >> 4) & 1)) ? 0x3f803f80u : 0u;
    const v8s sel = __builtin_bit_cast(v8s, v4u{one, one, one, one});
    v4f csa = {0.f, 0.f, 0.f, 0.f};
    Frag1 A, B;
    WAIT_VM(7);
    BARRIER();
    rd(A, smem, 0);
    round1_read();
    dma2(aoff[0], aoff[1], (const char*)x + ((2 + rot) & 7) * 128, lbase + 2 * 49152 + w * 2048);
    u32 c1[4], c2[4];
    static_for<0, 8>([&](auto T) {
      constexpr int t = T, slot = t % 3, nslot = (t + 1) % 3, pslot = (t + 2) % 3;
      LAS char* sb = smem + slot * 49152;
      {
        const int kt = (t + 2 + rot) & 7;
        const char* s1 = t < 6 ? (const char*)Wg + (size_t)kt * (64 * F_ * 2) : bsrc + (size_t)((t - 6) * 3 + 1) * 131072;
        const char* s2 = t < 6 ? (const char*)Wu + (size_t)kt * (64 * F_ * 2) : bsrc + (size_t)((t - 6) * 3 + 2) * 131072;
        const u32 o2 = t < 6 ? boff : ro0, o3 = t < 6 ? boff1 : ro1;
        const u32 dst = lbase + pslot * 49152 + w * 2048;
        rd(B, sb, 1); SCHED();
        acc[0][0] = mfma32b(A.w1, A.x0, acc[0][0]); acc[0][1] = mfma32b(A.w1, A.x1, acc[0][1]);
        if constexpr (t == 0) csa = mfma16b(sel, __builtin_bit_cast(v4u, A.w1), csa);
        SCHED();
        dma1<0>(o2, s1, dst + 16384);
        if constexpr (t == 2) load_xe();
        if constexpr (t == 7) load_wt();
        acc[1][0] = mfma32b(A.w2, A.x0, acc[1][0]); acc[1][1] = mfma32b(A.w2, A.x1, acc[1][1]); SCHED();
        if constexpr (t == 0) { round1_store(); round1_max(); SCHED(); }
        if constexpr (t == 3) { trig2(); SCHED(); }
        if constexpr (t == 4) { trig3(); SCHED(); }
        rd(A, sb, 2); SCHED();
        acc[0][0] = mfma32b(B.w1, B.x0, acc[0][0]); SCHED();
        dma1<1024>(o3, s1, dst + 16384);
        acc[0][1] = mfma32b(B.w1, B.x1, acc[0][1]);
        if constexpr (t == 0) csa = mfma16b(sel, __builtin_bit_cast(v4u, B.w1), csa);
        acc[1][0] = mfma32b(B.w2, B.x0, acc[1][0]); acc[1][1] = mfma32b(B.w2, B.x1, acc[1][1]); SCHED();
        dma1<0>(o2, s2, dst + 32768);
        if constexpr (t == 6) {
          load_rs();
        }
        if constexpr (t == 7) { WAIT_VM(13); segA1(); SCHED(); }
        rd(B, sb, 3); SCHED();
        if constexpr (t == 7) {
#pragma unroll
          for (int i = 0; i < 4; ++i) { PIN(c1[i]); PIN(c2[i]); }
          PIN(mbr[0]); PIN(mbr[1]);
          const v8s cs1 = __builtin_bit_cast(v8s, v4u{c1[0], c1[1], c1[2], c1[3]}), cs2 = __builtin_bit_cast(v8s, v4u{c2[0], c2[1], c2[2], c2[3]});
          const u32 m0_ = pkbf(mbr[0], mbr[0]), m1_ = pkbf(mbr[1], mbr[1]);
          const v8s mb0 = __builtin_bit_cast(v8s, v4u{m0_, m0_, m0_, m0_}), mb1 = __builtin_bit_cast(v8s, v4u{m1_, m1_, m1_, m1_});
          acc[0][0] = mfma32b(cs1, mb0, acc[0][0]); acc[1][0] = mfma32b(cs2, mb0, acc[1][0]);
          acc[0][1] = mfma32b(cs1, mb1, acc[0][1]); acc[1][1] = mfma32b(cs2, mb1, acc[1][1]); SCHED();
          segA2(); SCHED();
        }
        acc[0][0] = mfma32b(A.w1, A.x0, acc[0][0]); acc[0][1] = mfma32b(A.w1, A.x1, acc[0][1]);
        if constexpr (t == 0) csa = mfma16b(sel, __builtin_bit_cast(v4u, A.w1), csa);
        SCHED();
        dma1<1024>(o3, s2, dst + 32768);
        acc[1][0] = mfma32b(A.w2, A.x0, acc[1][0]); acc[1][1] = mfma32b(A.w2, A.x1, acc[1][1]); SCHED();
      }
      if constexpr (t < 7) {
        const char* s0 = t < 5 ? (const char*)x + ((t + 3 + rot) & 7) * 128 : bsrc + (size_t)((t - 5) * 3) * 131072;
        const u32 o0 = t < 5 ? aoff[0] : ro0, o1 = t < 5 ? aoff[1] : ro1;
        const u32 dst = lbase + slot * 49152 + w * 2048;
        if constexpr (t == 0) csa = mfma16b(sel, __builtin_bit_cast(v4u, B.w1), csa);
        WAIT_VM(t == 0 ? 8 : t == 2 ? 7 : t == 6 ? 18 : 6);
        if constexpr (t == 5) { if (w == 0) check_a(*(LAS v4u*)(smem + L_FA + l * 16)); }
        BARRIER();
        acc[0][0] = mfma32b(B.w1, B.x0, acc[0][0]); SCHED();
        rd(A, smem + nslot * 49152, 0);
        if constexpr (t == 6) segA0();
        SCHED();
        if constexpr (t == 0) {
          if (l < 16) {
#pragma unroll
            for (int i = 0; i < 2; ++i) {
              const float hi = rbf(csa[i]);
              st4c((u32)(((j * 16 + p) * 256 + sh * 128 + fq * 32 + i * 16 + l) * 4), ws + OFF_CSP, pkbf(hi, csa[i] - hi));
            }
          }
        }
        if constexpr (t == 0) { if (tid == 0) dma1s<0>(0u, BC, lbase + L_BC1); }
        if constexpr (t == 4) { if (w == 0) dma1s<0>((u32)(FL_A + l * 16), FL, lbase + L_FA); }
        if constexpr (t == 5) {
          const u32 co = (u32)(((j * 16 + (p & 1) + 8 * h) * 256 + fq * 32 + (l & 31)) * 4);
#pragma unroll
          for (int i = 0; i < 4; ++i) { ld4c(c1[i], co + sh * 512 + i * 2048, ws + OFF_CSP); ld4c(c2[i], co + (1 - sh) * 512 + i * 2048, ws + OFF_CSP); }
          ld4c(mbr[0], (u32)((p * 128 + xrow) * 4), ws + OFF_MB); ld4c(mbr[1], (u32)((p * 128 + xrow + 32) * 4), ws + OFF_MB);
          load_s1();
        }
        dma1<0>(o0, s0, dst);
        acc[0][1] = mfma32b(B.w1, B.x1, acc[0][1]);
        acc[1][0] = mfma32b(B.w2, B.x0, acc[1][0]);
        acc[1][1] = mfma32b(B.w2, B.x1, acc[1][1]); SCHED();
        dma1<1024>(o1, s0, dst);
        if constexpr (t == 1) { if (w == 0) { get_epoch(); signal_a(); } }
        SCHED();
      } else {
        acc[0][0] = mfma32b(B.w1, B.x0, acc[0][0]); acc[1][0] = mfma32b(B.w2, B.x0, acc[1][0]); SCHED();
        acc[0][1] = mfma32b(B.w1, B.x1, acc[0][1]); acc[1][1] = mfma32b(B.w2, B.x1, acc[1][1]); SCHED();
      }
    });
    auto fin = [&](const v16f& ag, const v16f& au, int rt) {
#pragma unroll
      for (int s2i = 0; s2i < 2; ++s2i) {
        v4u ov;
#pragma unroll
        for (int e = 0; e < 4; ++e) {
          const int r = s2i * 8 + 2 * e;
          v2f gg = v2f{relu(ag[r]), relu(ag[r + 1])} * v2f{au[r], au[r + 1]};
          ov[e] = pkbf(gg[0], gg[1]);
        }
        st16c((u32)(((p * 4 + sh * 2 + rt) * 112 + (j * 8 + fq * 2 + s2i)) * 1024 + l * 16), ws + OFF_H, ov);
      }
    };
    if (sh == 0) { fin(acc[0][0], acc[1][0], 0); fin(acc[0][1], acc[1][1], 1); }
    else { fin(acc[1][0], acc[0][0], 0); fin(acc[1][1], acc[0][1], 1); }
    segA3();
    WAIT_VM(4);
  } else {
    WAIT_VM(1);
    BARRIER();
    round1_read(); round1_store(); round1_max();
    if (tid == 0 && !coord) dma1s<0>(0u, BC, lbase + L_BC1);
    do_tick();
    if (w == 7) {
#pragma unroll
      for (int i = 0; i < 8; ++i) PIN(sq[i]);
      float war = bflo(sq[0]) + bflo(sq[2]), wai = bflo(sq[1]) - bflo(sq[3]);
      float wbr = bflo(sq[4]) + bflo(sq[6]), wbi = bflo(sq[5]) - bflo(sq[7]);
      v4f pv = {0.25f * (war + wbr), 0.25f * (wai + wbi), 0.25f * (war - wbr), 0.25f * (wai - wbi)};
      st16c((u32)((((sp >> 2) * 4 + (l & 3)) * 64 + (sp & 3) * 16 + (l >> 2)) * 16), ws + OFF_PQ, pv);
    }
    WAIT_VM(0);
    BARRIER();
    if (w == 0) { if (!coord) get_epoch(); signal_a(); do_tick(); WAIT_VM(0); }
    BARRIER();
    dma1c((u32)(sp * 57344 + (w * 112 + l) * 64), (const char*)Wd, lbase + 49152);
    dma1c((u32)(sp * 57344 + (w * 112 + 48 + l) * 64), (const char*)Wd, lbase + 49152);
    static_for<0, 6>([&](auto N) { pfl(N); });
    static_for<0, 6>([&](auto N) { issueW(N); });
    load_xe();
    trig2(); trig3(); segA0();
    if (w == 0) {
      check_a(v4u{~E, 0u, 0u, 0u});
    }
    BARRIER();
    load_s1(); load_wt(); load_rs();
    if (kd == 0) {
#pragma unroll
      for (int r = 0; r < 4; ++r) ld16c(pq[r], (u32)(w * 4096 + r * 1024 + l * 16), ws + OFF_PQ);
      pf_code();
      WAIT_VM(1);
    } else {
      pf_code();
      static_for<6, 14>([&](auto N) { pfl(N); });
      WAIT_VM(17);
    }
    segA1(); segA2(); segA3();
  }
  PIN(wt); PIN(xe); PIN(rs2);
  BARRIER();

  const u32 trG = (u32)(kq * 4096 + (4 * h + q) * 128 + ((dh ^ ((q >> 1) & 1)) << 6) + (blk ^ kh) * 32 + pp * 8);
  v8s Wf[14][2];
  auto readW = [&](auto N) {
    constexpr int n = N;
    LAS char* sb = smem + wslot(n) + trG;
    Wf[n][0] = cat8(trread(sb), trread(sb + 1024)); Wf[n][1] = cat8(trread(sb + 2048), trread(sb + 3072));
  };
  const char* hsrc = ws + OFF_H + ((size_t)(RG * 2 + (w >> 2)) * 112 + (w & 3) * 2) * 1024;
  const u32 so0 = (u32)((l >> 3) * S2P) + swz, so1 = so0 + 8 * S2P - 1024;
  const char* ssrc = ws + OFF_S2 + (size_t)(w * 16) * S2P + RG * 128;
  auto issueP = [&](int n, auto HALF) {
    constexpr int hf = HALF;
    if (n < 14) dma1s<hf * 1024>((u32)(l * 16), hsrc + n * 8192, lbase + aslot(n) + w * 2048);
    else dma1s<hf * 1024>(hf ? so1 : so0, ssrc + (size_t)(n - 14) * (128 * S2P), lbase + aslot(n) + w * 2048);
  };
  const int d = ct * 64 + dh * 32 + ((l & 31) ^ (kh << 4));
  const float gst = (float)d * (1.0f / 512.0f), ga0 = (float)(16 * kq + 2 * h) * gst;
  v8h G[8];
  auto gen = [&](auto FI) {
    constexpr int f = FI;
#pragma unroll
    for (int e2 = 0; e2 < 4; ++e2) {
      float a = fmaf((float)(64 * (f >> 1) + 8 * (f & 1) + 4 * (e2 >> 1) + (e2 & 1)), gst, ga0);
      float c = cosr(a), sn = sinr(a);
      if (f == 0 && e2 == 0) { if (kq == 0 && h == 0) { c = 0.5f; sn = (d & 1) ? 0.5f : -0.5f; } }
      G[f][2 * e2] = (_Float16)c; G[f][2 * e2 + 1] = (_Float16)sn;
    }
    PIN(G[f]);
  };

  float oscale;
  const u32 fhoff = (u32)(FL_H + (RG * 16 + (min(l, 55) >> 2)) * 64 + (l & 3) * 4);
  u32 fp;
  {
    v8h a;
    {
      LAS char* rp = smem + L_E1 + (4 * (g & 1) + (o >> 2)) * E1P + (2 * w + (g >> 1)) * 32 + (o & 3) * 8;
      a = __builtin_bit_cast(v8h, cat8(trread(rp), trread(rp + 8 * E1P)));
    }
    const v4u f8 = *(LAS v4u*)(smem + L_TAB + 1024 + l * 16);
    const v2u me = *(LAS v2u*)(smem + L_MX);
    issueW(6);
    static_for<0, 3>(readW);
    const int Ex = (int)me[0] < 16 ? 16 : ((int)me[0] > 240 ? 240 : (int)me[0]);
    const float wscale = __builtin_bit_cast(float, (u32)(246 - Ex) << 23);
    oscale = __builtin_bit_cast(float, (u32)(Ex - 7) << 23);
    re = mfma16h(__builtin_bit_cast(v8h, f8), a, z4); im = mfma16h(imf(f8), a, z4);
    static_for<3, 6>(readW);
    WAIT_VM(2);
    if (ffn && l == 0) st4c((u32)(FL_H + ((p * 2 + sh) * 16 + j) * 64 + fq * 4), FL, me[1]);
    issueW(7);
    if (kd != 0) {
#pragma unroll
      for (int r = 0; r < 4; ++r) {
        float fr = ((r & 1) ? bfhi(wt[r >> 1]) : bflo(wt[r >> 1])) * wscale, fi = ((r & 1) ? bfhi(wt[2 + (r >> 1)]) : bflo(wt[2 + (r >> 1)])) * wscale;
        float zr = re[r], zi = im[r];
        re[r] = zr * fr - zi * fi;
        im[r] = -(zr * fi + zi * fr);
      }
    } else {
      const int ka = 2 * w + (g >> 1), kr = o + 64 * (g & 1);
#pragma unroll
      for (int r = 0; r < 4; ++r) { v2f t = {re[r], im[r]}; *(LAS v2f*)(smem + L_ZX + ka * 1024 + (kr + 16 * r) * 8) = t; }
      BARRIER();
#pragma unroll
      for (int r = 0; r < 4; ++r) {
        PIN(pq[r]);
        const int km = (2048 - (ka + 16 * (kr + 16 * r))) & 2047;
        v2f zm = *(LAS v2f*)(smem + L_ZX + (km & 15) * 1024 + (km >> 4) * 8);
        float zr = re[r], zi = im[r];
        re[r] = (zr * pq[r][0] - zi * pq[r][1] + zm[0] * pq[r][2] + zm[1] * pq[r][3]) * wscale;
        im[r] = -(zr * pq[r][1] + zi * pq[r][0] + zm[0] * pq[r][3] - zm[1] * pq[r][2]) * wscale;
      }
    }
    LAS char* E2 = smem + (kd == 0 ? L_E1 : L_E2);
    a = pack_ri(re, im);
    re = mfma16h(a, __builtin_bit_cast(v8h, f8), z4); im = mfma16h(a, imf(f8), z4);
    twid(re, im, t3c, t3s);
    *(LAS v4h*)(E2 + (o & 7) * E2P + (2 * w + (o >> 3)) * 32 + g * 8) = pack4h(re);
    *(LAS v4h*)(E2 + (o & 7) * E2P + 512 + (2 * w + (o >> 3)) * 32 + g * 8) = pack4h(im);
    BARRIER();
    {
      LAS char* rp = E2 + w * E2P + o * 32 + g * 8;
      a = __builtin_bit_cast(v8h, cat8(*(LAS v4s*)rp, *(LAS v4s*)(rp + 512)));
    }
    issueW(8);
    re = mfma16h(a, __builtin_bit_cast(v8h, f16), z4); im = mfma16h(a, imf(f16), z4);
    issueW(9);
    twid(re, im, t1c, t1s);
    a = pack_ri(re, im);
    re = mfma16h(__builtin_bit_cast(v8h, f16), a, z4); im = mfma16h(imf(f16), a, z4);
    issueW(10);
    st8c((u32)(2 * kd * S2P + w * 512 + l * 8), ws + OFF_S2, pack4h(re));
    st8c((u32)((2 * kd + 1) * S2P + w * 512 + l * 8), ws + OFF_S2, pack4h(im));
  }
  gen(IC<0>{}); issueW(11);
  WAIT_VM(8);
  BARRIER();
  static_for<6, 9>(readW);
  gen(IC<1>{}); issueW(12); gen(IC<2>{}); issueW(13);
  if (!ffn) {
    WAIT_VM(6);
    BARRIER();
    if (tid == 0) st4c((u32)(FL_S + b * 4), FL, E);
  }
  if (w == 0) ld4c(fp, fhoff, FL);
  gen(IC<3>{}); gen(IC<4>{});
  WAIT_VM(0);
  if (w == 0) {
    PIN(fp);
    u32 f = fp;
    int spins = 0;
    while (!__all(f == E) && ++spins < SPIN_CAP) {
      __builtin_amdgcn_s_sleep(4);
      f = ld4c_sync(fhoff, FL);
    }
  }
  BARRIER();
  if (ffn && tid == 0) st4c((u32)(FL_S + b * 4), FL, E);
  issueP(0, IC<0>{}); issueP(0, IC<1>{}); issueP(1, IC<0>{}); issueP(1, IC<1>{});
  static_for<9, 14>(readW);
  gen(IC<5>{});
  issueP(2, IC<0>{}); issueP(2, IC<1>{});
  gen(IC<6>{});
  issueP(3, IC<0>{}); issueP(3, IC<1>{});
  gen(IC<7>{});

  const u32 trS = (u32)(kq * 4096 + (4 * h + q) * 128 + ((ph ^ ((q >> 1) & 1)) << 6) + blk * 32 + pp * 8);
  const u32 hb = (u32)(ph * 8192 + kq * 2048 + l * 16);
  v16f accG[2] = {}, accS[2] = {};
  v8s fa[8], fb[8];
  v4u fs;
  float gv[8];
  const u32 xw = (u32)(w * 6144 + l * 16);
  auto xchg_out = [&](const v16f (&ac)[2], int base) {
    LAS char* X = smem + base + xw;
#pragma unroll
    for (int i = 0; i < 6; ++i) {
      const v16f& a = ac[(i + 2) >> 2]; const int r = ((i + 2) & 3) * 4;
      *(LAS v4f*)(X + i * 1024) = v4f{a[r], a[r + 1], a[r + 2], a[r + 3]};
    }
  };
  auto xchg_in = [&](const v16f (&ac)[2], int base, float (&o8)[8]) {
    v4f s0 = {ac[0][0], ac[0][1], ac[0][2], ac[0][3]}, s1 = {ac[0][4], ac[0][5], ac[0][6], ac[0][7]};
#pragma unroll
    for (int x_ = 1; x_ < 4; ++x_) {
      LAS char* X = smem + base + (w ^ (2 * x_)) * 6144 + (x_ - 1) * 2048 + l * 16;
      s0 += *(LAS v4f*)X; s1 += *(LAS v4f*)(X + 1024);
    }
#pragma unroll
    for (int e = 0; e < 4; ++e) { o8[e] = s0[e]; o8[4 + e] = s1[e]; }
  };
  v4f xg[6];
  float ov[8], ex[8];
  auto sadd = [](float a, float b) { float r; asm("v_add_f32 %0, %1, %2" : "=v"(r) : "v"(a), "v"(b)); return r; };
  auto smul = [](float a, float b) { float r; asm("v_mul_f32 %0, %1, %2" : "=v"(r) : "v"(a), "v"(b)); return r; };
  auto silu_step = [&](int i) {
    if (i < 3) {
#pragma unroll
      for (int e = 0; e < 8; ++e) { ov[e] = sadd(i == 0 ? accG[0][e] : ov[e], xg[2 * i + (e >> 2)][e & 3]); PIN(ov[e]); }
    } else if (i == 3) {
#pragma unroll
      for (int e = 0; e < 8; e += 2) { u32 t = pkbf_o(smul(ov[e], rs2), smul(ov[e + 1], rs2)); ov[e] = bflo(t); ov[e + 1] = bfhi(t); PIN(ov[e]); PIN(ov[e + 1]); }
    } else if (i == 4) {
#pragma unroll
      for (int e = 0; e < 8; ++e) { ex[e] = __builtin_amdgcn_exp2f(ov[e] * -1.44269504088896f); PIN(ex[e]); }
    } else if (i == 5) {
#pragma unroll
      for (int e = 0; e < 8; ++e) { ex[e] = __builtin_amdgcn_rcpf(1.0f + ex[e]); PIN(ex[e]); }
    } else if (i == 6) {
#pragma unroll
      for (int e = 0; e < 8; e += 2) { u32 t = pkbf_o(ov[e] * ex[e], ov[e + 1] * ex[e + 1]); gv[e] = bflo(t); gv[e + 1] = bfhi(t); PIN(gv[e]); PIN(gv[e + 1]); }
    }
  };
  static_for<0, 10>([&](auto V) {
    constexpr int v = V;
    v8s(&cur)[8] = (v & 1) ? fb : fa;
    v8s(&prv)[8] = (v & 1) ? fa : fb;
    auto mm2 = [&](auto U, v8s (&fr)[8], int s) {
      constexpr int u = U;
      if constexpr (u >= 7) { accS[0] = mfma32h(G[(u - 7) * 4 + s], fr[2 * s], accS[0]); accS[1] = mfma32h(G[(u - 7) * 4 + s], fr[2 * s + 1], accS[1]); }
      else { accG[0] = mfma32b(Wf[2 * u + (s >> 1)][s & 1], fr[2 * s], accG[0]); accG[1] = mfma32b(Wf[2 * u + (s >> 1)][s & 1], fr[2 * s + 1], accG[1]); }
      SCHED();
    };
    if constexpr (v < 9) {
      WAIT_VM(v <= 7 ? 4 : 0);
      if constexpr (v == 5) { if (w == 0) { PIN(fs); poll4(fs, (u32)(FL_S + l * 16)); } }
      BARRIER();
      if constexpr (v == 4) { if (w == 0) ld16c(fs, (u32)(FL_S + l * 16), FL); }
      if constexpr (v >= 1) mm2(IC<(v >= 1 ? v - 1 : 0)>{}, prv, 2);
#pragma unroll
      for (int s = 0; s < 4; ++s) {
        if constexpr (v >= 7) {
          LAS char* sb = smem + aslot(2 * v + (s >> 1)) + (s & 1) * 2048;
          cur[2 * s] = cat8(trread(sb + trS), trread(sb + trS + 1024));
          cur[2 * s + 1] = cat8(trread(sb + (trS ^ 64)), trread(sb + (trS ^ 64) + 1024));
        } else {
          LAS char* sb = smem + aslot(2 * v + (s >> 1)) + (s & 1) * 1024;
          cur[2 * s] = *(LAS v8s*)(sb + hb);
          cur[2 * s + 1] = *(LAS v8s*)(sb + (hb ^ 8192));
        }
      }
      if constexpr (v == 8) {
#pragma unroll
        for (int x_ = 1; x_ < 4; ++x_) {
          LAS char* X = smem + 49152 + (w ^ (2 * x_)) * 6144 + (x_ - 1) * 2048 + l * 16;
          xg[2 * x_ - 2] = *(LAS v4f*)X; xg[2 * x_ - 1] = *(LAS v4f*)(X + 1024);
        }
      }
      SCHED();
      if constexpr (v <= 6) issueP(2 * v + 4, IC<0>{});
      if constexpr (v >= 1) mm2(IC<(v >= 1 ? v - 1 : 0)>{}, prv, 3);
      if constexpr (v <= 6) issueP(2 * v + 4, IC<1>{});
      if constexpr (v == 8) { silu_step(0); silu_step(1); SCHED(); }
      mm2(V, cur, 0);
      if constexpr (v <= 6) issueP(2 * v + 5, IC<0>{});
      if constexpr (v == 7) { xchg_out(accG, 49152); SCHED(); }
      if constexpr (v == 8) { silu_step(2); silu_step(3); SCHED(); }
      mm2(V, cur, 1);
      if constexpr (v <= 6) issueP(2 * v + 5, IC<1>{});
      if constexpr (v == 8) { silu_step(4); SCHED(); }
    } else {
      mm2(IC<8>{}, prv, 2);
      silu_step(5); SCHED();
      mm2(IC<8>{}, prv, 3);
      silu_step(6); SCHED();
    }
  });
  xchg_out(accS, 0);
  BARRIER();
  {
    float o8[8], pv[8];
    xchg_in(accS, 0, o8);
#pragma unroll
    for (int e = 0; e < 8; e += 2) { u32 t = pkbf_o(o8[e] * oscale, o8[e + 1] * oscale); pv[e] = bflo(t) * gv[e]; pv[e + 1] = bfhi(t) * gv[e + 1]; }
    u32 a0 = pkbf(pv[0], pv[1]), a1 = pkbf(pv[2], pv[3]), b0 = pkbf(pv[4], pv[5]), b1 = pkbf(pv[6], pv[7]);
    auto r0 = __builtin_amdgcn_permlane32_swap(a0, b0, false, false);
    auto r1 = __builtin_amdgcn_permlane32_swap(a1, b1, false, false);
    v4u pk = {r0[0], r1[0], r0[1], r1[1]};
    v4u ov4;
#pragma unroll
    for (int e = 0; e < 4; ++e) ov4[e] = pkbf(bflo(xe[e]) + bflo(pk[e]), bfhi(xe[e]) + bfhi(pk[e]));
    *(v4u*)((char*)out + eoff) = ov4;
  }
}

__global__ void kmain_end() { asm volatile("km_pf_b:"); }

extern "C" void kernel_launch(void* const* d_in, const int* in_sizes, int n_in, void* d_out, int out_size, void* d_ws,
                              size_t ws_size, hipStream_t stream) {
  kmain<<<256, 512, 0, stream>>>((const u16*)d_in[0], (const u16*)d_in[1], (const u16*)d_in[2], (const u16*)d_in[4],
                                 (const u16*)d_in[5], (const u16*)d_in[6], (char*)d_ws + WS_BASE(ws_size), (u16*)d_out);
}
```
